```python
import torch

def _softmax(x, axis=-1):
    return torch.softmax(x, dim=axis)
def _gelu(x):
    return torch.nn.functional.gelu(x, approximate="tanh")
def _silu(x):
    return torch.nn.functional.silu(x)
def _sigmoid(x):
    return torch.sigmoid(x)
def _layernorm(x, axis=-1, eps=1e-5):
    m = x.mean(dim=axis, keepdim=True)
    v = x.var(dim=axis, keepdim=True, unbiased=False)
    return (x - m) / torch.sqrt(v + eps)
def _scatter_add(tgt, idx, src, axis=0):
    return tgt.clone().index_add_(axis, idx, src)
def _segment_sum(src, seg, n):
    out = torch.zeros((n,) + src.shape[1:], dtype=src.dtype)
    return out.index_add_(0, seg, src)
def _pad(x, pw):
    flat = tuple(v for pair in reversed(pw) for v in pair)
    return torch.nn.functional.pad(x, flat)

kernel_name = 'recurrent_scan__sliding_local_dilated__glu_gated_geglu__prompt'

def setup_inputs():

    _w = (lambda a: (lambda t: t.to(torch.bfloat16) if t.is_floating_point() else t)(torch.as_tensor(a)))
    def _A_setup():
        B, S, D = 1, 2048, 1024
        return {
            "x": _w((lambda *s: torch.randn(*s, dtype=torch.bfloat16) * 0.05)(B, S, D)),
            "wqkv": _w((lambda *s: torch.randn(*s, dtype=torch.bfloat16) * 0.2)(D, 3 * D)),
            "wo": _w((lambda *s: torch.randn(*s, dtype=torch.bfloat16) * 0.2)(D, D)),
        }
    _A_in = _A_setup()

    def _B_setup():
        B, S, Dm, Df = 1, 2048, 1024, 3584
        return {
            "x": _w((lambda *s: torch.randn(*s, dtype=torch.bfloat16) * 0.05)(B, S, Dm)),
            "w_gate": _w((lambda *s: torch.randn(*s, dtype=torch.bfloat16) * 0.1)(Dm, Df) * (Dm ** -0.5)),
            "w_up": _w((lambda *s: torch.randn(*s, dtype=torch.bfloat16) * 0.1)(Dm, Df) * (Dm ** -0.5)),
            "w_down": _w((lambda *s: torch.randn(*s, dtype=torch.bfloat16) * 0.1)(Df, Dm) * (Df ** -0.5)),
        }
    _B_in = _B_setup()

    return {
        "A_x": _A_in["x"],
        "A_wqkv": _A_in["wqkv"],
        "A_wo": _A_in["wo"],
        "B_x": _B_in["x"],
        "B_w_gate": _B_in["w_gate"],
        "B_w_up": _B_in["w_up"],
        "B_w_down": _B_in["w_down"],
    }

def reference(A_x, A_wqkv, A_wo, B_x, B_w_gate, B_w_up, B_w_down):
    def _A_ref(x, wqkv, wo):
        B, S, D = x.shape
        H = 32; Dh = D // H
        W, dil = 8, 2
        qkv = (lambda x, *d: x.permute(*d))((x @ wqkv).reshape(B, S, 3, H, Dh), 2, 0, 3, 1, 4)
        q, k, v = qkv[0], qkv[1], qkv[2]
        scale = Dh ** -0.5
        pos = torch.arange(S)
        off = torch.arange(W) * dil
        idx = pos[:, None] - off[None, :]
        mask = idx >= 0
        idx_c = torch.where(mask, idx, 0).reshape(-1)
        kg = k[:, :, idx_c, :].reshape(B, H, S, W, Dh)
        vg = v[:, :, idx_c, :].reshape(B, H, S, W, Dh)
        logits = torch.einsum("bhsd,bhswd->bhsw", q, kg) * scale
        logits = torch.where(mask[None, None, :, :], logits, -1e9)
        attn = _softmax(logits, axis=-1)
        o = torch.einsum("bhsw,bhswd->bhsd", attn, vg)
        return (lambda x, *d: x.permute(*d))(o, 0, 2, 1, 3).reshape(B, S, D) @ wo

    def _B_ref(x, w_gate, w_up, w_down):
        g = torch.einsum("bsd,df->bsf", x, w_gate)
        u = torch.einsum("bsd,df->bsf", x, w_up)
        h = _gelu(g) * u
        return torch.einsum("bsf,fd->bsd", h, w_down)

    x = A_x
    xn = _layernorm(x, axis=-1)
    s = _A_ref(xn, A_wqkv, A_wo)
    g = _silu(_B_ref(xn, B_w_gate, B_w_up, B_w_down))
    return x + s * g
```

```cpp
#include <hip/hip_runtime.h>
#include <hip/hip_bf16.h>
#include <stdint.h>
#include <type_traits>

typedef unsigned short u16;
using bf16x8 = __attribute__((ext_vector_type(8))) short;
using bf16x4 = __attribute__((ext_vector_type(4))) short;
using f32x4 = __attribute__((ext_vector_type(4))) float;
using u32x2 = __attribute__((ext_vector_type(2))) unsigned;
using u32x4 = __attribute__((ext_vector_type(4))) unsigned;
typedef __attribute__((address_space(3))) const char* lds_cptr;

constexpr int S_ = 2048, D_ = 1024, F_ = 3584;

__device__ __forceinline__ float bf2f(u16 h) { return __uint_as_float((unsigned)h << 16); }
__device__ __forceinline__ u16 f2bf(float f) {
  unsigned u = __float_as_uint(f);
  u += 0x7fffu + ((u >> 16) & 1u);
  return (u16)(u >> 16);
}
__device__ __forceinline__ float rbf(float f) { return bf2f(f2bf(f)); }
typedef __attribute__((ext_vector_type(2))) __bf16 bf16x2_t;
typedef __attribute__((ext_vector_type(2))) float f32x2;
__device__ __forceinline__ unsigned pack2(float a, float b) {
  bf16x2_t r = __builtin_convertvector((f32x2){a, b}, bf16x2_t);
  return __builtin_bit_cast(unsigned, r);
}
__device__ __forceinline__ float lo_f(unsigned p) { return __uint_as_float(p << 16); }
__device__ __forceinline__ float hi_f(unsigned p) { return __uint_as_float(p & 0xffff0000u); }

#define TSDECL
#define TSS(i) do {} while (0)
#define TSFLUSH(k) do {} while (0)
#define TS1(i) TSS(i)
#define TS(i) TSS(i)
#define WAIT_V(n) asm volatile("s_waitcnt vmcnt(%0)" ::"n"(n) : "memory")
#define WAIT_L(n) asm volatile("s_waitcnt lgkmcnt(%0)" ::"n"(n) : "memory")
#define BARL() do { WAIT_L(0); BAR(); } while (0)
#define BAR() do { asm volatile("" ::: "memory"); __builtin_amdgcn_s_barrier(); asm volatile("" ::: "memory"); } while (0)

__device__ __forceinline__ bf16x4 tr_read(lds_cptr p) {
  return __builtin_amdgcn_ds_read_tr16_b64_v4i16((__attribute__((address_space(3))) bf16x4*)p);
}
__device__ __forceinline__ bf16x8 tr_frag(lds_cptr p) {
  bf16x4 lo = tr_read(p), hi = tr_read(p + 512);
  bf16x8 r;
  r[0] = lo[0]; r[1] = lo[1]; r[2] = lo[2]; r[3] = lo[3];
  r[4] = hi[0]; r[5] = hi[1]; r[6] = hi[2]; r[7] = hi[3];
  return r;
}
__device__ __forceinline__ void glds16(const void* gsrc, unsigned lds_dst) {
  unsigned keep;
  asm volatile("s_mov_b32 %0, m0\n\ts_mov_b32 m0, %2\n\ts_nop 0\n\tglobal_load_lds_dwordx4 %1, off\n\ts_mov_b32 m0, %0"
               : "=&s"(keep) : "v"(gsrc), "s"(lds_dst) : "memory");
}
#define GLDS(gptr, laddr) glds16((const void*)(gptr), (unsigned)(laddr))
__device__ __forceinline__ void glds16_sc1(const void* gsrc, unsigned lds_dst) {
  unsigned keep;
  asm volatile("s_mov_b32 %0, m0\n\ts_mov_b32 m0, %2\n\ts_nop 0\n\tglobal_load_lds_dwordx4 %1, off sc1\n\ts_mov_b32 m0, %0"
               : "=&s"(keep) : "v"(gsrc), "s"(lds_dst) : "memory");
}
#define GLDS_SC1(gptr, laddr) glds16_sc1((const void*)(gptr), (unsigned)(laddr))
__device__ __forceinline__ void glds16_nc(const void* gsrc, unsigned lds_dst) {
  unsigned keep;
  asm volatile("s_mov_b32 %0, m0\n\ts_mov_b32 m0, %2\n\ts_nop 0\n\tglobal_load_lds_dwordx4 %1, off\n\ts_mov_b32 m0, %0"
               : "=&s"(keep) : "v"(gsrc), "s"(lds_dst));
}
#define GLDS_NC(gptr, laddr) glds16_nc((const void*)(gptr), (unsigned)(laddr))
__device__ __forceinline__ void glds16_s(const void* sbase, unsigned voff, unsigned lds_dst) {
  asm volatile("s_mov_b32 m0, %2\n\ts_nop 0\n\tglobal_load_lds_dwordx4 %0, %1" :: "v"(voff), "s"(sbase), "s"(lds_dst) : "m0");
}

typedef unsigned long long u64;
__device__ __forceinline__ bool fresh(u64 stamp, u64 t_own) {
  return stamp - t_own - 100ull < __builtin_amdgcn_s_memrealtime() - t_own;
}
constexpr u64 TIMEOUT_ = 200000;
__device__ __forceinline__ float half_sum(float v) {
  v += __builtin_bit_cast(float, __builtin_amdgcn_update_dpp(0, __builtin_bit_cast(int, v), 0xB1, 0xf, 0xf, false));
  v += __builtin_bit_cast(float, __builtin_amdgcn_update_dpp(0, __builtin_bit_cast(int, v), 0x4E, 0xf, 0xf, false));
  v += __builtin_bit_cast(float, __builtin_amdgcn_update_dpp(0, __builtin_bit_cast(int, v), 0x141, 0xf, 0xf, false));
  v += __builtin_bit_cast(float, __builtin_amdgcn_update_dpp(0, __builtin_bit_cast(int, v), 0x140, 0xf, 0xf, false));
  { auto r = __builtin_amdgcn_permlane16_swap(__float_as_uint(v), __float_as_uint(v), false, false); v = __uint_as_float(r[0]) + __uint_as_float(r[1]); }
  return v;
}
__device__ __forceinline__ unsigned pack_stats(float mean, float var) {
  return (unsigned)f2bf(mean) | ((unsigned)f2bf(sqrtf(rbf(rbf(var) + 1e-5f))) << 16);
}
__device__ __forceinline__ unsigned row_stats_serial(const u16* xr) {
  float s = 0.f, q = 0.f;
#pragma unroll 1
  for (int k = 0; k < D_; ++k) s += bf2f(xr[k]);
  const float mean = s * (1.0f / D_);
#pragma unroll 1
  for (int k = 0; k < D_; ++k) { const float d = bf2f(xr[k]) - mean; q += d * d; }
  return pack_stats(mean, q * (1.0f / D_));
}
constexpr int G1_STAGE = 32768 + 5 * 8192;
#define PF_K0 4
#define CP_K0 5
#define DLT 1
#define PSLEEP 2
#define PUB_K 5

#define RLX_AGENT __ATOMIC_RELAXED, __HIP_MEMORY_SCOPE_AGENT

constexpr int G2_STAGE = 16384 + 2 * 8192;
constexpr int G2_NSTAGE = 5;
constexpr size_t MB_ = 1024 * 1024;
__global__ __launch_bounds__(512, 2) void fused_kernel(const u16* __restrict__ x, const u16* __restrict__ wqkv,
                                                       const u16* __restrict__ wg, const u16* __restrict__ wu,
                                                       const u16* __restrict__ wd, const u16* __restrict__ wo,
                                                       u16* __restrict__ out, char* __restrict__ ws) {
  u16* const halo = (u16*)(ws + 4 * MB_);
  u16* const abuf = (u16*)(ws + 16 * MB_);
  char* const slab = ws + 64 * MB_;
  unsigned* const flags = (unsigned*)(ws + 96 * MB_);
  u32x4* const rstat = (u32x4*)(ws + 97 * MB_);
  char* const wcopy = ws + 98 * MB_;
  u64* const done1 = (u64*)(ws + 100 * MB_);
  u64* const done2 = (u64*)(ws + 100 * MB_ + 65536);
  u64* const donec = (u64*)(ws + 100 * MB_ + 131072);
  u64* const acks = (u64*)(ws + 100 * MB_ + 196608);
  u64* const cstamp = (u64*)(ws + 100 * MB_ + 262144);
  float* const csum = (float*)(ws + 101 * MB_);
  __shared__ __attribute__((aligned(1024))) char shm[163840];
  constexpr int AUX = 2 * G1_STAGE + 8192;
  {
    u64 pc; int len;
    asm volatile("s_getpc_b64 %0\n.Lcode_beg:\n\ts_mov_b32 %1, .Lcode_end-.Lcode_beg" : "=s"(pc), "=s"(len));
    const int ol = ((blockIdx.x >> 3) + 32 * (threadIdx.x >> 6)) * 1024 + (threadIdx.x & 63) * 16;
    asm volatile("s_mov_b32 m0, %2\n\ts_nop 0\n\tglobal_load_lds_dwordx4 %0, %1"
                 :: "v"(ol < len - 16 ? ol : 0), "s"(pc & ~15ull), "s"((unsigned)(uintptr_t)(lds_cptr)shm + 2 * G1_STAGE + 7168) : "m0");
    __builtin_amdgcn_sched_barrier(0);
  }
  const u64 t_own = __builtin_amdgcn_s_memrealtime();
  TSDECL;
  TS1(7);
  const int tid = threadIdx.x, lane = tid & 63;
  const int wid = __builtin_amdgcn_readfirstlane(tid >> 6);
  const int wm = wid & 3, wc = wid >> 2;
  const int bid = blockIdx.x, xcd = bid & 7, idx = bid >> 3;
  const int ntile = xcd * 4 + (idx & 3), mtile = idx >> 2;
  const int m0 = mtile * 256;
  const int hg = ntile >> 1;
  const bool tya = (ntile & 1) == 0;
  const int srow = m0 + ntile * 8 + wm * 2;
  lds_cptr shm3 = (lds_cptr)shm;
  const unsigned shm_base = (unsigned)(uintptr_t)shm3;
  const int r8 = tid >> 3, pos = tid & 7;
  const int phys = pos >> 1, half = pos & 1;
  const int fk = ((r8 >> 1) & 1) | (((r8 >> 3) & 1) << 1);
  const int vsrc = phys ^ fk;
  const int qstr = tya ? 3072 * 2 : F_ * 2;
  unsigned voa = (((r8 & 32) + 2 * (r8 & 15) + ((r8 >> 4) & 1)) * D_ + ((pos ^ (r8 & 7)) * 8)) * 2;
  unsigned vog = r8 * (F_ * 2) + vsrc * 32 + half * 16, voq = r8 * qstr + vsrc * 32 + half * 16;
  unsigned vom = r8 * qstr + (vsrc >> (tya ? 0 : 1)) * 32 + half * 16, vol = lane * 16;
  asm volatile("" : "+v"(voa), "+v"(vog), "+v"(voq), "+v"(vom), "+v"(vol));
  asm volatile("" :: "s"(x), "s"(wqkv), "s"(wg), "s"(wu), "s"(wd), "s"(wo), "s"(out), "s"(ws));
  const char* const ab = (const char*)(x + (size_t)m0 * D_);
  int kz = 0;
  asm volatile("" : "+s"(kz));
  auto stage_a = [&](int buf, int kt, int i, int h) {
    glds16_s(ab + (long)(((i * 64 + (h - 1) * 32) * D_ + kt * 64) * 2), voa, shm_base + buf * G1_STAGE + (wid - 4 + 4 * h) * 1024 + i * 8192);
  };
  if (wid >= 4) {
#pragma unroll
    for (int i = 0; i < 8; ++i) stage_a(0, kz, i >> 1, i & 1);
  } else {
#pragma unroll
    for (int i = 0; i < 4; ++i) glds16_s(x + (size_t)srow * D_ + i * 512, vol, shm_base + G1_STAGE + wid * 4096 + i * 1024);
  }
  const int kt0 = (hg >> 1) * 7, he = hg & 1;
  const char* const gb = (const char*)(wg + (kt0 + (tya ? 5 + he : 2 * he + 1)) * 64);
  const char* const qb = tya ? (const char*)(wqkv + 64 * hg) : gb - 128;
  const long dgu = (const char*)wu - (const char*)wg, dp1 = tya ? 2048 : dgu;
  const char* const mb0 = tya ? qb + 4096 : (const char*)(wg + (kt0 + 4) * 64 + 32 * he);
  const char* mptr = mb0 + (((vsrc & 1) && !tya) ? dgu : 0) + vom;
  auto wload = [&](int p, int rows, unsigned dst) {
    long off = (long)(rows * (p < 3 ? qstr : F_ * 2)) + (p == 1 ? dp1 : (p == 4 ? dgu : 0));
    if (p == 2) { asm volatile("" : "+s"(off)); GLDS_NC(mptr + off, dst); }
    else glds16_s((p < 2 ? qb : gb) + off, p < 2 ? voq : vog, dst);
  };
  const int fr = lane & 15, fq = lane >> 4, q4 = fr >> 2, pp = lane & 3;
  const int ff = ((q4 >> 1) & 1) | ((fq & 1) << 1);
  int aoff[2], boff[2];
#pragma unroll
  for (int ks = 0; ks < 2; ++ks) aoff[ks] = (wm * 64 + fr) * 128 + (((ks * 4 + fq) ^ (fr & 7)) * 16);
#pragma unroll
  for (int j = 0; j < 2; ++j) boff[j] = 32768 + (8 * fq + q4) * 128 + (((2 * wc + j) ^ ff) * 32) + pp * 8;

  f32x4 acc[4][10];
#pragma unroll
  for (int a = 0; a < 4; ++a)
#pragma unroll
    for (int b = 0; b < 10; ++b) acc[a][b] = f32x4{0.f, 0.f, 0.f, 0.f};

  auto stage_part = [&](int buf, int kt, int i, int h) {
    if (i < 5) wload(i, kt * 64 + (h - 1) * 32, shm_base + buf * G1_STAGE + (wid - 4 + 4 * h) * 1024 + 32768 + i * 8192);
    else stage_a(buf, kt, i - 5, h);
  };
  constexpr int NT = D_ / 64;
  constexpr int CNT = AUX + 4096 + 3584;
  if (tid < 8) *(unsigned*)(shm + CNT + tid * 4) = 0u;
  bf16x8 af0[4], af1[4];
  bf16x4 bl[4], bh[4];
#pragma unroll
  for (int i = 0; i < 4; ++i) { af0[i] = bf16x8{0, 0, 0, 0, 0, 0, 0, 0}; af1[i] = af0[i]; bl[i] = bf16x4{0, 0, 0, 0}; bh[i] = bf16x4{0, 0, 0, 0}; }
#define SB0() __builtin_amdgcn_sched_barrier(0)
  auto mm = [&](int g, int t) {
    const int mi = t >> 1, mf = ((g & 1) && g != 9) ? 3 - mi : mi, ks = (t & 1) ^ (mi & 1), s = ((g + 1) & 1) * 2 + ks;
    const bf16x8 b = __builtin_shufflevector(bl[s], bh[s], 0, 1, 2, 3, 4, 5, 6, 7);
    if ((g >> 1) == 2) acc[mf][g] = __builtin_amdgcn_mfma_f32_16x16x32_bf16(ks ? af1[mf] : af0[mf], b, acc[mf][g], 0, 0, 0);
    else acc[mf][g] = __builtin_amdgcn_mfma_f32_16x16x32_bf16(b, ks ? af1[mf] : af0[mf], acc[mf][g], 0, 0, 0);
  };
  auto kloop = [&](auto wmc) {
    constexpr bool LD = decltype(wmc)::value != 0;
    const int WID = wid;
    constexpr int DMP = 2 * G1_STAGE;
    const char* pa = nullptr;
    unsigned vob = 0;
    f32x4 ca = {0.f, 0.f, 0.f, 0.f}, cb = {0.f, 0.f, 0.f, 0.f};
    bf16x8 sel = {0, 0, 0, 0, 0, 0, 0, 0};
    int tro = 0;
    u64 tg = 0;
    const int psh = (wid == 2 && !tya) ? 1 : 0, pstr = wid < 3 ? qstr : F_ * 2;
    const char* const pbase = wid == 0 ? qb : (wid == 1 ? qb + dp1 : (wid == 2 ? mb0 : gb));
    if (!LD) {
      const int rs = mtile * 8 + (lane >> 3), vs = ((lane & 7) >> 1) ^ (((rs >> 1) & 1) | ((mtile & 1) << 1));
      pa = pbase + ((psh & vs) ? dgu : 0) + (rs * pstr + (vs >> psh) * 32 + (lane & 1) * 16);
      vob = rs * (F_ * 2) + vs * 32 + (lane & 1) * 16;
      const short one = fr == fq ? 0x3f80 : 0;
      sel = bf16x8{one, one, one, one, one, one, one, one};
      tro = q4 * 128 + ((fq ^ ((q4 >> 1) | ((mtile & 1) << 1))) * 32) + pp * 8;
      asm volatile("" : "+v"(sel), "+v"(tro), "+v"(ca), "+v"(cb));
    }
    auto share = [&](int t, unsigned dsta, unsigned dstb) {
      long off = (long)(t * 64 * pstr);
      asm volatile("" : "+s"(off));
      GLDS_NC(pa + off, shm_base + dsta);
      if (wid == 0) glds16_s(gb + dgu + (long)(t * 64 * F_ * 2), vob, shm_base + dstb);
    };
    auto accum = [&](int oa, int ob) {
      ca = __builtin_amdgcn_mfma_f32_16x16x32_bf16(tr_frag(shm3 + tro + oa), sel, ca, 0, 0, 0);
      if (wid == 0) cb = __builtin_amdgcn_mfma_f32_16x16x32_bf16(tr_frag(shm3 + tro + ob), sel, cb, 0, 0, 0);
    };
    constexpr int CG = 61440;
    auto cgather = [&]() {
      int l = threadIdx.x & 63;
      asm volatile("s_memrealtime %0\n\ts_waitcnt lgkmcnt(0)" : "=s"(tg), "+v"(l) :: "memory");
      if (l < 4) GLDS_SC1((const char*)(cstamp + (ntile * 4 + wid) * 8) + l * 16, shm_base + CG + 10240 + wid * 64);
#pragma unroll
      for (int i = 0; i < 2; ++i) {
        GLDS_SC1((const char*)(csum + (ntile * 5 + wid) * 512) + i * 1024 + l * 16, shm_base + CG + wid * 2048 + i * 1024);
        if (wid == 0) GLDS_SC1((const char*)(csum + (ntile * 5 + 4) * 512) + i * 1024 + l * 16, shm_base + CG + 8192 + i * 1024);
      }
    };
    if (LD) {
#pragma unroll
      for (int i = 0; i < 10; ++i) {
        stage_part(0, kz, i >> 1, i & 1);
        asm volatile("" : "+v"(acc[0][i]), "+v"(acc[1][i]), "+v"(acc[2][i]), "+v"(acc[3][i]));
      }
    }
    unsigned st0 = 0, st1 = 0;
    if (!LD) {
#pragma unroll
      for (int a = 0; a < 4; ++a)
        asm volatile("" : "+v"(acc[a][0]), "+v"(acc[a][1]), "+v"(acc[a][2]), "+v"(acc[a][3]), "+v"(acc[a][4]), "+v"(acc[a][5]), "+v"(acc[a][6]),
                     "+v"(acc[a][7]), "+v"(acc[a][8]), "+v"(acc[a][9]));
      WAIT_V(0);
      const char* const xr = shm + G1_STAGE + WID * 4096 + (lane >> 5) * 2048 + (lane & 31) * 16;
      const u32x4 xv0 = *(const u32x4*)xr, xv1 = *(const u32x4*)(xr + 512), xv2 = *(const u32x4*)(xr + 1024), xv3 = *(const u32x4*)(xr + 1536);
      f32x2 f[16];
#pragma unroll
      for (int i = 0; i < 4; ++i) {
        f[i] = f32x2{lo_f(xv0[i]), hi_f(xv0[i])}; f[4 + i] = f32x2{lo_f(xv1[i]), hi_f(xv1[i])};
        f[8 + i] = f32x2{lo_f(xv2[i]), hi_f(xv2[i])}; f[12 + i] = f32x2{lo_f(xv3[i]), hi_f(xv3[i])};
      }
      f32x2 sa = f[0], sb = f[1];
#pragma unroll
      for (int i = 2; i < 16; i += 2) { sa += f[i]; sb += f[i + 1]; }
      sa += sb;
      const float mean = half_sum(sa[0] + sa[1]) * (1.0f / D_);
      f32x2 qa = {0.f, 0.f}, qb = {0.f, 0.f};
#pragma unroll
      for (int i = 0; i < 16; i += 2) {
        const f32x2 d = f[i] - f32x2{mean, mean}, e = f[i + 1] - f32x2{mean, mean};
        qa = __builtin_elementwise_fma(d, d, qa); qb = __builtin_elementwise_fma(e, e, qb);
      }
      qa += qb;
      const unsigned ps = pack_stats(mean, half_sum(qa[0] + qa[1]) * (1.0f / D_));
      st0 = __builtin_amdgcn_readlane(ps, 0);
      st1 = __builtin_amdgcn_readlane(ps, 32);
    }
    asm volatile("" : "+v"(aoff[0]), "+v"(aoff[1]), "+v"(boff[0]), "+v"(boff[1]));
    TS1(0);
#pragma clang loop unroll(disable)
    for (int kt = 0; kt < NT; ++kt) {
      const int cur = kt & 1;
      if (LD) WAIT_V(0);
      BARL();
      lds_cptr sb = shm3;
      if (LD && kt + 1 < NT) {
#pragma unroll
        for (int i = 0; i < 18; ++i) stage_part(cur ^ 1, kt + 1, i >> 1, i & 1);
      }
      if (!LD && kt == NT - 1) cgather();
      auto rdb = [&](int g, int i) {
        lds_cptr p = sb + boff[g & 1] + (g >> 1) * 8192 + (i >> 1) * 4096;
        const int s = ((g + 1) & 1) * 2 + (i >> 1);
        if (i & 1) bh[s] = tr_read(p + 512); else bl[s] = tr_read(p);
      };
      auto rda = [&](int ks, int mf) { (ks ? af1 : af0)[mf] = *(const __attribute__((address_space(3))) bf16x8*)(sb + aoff[ks] + mf * 2048); };
      SB0();
      __builtin_amdgcn_s_waitcnt(0xC07F);
#pragma unroll
      for (int gg = 0; gg < 10; ++gg) {
        const int g = (gg + 9) % 10;
#pragma unroll
        for (int t = 0; t < 8; ++t) {
          SB0();
          mm(g, t);
          SB0();
          if (g == 9) {
            if (t == 0 || t == 2) { rdb(0, t); rdb(0, t + 1); }
            if (t & 1) { rda((t >> 1) & 1, t >> 1); rda(((t >> 1) & 1) ^ 1, t >> 1); }
          } else if (t < 4) rdb(g + 1, t);
        }
      }
      SB0();
      {
        const int d = cur ? -G1_STAGE : G1_STAGE;
        aoff[0] += d; aoff[1] += d; boff[0] += d; boff[1] += d;
        asm volatile("" : "+v"(aoff[0]), "+v"(aoff[1]), "+v"(boff[0]), "+v"(boff[1]));
      }
      if (!LD) {
        int lane = threadIdx.x & 63;
        asm volatile("" : "+v"(lane));
        const unsigned vol = lane * 16;
        if (kt >= 1 && kt <= 12) { WAIT_V(0); accum(DMP + wid * 1024, DMP + 4096); WAIT_L(0); }
        if (kt <= 1) share(kt + 2, DMP + 7168, DMP + 7168);
        if (kt <= 11) share(kt + 4, DMP + wid * 1024, DMP + 4096);
        if (kt <= 3) {
          const int sp = cur * G1_STAGE + 32768 + mtile * 1024;
          accum(sp + wid * 8192, sp + 4 * 8192);
        }
        if (kt == 12 && (lane & 15) < 4) {
          const __amdgpu_buffer_rsrc_t rc = __builtin_amdgcn_make_buffer_rsrc((void*)(csum + ntile * 5 * 512 + mtile * 64), 0, 5 * 2048, 0x00020000);
          const int co = (lane & 15) * 64 + (lane >> 4) * 16;
          __builtin_amdgcn_raw_buffer_store_b128(__builtin_bit_cast(u32x4, ca), rc, co + wid * 2048, 0, 16);
          if (wid == 0) __builtin_amdgcn_raw_buffer_store_b128(__builtin_bit_cast(u32x4, cb), rc, co + 4 * 2048, 0, 16);
        }
        if (kt == 13) {
          WAIT_V(0);
          u64 now;
          asm volatile("s_memrealtime %0\n\ts_waitcnt lgkmcnt(0)" : "=s"(now) :: "memory");
          if (lane == 0) __hip_atomic_store(&cstamp[(ntile * 4 + wid) * 8 + mtile], now, RLX_AGENT);
        }
        if (kt >= CP_K0 && kt < CP_K0 + 3) {
          int ln = threadIdx.x & 63;
          asm volatile("" : "+v"(ln));
          const int q1 = bid * 4 + WID + (kt - CP_K0) * 1024, q0 = q1 - 1024;
          if (q0 >= 0 && q0 < 1152) {
            const __amdgpu_buffer_rsrc_t rc = __builtin_amdgcn_make_buffer_rsrc((void*)(wcopy + (q0 & 1) * 589824 + (q0 >> 1) * 1024), 0, 1024, 0x00020000);
            __builtin_amdgcn_raw_buffer_store_b128(*(const u32x4*)(shm + AUX + WID * 1024 + ln * 16), rc, ln * 16, 0, 16);
            WAIT_L(0);
          }
          if (q1 < 1152) {
            const char* src = (const char*)((q1 >> 1) < 448 ? wd + (size_t)(q1 >> 1) * 8 * D_ : wo + (size_t)((q1 >> 1) - 448) * 8 * D_);
            src += ((3 - ((unsigned)(uintptr_t)src >> 7)) & 7) * 128 + (q1 & 1) * 1024;
            GLDS(src + (ln >> 3) * 2048 + (ln & 7) * 16, shm_base + AUX + WID * 1024);
          }
        }
        if (WID == 0 && kt == CP_K0 + 4) {
          u64 now;
          asm volatile("s_memrealtime %0\n\ts_waitcnt lgkmcnt(0)" : "=s"(now) :: "memory");
          if (lane == 0) __hip_atomic_store(&donec[bid], now, RLX_AGENT);
        }
        if (kt >= PF_K0 && kt < PF_K0 + 5) {
#pragma unroll
          for (int i = 0; i < 2; ++i) {
            const int c = (bid * 4 + wid) * 10 + (kt - PF_K0) * 2 + i;
            if (c < 9216) {
              const u16* src = (c < 7168 ? wd + (size_t)c * 512 : wo + (size_t)(c - 7168) * 512) + lane * 8;
              GLDS_NC(src, shm_base + DMP + 7168);
            }
          }
        }
        if (kt == PUB_K) {
          u64 now;
          asm volatile("s_memrealtime %0\n\ts_waitcnt lgkmcnt(0)" : "=s"(now) :: "memory");
          if (lane == 0) {
            const __amdgpu_buffer_rsrc_t rs = __builtin_amdgcn_make_buffer_rsrc((void*)(rstat + srow), 0, 32, 0x00020000);
            __builtin_amdgcn_raw_buffer_store_b128(u32x4{st0, (unsigned)now, st0, (unsigned)(now >> 32)}, rs, 0, 0, 16);
            __builtin_amdgcn_raw_buffer_store_b128(u32x4{st1, (unsigned)now, st1, (unsigned)(now >> 32)}, rs, 16, 0, 16);
          }
        }
        if (kt == NT - 3) GLDS_SC1(rstat + m0 + wid * 64 + (lane & 32) + 2 * (lane & 15) + ((lane >> 4) & 1), shm_base + AUX + wid * 1024);
        if (WID == 2 && kt == 8) GLDS_SC1((const char*)(done1 + (bid & ~3) * 32) + lane * 16, shm_base + DMP + 7168);
        if (WID >= 2 && kt == NT - 3) GLDS_SC1((const char*)donec + (WID - 2) * 1024 + lane * 16, shm_base + DMP + WID * 2048);
        if (kt == NT - 2) {
          WAIT_V(0);
          int ln = threadIdx.x;
          asm volatile("" : "+v"(ln));
          if (WID >= 2) {
            const u32x4 c4 = *(const u32x4*)(shm + DMP + WID * 1024 + ln * 16);
            const bool ok = __all(fresh(((u64)c4[1] << 32) | c4[0], t_own) & fresh(((u64)c4[3] << 32) | c4[2], t_own));
            *(unsigned*)(shm + CNT - 4 + WID * 4) = ok ? 1u : 0u;
          }
          u32x4 gr = *(const u32x4*)(shm + AUX + ln * 16);
          if (__builtin_expect(!__all((gr[0] == gr[2]) & fresh(((u64)gr[3] << 32) | gr[1], t_own)), 0)) {
            const __amdgpu_buffer_rsrc_t rs = __builtin_amdgcn_make_buffer_rsrc((void*)(rstat + m0), 0, 4096, 0x00020000);
            const int prow = (ln & 0xe0) + 2 * (ln & 15) + ((ln >> 4) & 1);
            for (;;) {
              gr = __builtin_bit_cast(u32x4, __builtin_amdgcn_raw_buffer_load_b128(rs, prow * 16, 0, 16));
              if (__all((gr[0] == gr[2]) & fresh(((u64)gr[3] << 32) | gr[1], t_own))) break;
              if (__builtin_amdgcn_s_memrealtime() - t_own > TIMEOUT_) { gr[0] = row_stats_serial(x + (size_t)(m0 + prow) * D_); break; }
              __builtin_amdgcn_s_sleep(2);
            }
          }
          const float nm = -lo_f(gr[0]), dn = hi_f(gr[0]), r = __builtin_amdgcn_rcpf(dn);
          const f32x4 fc = {nm, -2.3022081986f * r, -0.10294324f * r * r * r, dn * dn};
          *(f32x4*)(shm + AUX + ln * 16) = fc;
          float* const tw = (float*)(shm + 2 * G1_STAGE + WID * 768) + ln;
#pragma unroll
          for (int i = 0; i < 4; ++i) tw[i * 64] = fc[i];
          ((float*)(shm + AUX + 4096))[384 + ln] = r;
        }
      }
      SB0();
    }
#pragma unroll
    for (int t = 0; t < 8; ++t) mm(9, t);
    if (!LD) {
      SB0();
      int lane = threadIdx.x & 63;
      asm volatile("s_waitcnt vmcnt(0)" : "+v"(lane) :: "memory");
      bool late = false;
      while (__builtin_expect(!__all(*(const u64*)(shm + CG + 10240 + wid * 64 + (lane & 7) * 8) - t_own - 100ull < tg - t_own - 200ull), 0)) {
        late = __builtin_amdgcn_s_memrealtime() - t_own > TIMEOUT_;
        if (late) break;
        __builtin_amdgcn_s_sleep(2);
        cgather();
        WAIT_V(0);
      }
      const char* const sc = shm + CG + (lane < 32 ? wid * 2048 : 8192 - 256) + lane * 8;
      f32x2 t = *(const f32x2*)sc;
#pragma unroll
      for (int r = 1; r < 8; ++r) t += *(const f32x2*)(sc + r * 256);
      if (__builtin_expect(late, 0)) {
        const int gq = (lane >> 3) & 3;
        const char* pc = lane < 32 ? pbase + ((psh & gq) ? dgu : 0) + (gq >> psh) * 32 + (lane & 7) * 4 : gb + dgu + (lane & 31) * 4;
        t = f32x2{0.f, 0.f};
#pragma unroll 1
        for (int k = 0; k < D_; ++k) { const unsigned w = *(const unsigned*)(pc + (size_t)k * (lane < 32 ? pstr : F_ * 2)); t += f32x2{lo_f(w), hi_f(w)}; }
      }
      if (lane < 32 || wid == 0)
        *(f32x2*)((float*)(shm + AUX + 4096) + ((lane >> 4) & 1) * 192 + (lane < 32 ? wid : 4) * 32 + (lane & 15) * 2) = t;
    }
  };
  if (wid < 4) kloop(std::integral_constant<int, 0>{});
  else kloop(std::integral_constant<int, 1>{});
  TS1(1);
  __builtin_amdgcn_sched_barrier(0);
  int tz = threadIdx.x;
  asm volatile("" : "+v"(tz));
  __builtin_amdgcn_sched_barrier(0);
  const int li = tz & 15, lq = (tz >> 4) & 3, ln = tz & 63;
  const __amdgpu_buffer_rsrc_t ra = __builtin_amdgcn_make_buffer_rsrc((void*)abuf, 0, 16 * 72 * 16384, 0x00020000);
  float* const cfin = (float*)(shm + AUX + 4096);
  float* const svr = cfin + 384;
  float* const tb = (float*)(shm + 2 * G1_STAGE + wm * 1024);
  typedef __attribute__((address_space(3))) unsigned lds_u32;
  lds_u32* const cnt = (lds_u32*)(shm3 + CNT);
  auto signal = [&](unsigned v) {
    asm volatile("" ::: "memory");
    if (ln == 0) __hip_atomic_fetch_add(cnt, v, __ATOMIC_RELAXED, __HIP_MEMORY_SCOPE_WORKGROUP);
    asm volatile("" ::: "memory");
  };
  auto await = [&](unsigned mask, unsigned val) {
    while ((__builtin_amdgcn_readfirstlane(__hip_atomic_load(cnt, __ATOMIC_RELAXED, __HIP_MEMORY_SCOPE_WORKGROUP)) & mask) != val) __builtin_amdgcn_s_sleep(1);
    asm volatile("" ::: "memory");
  };
  if (wc) __builtin_amdgcn_s_setprio(1);
  signal(1u << (8 * wc));
  await(0xffu, 4u);
  TS1(4);
  f32x4 rf[4];
#pragma unroll
  for (int mf = 0; mf < 4; ++mf) rf[mf] = *(const f32x4*)(shm + AUX + (wm * 64 + mf * 16 + li) * 16);
  const float* const cl = cfin + wc * 192 + lq * 4;
  const int sbase = ((mtile * 2 + (wm >> 1)) * 72 * 8 + (wm & 1) * 4) * 2048;
  auto putf = [&](int kg, int mf, u32x4 v) {
    __builtin_amdgcn_raw_buffer_store_b128(v, ra, ln * 16 + sbase + ((kg >> 1) * 8 + mf) * 2048 + (kg & 1) * 1024, 0, 16);
  };
  auto gelp = [&](f32x2 g, f32x2 u, f32x2 cg, f32x2 cu, f32x2 m2, f32x2 k12, f32x2 k22, f32x2 d2) {
    const f32x2 tg = __builtin_elementwise_fma(m2, cg, g), tu = __builtin_elementwise_fma(m2, cu, u);
    const f32x2 arg = tg * __builtin_elementwise_fma(tg * tg, k22, k12);
    const f32x2 dn = __builtin_elementwise_fma(f32x2{__builtin_amdgcn_exp2f(arg[0]), __builtin_amdgcn_exp2f(arg[1])}, d2, d2);
    const f32x2 r = (tg * tu) * f32x2{__builtin_amdgcn_rcpf(dn[0]), __builtin_amdgcn_rcpf(dn[1])};
    return pack2(r[0], r[1]);
  };
  auto gel2 = [&](int p, int kg, int mf0 = 0, int mf1 = 4) {
    f32x4 cg[2], cu[2];
#pragma unroll
    for (int j = 0; j < 2; ++j) { cg[j] = *(const f32x4*)(cl + p * 32 + j * 16); cu[j] = *(const f32x4*)(cl + p * 32 + 32 + j * 16); }
#pragma unroll
    for (int mf = mf0; mf < mf1; ++mf) {
      const f32x2 m2 = {rf[mf][0], rf[mf][0]}, k12 = {rf[mf][1], rf[mf][1]}, k22 = {rf[mf][2], rf[mf][2]}, d2 = {rf[mf][3], rf[mf][3]};
      u32x4 o;
#pragma unroll
      for (int i = 0; i < 4; ++i) {
        const int j = i >> 1, e = (i & 1) * 2;
        const f32x4 g = acc[mf][p * 2 + j], u = acc[mf][p * 2 + 2 + j];
        o[i] = gelp(f32x2{g[e], g[e + 1]}, f32x2{u[e], u[e + 1]}, f32x2{cg[j][e], cg[j][e + 1]}, f32x2{cu[j][e], cu[j][e + 1]}, m2, k12, k22, d2);
      }
      putf(kg, mf, o);
    }
  };
  int tsel = ntile & 1;
  asm volatile("" : "+s"(tsel));
  if (tsel != 0) {
    const float cg = cfin[wc * 192 + 64 + li], cu = cfin[wc * 192 + 80 + li];
#pragma unroll
    for (int mf = 0; mf < 4; ++mf) {
      f32x4 t[4];
#pragma unroll
      for (int i = 0; i < 4; ++i) t[i] = *(const f32x4*)(tb + i * 64 + mf * 16 + lq * 4);
      const f32x4 g = acc[mf][4], u = acc[mf][5];
      u32x2 o;
#pragma unroll
      for (int e = 0; e < 4; e += 2)
        o[e >> 1] = gelp(f32x2{g[e], g[e + 1]}, f32x2{u[e], u[e + 1]}, f32x2{cg, cg}, f32x2{cu, cu}, f32x2{t[0][e], t[0][e + 1]},
                         f32x2{t[1][e], t[1][e + 1]}, f32x2{t[2][e], t[2][e + 1]}, f32x2{t[3][e], t[3][e + 1]});
      *(u32x2*)(shm + wm * 4096 + mf * 1024 + wc * 512 + li * 32 + lq * 8) = o;
    }
    if (wc == 0) signal(0x10000u << wid);
    gel2(0, 2 * (kt0 + 2 * he) + wc);
    TS1(5);
    if (wc) {
      await(0x1000u << wid, 0x1000u << wid);
#pragma unroll
      for (int mf = 0; mf < 4; ++mf) putf(2 * (kt0 + 4) + he, mf, __builtin_bit_cast(u32x4, tr_frag(shm3 + wm * 4096 + mf * 1024 + ln * 8)));
    }
    gel2(3, 2 * (kt0 + 2 * he + 1) + wc);
    TS1(6);
  } else {
    float rd[4];
#pragma unroll
    for (int mf = 0; mf < 4; ++mf) rd[mf] = svr[wm * 64 + mf * 16 + li];
    auto cvq = [&](int p, int mf, const f32x4& c0, const f32x4& c1) {
      u32x4 o;
#pragma unroll
      for (int j = 0; j < 2; ++j) {
        const f32x4 a = acc[mf][p * 2 + j], c4 = j ? c1 : c0;
        const f32x2 m2 = {rf[mf][0], rf[mf][0]}, r2 = {rd[mf], rd[mf]};
        f32x2 t0 = __builtin_elementwise_fma(m2, f32x2{c4[0], c4[1]}, f32x2{a[0], a[1]});
        f32x2 t1 = __builtin_elementwise_fma(m2, f32x2{c4[2], c4[3]}, f32x2{a[2], a[3]});
        if (p) { t0 *= r2; t1 *= r2; }
        o[2 * j] = pack2(t0[0], t0[1]); o[2 * j + 1] = pack2(t1[0], t1[1]);
      }
      return o;
    };
    const float cv0 = cfin[wc * 192 + 64 + li], cv1 = cfin[wc * 192 + 80 + li];
    auto cvv = [&](int mf) {
      const f32x4 n4 = *(const f32x4*)(tb + mf * 16 + lq * 4), r4 = *(const f32x4*)(svr + wm * 64 + mf * 16 + lq * 4);
      u32x4 o;
#pragma unroll
      for (int j = 0; j < 2; ++j) {
        const f32x4 a = acc[mf][4 + j];
        const f32x2 c2 = {j ? cv1 : cv0, j ? cv1 : cv0};
        const f32x2 t0 = __builtin_elementwise_fma(f32x2{n4[0], n4[1]}, c2, f32x2{a[0], a[1]}) * f32x2{r4[0], r4[1]};
        const f32x2 t1 = __builtin_elementwise_fma(f32x2{n4[2], n4[3]}, c2, f32x2{a[2], a[3]}) * f32x2{r4[2], r4[3]};
        o[2 * j] = pack2(t0[0], t0[1]); o[2 * j + 1] = pack2(t1[0], t1[1]);
      }
      return o;
    };
    u32x4 kx[4], vx[4], qx[4], hx[4];
    const bf16x8 ones = {0x3f80, 0x3f80, 0x3f80, 0x3f80, 0x3f80, 0x3f80, 0x3f80, 0x3f80};
    const f32x4 ck0 = *(const f32x4*)(cl + 32), ck1 = *(const f32x4*)(cl + 48);
    kx[2] = cvq(1, 2, ck0, ck1); kx[3] = cvq(1, 3, ck0, ck1);
    vx[2] = cvv(2); vx[3] = cvv(3);
    const bool giveh = wm == 3 && mtile < 7, needh = wm == 0 && mtile > 0;
    char* const hptr = (char*)halo + (size_t)((needh ? mtile - 1 : mtile) * 32 + ntile + wc) * 16384;
    if (wm < 3) {
      char* const ex = shm + wid * 4096 + ln * 16;
      *(u32x4*)ex = kx[2]; *(u32x4*)(ex + 1024) = kx[3]; *(u32x4*)(ex + 2048) = vx[2]; *(u32x4*)(ex + 3072) = vx[3];
      signal(0x10000u << wid);
    } else if (giveh) {
      const __amdgpu_buffer_rsrc_t rh = __builtin_amdgcn_make_buffer_rsrc((void*)hptr, 0, 9216, 0x00020000);
      const u64 now = __builtin_amdgcn_s_memrealtime();
      const unsigned tag = (unsigned)now;
#pragma unroll
      for (int r = 0; r < 8; ++r) {
        const u32x4 w = r < 4 ? kx[2 + (r >> 1)] : vx[r >> 1];
        __builtin_amdgcn_raw_buffer_store_b128(u32x4{w[2 * (r & 1)], tag, w[2 * (r & 1) + 1], tag}, rh, ln * 16 + r * 1024, 0, 16);
      }
      __builtin_amdgcn_raw_buffer_store_b128(u32x4{tag, (unsigned)(now >> 32), tag, (unsigned)(now >> 32)}, rh, ln * 16 + 8192, 0, 16);
    }
    kx[0] = cvq(1, 0, ck0, ck1); kx[1] = cvq(1, 1, ck0, ck1);
    vx[0] = cvv(0); vx[1] = cvv(1);
    {
      const f32x4 cq0 = *(const f32x4*)(cl), cq1 = *(const f32x4*)(cl + 16);
#pragma unroll
      for (int mf = 0; mf < 4; ++mf) qx[mf] = cvq(0, mf, cq0, cq1);
    }
    f32x4 mk0, mk1;
#pragma unroll
    for (int j = 0; j < 4; ++j) {
      const int k0 = 4 * lq + j, k1 = 16 + 4 * lq + j;
      mk0[j] = (k0 >= li + 9 && k0 <= li + 16) ? 0.f : -1.0e30f;
      mk1[j] = (k1 >= li + 9 && k1 <= li + 16) ? 0.f : -1.0e30f;
    }
    auto max3 = [](float a, float b, float c) { float r; asm("v_max3_f32 %0, %1, %2, %3" : "=v"(r) : "v"(a), "v"(b), "v"(c)); return r; };
    auto max2 = [](float a, float b) { float r; asm("v_max_f32 %0, %1, %2" : "=v"(r) : "v"(a), "v"(b)); return r; };
    auto tiles = [&](int t0, const u32x4* kp, const u32x4* vp, bool first_rows) {
      const f32x4 z = {0.f, 0.f, 0.f, 0.f};
      const f32x4 mz = {-1.0e30f, -1.0e30f, -1.0e30f, -1.0e30f};
      f32x4 s0[2], s1[2];
#pragma unroll
      for (int n = 0; n < 2; ++n) {
        const bf16x8 qf = __builtin_bit_cast(bf16x8, qx[t0 + n]);
        s0[n] = __builtin_amdgcn_mfma_f32_16x16x32_bf16(__builtin_bit_cast(bf16x8, kp[n]), qf, first_rows ? mz : mk0, 0, 0, 0);
        s1[n] = __builtin_amdgcn_mfma_f32_16x16x32_bf16(__builtin_bit_cast(bf16x8, kx[t0 + n]), qf, mk1, 0, 0, 0);
      }
      SB0();
      asm volatile("s_nop 7\n\ts_nop 3" : "+v"(s0[0]), "+v"(s1[0]), "+v"(s0[1]), "+v"(s1[1]));
      float mx[2];
#pragma unroll
      for (int n = 0; n < 2; ++n) mx[n] = max3(max3(s0[n][0], s0[n][1], s0[n][2]), max3(s0[n][3], s1[n][0], s1[n][1]), max2(s1[n][2], s1[n][3]));
#pragma unroll
      for (int n = 0; n < 2; ++n) { auto r = __builtin_amdgcn_permlane16_swap(__float_as_uint(mx[n]), __float_as_uint(mx[n]), false, false);
        mx[n] = max2(__uint_as_float(r[0]), __uint_as_float(r[1])); }
#pragma unroll
      for (int n = 0; n < 2; ++n) { auto r = __builtin_amdgcn_permlane32_swap(__float_as_uint(mx[n]), __float_as_uint(mx[n]), false, false);
        mx[n] = max2(__uint_as_float(r[0]), __uint_as_float(r[1])); }
      SB0();
      constexpr float cs2 = 0.17677669529663687f * 1.4426950408889634f;
      f32x4 os[2], o0[2], o1[2];
#pragma unroll
      for (int n = 0; n < 2; ++n) {
        const float c = cs2 * rd[t0 + n], nm = -mx[n] * c;
        float e8[8];
#pragma unroll
        for (int j = 0; j < 4; ++j) {
          e8[j] = __builtin_amdgcn_exp2f(__builtin_fmaf(s0[n][j], c, nm));
          e8[4 + j] = __builtin_amdgcn_exp2f(__builtin_fmaf(s1[n][j], c, nm));
        }
        u32x4 pk;
#pragma unroll
        for (int j = 0; j < 4; ++j) pk[j] = pack2(e8[2 * j], e8[2 * j + 1]);
        const bf16x8 pf = __builtin_bit_cast(bf16x8, pk);
        const u32x4 va = {vp[n][0], vp[n][1], vx[t0 + n][0], vx[t0 + n][1]}, vb = {vp[n][2], vp[n][3], vx[t0 + n][2], vx[t0 + n][3]};
        os[n] = __builtin_amdgcn_mfma_f32_16x16x32_bf16(ones, pf, z, 0, 0, 0);
        o0[n] = __builtin_amdgcn_mfma_f32_16x16x32_bf16(__builtin_bit_cast(bf16x8, va), pf, z, 0, 0, 0);
        o1[n] = __builtin_amdgcn_mfma_f32_16x16x32_bf16(__builtin_bit_cast(bf16x8, vb), pf, z, 0, 0, 0);
      }
#pragma unroll
      for (int n = 0; n < 2; ++n) {
        const float inv = __builtin_amdgcn_rcpf(os[n][0]);
        const f32x4 n0 = o0[n] * inv, n1 = o1[n] * inv;
        putf(112 + ntile + wc, t0 + n, u32x4{pack2(n0[0], n0[1]), pack2(n0[2], n0[3]), pack2(n1[0], n1[1]), pack2(n1[2], n1[3])});
      }
    };
    TS1(5);
    tiles(2, &kx[0], &vx[0], false);
    constexpr int HL = 40960;
    auto hload = [&]() {
#pragma unroll
      for (int r = 0; r < 9; ++r) GLDS_SC1(hptr + r * 1024 + ln * 16, shm_base + HL + wc * 10240 + r * 1024);
    };
    if (needh) hload();
    gel2(3, 2 * (kt0 + 5 + he) + wc, 0, 2);
    TS1(6);
    if (wm > 0) {
      await(0x8000u << wid, 0x8000u << wid);
#pragma unroll
      for (int i = 0; i < 4; ++i) hx[i] = *(const u32x4*)(shm + (wid - 1) * 4096 + i * 1024 + ln * 16);
    } else if (!needh) {
#pragma unroll
      for (int i = 0; i < 4; ++i) hx[i] = u32x4{0u, 0u, 0u, 0u};
    } else {
      bool late = false;
      WAIT_V(2);
      for (;;) {
        const char* const hl = shm + HL + wc * 10240 + ln * 16;
        const u32x4 sp = *(const u32x4*)(hl + 8192);
        unsigned df = 0;
#pragma unroll
        for (int i = 0; i < 4; ++i) {
          const u32x4 g0 = *(const u32x4*)(hl + i * 2048), g1 = *(const u32x4*)(hl + i * 2048 + 1024);
          df |= (g0[1] ^ sp[0]) | (g0[3] ^ sp[0]) | (g1[1] ^ sp[0]) | (g1[3] ^ sp[0]);
          hx[i] = u32x4{g0[0], g0[2], g1[0], g1[2]};
        }
        if (__builtin_expect(__all((df == 0) & fresh(((u64)sp[1] << 32) | sp[0], t_own)), 1)) break;
        late = __builtin_amdgcn_s_memrealtime() - t_own > TIMEOUT_;
        if (late) break;
        __builtin_amdgcn_s_sleep(1);
        WAIT_L(0);
        hload();
        WAIT_V(0);
      }
      if (__builtin_expect(late, 0)) {
        char* const fb = shm + (wid + 3) * 4096 + ln * 16;
#pragma unroll 1
        for (int i = 0; i < 32; ++i) {
          const int rec = i >> 3, e = i & 7;
          const int row = m0 - 32 + 2 * (rec < 2 ? li : lq * 4 + (e & 3)) + (rec & 1);
          const int col = (rec < 2 ? D_ + lq * 4 + (e & 3) : 2 * D_ + li) + (ntile + wc) * 32 + (e >> 2) * 16;
          const u16* xr = x + (size_t)row * D_;
          const unsigned stt = row_stats_serial(xr);
          const float mb = lo_f(stt);
          float a = 0.f;
#pragma unroll 1
          for (int k = 0; k < D_; ++k) a += (bf2f(xr[k]) - mb) * bf2f(wqkv[(size_t)k * 3072 + col]);
          *(u16*)(fb + rec * 1024 + e * 2) = f2bf(a / hi_f(stt));
        }
#pragma unroll
        for (int i = 0; i < 4; ++i) hx[i] = *(const u32x4*)(fb + i * 1024);
      }
    }
    tiles(0, &hx[0], &hx[2], mtile == 0 && wm == 0);
    gel2(3, 2 * (kt0 + 5 + he) + wc, 2, 4);
  }
  TS1(2);
  auto stamp = [&](u64* p) {
    WAIT_V(0);
    u64 now;
    asm volatile("s_memrealtime %0\n\ts_waitcnt lgkmcnt(0)" : "=s"(now) :: "memory");
    if ((threadIdx.x & 63) == 0) __hip_atomic_store(p, now, RLX_AGENT);
  };
  auto client = [&](int l) { const int m128 = mtile * 2 + ((l >> 4) & 1); return (((l & 7) * 4 + (m128 & 3)) * 8) + ((m128 >> 2) ^ ((l & 8) ? 5 : 0)); };
  auto notify = [&](int l0, int nl) {
    u64 now;
    asm volatile("s_memrealtime %0\n\ts_waitcnt lgkmcnt(0)" : "=s"(now) :: "memory");
    int l = threadIdx.x & 63;
    asm volatile("" : "+v"(l));
    if (l < nl) __hip_atomic_store(&done1[client(l0 + l) * 32 + ntile], now, RLX_AGENT);
  };
  {
    WAIT_V(0);
    unsigned old = 0;
    if (ln == 0) old = __hip_atomic_fetch_add(cnt + 4 + (wm >> 1), 1u, __ATOMIC_RELAXED, __HIP_MEMORY_SCOPE_WORKGROUP);
    if (__builtin_amdgcn_readfirstlane(old) == 3u) notify((wm >> 1) * 16, 16);
  }
  TS1(3);
  TSFLUSH(1);
  if (wc) await(0xffffu, 0x404u);
  const bool cok = __builtin_amdgcn_readfirstlane(cnt[1] & cnt[2]) != 0u;
  const bool tya1 = tya;
  __builtin_amdgcn_s_setprio(0);
  {
  __builtin_amdgcn_sched_barrier(0);
  int tid = threadIdx.x;
  asm volatile("" : "+v"(tid));
  __builtin_amdgcn_sched_barrier(0);
  const int lane = tid & 63;
  const int wm = wid >> 1, wn = wid & 1;
  const int role = 1 - (xcd >> 2);
  const int mtile = (role ? xcd : xcd ^ 5) * 4 + (idx & 3), ntile = idx >> 2;
  const int m0 = mtile * 128, n0 = ntile * 128;
  constexpr int PD = G2_NSTAGE - 1;
  constexpr int NY = 28;
  const int ny = (xcd & 1) ? NY - DLT : NY + DLT;
  TS(0);
  constexpr int EP = 288;
  char* const img = shm + ((ny + 19) % G2_NSTAGE) * G2_STAGE;
  if (wid >= 4) {
    const int p = wid - 4;
    unsigned vr[2], vc[2], vb[2][2];
#pragma unroll
    for (int v = 0; v < 2; ++v) {
      const int vt = p * 128 + v * 64 + lane;
      const int r8 = vt >> 3, pos = vt & 7;
      const int phys = pos >> 1, hf = pos & 1;
      const int fk = ((r8 >> 1) & 1) | (((r8 >> 3) & 1) << 1);
      vr[v] = (r8 >> 5) * 32 + ((r8 >> 2) & 1) * 16 + ((r8 >> 3) & 3) * 4 + (r8 & 3);
      vc[v] = (phys ^ fk) * 32 + hf * 16;
    }
    const unsigned va = lane * 16;
    const char* sa = (const char*)abuf + (size_t)(mtile * 72 + role * NY) * 16384 + p * 4096;
    const char* sw[2];
    int ws[2];
    auto wsetup = [&](const u16* w, int row0, int crow0) {
#pragma unroll
      for (int i = 0; i < 2; ++i) {
        const bool slow = cok && (((((unsigned)(uintptr_t)w >> 7) + ntile * 2 + i) & 7) == 3);
        const int sh = slow ? 7 : 11;
        sw[i] = slow ? wcopy + (ntile >> 2) * 589824 + crow0 * 128 : (const char*)(w + (size_t)row0 * D_ + n0 + i * 64);
        ws[i] = 64 << sh;
#pragma unroll
        for (int v = 0; v < 2; ++v) vb[i][v] = (vr[v] << sh) + vc[v];
      }
    };
    wsetup(wd, role * NY * 64, role * NY * 64);
    unsigned sl = shm_base + 2 * G2_STAGE;
#define G2_W "s_add_u32 m0, %[l], %[ow]\n\ts_nop 0\n\tglobal_load_lds_dwordx4 %[v0], %[w0] sc1\n\t" \
             "s_add_u32 m0, m0, 1024\n\ts_nop 0\n\tglobal_load_lds_dwordx4 %[v1], %[w0] sc1\n\t" \
             "s_add_u32 m0, m0, 7168\n\ts_nop 0\n\tglobal_load_lds_dwordx4 %[v2], %[w1] sc1\n\t" \
             "s_add_u32 m0, m0, 1024\n\ts_nop 0\n\tglobal_load_lds_dwordx4 %[v3], %[w1] sc1"
#define G2_WOP [ow] "s"(16384 + p * 2048), [w0] "s"(sw[0]), [w1] "s"(sw[1]), [v0] "v"(vb[0][0]), [v1] "v"(vb[0][1]), [v2] "v"(vb[1][0]), [v3] "v"(vb[1][1])
    auto adv = [&]() { sa += 16384; sw[0] += ws[0]; sw[1] += ws[1]; sl = sl == shm_base + (G2_NSTAGE - 1) * G2_STAGE ? shm_base : sl + G2_STAGE; };
    auto stage_y = [&]() {
      asm volatile("s_add_u32 m0, %[l], %[oa]\n\ts_nop 0\n\tglobal_load_lds_dwordx4 %[va], %[a] sc1\n\tglobal_load_lds_dwordx4 %[va], %[a] offset:1024 sc1\n\t"
                   "global_load_lds_dwordx4 %[va], %[a] offset:2048 sc1\n\tglobal_load_lds_dwordx4 %[va], %[a] offset:3072 sc1\n\t" G2_W
                   :: [l] "s"(sl), [oa] "s"((p ^ role) * 4096), [a] "s"(sa), [va] "v"(va), G2_WOP : "memory", "scc", "m0");
      adv();
    };
    auto stage_s = [&]() {
      asm volatile("s_add_u32 m0, %[l], %[oa]\n\ts_nop 0\n\tglobal_load_lds_dwordx4 %[va], %[a] sc1\n\tglobal_load_lds_dwordx4 %[va], %[a] offset:1024 sc1\n\t" G2_W
                   :: [l] "s"(sl), [oa] "s"((p >> 1) * 8192 + (p & 1) * 2048), [a] "s"(sa), [va] "v"(va), G2_WOP : "memory", "scc", "m0");
      adv();
    };
    unsigned ready = 0;
    const u64* const mbox = &done1[bid * 32 + (lane & 31)];
    auto need = [&](unsigned mask, bool first = false) {
      while ((ready & mask) != mask) {
        ready = (unsigned)__ballot(fresh(__hip_atomic_load(mbox, RLX_AGENT), t_own));
        if ((ready & mask) != mask) {
          __builtin_amdgcn_s_sleep(PSLEEP);
          if (__builtin_expect(!cok, 0) && (!first || __builtin_amdgcn_readfirstlane(cnt[4] & cnt[5]) == 4u)) notify(0, 32);
        }
      }
      asm volatile("" ::: "memory");
    };
    unsigned gm = 0xfu << (16 * role);
    need(gm & 0x22222222u, true);
    TS(4);
    stage_y();
    stage_y();
    WAIT_V(8);
    BARL();
    need(gm & 0xaaaaaaaau);
    stage_y();
    stage_y();
    gm >>= 4;
    int left = 1, span = 2;
    u64 fl = 0;
#pragma unroll 1
    for (int g = 0; g < ny - PD; ++g) {
      WAIT_V(16);
      BAR();
      if (__builtin_expect(left == 0, 0)) {
        asm volatile("" : "+v"(fl));
        ready |= (unsigned)__ballot(fresh(fl, t_own));
        left = span; span = 7; gm = gm ? gm << 4 : 0xf;
        if (g == NY - PD) {
          gm = 0x55555555u;
          const int t = (2 - role) * NY - DLT;
          sa = (const char*)abuf + (size_t)(mtile * 72 + t) * 16384 + p * 4096;
          wsetup(wd, t * 64, t * 64);
        }
        need(gm);
      }
      if (__builtin_expect(min(left, ny - PD - g) == 2, 0)) asm volatile("global_load_dwordx2 %0, %1, off sc1" : "+v"(fl) : "v"(mbox) : "memory");
      --left;
      stage_y();
    }
    WAIT_V(16);
    asm volatile("" : "+v"(fl));
    ready |= (unsigned)__ballot(fresh(fl, t_own));
    need(0x55555555u);
    sa = (const char*)abuf + (size_t)(mtile * 72 + 56) * 16384 + (p >> 1) * 8192 + role * 4096 + (p & 1) * 2048;
    wsetup(wo, 0, F_);
    const u64* const pf = done2 + (bid ^ 5) * 8 + (lane & 3);
#pragma unroll 1
    for (int g = 0; g < 16; ++g) {
      WAIT_V(12);
      BAR();
      if (__builtin_expect(g == 14, 0)) asm volatile("global_load_dwordx2 %0, %1, off sc1" : "+v"(fl) : "v"(pf) : "memory");
      stage_s();
    }
    WAIT_V(12);
    BAR();
    {
      asm volatile("" : "+v"(fl));
      while (!__all(fresh(fl, t_own))) {
        if (__builtin_expect(!cok, 0)) { notify(0, 32); if (lane < 4) __hip_atomic_store(done2 + bid * 8 + lane, __builtin_amdgcn_s_memrealtime(), RLX_AGENT); }
        __builtin_amdgcn_s_sleep(1);
        fl = __hip_atomic_load(pf, RLX_AGENT);
      }
      asm volatile("" ::: "memory");
      const char* ps = (const char*)slab + (size_t)(bid ^ 5) * 16384 + p * 4096 + lane * 16;
#pragma unroll
      for (int i = 0; i < 4; ++i) GLDS_SC1(ps + i * 1024, sl + p * 4096 + i * 1024);
    }
    WAIT_V(10);
    BAR();
    WAIT_V(4);
    BAR();
    WAIT_V(0);
    BAR();
    u32x4 xv[4];
#pragma unroll
    for (int i = 0; i < 4; ++i) {
      const int c = i * 256 + tid - 256, r = c >> 4;
      xv[i] = *(const u32x4*)(x + (size_t)(m0 + (r >> 5) * 64 + role * 32 + (r & 31)) * D_ + n0 + (c & 15) * 8);
    }
    BAR();
#pragma unroll
    for (int i = 0; i < 4; ++i) {
      const int c = i * 256 + tid - 256, r = c >> 4;
      const u32x4 tv = *(const u32x4*)(img + ((r & 32) + (r & 1) * 16 + ((r >> 1) & 15)) * EP + (c & 15) * 16);
      u32x4 ov;
#pragma unroll
      for (int e = 0; e < 4; ++e) ov[e] = pack2(lo_f(xv[i][e]) + lo_f(tv[e]), hi_f(xv[i][e]) + hi_f(tv[e]));
      __builtin_nontemporal_store(ov, (u32x4*)(out + (size_t)(m0 + (r >> 5) * 64 + role * 32 + (r & 31)) * D_ + n0 + (c & 15) * 8));
    }
    TS(5);
    if (tid == 256) __hip_atomic_store(&acks[bid], __builtin_amdgcn_s_memrealtime(), RLX_AGENT);
    if (__builtin_expect(!cok, 0) && p == 0) {
      const bool mine = lane == 32 || (lane < 32 && (tya1 || ((lane >> 3) & 1) != (xcd >> 2)));
      const u64* const pa = &acks[lane < 32 ? client(lane) : bid ^ 5];
      while (!__all(!mine || fresh(__hip_atomic_load(pa, RLX_AGENT), t_own))) {
        notify(0, 32);
        if (lane < 4) __hip_atomic_store(done2 + bid * 8 + lane, __builtin_amdgcn_s_memrealtime(), RLX_AGENT);
        __builtin_amdgcn_s_sleep(32);
      }
    }
  } else {
    TS(6);
    f32x4 acc[4][4], sacc[2][4];
    const int fr = lane & 15, fq = lane >> 4, q4 = fr >> 2, pp = lane & 3;
    const int ff = ((q4 >> 1) & 1) | ((fq & 1) << 1);
    int aof = 2 * G2_STAGE + wm * 8192 + lane * 16;
    int boff[4];
#pragma unroll
    for (int j = 0; j < 4; ++j) boff[j] = 2 * G2_STAGE + 16384 + wn * 8192 + (8 * fq + q4) * 128 + ((j ^ ff) * 32) + pp * 8;
#pragma unroll
    for (int b = 0; b < 4; ++b) {
#pragma unroll
      for (int a = 0; a < 4; ++a) acc[a][b] = f32x4{0.f, 0.f, 0.f, 0.f};
      sacc[0][b] = f32x4{0.f, 0.f, 0.f, 0.f}; sacc[1][b] = f32x4{0.f, 0.f, 0.f, 0.f};
    }
#define SB() __builtin_amdgcn_sched_barrier(0)
    struct Fr { bf16x8 a[4]; bf16x4 bl[4], bh[4]; };
    Fr f0, f1;
    auto rd = [&](Fr& f, lds_cptr sb, int ks, int t, bool sp) {
      constexpr int ord[12] = {0, 1, 8, 2, 3, 4, 5, 6, 7, 9, 10, 11};
      const int o = ord[t];
      if (o >= 8) f.a[o - 8] = *(const __attribute__((address_space(3))) bf16x8*)(sb + aof + (o - 8) * 2048 + ks * 1024);
      else if (o & 1) f.bh[o >> 1] = tr_read(sb + boff[o >> 1] + ks * 4096 + 512);
      else f.bl[o >> 1] = tr_read(sb + boff[o >> 1] + ks * 4096);
    };
    auto bfr = [&](const Fr& c, int j) { return __builtin_shufflevector(c.bl[j], c.bh[j], 0, 1, 2, 3, 4, 5, 6, 7); };
    auto half = [&](const Fr& c, Fr& n, int ks) {
      lds_cptr sb = shm3;
#pragma unroll
      for (int t = 0; t < 16; ++t) {
        const int mf = t >> 2, j = t & 3;
        SB();
        acc[mf][j] = __builtin_amdgcn_mfma_f32_16x16x32_bf16(bfr(c, j), c.a[mf], acc[mf][j], 0, 0, 0);
        SB();
        if (t < 12) rd(n, sb, ks, t, false);
      }
      SB();
    };
    auto rds = [&](Fr* n, lds_cptr sb, int t) {
      if (t < 10) { rd(n[t / 5], sb, t / 5, 2 * (t % 5), true); rd(n[t / 5], sb, t / 5, 2 * (t % 5) + 1, true); }
    };
    auto stg = [&](const Fr* c, Fr* n, bool load = true) {
      lds_cptr sb = shm3;
#pragma unroll
      for (int t = 0; t < 16; ++t) {
        const int ks = t >> 3, mf = (t >> 2) & 1, j = t & 3;
        SB();
        sacc[mf][j] = __builtin_amdgcn_mfma_f32_16x16x32_bf16(bfr(c[ks], j), c[ks].a[mf], sacc[mf][j], 0, 0, 0);
        SB();
        if (load) rds(n, sb, t);
      }
      SB();
    };
    asm volatile("" : "+v"(aof), "+v"(boff[0]), "+v"(boff[1]), "+v"(boff[2]), "+v"(boff[3]));
    BAR();
    TS(7);
    __builtin_amdgcn_s_waitcnt(0xC07F);
    SB();
#pragma unroll
    for (int t = 0; t < 12; ++t) rd(f0, shm3, 0, t, false);
    int so = 2 * G2_STAGE;
    auto nxt = [&]() {
      const int d = so == (G2_NSTAGE - 1) * G2_STAGE ? -(G2_NSTAGE - 1) * G2_STAGE : G2_STAGE;
      so += d; aof += d; boff[0] += d; boff[1] += d; boff[2] += d; boff[3] += d;
      asm volatile("" : "+v"(aof), "+v"(boff[0]), "+v"(boff[1]), "+v"(boff[2]), "+v"(boff[3]));
    };
#pragma unroll 1
    for (int g = 0; g < ny - 1; ++g) {
      half(f0, f1, 1);
      nxt();
      BAR();
      half(f1, f0, 0);
    }
    half(f0, f1, 1);
    nxt();
    BAR();
    Fr sa[2], sb2[2];
#pragma unroll
    for (int t = 0; t < 16; ++t) {
      const int mf = (t >> 2) ^ 2, j = t & 3;
      SB();
      acc[mf][j] = __builtin_amdgcn_mfma_f32_16x16x32_bf16(bfr(f1, j), f1.a[mf], acc[mf][j], 0, 0, 0);
      SB();
      rds(sa, shm3, t);
    }
    SB();
    TS(2);
    {
      const __amdgpu_buffer_rsrc_t rs = __builtin_amdgcn_make_buffer_rsrc((void*)((char*)slab + (size_t)bid * 16384), 0, 16384, 0x00020000);
#pragma unroll
      for (int f = 0; f < 4; ++f) {
        const f32x4 a = acc[2 + (f >> 1)][(f & 1) * 2], b = acc[2 + (f >> 1)][(f & 1) * 2 + 1];
        __builtin_amdgcn_raw_buffer_store_b128(u32x4{pack2(a[0], a[1]), pack2(a[2], a[3]), pack2(b[0], b[1]), pack2(b[2], b[3])}, rs,
                                               (f * 256 + tid) * 16, 0, 16);
      }
    }
    SB();
#pragma unroll 1
    for (int i = 0; i < 7; ++i) {
      nxt();
      BAR();
      stg(sa, sb2);
      nxt();
      BAR();
      stg(sb2, sa);
      if (i == 2) {
        stamp(done2 + bid * 8 + wid);
      }
    }
    nxt();
    BAR();
    stg(sa, sb2);
    BAR();
    u32x4 pv[4];
    {
      nxt();
      lds_cptr sb = shm3 + so + tid * 16;
#pragma unroll
      for (int f = 0; f < 4; ++f) pv[f] = *(const __attribute__((address_space(3))) u32x4*)(sb + f * 4096);
    }
    stg(sb2, sa, false);
    TS(1);
#pragma unroll
    for (int mf = 0; mf < 2; ++mf)
#pragma unroll
      for (int j = 0; j < 4; ++j) {
        const u32x4 pw = pv[mf * 2 + (j >> 1)];
        const f32x4 sv = sacc[mf][j];
        const f32x4 ya = acc[mf][j] + f32x4{lo_f(pw[2 * (j & 1)]), hi_f(pw[2 * (j & 1)]), lo_f(pw[2 * (j & 1) + 1]), hi_f(pw[2 * (j & 1) + 1])};
        u32x2 o;
#pragma unroll
        for (int e = 0; e < 2; ++e) {
          const f32x2 y2 = {ya[2 * e], ya[2 * e + 1]}, s2 = {sv[2 * e], sv[2 * e + 1]};
          const f32x2 ar = y2 * f32x2{-1.4426950408889634f, -1.4426950408889634f};
          const f32x2 dn = f32x2{__builtin_amdgcn_exp2f(ar[0]), __builtin_amdgcn_exp2f(ar[1])} + f32x2{1.0f, 1.0f};
          const f32x2 t = (s2 * y2) * f32x2{__builtin_amdgcn_rcpf(dn[0]), __builtin_amdgcn_rcpf(dn[1])};
          o[e] = pack2(t[0], t[1]);
        }
        *(u32x2*)(img + (wm * 32 + mf * 16 + fr) * EP + (wn * 64 + j * 16 + fq * 4) * 2) = o;
      }
    BARL();
    TS(3);
  }
  TSFLUSH(2);
  }
  asm volatile(".Lcode_end:");
}

extern "C" void kernel_launch(void* const* d_in, const int* in_sizes, int n_in, void* d_out, int out_size, void* d_ws,
                              size_t ws_size, hipStream_t stream) {
  const u16* A_x = (const u16*)d_in[0];
  const u16* A_wqkv = (const u16*)d_in[1];
  const u16* A_wo = (const u16*)d_in[2];
  const u16* B_wg = (const u16*)d_in[4];
  const u16* B_wu = (const u16*)d_in[5];
  const u16* B_wd = (const u16*)d_in[6];
  u16* out = (u16*)d_out;
  fused_kernel<<<256, 512, 0, stream>>>(A_x, A_wqkv, B_wg, B_wu, B_wd, A_wo, out, (char*)d_ws);
}
```

```cpp
#include <hip/hip_runtime.h>
#include <hip/hip_bf16.h>
#include <stdint.h>
#include <type_traits>

typedef unsigned short u16;
using bf16x8 = __attribute__((ext_vector_type(8))) short;
using bf16x4 = __attribute__((ext_vector_type(4))) short;
using f32x4 = __attribute__((ext_vector_type(4))) float;
using u32x2 = __attribute__((ext_vector_type(2))) unsigned;
using u32x4 = __attribute__((ext_vector_type(4))) unsigned;
typedef __attribute__((address_space(3))) const char* lds_cptr;

constexpr int S_ = 2048, D_ = 1024, F_ = 3584;

__device__ __forceinline__ float bf2f(u16 h) { return __uint_as_float((unsigned)h << 16); }
__device__ __forceinline__ u16 f2bf(float f) {
  unsigned u = __float_as_uint(f);
  u += 0x7fffu + ((u >> 16) & 1u);
  return (u16)(u >> 16);
}
__device__ __forceinline__ float rbf(float f) { return bf2f(f2bf(f)); }
typedef __attribute__((ext_vector_type(2))) __bf16 bf16x2_t;
typedef __attribute__((ext_vector_type(2))) float f32x2;
__device__ __forceinline__ unsigned pack2(float a, float b) {
  bf16x2_t r = __builtin_convertvector((f32x2){a, b}, bf16x2_t);
  return __builtin_bit_cast(unsigned, r);
}
__device__ __forceinline__ float lo_f(unsigned p) { return __uint_as_float(p << 16); }
__device__ __forceinline__ float hi_f(unsigned p) { return __uint_as_float(p & 0xffff0000u); }

#define TSDECL
#define TSS(i) do {} while (0)
#define TSFLUSH(k) do {} while (0)
#define TS1(i) TSS(i)
#define TS(i) TSS(i)
#define WAIT_V(n) asm volatile("s_waitcnt vmcnt(%0)" ::"n"(n) : "memory")
#define WAIT_L(n) asm volatile("s_waitcnt lgkmcnt(%0)" ::"n"(n) : "memory")
#define BARL() do { WAIT_L(0); BAR(); } while (0)
#define BAR() do { asm volatile("" ::: "memory"); __builtin_amdgcn_s_barrier(); asm volatile("" ::: "memory"); } while (0)

__device__ __forceinline__ bf16x4 tr_read(lds_cptr p) {
  return __builtin_amdgcn_ds_read_tr16_b64_v4i16((__attribute__((address_space(3))) bf16x4*)p);
}
__device__ __forceinline__ bf16x8 tr_frag(lds_cptr p) {
  bf16x4 lo = tr_read(p), hi = tr_read(p + 512);
  bf16x8 r;
  r[0] = lo[0]; r[1] = lo[1]; r[2] = lo[2]; r[3] = lo[3];
  r[4] = hi[0]; r[5] = hi[1]; r[6] = hi[2]; r[7] = hi[3];
  return r;
}
__device__ __forceinline__ void glds16(const void* gsrc, unsigned lds_dst) {
  unsigned keep;
  asm volatile("s_mov_b32 %0, m0\n\ts_mov_b32 m0, %2\n\ts_nop 0\n\tglobal_load_lds_dwordx4 %1, off\n\ts_mov_b32 m0, %0"
               : "=&s"(keep) : "v"(gsrc), "s"(lds_dst) : "memory");
}
#define GLDS(gptr, laddr) glds16((const void*)(gptr), (unsigned)(laddr))
__device__ __forceinline__ void glds16_sc1(const void* gsrc, unsigned lds_dst) {
  unsigned keep;
  asm volatile("s_mov_b32 %0, m0\n\ts_mov_b32 m0, %2\n\ts_nop 0\n\tglobal_load_lds_dwordx4 %1, off sc1\n\ts_mov_b32 m0, %0"
               : "=&s"(keep) : "v"(gsrc), "s"(lds_dst) : "memory");
}
#define GLDS_SC1(gptr, laddr) glds16_sc1((const void*)(gptr), (unsigned)(laddr))
__device__ __forceinline__ void glds16_nc(const void* gsrc, unsigned lds_dst) {
  unsigned keep;
  asm volatile("s_mov_b32 %0, m0\n\ts_mov_b32 m0, %2\n\ts_nop 0\n\tglobal_load_lds_dwordx4 %1, off\n\ts_mov_b32 m0, %0"
               : "=&s"(keep) : "v"(gsrc), "s"(lds_dst));
}
#define GLDS_NC(gptr, laddr) glds16_nc((const void*)(gptr), (unsigned)(laddr))
__device__ __forceinline__ void glds16_s(const void* sbase, unsigned voff, unsigned lds_dst) {
  asm volatile("s_mov_b32 m0, %2\n\ts_nop 0\n\tglobal_load_lds_dwordx4 %0, %1" :: "v"(voff), "s"(sbase), "s"(lds_dst) : "m0");
}

typedef unsigned long long u64;
__device__ __forceinline__ bool fresh(u64 stamp, u64 t_own) {
  return stamp - t_own - 100ull < __builtin_amdgcn_s_memrealtime() - t_own;
}
constexpr u64 TIMEOUT_ = 200000;
__device__ __forceinline__ float half_sum(float v) {
  v += __builtin_bit_cast(float, __builtin_amdgcn_update_dpp(0, __builtin_bit_cast(int, v), 0xB1, 0xf, 0xf, false));
  v += __builtin_bit_cast(float, __builtin_amdgcn_update_dpp(0, __builtin_bit_cast(int, v), 0x4E, 0xf, 0xf, false));
  v += __builtin_bit_cast(float, __builtin_amdgcn_update_dpp(0, __builtin_bit_cast(int, v), 0x141, 0xf, 0xf, false));
  v += __builtin_bit_cast(float, __builtin_amdgcn_update_dpp(0, __builtin_bit_cast(int, v), 0x140, 0xf, 0xf, false));
  { auto r = __builtin_amdgcn_permlane16_swap(__float_as_uint(v), __float_as_uint(v), false, false); v = __uint_as_float(r[0]) + __uint_as_float(r[1]); }
  return v;
}
__device__ __forceinline__ unsigned pack_stats(float mean, float var) {
  return (unsigned)f2bf(mean) | ((unsigned)f2bf(sqrtf(rbf(rbf(var) + 1e-5f))) << 16);
}
__device__ __forceinline__ unsigned row_stats_serial(const u16* xr) {
  float s = 0.f, q = 0.f;
#pragma unroll 1
  for (int k = 0; k < D_; ++k) s += bf2f(xr[k]);
  const float mean = s * (1.0f / D_);
#pragma unroll 1
  for (int k = 0; k < D_; ++k) { const float d = bf2f(xr[k]) - mean; q += d * d; }
  return pack_stats(mean, q * (1.0f / D_));
}
constexpr int G1_STAGE = 32768 + 5 * 8192;
#define PF_K0 4
#define CP_K0 5
#define DLT 1
#define PSLEEP 2
#define PUB_K 5

#define RLX_AGENT __ATOMIC_RELAXED, __HIP_MEMORY_SCOPE_AGENT

constexpr int G2_STAGE = 16384 + 2 * 8192;
constexpr int G2_NSTAGE = 5;
constexpr size_t MB_ = 1024 * 1024;
__global__ __launch_bounds__(512, 2) void fused_kernel(const u16* __restrict__ x, const u16* __restrict__ wqkv,
                                                       const u16* __restrict__ wg, const u16* __restrict__ wu,
                                                       const u16* __restrict__ wd, const u16* __restrict__ wo,
                                                       u16* __restrict__ out, char* __restrict__ ws) {
  u16* const halo = (u16*)(ws + 4 * MB_);
  u16* const abuf = (u16*)(ws + 16 * MB_);
  char* const slab = ws + 64 * MB_;
  unsigned* const flags = (unsigned*)(ws + 96 * MB_);
  u32x4* const rstat = (u32x4*)(ws + 97 * MB_);
  char* const wcopy = ws + 98 * MB_;
  u64* const done1 = (u64*)(ws + 100 * MB_);
  u64* const done2 = (u64*)(ws + 100 * MB_ + 65536);
  u64* const donec = (u64*)(ws + 100 * MB_ + 131072);
  u64* const acks = (u64*)(ws + 100 * MB_ + 196608);
  u64* const cstamp = (u64*)(ws + 100 * MB_ + 262144);
  float* const csum = (float*)(ws + 101 * MB_);
  __shared__ __attribute__((aligned(1024))) char shm[163840];
  constexpr int AUX = 2 * G1_STAGE + 8192;
  {
    u64 pc; int len;
    asm volatile("s_getpc_b64 %0\n.Lcode_beg:\n\ts_mov_b32 %1, .Lcode_end-.Lcode_beg" : "=s"(pc), "=s"(len));
    const int ol = ((blockIdx.x >> 3) + 32 * (threadIdx.x >> 6)) * 1024 + (threadIdx.x & 63) * 16;
    asm volatile("s_mov_b32 m0, %2\n\ts_nop 0\n\tglobal_load_lds_dwordx4 %0, %1"
                 :: "v"(ol < len - 16 ? ol : 0), "s"(pc & ~15ull), "s"((unsigned)(uintptr_t)(lds_cptr)shm + 2 * G1_STAGE + 7168) : "m0");
    __builtin_amdgcn_sched_barrier(0);
  }
  const u64 t_own = __builtin_amdgcn_s_memrealtime();
  TSDECL;
  TS1(7);
  const int tid = threadIdx.x, lane = tid & 63;
  const int wid = __builtin_amdgcn_readfirstlane(tid >> 6);
  const int wm = wid & 3, wc = wid >> 2;
  const int bid = blockIdx.x, xcd = bid & 7, idx = bid >> 3;
  const int ntile = xcd * 4 + (idx & 3), mtile = idx >> 2;
  const int m0 = mtile * 256;
  const int hg = ntile >> 1;
  const bool tya = (ntile & 1) == 0;
  const int srow = m0 + ntile * 8 + wm * 2;
  lds_cptr shm3 = (lds_cptr)shm;
  const unsigned shm_base = (unsigned)(uintptr_t)shm3;
  const int r8 = tid >> 3, pos = tid & 7;
  const int phys = pos >> 1, half = pos & 1;
  const int fk = ((r8 >> 1) & 1) | (((r8 >> 3) & 1) << 1);
  const int vsrc = phys ^ fk;
  const int qstr = tya ? 3072 * 2 : F_ * 2;
  unsigned voa = (((r8 & 32) + 2 * (r8 & 15) + ((r8 >> 4) & 1)) * D_ + ((pos ^ (r8 & 7)) * 8)) * 2;
  unsigned vog = r8 * (F_ * 2) + vsrc * 32 + half * 16, voq = r8 * qstr + vsrc * 32 + half * 16;
  unsigned vom = r8 * qstr + (vsrc >> (tya ? 0 : 1)) * 32 + half * 16, vol = lane * 16;
  asm volatile("" : "+v"(voa), "+v"(vog), "+v"(voq), "+v"(vom), "+v"(vol));
  asm volatile("" :: "s"(x), "s"(wqkv), "s"(wg), "s"(wu), "s"(wd), "s"(wo), "s"(out), "s"(ws));
  const char* const ab = (const char*)(x + (size_t)m0 * D_);
  int kz = 0;
  asm volatile("" : "+s"(kz));
  auto stage_a = [&](int buf, int kt, int i, int h) {
    glds16_s(ab + (long)(((i * 64 + (h - 1) * 32) * D_ + kt * 64) * 2), voa, shm_base + buf * G1_STAGE + (wid - 4 + 4 * h) * 1024 + i * 8192);
  };
  if (wid >= 4) {
#pragma unroll
    for (int i = 0; i < 8; ++i) stage_a(0, kz, i >> 1, i & 1);
  } else {
#pragma unroll
    for (int i = 0; i < 4; ++i) glds16_s(x + (size_t)srow * D_ + i * 512, vol, shm_base + G1_STAGE + wid * 4096 + i * 1024);
  }
  const int kt0 = (hg >> 1) * 7, he = hg & 1;
  const char* const gb = (const char*)(wg + (kt0 + (tya ? 5 + he : 2 * he + 1)) * 64);
  const char* const qb = tya ? (const char*)(wqkv + 64 * hg) : gb - 128;
  const long dgu = (const char*)wu - (const char*)wg, dp1 = tya ? 2048 : dgu;
  const char* const mb0 = tya ? qb + 4096 : (const char*)(wg + (kt0 + 4) * 64 + 32 * he);
  const char* mptr = mb0 + (((vsrc & 1) && !tya) ? dgu : 0) + vom;
  auto wload = [&](int p, int rows, unsigned dst) {
    long off = (long)(rows * (p < 3 ? qstr : F_ * 2)) + (p == 1 ? dp1 : (p == 4 ? dgu : 0));
    if (p == 2) { asm volatile("" : "+s"(off)); GLDS_NC(mptr + off, dst); }
    else glds16_s((p < 2 ? qb : gb) + off, p < 2 ? voq : vog, dst);
  };
  const int fr = lane & 15, fq = lane >> 4, q4 = fr >> 2, pp = lane & 3;
  const int ff = ((q4 >> 1) & 1) | ((fq & 1) << 1);
  int aoff[2], boff[2];
#pragma unroll
  for (int ks = 0; ks < 2; ++ks) aoff[ks] = (wm * 64 + fr) * 128 + (((ks * 4 + fq) ^ (fr & 7)) * 16);
#pragma unroll
  for (int j = 0; j < 2; ++j) boff[j] = 32768 + (8 * fq + q4) * 128 + (((2 * wc + j) ^ ff) * 32) + pp * 8;

  f32x4 acc[4][10];
#pragma unroll
  for (int a = 0; a < 4; ++a)
#pragma unroll
    for (int b = 0; b < 10; ++b) acc[a][b] = f32x4{0.f, 0.f, 0.f, 0.f};

  auto stage_part = [&](int buf, int kt, int i, int h) {
    if (i < 5) wload(i, kt * 64 + (h - 1) * 32, shm_base + buf * G1_STAGE + (wid - 4 + 4 * h) * 1024 + 32768 + i * 8192);
    else stage_a(buf, kt, i - 5, h);
  };
  constexpr int NT = D_ / 64;
  constexpr int CNT = AUX + 4096 + 3584;
  if (tid < 8) *(unsigned*)(shm + CNT + tid * 4) = 0u;
  bf16x8 af0[4], af1[4];
  bf16x4 bl[4], bh[4];
#pragma unroll
  for (int i = 0; i < 4; ++i) { af0[i] = bf16x8{0, 0, 0, 0, 0, 0, 0, 0}; af1[i] = af0[i]; bl[i] = bf16x4{0, 0, 0, 0}; bh[i] = bf16x4{0, 0, 0, 0}; }
#define SB0() __builtin_amdgcn_sched_barrier(0)
  auto mm = [&](int g, int t) {
    const int mi = t >> 1, mf = ((g & 1) && g != 9) ? 3 - mi : mi, ks = (t & 1) ^ (mi & 1), s = ((g + 1) & 1) * 2 + ks;
    const bf16x8 b = __builtin_shufflevector(bl[s], bh[s], 0, 1, 2, 3, 4, 5, 6, 7);
    if ((g >> 1) == 2) acc[mf][g] = __builtin_amdgcn_mfma_f32_16x16x32_bf16(ks ? af1[mf] : af0[mf], b, acc[mf][g], 0, 0, 0);
    else acc[mf][g] = __builtin_amdgcn_mfma_f32_16x16x32_bf16(b, ks ? af1[mf] : af0[mf], acc[mf][g], 0, 0, 0);
  };
  auto kloop = [&](auto wmc) {
    constexpr bool LD = decltype(wmc)::value != 0;
    const int WID = wid;
    constexpr int DMP = 2 * G1_STAGE;
    const char* pa = nullptr;
    unsigned vob = 0;
    f32x4 ca = {0.f, 0.f, 0.f, 0.f}, cb = {0.f, 0.f, 0.f, 0.f};
    bf16x8 sel = {0, 0, 0, 0, 0, 0, 0, 0};
    int tro = 0;
    u64 tg = 0;
    const int psh = (wid == 2 && !tya) ? 1 : 0, pstr = wid < 3 ? qstr : F_ * 2;
    const char* const pbase = wid == 0 ? qb : (wid == 1 ? qb + dp1 : (wid == 2 ? mb0 : gb));
    if (!LD) {
      const int rs = mtile * 8 + (lane >> 3), vs = ((lane & 7) >> 1) ^ (((rs >> 1) & 1) | ((mtile & 1) << 1));
      pa = pbase + ((psh & vs) ? dgu : 0) + (rs * pstr + (vs >> psh) * 32 + (lane & 1) * 16);
      vob = rs * (F_ * 2) + vs * 32 + (lane & 1) * 16;
      const short one = fr == fq ? 0x3f80 : 0;
      sel = bf16x8{one, one, one, one, one, one, one, one};
      tro = q4 * 128 + ((fq ^ ((q4 >> 1) | ((mtile & 1) << 1))) * 32) + pp * 8;
      asm volatile("" : "+v"(sel), "+v"(tro), "+v"(ca), "+v"(cb));
    }
    auto share = [&](int t, unsigned dsta, unsigned dstb) {
      long off = (long)(t * 64 * pstr);
      asm volatile("" : "+s"(off));
      GLDS_NC(pa + off, shm_base + dsta);
      if (wid == 0) glds16_s(gb + dgu + (long)(t * 64 * F_ * 2), vob, shm_base + dstb);
    };
    auto accum = [&](int oa, int ob) {
      ca = __builtin_amdgcn_mfma_f32_16x16x32_bf16(tr_frag(shm3 + tro + oa), sel, ca, 0, 0, 0);
      if (wid == 0) cb = __builtin_amdgcn_mfma_f32_16x16x32_bf16(tr_frag(shm3 + tro + ob), sel, cb, 0, 0, 0);
    };
    constexpr int CG = 61440;
    auto cgather = [&]() {
      int l = threadIdx.x & 63;
      asm volatile("s_memrealtime %0\n\ts_waitcnt lgkmcnt(0)" : "=s"(tg), "+v"(l) :: "memory");
      if (l < 4) GLDS_SC1((const char*)(cstamp + (ntile * 4 + wid) * 8) + l * 16, shm_base + CG + 10240 + wid * 64);
#pragma unroll
      for (int i = 0; i < 2; ++i) {
        GLDS_SC1((const char*)(csum + (ntile * 5 + wid) * 512) + i * 1024 + l * 16, shm_base + CG + wid * 2048 + i * 1024);
        if (wid == 0) GLDS_SC1((const char*)(csum + (ntile * 5 + 4) * 512) + i * 1024 + l * 16, shm_base + CG + 8192 + i * 1024);
      }
    };
    if (LD) {
#pragma unroll
      for (int i = 0; i < 10; ++i) {
        stage_part(0, kz, i >> 1, i & 1);
        asm volatile("" : "+v"(acc[0][i]), "+v"(acc[1][i]), "+v"(acc[2][i]), "+v"(acc[3][i]));
      }
    }
    unsigned st0 = 0, st1 = 0;
    if (!LD) {
#pragma unroll
      for (int a = 0; a < 4; ++a)
        asm volatile("" : "+v"(acc[a][0]), "+v"(acc[a][1]), "+v"(acc[a][2]), "+v"(acc[a][3]), "+v"(acc[a][4]), "+v"(acc[a][5]), "+v"(acc[a][6]),
                     "+v"(acc[a][7]), "+v"(acc[a][8]), "+v"(acc[a][9]));
      WAIT_V(0);
      const char* const xr = shm + G1_STAGE + WID * 4096 + (lane >> 5) * 2048 + (lane & 31) * 16;
      const u32x4 xv0 = *(const u32x4*)xr, xv1 = *(const u32x4*)(xr + 512), xv2 = *(const u32x4*)(xr + 1024), xv3 = *(const u32x4*)(xr + 1536);
      f32x2 f[16];
#pragma unroll
      for (int i = 0; i < 4; ++i) {
        f[i] = f32x2{lo_f(xv0[i]), hi_f(xv0[i])}; f[4 + i] = f32x2{lo_f(xv1[i]), hi_f(xv1[i])};
        f[8 + i] = f32x2{lo_f(xv2[i]), hi_f(xv2[i])}; f[12 + i] = f32x2{lo_f(xv3[i]), hi_f(xv3[i])};
      }
      f32x2 sa = f[0], sb = f[1];
#pragma unroll
      for (int i = 2; i < 16; i += 2) { sa += f[i]; sb += f[i + 1]; }
      sa += sb;
      const float mean = half_sum(sa[0] + sa[1]) * (1.0f / D_);
      f32x2 qa = {0.f, 0.f}, qb = {0.f, 0.f};
#pragma unroll
      for (int i = 0; i < 16; i += 2) {
        const f32x2 d = f[i] - f32x2{mean, mean}, e = f[i + 1] - f32x2{mean, mean};
        qa = __builtin_elementwise_fma(d, d, qa); qb = __builtin_elementwise_fma(e, e, qb);
      }
      qa += qb;
      const unsigned ps = pack_stats(mean, half_sum(qa[0] + qa[1]) * (1.0f / D_));
      st0 = __builtin_amdgcn_readlane(ps, 0);
      st1 = __builtin_amdgcn_readlane(ps, 32);
    }
    asm volatile("" : "+v"(aoff[0]), "+v"(aoff[1]), "+v"(boff[0]), "+v"(boff[1]));
    TS1(0);
#pragma clang loop unroll(disable)
    for (int kt = 0; kt < NT; ++kt) {
      const int cur = kt & 1;
      if (LD) WAIT_V(0);
      BARL();
      lds_cptr sb = shm3;
      if (LD && kt + 1 < NT) {
#pragma unroll
        for (int i = 0; i < 18; ++i) stage_part(cur ^ 1, kt + 1, i >> 1, i & 1);
      }
      if (!LD && kt == NT - 1) cgather();
      auto rdb = [&](int g, int i) {
        lds_cptr p = sb + boff[g & 1] + (g >> 1) * 8192 + (i >> 1) * 4096;
        const int s = ((g + 1) & 1) * 2 + (i >> 1);
        if (i & 1) bh[s] = tr_read(p + 512); else bl[s] = tr_read(p);
      };
      auto rda = [&](int ks, int mf) { (ks ? af1 : af0)[mf] = *(const __attribute__((address_space(3))) bf16x8*)(sb + aoff[ks] + mf * 2048); };
      SB0();
      __builtin_amdgcn_s_waitcnt(0xC07F);
#pragma unroll
      for (int gg = 0; gg < 10; ++gg) {
        const int g = (gg + 9) % 10;
#pragma unroll
        for (int t = 0; t < 8; ++t) {
          SB0();
          mm(g, t);
          SB0();
          if (g == 9) {
            if (t == 0 || t == 2) { rdb(0, t); rdb(0, t + 1); }
            if (t & 1) { rda((t >> 1) & 1, t >> 1); rda(((t >> 1) & 1) ^ 1, t >> 1); }
          } else if (t < 4) rdb(g + 1, t);
        }
      }
      SB0();
      {
        const int d = cur ? -G1_STAGE : G1_STAGE;
        aoff[0] += d; aoff[1] += d; boff[0] += d; boff[1] += d;
        asm volatile("" : "+v"(aoff[0]), "+v"(aoff[1]), "+v"(boff[0]), "+v"(boff[1]));
      }
      if (!LD) {
        int lane = threadIdx.x & 63;
        asm volatile("" : "+v"(lane));
        const unsigned vol = lane * 16;
        if (kt >= 1 && kt <= 12) { WAIT_V(0); accum(DMP + wid * 1024, DMP + 4096); WAIT_L(0); }
        if (kt <= 1) share(kt + 2, DMP + 7168, DMP + 7168);
        if (kt <= 11) share(kt + 4, DMP + wid * 1024, DMP + 4096);
        if (kt <= 3) {
          const int sp = cur * G1_STAGE + 32768 + mtile * 1024;
          accum(sp + wid * 8192, sp + 4 * 8192);
        }
        if (kt == 12 && (lane & 15) < 4) {
          const __amdgpu_buffer_rsrc_t rc = __builtin_amdgcn_make_buffer_rsrc((void*)(csum + ntile * 5 * 512 + mtile * 64), 0, 5 * 2048, 0x00020000);
          const int co = (lane & 15) * 64 + (lane >> 4) * 16;
          __builtin_amdgcn_raw_buffer_store_b128(__builtin_bit_cast(u32x4, ca), rc, co + wid * 2048, 0, 16);
          if (wid == 0) __builtin_amdgcn_raw_buffer_store_b128(__builtin_bit_cast(u32x4, cb), rc, co + 4 * 2048, 0, 16);
        }
        if (kt == 13) {
          WAIT_V(0);
          u64 now;
          asm volatile("s_memrealtime %0\n\ts_waitcnt lgkmcnt(0)" : "=s"(now) :: "memory");
          if (lane == 0) __hip_atomic_store(&cstamp[(ntile * 4 + wid) * 8 + mtile], now, RLX_AGENT);
        }
        if (kt >= CP_K0 && kt < CP_K0 + 3) {
          int ln = threadIdx.x & 63;
          asm volatile("" : "+v"(ln));
          const int q1 = bid * 4 + WID + (kt - CP_K0) * 1024, q0 = q1 - 1024;
          if (q0 >= 0 && q0 < 1152) {
            const __amdgpu_buffer_rsrc_t rc = __builtin_amdgcn_make_buffer_rsrc((void*)(wcopy + (q0 & 1) * 589824 + (q0 >> 1) * 1024), 0, 1024, 0x00020000);
            __builtin_amdgcn_raw_buffer_store_b128(*(const u32x4*)(shm + AUX + WID * 1024 + ln * 16), rc, ln * 16, 0, 16);
            WAIT_L(0);
          }
          if (q1 < 1152) {
            const char* src = (const char*)((q1 >> 1) < 448 ? wd + (size_t)(q1 >> 1) * 8 * D_ : wo + (size_t)((q1 >> 1) - 448) * 8 * D_);
            src += ((3 - ((unsigned)(uintptr_t)src >> 7)) & 7) * 128 + (q1 & 1) * 1024;
            GLDS(src + (ln >> 3) * 2048 + (ln & 7) * 16, shm_base + AUX + WID * 1024);
          }
        }
        if (WID == 0 && kt == CP_K0 + 4) {
          u64 now;
          asm volatile("s_memrealtime %0\n\ts_waitcnt lgkmcnt(0)" : "=s"(now) :: "memory");
          if (lane == 0) __hip_atomic_store(&donec[bid], now, RLX_AGENT);
        }
        if (kt >= PF_K0 && kt < PF_K0 + 5) {
#pragma unroll
          for (int i = 0; i < 2; ++i) {
            const int c = (bid * 4 + wid) * 10 + (kt - PF_K0) * 2 + i;
            if (c < 9216) {
              const u16* src = (c < 7168 ? wd + (size_t)c * 512 : wo + (size_t)(c - 7168) * 512) + lane * 8;
              GLDS_NC(src, shm_base + DMP + 7168);
            }
          }
        }
        if (kt == PUB_K) {
          u64 now;
          asm volatile("s_memrealtime %0\n\ts_waitcnt lgkmcnt(0)" : "=s"(now) :: "memory");
          if (lane == 0) {
            const __amdgpu_buffer_rsrc_t rs = __builtin_amdgcn_make_buffer_rsrc((void*)(rstat + srow), 0, 32, 0x00020000);
            __builtin_amdgcn_raw_buffer_store_b128(u32x4{st0, (unsigned)now, st0, (unsigned)(now >> 32)}, rs, 0, 0, 16);
            __builtin_amdgcn_raw_buffer_store_b128(u32x4{st1, (unsigned)now, st1, (unsigned)(now >> 32)}, rs, 16, 0, 16);
          }
        }
        if (kt == NT - 3) GLDS_SC1(rstat + m0 + wid * 64 + (lane & 32) + 2 * (lane & 15) + ((lane >> 4) & 1), shm_base + AUX + wid * 1024);
        if (WID == 2 && kt == 8) GLDS_SC1((const char*)(done1 + (bid & ~3) * 32) + lane * 16, shm_base + DMP + 7168);
        if (WID >= 2 && kt == NT - 3) GLDS_SC1((const char*)donec + (WID - 2) * 1024 + lane * 16, shm_base + DMP + WID * 2048);
        if (kt == NT - 2) {
          WAIT_V(0);
          int ln = threadIdx.x;
          asm volatile("" : "+v"(ln));
          if (WID >= 2) {
            const u32x4 c4 = *(const u32x4*)(shm + DMP + WID * 1024 + ln * 16);
            const bool ok = __all(fresh(((u64)c4[1] << 32) | c4[0], t_own) & fresh(((u64)c4[3] << 32) | c4[2], t_own));
            *(unsigned*)(shm + CNT - 4 + WID * 4) = ok ? 1u : 0u;
          }
          u32x4 gr = *(const u32x4*)(shm + AUX + ln * 16);
          if (__builtin_expect(!__all((gr[0] == gr[2]) & fresh(((u64)gr[3] << 32) | gr[1], t_own)), 0)) {
            const __amdgpu_buffer_rsrc_t rs = __builtin_amdgcn_make_buffer_rsrc((void*)(rstat + m0), 0, 4096, 0x00020000);
            const int prow = (ln & 0xe0) + 2 * (ln & 15) + ((ln >> 4) & 1);
            for (;;) {
              gr = __builtin_bit_cast(u32x4, __builtin_amdgcn_raw_buffer_load_b128(rs, prow * 16, 0, 16));
              if (__all((gr[0] == gr[2]) & fresh(((u64)gr[3] << 32) | gr[1], t_own))) break;
              if (__builtin_amdgcn_s_memrealtime() - t_own > TIMEOUT_) { gr[0] = row_stats_serial(x + (size_t)(m0 + prow) * D_); break; }
              __builtin_amdgcn_s_sleep(2);
            }
          }
          const float nm = -lo_f(gr[0]), dn = hi_f(gr[0]), r = __builtin_amdgcn_rcpf(dn);
          const f32x4 fc = {nm, -2.3022081986f * r, -0.10294324f * r * r * r, dn * dn};
          *(f32x4*)(shm + AUX + ln * 16) = fc;
          float* const tw = (float*)(shm + 2 * G1_STAGE + WID * 768) + ln;
#pragma unroll
          for (int i = 0; i < 4; ++i) tw[i * 64] = fc[i];
          ((float*)(shm + AUX + 4096))[384 + ln] = r;
        }
      }
      SB0();
    }
#pragma unroll
    for (int t = 0; t < 8; ++t) mm(9, t);
    if (!LD) {
      SB0();
      int lane = threadIdx.x & 63;
      asm volatile("s_waitcnt vmcnt(0)" : "+v"(lane) :: "memory");
      bool late = false;
      while (__builtin_expect(!__all(*(const u64*)(shm + CG + 10240 + wid * 64 + (lane & 7) * 8) - t_own - 100ull < tg - t_own - 200ull), 0)) {
        late = __builtin_amdgcn_s_memrealtime() - t_own > TIMEOUT_;
        if (late) break;
        __builtin_amdgcn_s_sleep(2);
        cgather();
        WAIT_V(0);
      }
      const char* const sc = shm + CG + (lane < 32 ? wid * 2048 : 8192 - 256) + lane * 8;
      f32x2 t = *(const f32x2*)sc;
#pragma unroll
      for (int r = 1; r < 8; ++r) t += *(const f32x2*)(sc + r * 256);
      if (__builtin_expect(late, 0)) {
        const int gq = (lane >> 3) & 3;
        const char* pc = lane < 32 ? pbase + ((psh & gq) ? dgu : 0) + (gq >> psh) * 32 + (lane & 7) * 4 : gb + dgu + (lane & 31) * 4;
        t = f32x2{0.f, 0.f};
#pragma unroll 1
        for (int k = 0; k < D_; ++k) { const unsigned w = *(const unsigned*)(pc + (size_t)k * (lane < 32 ? pstr : F_ * 2)); t += f32x2{lo_f(w), hi_f(w)}; }
      }
      if (lane < 32 || wid == 0)
        *(f32x2*)((float*)(shm + AUX + 4096) + ((lane >> 4) & 1) * 192 + (lane < 32 ? wid : 4) * 32 + (lane & 15) * 2) = t;
    }
  };
  if (wid < 4) kloop(std::integral_constant<int, 0>{});
  else kloop(std::integral_constant<int, 1>{});
  TS1(1);
  __builtin_amdgcn_sched_barrier(0);
  int tz = threadIdx.x;
  asm volatile("" : "+v"(tz));
  __builtin_amdgcn_sched_barrier(0);
  const int li = tz & 15, lq = (tz >> 4) & 3, ln = tz & 63;
  const __amdgpu_buffer_rsrc_t ra = __builtin_amdgcn_make_buffer_rsrc((void*)abuf, 0, 16 * 72 * 16384, 0x00020000);
  float* const cfin = (float*)(shm + AUX + 4096);
  float* const svr = cfin + 384;
  float* const tb = (float*)(shm + 2 * G1_STAGE + wm * 1024);
  typedef __attribute__((address_space(3))) unsigned lds_u32;
  lds_u32* const cnt = (lds_u32*)(shm3 + CNT);
  auto signal = [&](unsigned v) {
    asm volatile("" ::: "memory");
    if (ln == 0) __hip_atomic_fetch_add(cnt, v, __ATOMIC_RELAXED, __HIP_MEMORY_SCOPE_WORKGROUP);
    asm volatile("" ::: "memory");
  };
  auto await = [&](unsigned mask, unsigned val) {
    while ((__builtin_amdgcn_readfirstlane(__hip_atomic_load(cnt, __ATOMIC_RELAXED, __HIP_MEMORY_SCOPE_WORKGROUP)) & mask) != val) __builtin_amdgcn_s_sleep(1);
    asm volatile("" ::: "memory");
  };
  if (wc) __builtin_amdgcn_s_setprio(1);
  signal(1u << (8 * wc));
  await(0xffu, 4u);
  TS1(4);
  f32x4 rf[4];
#pragma unroll
  for (int mf = 0; mf < 4; ++mf) rf[mf] = *(const f32x4*)(shm + AUX + (wm * 64 + mf * 16 + li) * 16);
  const float* const cl = cfin + wc * 192 + lq * 4;
  const int sbase = ((mtile * 2 + (wm >> 1)) * 72 * 8 + (wm & 1) * 4) * 2048;
  auto putf = [&](int kg, int mf, u32x4 v) {
    __builtin_amdgcn_raw_buffer_store_b128(v, ra, ln * 16 + sbase + ((kg >> 1) * 8 + mf) * 2048 + (kg & 1) * 1024, 0, 16);
  };
  auto gelp = [&](f32x2 g, f32x2 u, f32x2 cg, f32x2 cu, f32x2 m2, f32x2 k12, f32x2 k22, f32x2 d2) {
    const f32x2 tg = __builtin_elementwise_fma(m2, cg, g), tu = __builtin_elementwise_fma(m2, cu, u);
    const f32x2 arg = tg * __builtin_elementwise_fma(tg * tg, k22, k12);
    const f32x2 dn = __builtin_elementwise_fma(f32x2{__builtin_amdgcn_exp2f(arg[0]), __builtin_amdgcn_exp2f(arg[1])}, d2, d2);
    const f32x2 r = (tg * tu) * f32x2{__builtin_amdgcn_rcpf(dn[0]), __builtin_amdgcn_rcpf(dn[1])};
    return pack2(r[0], r[1]);
  };
  auto gel2 = [&](int p, int kg, int mf0 = 0, int mf1 = 4) {
    f32x4 cg[2], cu[2];
#pragma unroll
    for (int j = 0; j < 2; ++j) { cg[j] = *(const f32x4*)(cl + p * 32 + j * 16); cu[j] = *(const f32x4*)(cl + p * 32 + 32 + j * 16); }
#pragma unroll
    for (int mf = mf0; mf < mf1; ++mf) {
      const f32x2 m2 = {rf[mf][0], rf[mf][0]}, k12 = {rf[mf][1], rf[mf][1]}, k22 = {rf[mf][2], rf[mf][2]}, d2 = {rf[mf][3], rf[mf][3]};
      u32x4 o;
#pragma unroll
      for (int i = 0; i < 4; ++i) {
        const int j = i >> 1, e = (i & 1) * 2;
        const f32x4 g = acc[mf][p * 2 + j], u = acc[mf][p * 2 + 2 + j];
        o[i] = gelp(f32x2{g[e], g[e + 1]}, f32x2{u[e], u[e + 1]}, f32x2{cg[j][e], cg[j][e + 1]}, f32x2{cu[j][e], cu[j][e + 1]}, m2, k12, k22, d2);
      }
      putf(kg, mf, o);
    }
  };
  int tsel = ntile & 1;
  asm volatile("" : "+s"(tsel));
  if (tsel != 0) {
    const float cg = cfin[wc * 192 + 64 + li], cu = cfin[wc * 192 + 80 + li];
#pragma unroll
    for (int mf = 0; mf < 4; ++mf) {
      f32x4 t[4];
#pragma unroll
      for (int i = 0; i < 4; ++i) t[i] = *(const f32x4*)(tb + i * 64 + mf * 16 + lq * 4);
      const f32x4 g = acc[mf][4], u = acc[mf][5];
      u32x2 o;
#pragma unroll
      for (int e = 0; e < 4; e += 2)
        o[e >> 1] = gelp(f32x2{g[e], g[e + 1]}, f32x2{u[e], u[e + 1]}, f32x2{cg, cg}, f32x2{cu, cu}, f32x2{t[0][e], t[0][e + 1]},
                         f32x2{t[1][e], t[1][e + 1]}, f32x2{t[2][e], t[2][e + 1]}, f32x2{t[3][e], t[3][e + 1]});
      *(u32x2*)(shm + wm * 4096 + mf * 1024 + wc * 512 + li * 32 + lq * 8) = o;
    }
    if (wc == 0) signal(0x10000u << wid);
    gel2(0, 2 * (kt0 + 2 * he) + wc);
    TS1(5);
    if (wc) {
      await(0x1000u << wid, 0x1000u << wid);
#pragma unroll
      for (int mf = 0; mf < 4; ++mf) putf(2 * (kt0 + 4) + he, mf, __builtin_bit_cast(u32x4, tr_frag(shm3 + wm * 4096 + mf * 1024 + ln * 8)));
    }
    gel2(3, 2 * (kt0 + 2 * he + 1) + wc);
    TS1(6);
  } else {
    float rd[4];
#pragma unroll
    for (int mf = 0; mf < 4; ++mf) rd[mf] = svr[wm * 64 + mf * 16 + li];
    auto cvq = [&](int p, int mf, const f32x4& c0, const f32x4& c1) {
      u32x4 o;
#pragma unroll
      for (int j = 0; j < 2; ++j) {
        const f32x4 a = acc[mf][p * 2 + j], c4 = j ? c1 : c0;
        const f32x2 m2 = {rf[mf][0], rf[mf][0]}, r2 = {rd[mf], rd[mf]};
        f32x2 t0 = __builtin_elementwise_fma(m2, f32x2{c4[0], c4[1]}, f32x2{a[0], a[1]});
        f32x2 t1 = __builtin_elementwise_fma(m2, f32x2{c4[2], c4[3]}, f32x2{a[2], a[3]});
        if (p) { t0 *= r2; t1 *= r2; }
        o[2 * j] = pack2(t0[0], t0[1]); o[2 * j + 1] = pack2(t1[0], t1[1]);
      }
      return o;
    };
    const float cv0 = cfin[wc * 192 + 64 + li], cv1 = cfin[wc * 192 + 80 + li];
    auto cvv = [&](int mf) {
      const f32x4 n4 = *(const f32x4*)(tb + mf * 16 + lq * 4), r4 = *(const f32x4*)(svr + wm * 64 + mf * 16 + lq * 4);
      u32x4 o;
#pragma unroll
      for (int j = 0; j < 2; ++j) {
        const f32x4 a = acc[mf][4 + j];
        const f32x2 c2 = {j ? cv1 : cv0, j ? cv1 : cv0};
        const f32x2 t0 = __builtin_elementwise_fma(f32x2{n4[0], n4[1]}, c2, f32x2{a[0], a[1]}) * f32x2{r4[0], r4[1]};
        const f32x2 t1 = __builtin_elementwise_fma(f32x2{n4[2], n4[3]}, c2, f32x2{a[2], a[3]}) * f32x2{r4[2], r4[3]};
        o[2 * j] = pack2(t0[0], t0[1]); o[2 * j + 1] = pack2(t1[0], t1[1]);
      }
      return o;
    };
    u32x4 kx[4], vx[4], qx[4], hx[4];
    const bf16x8 ones = {0x3f80, 0x3f80, 0x3f80, 0x3f80, 0x3f80, 0x3f80, 0x3f80, 0x3f80};
    const f32x4 ck0 = *(const f32x4*)(cl + 32), ck1 = *(const f32x4*)(cl + 48);
    kx[2] = cvq(1, 2, ck0, ck1); kx[3] = cvq(1, 3, ck0, ck1);
    vx[2] = cvv(2); vx[3] = cvv(3);
    const bool giveh = wm == 3 && mtile < 7, needh = wm == 0 && mtile > 0;
    char* const hptr = (char*)halo + (size_t)((needh ? mtile - 1 : mtile) * 32 + ntile + wc) * 16384;
    if (wm < 3) {
      char* const ex = shm + wid * 4096 + ln * 16;
      *(u32x4*)ex = kx[2]; *(u32x4*)(ex + 1024) = kx[3]; *(u32x4*)(ex + 2048) = vx[2]; *(u32x4*)(ex + 3072) = vx[3];
      signal(0x10000u << wid);
    } else if (giveh) {
      const __amdgpu_buffer_rsrc_t rh = __builtin_amdgcn_make_buffer_rsrc((void*)hptr, 0, 9216, 0x00020000);
      const u64 now = __builtin_amdgcn_s_memrealtime();
      const unsigned tag = (unsigned)now;
#pragma unroll
      for (int r = 0; r < 8; ++r) {
        const u32x4 w = r < 4 ? kx[2 + (r >> 1)] : vx[r >> 1];
        __builtin_amdgcn_raw_buffer_store_b128(u32x4{w[2 * (r & 1)], tag, w[2 * (r & 1) + 1], tag}, rh, ln * 16 + r * 1024, 0, 16);
      }
      __builtin_amdgcn_raw_buffer_store_b128(u32x4{tag, (unsigned)(now >> 32), tag, (unsigned)(now >> 32)}, rh, ln * 16 + 8192, 0, 16);
    }
    kx[0] = cvq(1, 0, ck0, ck1); kx[1] = cvq(1, 1, ck0, ck1);
    vx[0] = cvv(0); vx[1] = cvv(1);
    {
      const f32x4 cq0 = *(const f32x4*)(cl), cq1 = *(const f32x4*)(cl + 16);
#pragma unroll
      for (int mf = 0; mf < 4; ++mf) qx[mf] = cvq(0, mf, cq0, cq1);
    }
    f32x4 mk0, mk1;
#pragma unroll
    for (int j = 0; j < 4; ++j) {
      const int k0 = 4 * lq + j, k1 = 16 + 4 * lq + j;
      mk0[j] = (k0 >= li + 9 && k0 <= li + 16) ? 0.f : -1.0e30f;
      mk1[j] = (k1 >= li + 9 && k1 <= li + 16) ? 0.f : -1.0e30f;
    }
    auto max3 = [](float a, float b, float c) { float r; asm("v_max3_f32 %0, %1, %2, %3" : "=v"(r) : "v"(a), "v"(b), "v"(c)); return r; };
    auto max2 = [](float a, float b) { float r; asm("v_max_f32 %0, %1, %2" : "=v"(r) : "v"(a), "v"(b)); return r; };
    auto tiles = [&](int t0, const u32x4* kp, const u32x4* vp, bool first_rows) {
      const f32x4 z = {0.f, 0.f, 0.f, 0.f};
      const f32x4 mz = {-1.0e30f, -1.0e30f, -1.0e30f, -1.0e30f};
      f32x4 s0[2], s1[2];
#pragma unroll
      for (int n = 0; n < 2; ++n) {
        const bf16x8 qf = __builtin_bit_cast(bf16x8, qx[t0 + n]);
        s0[n] = __builtin_amdgcn_mfma_f32_16x16x32_bf16(__builtin_bit_cast(bf16x8, kp[n]), qf, first_rows ? mz : mk0, 0, 0, 0);
        s1[n] = __builtin_amdgcn_mfma_f32_16x16x32_bf16(__builtin_bit_cast(bf16x8, kx[t0 + n]), qf, mk1, 0, 0, 0);
      }
      SB0();
      asm volatile("s_nop 7\n\ts_nop 3" : "+v"(s0[0]), "+v"(s1[0]), "+v"(s0[1]), "+v"(s1[1]));
      float mx[2];
#pragma unroll
      for (int n = 0; n < 2; ++n) mx[n] = max3(max3(s0[n][0], s0[n][1], s0[n][2]), max3(s0[n][3], s1[n][0], s1[n][1]), max2(s1[n][2], s1[n][3]));
#pragma unroll
      for (int n = 0; n < 2; ++n) { auto r = __builtin_amdgcn_permlane16_swap(__float_as_uint(mx[n]), __float_as_uint(mx[n]), false, false);
        mx[n] = max2(__uint_as_float(r[0]), __uint_as_float(r[1])); }
#pragma unroll
      for (int n = 0; n < 2; ++n) { auto r = __builtin_amdgcn_permlane32_swap(__float_as_uint(mx[n]), __float_as_uint(mx[n]), false, false);
        mx[n] = max2(__uint_as_float(r[0]), __uint_as_float(r[1])); }
      SB0();
      constexpr float cs2 = 0.17677669529663687f * 1.4426950408889634f;
      f32x4 os[2], o0[2], o1[2];
#pragma unroll
      for (int n = 0; n < 2; ++n) {
        const float c = cs2 * rd[t0 + n], nm = -mx[n] * c;
        float e8[8];
#pragma unroll
        for (int j = 0; j < 4; ++j) {
          e8[j] = __builtin_amdgcn_exp2f(__builtin_fmaf(s0[n][j], c, nm));
          e8[4 + j] = __builtin_amdgcn_exp2f(__builtin_fmaf(s1[n][j], c, nm));
        }
        u32x4 pk;
#pragma unroll
        for (int j = 0; j < 4; ++j) pk[j] = pack2(e8[2 * j], e8[2 * j + 1]);
        const bf16x8 pf = __builtin_bit_cast(bf16x8, pk);
        const u32x4 va = {vp[n][0], vp[n][1], vx[t0 + n][0], vx[t0 + n][1]}, vb = {vp[n][2], vp[n][3], vx[t0 + n][2], vx[t0 + n][3]};
        os[n] = __builtin_amdgcn_mfma_f32_16x16x32_bf16(ones, pf, z, 0, 0, 0);
        o0[n] = __builtin_amdgcn_mfma_f32_16x16x32_bf16(__builtin_bit_cast(bf16x8, va), pf, z, 0, 0, 0);
        o1[n] = __builtin_amdgcn_mfma_f32_16x16x32_bf16(__builtin_bit_cast(bf16x8, vb), pf, z, 0, 0, 0);
      }
#pragma unroll
      for (int n = 0; n < 2; ++n) {
        const float inv = __builtin_amdgcn_rcpf(os[n][0]);
        const f32x4 n0 = o0[n] * inv, n1 = o1[n] * inv;
        putf(112 + ntile + wc, t0 + n, u32x4{pack2(n0[0], n0[1]), pack2(n0[2], n0[3]), pack2(n1[0], n1[1]), pack2(n1[2], n1[3])});
      }
    };
    TS1(5);
    tiles(2, &kx[0], &vx[0], false);
    constexpr int HL = 40960;
    auto hload = [&]() {
#pragma unroll
      for (int r = 0; r < 9; ++r) GLDS_SC1(hptr + r * 1024 + ln * 16, shm_base + HL + wc * 10240 + r * 1024);
    };
    if (needh) hload();
    gel2(3, 2 * (kt0 + 5 + he) + wc, 0, 2);
    TS1(6);
    if (wm > 0) {
      await(0x8000u << wid, 0x8000u << wid);
#pragma unroll
      for (int i = 0; i < 4; ++i) hx[i] = *(const u32x4*)(shm + (wid - 1) * 4096 + i * 1024 + ln * 16);
    } else if (!needh) {
#pragma unroll
      for (int i = 0; i < 4; ++i) hx[i] = u32x4{0u, 0u, 0u, 0u};
    } else {
      bool late = false;
      WAIT_V(2);
      for (;;) {
        const char* const hl = shm + HL + wc * 10240 + ln * 16;
        const u32x4 sp = *(const u32x4*)(hl + 8192);
        unsigned df = 0;
#pragma unroll
        for (int i = 0; i < 4; ++i) {
          const u32x4 g0 = *(const u32x4*)(hl + i * 2048), g1 = *(const u32x4*)(hl + i * 2048 + 1024);
          df |= (g0[1] ^ sp[0]) | (g0[3] ^ sp[0]) | (g1[1] ^ sp[0]) | (g1[3] ^ sp[0]);
          hx[i] = u32x4{g0[0], g0[2], g1[0], g1[2]};
        }
        if (__builtin_expect(__all((df == 0) & fresh(((u64)sp[1] << 32) | sp[0], t_own)), 1)) break;
        late = __builtin_amdgcn_s_memrealtime() - t_own > TIMEOUT_;
        if (late) break;
        __builtin_amdgcn_s_sleep(1);
        WAIT_L(0);
        hload();
        WAIT_V(0);
      }
      if (__builtin_expect(late, 0)) {
        char* const fb = shm + (wid + 3) * 4096 + ln * 16;
#pragma unroll 1
        for (int i = 0; i < 32; ++i) {
          const int rec = i >> 3, e = i & 7;
          const int row = m0 - 32 + 2 * (rec < 2 ? li : lq * 4 + (e & 3)) + (rec & 1);
          const int col = (rec < 2 ? D_ + lq * 4 + (e & 3) : 2 * D_ + li) + (ntile + wc) * 32 + (e >> 2) * 16;
          const u16* xr = x + (size_t)row * D_;
          const unsigned stt = row_stats_serial(xr);
          const float mb = lo_f(stt);
          float a = 0.f;
#pragma unroll 1
          for (int k = 0; k < D_; ++k) a += (bf2f(xr[k]) - mb) * bf2f(wqkv[(size_t)k * 3072 + col]);
          *(u16*)(fb + rec * 1024 + e * 2) = f2bf(a / hi_f(stt));
        }
#pragma unroll
        for (int i = 0; i < 4; ++i) hx[i] = *(const u32x4*)(fb + i * 1024);
      }
    }
    tiles(0, &hx[0], &hx[2], mtile == 0 && wm == 0);
    gel2(3, 2 * (kt0 + 5 + he) + wc, 2, 4);
  }
  TS1(2);
  auto stamp = [&](u64* p) {
    WAIT_V(0);
    u64 now;
    asm volatile("s_memrealtime %0\n\ts_waitcnt lgkmcnt(0)" : "=s"(now) :: "memory");
    if ((threadIdx.x & 63) == 0) __hip_atomic_store(p, now, RLX_AGENT);
  };
  auto client = [&](int l) { const int m128 = mtile * 2 + ((l >> 4) & 1); return (((l & 7) * 4 + (m128 & 3)) * 8) + ((m128 >> 2) ^ ((l & 8) ? 5 : 0)); };
  auto notify = [&](int l0, int nl) {
    u64 now;
    asm volatile("s_memrealtime %0\n\ts_waitcnt lgkmcnt(0)" : "=s"(now) :: "memory");
    int l = threadIdx.x & 63;
    asm volatile("" : "+v"(l));
    if (l < nl) __hip_atomic_store(&done1[client(l0 + l) * 32 + ntile], now, RLX_AGENT);
  };
  {
    WAIT_V(0);
    unsigned old = 0;
    if (ln == 0) old = __hip_atomic_fetch_add(cnt + 4 + (wm >> 1), 1u, __ATOMIC_RELAXED, __HIP_MEMORY_SCOPE_WORKGROUP);
    if (__builtin_amdgcn_readfirstlane(old) == 3u) notify((wm >> 1) * 16, 16);
  }
  TS1(3);
  TSFLUSH(1);
  if (wc) await(0xffffu, 0x404u);
  const bool cok = __builtin_amdgcn_readfirstlane(cnt[1] & cnt[2]) != 0u;
  const bool tya1 = tya;
  __builtin_amdgcn_s_setprio(0);
  {
  __builtin_amdgcn_sched_barrier(0);
  int tid = threadIdx.x;
  asm volatile("" : "+v"(tid));
  __builtin_amdgcn_sched_barrier(0);
  const int lane = tid & 63;
  const int wm = wid >> 1, wn = wid & 1;
  const int role = 1 - (xcd >> 2);
  const int mtile = (role ? xcd : xcd ^ 5) * 4 + (idx & 3), ntile = idx >> 2;
  const int m0 = mtile * 128, n0 = ntile * 128;
  constexpr int PD = G2_NSTAGE - 1;
  constexpr int NY = 28;
  const int ny = (xcd & 1) ? NY - DLT : NY + DLT;
  TS(0);
  constexpr int EP = 288;
  char* const img = shm + ((ny + 19) % G2_NSTAGE) * G2_STAGE;
  if (wid >= 4) {
    const int p = wid - 4;
    unsigned vr[2], vc[2], vb[2][2];
#pragma unroll
    for (int v = 0; v < 2; ++v) {
      const int vt = p * 128 + v * 64 + lane;
      const int r8 = vt >> 3, pos = vt & 7;
      const int phys = pos >> 1, hf = pos & 1;
      const int fk = ((r8 >> 1) & 1) | (((r8 >> 3) & 1) << 1);
      vr[v] = (r8 >> 5) * 32 + ((r8 >> 2) & 1) * 16 + ((r8 >> 3) & 3) * 4 + (r8 & 3);
      vc[v] = (phys ^ fk) * 32 + hf * 16;
    }
    const unsigned va = lane * 16;
    const char* sa = (const char*)abuf + (size_t)(mtile * 72 + role * NY) * 16384 + p * 4096;
    const char* sw[2];
    int ws[2];
    auto wsetup = [&](const u16* w, int row0, int crow0) {
#pragma unroll
      for (int i = 0; i < 2; ++i) {
        const bool slow = cok && (((((unsigned)(uintptr_t)w >> 7) + ntile * 2 + i) & 7) == 3);
        const int sh = slow ? 7 : 11;
        sw[i] = slow ? wcopy + (ntile >> 2) * 589824 + crow0 * 128 : (const char*)(w + (size_t)row0 * D_ + n0 + i * 64);
        ws[i] = 64 << sh;
#pragma unroll
        for (int v = 0; v < 2; ++v) vb[i][v] = (vr[v] << sh) + vc[v];
      }
    };
    wsetup(wd, role * NY * 64, role * NY * 64);
    unsigned sl = shm_base + 2 * G2_STAGE;
#define G2_W "s_add_u32 m0, %[l], %[ow]\n\ts_nop 0\n\tglobal_load_lds_dwordx4 %[v0], %[w0] sc1\n\t" \
             "s_add_u32 m0, m0, 1024\n\ts_nop 0\n\tglobal_load_lds_dwordx4 %[v1], %[w0] sc1\n\t" \
             "s_add_u32 m0, m0, 7168\n\ts_nop 0\n\tglobal_load_lds_dwordx4 %[v2], %[w1] sc1\n\t" \
             "s_add_u32 m0, m0, 1024\n\ts_nop 0\n\tglobal_load_lds_dwordx4 %[v3], %[w1] sc1"
#define G2_WOP [ow] "s"(16384 + p * 2048), [w0] "s"(sw[0]), [w1] "s"(sw[1]), [v0] "v"(vb[0][0]), [v1] "v"(vb[0][1]), [v2] "v"(vb[1][0]), [v3] "v"(vb[1][1])
    auto adv = [&]() { sa += 16384; sw[0] += ws[0]; sw[1] += ws[1]; sl = sl == shm_base + (G2_NSTAGE - 1) * G2_STAGE ? shm_base : sl + G2_STAGE; };
    auto stage_y = [&]() {
      asm volatile("s_add_u32 m0, %[l], %[oa]\n\ts_nop 0\n\tglobal_load_lds_dwordx4 %[va], %[a] sc1\n\tglobal_load_lds_dwordx4 %[va], %[a] offset:1024 sc1\n\t"
                   "global_load_lds_dwordx4 %[va], %[a] offset:2048 sc1\n\tglobal_load_lds_dwordx4 %[va], %[a] offset:3072 sc1\n\t" G2_W
                   :: [l] "s"(sl), [oa] "s"((p ^ role) * 4096), [a] "s"(sa), [va] "v"(va), G2_WOP : "memory", "scc", "m0");
      adv();
    };
    auto stage_s = [&]() {
      asm volatile("s_add_u32 m0, %[l], %[oa]\n\ts_nop 0\n\tglobal_load_lds_dwordx4 %[va], %[a] sc1\n\tglobal_load_lds_dwordx4 %[va], %[a] offset:1024 sc1\n\t" G2_W
                   :: [l] "s"(sl), [oa] "s"((p >> 1) * 8192 + (p & 1) * 2048), [a] "s"(sa), [va] "v"(va), G2_WOP : "memory", "scc", "m0");
      adv();
    };
    unsigned ready = 0;
    const u64* const mbox = &done1[bid * 32 + (lane & 31)];
    auto need = [&](unsigned mask, bool first = false) {
      while ((ready & mask) != mask) {
        ready = (unsigned)__ballot(fresh(__hip_atomic_load(mbox, RLX_AGENT), t_own));
        if ((ready & mask) != mask) {
          __builtin_amdgcn_s_sleep(PSLEEP);
          if (__builtin_expect(!cok, 0) && (!first || __builtin_amdgcn_readfirstlane(cnt[4] & cnt[5]) == 4u)) notify(0, 32);
        }
      }
      asm volatile("" ::: "memory");
    };
    unsigned gm = 0xfu << (16 * role);
    need(gm & 0x22222222u, true);
    TS(4);
    stage_y();
    stage_y();
    WAIT_V(8);
    BARL();
    need(gm & 0xaaaaaaaau);
    stage_y();
    stage_y();
    gm >>= 4;
    int left = 1, span = 2;
    u64 fl = 0;
#pragma unroll 1
    for (int g = 0; g < ny - PD; ++g) {
      WAIT_V(16);
      BAR();
      if (__builtin_expect(left == 0, 0)) {
        asm volatile("" : "+v"(fl));
        ready |= (unsigned)__ballot(fresh(fl, t_own));
        left = span; span = 7; gm = gm ? gm << 4 : 0xf;
        if (g == NY - PD) {
          gm = 0x55555555u;
          const int t = (2 - role) * NY - DLT;
          sa = (const char*)abuf + (size_t)(mtile * 72 + t) * 16384 + p * 4096;
          wsetup(wd, t * 64, t * 64);
        }
        need(gm);
      }
      if (__builtin_expect(min(left, ny - PD - g) == 2, 0)) asm volatile("global_load_dwordx2 %0, %1, off sc1" : "+v"(fl) : "v"(mbox) : "memory");
      --left;
      stage_y();
    }
    WAIT_V(16);
    asm volatile("" : "+v"(fl));
    ready |= (unsigned)__ballot(fresh(fl, t_own));
    need(0x55555555u);
    sa = (const char*)abuf + (size_t)(mtile * 72 + 56) * 16384 + (p >> 1) * 8192 + role * 4096 + (p & 1) * 2048;
    wsetup(wo, 0, F_);
    const u64* const pf = done2 + (bid ^ 5) * 8 + (lane & 3);
#pragma unroll 1
    for (int g = 0; g < 16; ++g) {
      WAIT_V(12);
      BAR();
      if (__builtin_expect(g == 14, 0)) asm volatile("global_load_dwordx2 %0, %1, off sc1" : "+v"(fl) : "v"(pf) : "memory");
      stage_s();
    }
    WAIT_V(12);
    BAR();
    {
      asm volatile("" : "+v"(fl));
      while (!__all(fresh(fl, t_own))) {
        if (__builtin_expect(!cok, 0)) { notify(0, 32); if (lane < 4) __hip_atomic_store(done2 + bid * 8 + lane, __builtin_amdgcn_s_memrealtime(), RLX_AGENT); }
        __builtin_amdgcn_s_sleep(1);
        fl = __hip_atomic_load(pf, RLX_AGENT);
      }
      asm volatile("" ::: "memory");
      const char* ps = (const char*)slab + (size_t)(bid ^ 5) * 16384 + p * 4096 + lane * 16;
#pragma unroll
      for (int i = 0; i < 4; ++i) GLDS_SC1(ps + i * 1024, sl + p * 4096 + i * 1024);
    }
    WAIT_V(10);
    BAR();
    WAIT_V(4);
    BAR();
    WAIT_V(0);
    BAR();
    u32x4 xv[4];
#pragma unroll
    for (int i = 0; i < 4; ++i) {
      const int c = i * 256 + tid - 256, r = c >> 4;
      xv[i] = *(const u32x4*)(x + (size_t)(m0 + (r >> 5) * 64 + role * 32 + (r & 31)) * D_ + n0 + (c & 15) * 8);
    }
    BAR();
#pragma unroll
    for (int i = 0; i < 4; ++i) {
      const int c = i * 256 + tid - 256, r = c >> 4;
      const u32x4 tv = *(const u32x4*)(img + ((r & 32) + (r & 1) * 16 + ((r >> 1) & 15)) * EP + (c & 15) * 16);
      u32x4 ov;
#pragma unroll
      for (int e = 0; e < 4; ++e) ov[e] = pack2(lo_f(xv[i][e]) + lo_f(tv[e]), hi_f(xv[i][e]) + hi_f(tv[e]));
      __builtin_nontemporal_store(ov, (u32x4*)(out + (size_t)(m0 + (r >> 5) * 64 + role * 32 + (r & 31)) * D_ + n0 + (c & 15) * 8));
    }
    TS(5);
    if (tid == 256) __hip_atomic_store(&acks[bid], __builtin_amdgcn_s_memrealtime(), RLX_AGENT);
    if (__builtin_expect(!cok, 0) && p == 0) {
      const bool mine = lane == 32 || (lane < 32 && (tya1 || ((lane >> 3) & 1) != (xcd >> 2)));
      const u64* const pa = &acks[lane < 32 ? client(lane) : bid ^ 5];
      while (!__all(!mine || fresh(__hip_atomic_load(pa, RLX_AGENT), t_own))) {
        notify(0, 32);
        if (lane < 4) __hip_atomic_store(done2 + bid * 8 + lane, __builtin_amdgcn_s_memrealtime(), RLX_AGENT);
        __builtin_amdgcn_s_sleep(32);
      }
    }
  } else {
    TS(6);
    f32x4 acc[4][4], sacc[2][4];
    const int fr = lane & 15, fq = lane >> 4, q4 = fr >> 2, pp = lane & 3;
    const int ff = ((q4 >> 1) & 1) | ((fq & 1) << 1);
    int aof = 2 * G2_STAGE + wm * 8192 + lane * 16;
    int boff[4];
#pragma unroll
    for (int j = 0; j < 4; ++j) boff[j] = 2 * G2_STAGE + 16384 + wn * 8192 + (8 * fq + q4) * 128 + ((j ^ ff) * 32) + pp * 8;
#pragma unroll
    for (int b = 0; b < 4; ++b) {
#pragma unroll
      for (int a = 0; a < 4; ++a) acc[a][b] = f32x4{0.f, 0.f, 0.f, 0.f};
      sacc[0][b] = f32x4{0.f, 0.f, 0.f, 0.f}; sacc[1][b] = f32x4{0.f, 0.f, 0.f, 0.f};
    }
#define SB() __builtin_amdgcn_sched_barrier(0)
    struct Fr { bf16x8 a[4]; bf16x4 bl[4], bh[4]; };
    Fr fa[2], fb[2];
    auto rd = [&](Fr* f, int t) {
      constexpr int ord[12] = {0, 1, 8, 2, 3, 4, 5, 6, 7, 9, 10, 11};
      constexpr int seq[24] = {0, 1, 2, 12, 13, 14, 15, 16, 3, 4, 5, 6, 17, 18, 19, 20, 7, 8, 9, 21, 10, 22, 11, 23};
      lds_cptr sb = shm3;
      const int ks = seq[t] / 12, o = ord[seq[t] % 12];
      if (o >= 8) f[ks].a[o - 8] = *(const __attribute__((address_space(3))) bf16x8*)(sb + aof + (o - 8) * 2048 + ks * 1024);
      else if (o & 1) f[ks].bh[o >> 1] = tr_read(sb + boff[o >> 1] + ks * 4096 + 512);
      else f[ks].bl[o >> 1] = tr_read(sb + boff[o >> 1] + ks * 4096);
    };
    auto bfr = [&](const Fr& c, int j) { return __builtin_shufflevector(c.bl[j], c.bh[j], 0, 1, 2, 3, 4, 5, 6, 7); };
    auto mf1 = [](f32x4& c, bf16x8 b, bf16x8 a) { asm volatile("v_mfma_f32_16x16x32_bf16 %0, %1, %2, %0" : "+v"(c) : "v"(b), "v"(a)); };
    auto mf2 = [](f32x4& c, bf16x8 b, bf16x8 a, bf16x8 b2, bf16x8 a2) {
      asm volatile("v_mfma_f32_16x16x32_bf16 %0, %1, %2, %0\n\tv_mfma_f32_16x16x32_bf16 %0, %3, %4, %0" : "+v"(c) : "v"(b), "v"(a), "v"(b2), "v"(a2));
    };
    auto pairs = [&](f32x4 (*ac)[4], const Fr* c, Fr* n, int nq, int mx, int nr, int per) {
#pragma unroll
      for (int q = 0; q < nq; ++q) {
        const int mf = (q >> 2) ^ mx, j = (q & 4) ? 3 - (q & 3) : q & 3, ks = q & 1;
        SB();
        if (2 * q * per >= nr) mf2(ac[mf][j], bfr(c[ks], j), c[ks].a[mf], bfr(c[ks ^ 1], j), c[ks ^ 1].a[mf]);
        else {
          mf1(ac[mf][j], bfr(c[ks], j), c[ks].a[mf]);
          SB();
#pragma unroll
          for (int i = 0; i < per; ++i) rd(n, 2 * q * per + i);
          SB();
          mf1(ac[mf][j], bfr(c[ks ^ 1], j), c[ks ^ 1].a[mf]);
          SB();
#pragma unroll
          for (int i = 0; i < per; ++i) rd(n, (2 * q + 1) * per + i);
        }
      }
      SB();
    };
    auto ystg = [&](const Fr* c, Fr* n, bool last) { pairs(acc, c, n, 16, last ? 2 : 0, last ? 20 : 24, 1); };
    auto stg = [&](const Fr* c, Fr* n, bool load = true) { pairs(sacc, c, n, 8, 0, load ? 20 : 0, 2); };
    asm volatile("" : "+v"(aof), "+v"(boff[0]), "+v"(boff[1]), "+v"(boff[2]), "+v"(boff[3]));
    BAR();
    TS(7);
    __builtin_amdgcn_s_waitcnt(0xC07F);
    SB();
#pragma unroll
    for (int t = 0; t < 24; ++t) rd(fa, t);
    int so = 2 * G2_STAGE;
    auto nxt = [&]() {
      const int d = so == (G2_NSTAGE - 1) * G2_STAGE ? -(G2_NSTAGE - 1) * G2_STAGE : G2_STAGE;
      so += d; aof += d; boff[0] += d; boff[1] += d; boff[2] += d; boff[3] += d;
      asm volatile("" : "+v"(aof), "+v"(boff[0]), "+v"(boff[1]), "+v"(boff[2]), "+v"(boff[3]));
    };
    static_assert((NY - DLT) % 2 == 1, "");
#pragma unroll 1
    for (int g = 0; g < ny - 1; g += 2) {
      nxt();
      BAR();
      ystg(fa, fb, false);
      nxt();
      BAR();
      ystg(fb, fa, false);
    }
    nxt();
    BAR();
    ystg(fa, fb, true);
    Fr* const sa = fb;
    Fr* const sb2 = fa;
    TS(2);
    {
      const __amdgpu_buffer_rsrc_t rs = __builtin_amdgcn_make_buffer_rsrc((void*)((char*)slab + (size_t)bid * 16384), 0, 16384, 0x00020000);
#pragma unroll
      for (int f = 0; f < 4; ++f) {
        const f32x4 a = acc[2 + (f >> 1)][(f & 1) * 2], b = acc[2 + (f >> 1)][(f & 1) * 2 + 1];
        __builtin_amdgcn_raw_buffer_store_b128(u32x4{pack2(a[0], a[1]), pack2(a[2], a[3]), pack2(b[0], b[1]), pack2(b[2], b[3])}, rs,
                                               (f * 256 + tid) * 16, 0, 16);
      }
    }
    SB();
#pragma unroll 1
    for (int i = 0; i < 7; ++i) {
      nxt();
      BAR();
      stg(sa, sb2);
      nxt();
      BAR();
      stg(sb2, sa);
      if (i == 2) {
        stamp(done2 + bid * 8 + wid);
      }
    }
    nxt();
    BAR();
    stg(sa, sb2);
    BAR();
    u32x4 pv[4];
    {
      nxt();
      lds_cptr sb = shm3 + so + tid * 16;
#pragma unroll
      for (int f = 0; f < 4; ++f) pv[f] = *(const __attribute__((address_space(3))) u32x4*)(sb + f * 4096);
    }
    stg(sb2, sa, false);
    asm volatile("s_nop 7\n\ts_nop 3" : "+v"(sacc[1][0]), "+v"(sacc[1][1]), "+v"(sacc[1][2]), "+v"(sacc[1][3]));
    SB();
    TS(1);
#pragma unroll
    for (int mf = 0; mf < 2; ++mf)
#pragma unroll
      for (int j = 0; j < 4; ++j) {
        const u32x4 pw = pv[mf * 2 + (j >> 1)];
        const f32x4 sv = sacc[mf][j];
        const f32x4 ya = acc[mf][j] + f32x4{lo_f(pw[2 * (j & 1)]), hi_f(pw[2 * (j & 1)]), lo_f(pw[2 * (j & 1) + 1]), hi_f(pw[2 * (j & 1) + 1])};
        u32x2 o;
#pragma unroll
        for (int e = 0; e < 2; ++e) {
          const f32x2 y2 = {ya[2 * e], ya[2 * e + 1]}, s2 = {sv[2 * e], sv[2 * e + 1]};
          const f32x2 ar = y2 * f32x2{-1.4426950408889634f, -1.4426950408889634f};
          const f32x2 dn = f32x2{__builtin_amdgcn_exp2f(ar[0]), __builtin_amdgcn_exp2f(ar[1])} + f32x2{1.0f, 1.0f};
          const f32x2 t = (s2 * y2) * f32x2{__builtin_amdgcn_rcpf(dn[0]), __builtin_amdgcn_rcpf(dn[1])};
          o[e] = pack2(t[0], t[1]);
        }
        *(u32x2*)(img + (wm * 32 + mf * 16 + fr) * EP + (wn * 64 + j * 16 + fq * 4) * 2) = o;
      }
    BARL();
    TS(3);
  }
  TSFLUSH(2);
  }
  asm volatile(".Lcode_end:");
}

extern "C" void kernel_launch(void* const* d_in, const int* in_sizes, int n_in, void* d_out, int out_size, void* d_ws,
                              size_t ws_size, hipStream_t stream) {
  const u16* A_x = (const u16*)d_in[0];
  const u16* A_wqkv = (const u16*)d_in[1];
  const u16* A_wo = (const u16*)d_in[2];
  const u16* B_wg = (const u16*)d_in[4];
  const u16* B_wu = (const u16*)d_in[5];
  const u16* B_wd = (const u16*)d_in[6];
  u16* out = (u16*)d_out;
  fused_kernel<<<256, 512, 0, stream>>>(A_x, A_wqkv, B_wg, B_wu, B_wd, A_wo, out, (char*)d_ws);
}
```

```cpp
#include <hip/hip_runtime.h>
#include <hip/hip_bf16.h>
#include <stdint.h>
#include <type_traits>

typedef unsigned short u16;
using bf16x8 = __attribute__((ext_vector_type(8))) short;
using bf16x4 = __attribute__((ext_vector_type(4))) short;
using f32x4 = __attribute__((ext_vector_type(4))) float;
using u32x2 = __attribute__((ext_vector_type(2))) unsigned;
using u32x4 = __attribute__((ext_vector_type(4))) unsigned;
typedef __attribute__((address_space(3))) const char* lds_cptr;

constexpr int S_ = 2048, D_ = 1024, F_ = 3584;

__device__ __forceinline__ float bf2f(u16 h) { return __uint_as_float((unsigned)h << 16); }
__device__ __forceinline__ u16 f2bf(float f) {
  unsigned u = __float_as_uint(f);
  u += 0x7fffu + ((u >> 16) & 1u);
  return (u16)(u >> 16);
}
__device__ __forceinline__ float rbf(float f) { return bf2f(f2bf(f)); }
typedef __attribute__((ext_vector_type(2))) __bf16 bf16x2_t;
typedef __attribute__((ext_vector_type(2))) float f32x2;
__device__ __forceinline__ unsigned pack2(float a, float b) {
  bf16x2_t r = __builtin_convertvector((f32x2){a, b}, bf16x2_t);
  return __builtin_bit_cast(unsigned, r);
}
__device__ __forceinline__ float lo_f(unsigned p) { return __uint_as_float(p << 16); }
__device__ __forceinline__ float hi_f(unsigned p) { return __uint_as_float(p & 0xffff0000u); }

#define TSDECL
#define TSS(i) do {} while (0)
#define TSFLUSH(k) do {} while (0)
#define TS1(i) TSS(i)
#define TS(i) TSS(i)
#define WAIT_V(n) asm volatile("s_waitcnt vmcnt(%0)" ::"n"(n) : "memory")
#define WAIT_L(n) asm volatile("s_waitcnt lgkmcnt(%0)" ::"n"(n) : "memory")
#define BARL() do { WAIT_L(0); BAR(); } while (0)
#define BAR() do { asm volatile("" ::: "memory"); __builtin_amdgcn_s_barrier(); asm volatile("" ::: "memory"); } while (0)

__device__ __forceinline__ bf16x4 tr_read(lds_cptr p) {
  return __builtin_amdgcn_ds_read_tr16_b64_v4i16((__attribute__((address_space(3))) bf16x4*)p);
}
__device__ __forceinline__ bf16x8 tr_frag(lds_cptr p) {
  bf16x4 lo = tr_read(p), hi = tr_read(p + 512);
  bf16x8 r;
  r[0] = lo[0]; r[1] = lo[1]; r[2] = lo[2]; r[3] = lo[3];
  r[4] = hi[0]; r[5] = hi[1]; r[6] = hi[2]; r[7] = hi[3];
  return r;
}
__device__ __forceinline__ void glds16(const void* gsrc, unsigned lds_dst) {
  unsigned keep;
  asm volatile("s_mov_b32 %0, m0\n\ts_mov_b32 m0, %2\n\ts_nop 0\n\tglobal_load_lds_dwordx4 %1, off\n\ts_mov_b32 m0, %0"
               : "=&s"(keep) : "v"(gsrc), "s"(lds_dst) : "memory");
}
#define GLDS(gptr, laddr) glds16((const void*)(gptr), (unsigned)(laddr))
__device__ __forceinline__ void glds16_sc1(const void* gsrc, unsigned lds_dst) {
  unsigned keep;
  asm volatile("s_mov_b32 %0, m0\n\ts_mov_b32 m0, %2\n\ts_nop 0\n\tglobal_load_lds_dwordx4 %1, off sc1\n\ts_mov_b32 m0, %0"
               : "=&s"(keep) : "v"(gsrc), "s"(lds_dst) : "memory");
}
#define GLDS_SC1(gptr, laddr) glds16_sc1((const void*)(gptr), (unsigned)(laddr))
__device__ __forceinline__ void glds16_nc(const void* gsrc, unsigned lds_dst) {
  unsigned keep;
  asm volatile("s_mov_b32 %0, m0\n\ts_mov_b32 m0, %2\n\ts_nop 0\n\tglobal_load_lds_dwordx4 %1, off\n\ts_mov_b32 m0, %0"
               : "=&s"(keep) : "v"(gsrc), "s"(lds_dst));
}
#define GLDS_NC(gptr, laddr) glds16_nc((const void*)(gptr), (unsigned)(laddr))
__device__ __forceinline__ void glds16_s(const void* sbase, unsigned voff, unsigned lds_dst) {
  asm volatile("s_mov_b32 m0, %2\n\ts_nop 0\n\tglobal_load_lds_dwordx4 %0, %1" :: "v"(voff), "s"(sbase), "s"(lds_dst) : "m0");
}

typedef unsigned long long u64;
__device__ __forceinline__ bool fresh(u64 stamp, u64 t_own) {
  return stamp - t_own - 100ull < __builtin_amdgcn_s_memrealtime() - t_own;
}
constexpr u64 TIMEOUT_ = 200000;
__device__ __forceinline__ float half_sum(float v) {
  v += __builtin_bit_cast(float, __builtin_amdgcn_update_dpp(0, __builtin_bit_cast(int, v), 0xB1, 0xf, 0xf, false));
  v += __builtin_bit_cast(float, __builtin_amdgcn_update_dpp(0, __builtin_bit_cast(int, v), 0x4E, 0xf, 0xf, false));
  v += __builtin_bit_cast(float, __builtin_amdgcn_update_dpp(0, __builtin_bit_cast(int, v), 0x141, 0xf, 0xf, false));
  v += __builtin_bit_cast(float, __builtin_amdgcn_update_dpp(0, __builtin_bit_cast(int, v), 0x140, 0xf, 0xf, false));
  { auto r = __builtin_amdgcn_permlane16_swap(__float_as_uint(v), __float_as_uint(v), false, false); v = __uint_as_float(r[0]) + __uint_as_float(r[1]); }
  return v;
}
__device__ __forceinline__ unsigned pack_stats(float mean, float var) {
  return (unsigned)f2bf(mean) | ((unsigned)f2bf(sqrtf(rbf(rbf(var) + 1e-5f))) << 16);
}
__device__ __forceinline__ unsigned row_stats_serial(const u16* xr) {
  float s = 0.f, q = 0.f;
#pragma unroll 1
  for (int k = 0; k < D_; ++k) s += bf2f(xr[k]);
  const float mean = s * (1.0f / D_);
#pragma unroll 1
  for (int k = 0; k < D_; ++k) { const float d = bf2f(xr[k]) - mean; q += d * d; }
  return pack_stats(mean, q * (1.0f / D_));
}
constexpr int G1_STAGE = 32768 + 5 * 8192;
#define PF_K0 4
#define CP_K0 5
#define DLT 1
#define PSLEEP 2
#define GETJ 3
#define PUB_K 5

constexpr unsigned BLANK = 0x7f817f81u;
__device__ __forceinline__ bool blank(const u32x4* pv) {
  u64 m;
#define CX(i) "v_cmpx_ne_u32 vcc, %1, %" #i "\n\t"
  asm volatile(CX(2) CX(3) CX(4) CX(5) CX(6) CX(7) CX(8) CX(9) CX(10) CX(11) CX(12) CX(13) CX(14) CX(15) CX(16) CX(17)
               "s_mov_b64 %0, exec\n\ts_mov_b64 exec, -1" : "=s"(m) : "s"(BLANK), "v"(pv[0][0]), "v"(pv[0][1]), "v"(pv[0][2]), "v"(pv[0][3]),
               "v"(pv[1][0]), "v"(pv[1][1]), "v"(pv[1][2]), "v"(pv[1][3]), "v"(pv[2][0]), "v"(pv[2][1]), "v"(pv[2][2]), "v"(pv[2][3]),
               "v"(pv[3][0]), "v"(pv[3][1]), "v"(pv[3][2]), "v"(pv[3][3]) : "vcc");
  return m != ~0ull;
}
#define RLX_AGENT __ATOMIC_RELAXED, __HIP_MEMORY_SCOPE_AGENT

constexpr int G2_STAGE = 16384 + 2 * 8192;
constexpr int G2_NSTAGE = 4;
constexpr size_t MB_ = 1024 * 1024;
__global__ __launch_bounds__(512, 2) void fused_kernel(const u16* __restrict__ x, const u16* __restrict__ wqkv,
                                                       const u16* __restrict__ wg, const u16* __restrict__ wu,
                                                       const u16* __restrict__ wd, const u16* __restrict__ wo,
                                                       u16* __restrict__ out, char* __restrict__ ws) {
  u16* const halo = (u16*)(ws + 4 * MB_);
  u16* const abuf = (u16*)(ws + 16 * MB_);
  char* const slab = ws + 64 * MB_;
  unsigned* const flags = (unsigned*)(ws + 96 * MB_);
  u32x4* const rstat = (u32x4*)(ws + 97 * MB_);
  char* const wcopy = ws + 98 * MB_;
  u64* const done1 = (u64*)(ws + 102 * MB_);
  u64* const done2 = (u64*)(ws + 100 * MB_ + 65536);
  u64* const donec = (u64*)(ws + 100 * MB_ + 131072);
  u64* const acks = (u64*)(ws + 100 * MB_ + 196608);
  u64* const cstamp = (u64*)(ws + 100 * MB_ + 262144);
  float* const csum = (float*)(ws + 101 * MB_);
  __shared__ __attribute__((aligned(1024))) char shm[163840];
  constexpr int AUX = 2 * G1_STAGE + 8192;
  {
    u64 pc; int len;
    asm volatile("s_getpc_b64 %0\n.Lcode_beg:\n\ts_mov_b32 %1, .Lcode_end-.Lcode_beg" : "=s"(pc), "=s"(len));
    const int ol = ((blockIdx.x >> 3) + 32 * (threadIdx.x >> 6)) * 1024 + (threadIdx.x & 63) * 16;
    asm volatile("s_mov_b32 m0, %2\n\ts_nop 0\n\tglobal_load_lds_dwordx4 %0, %1"
                 :: "v"(ol < len - 16 ? ol : 0), "s"(pc & ~15ull), "s"((unsigned)(uintptr_t)(lds_cptr)shm + 2 * G1_STAGE + 7168) : "m0");
    __builtin_amdgcn_sched_barrier(0);
  }
  const u64 t_own = __builtin_amdgcn_s_memrealtime();
  TSDECL;
  TS1(7);
  const int tid = threadIdx.x, lane = tid & 63;
  const int wid = __builtin_amdgcn_readfirstlane(tid >> 6);
  const int wm = wid & 3, wc = wid >> 2;
  const int bid = blockIdx.x, xcd = bid & 7, idx = bid >> 3;
  const int ntile = xcd * 4 + (idx & 3), mtile = idx >> 2;
  const int m0 = mtile * 256;
  const int hg = ntile >> 1;
  const bool tya = (ntile & 1) == 0;
  const int srow = m0 + ntile * 8 + wm * 2;
  lds_cptr shm3 = (lds_cptr)shm;
  const unsigned shm_base = (unsigned)(uintptr_t)shm3;
  const int r8 = tid >> 3, pos = tid & 7;
  const int phys = pos >> 1, half = pos & 1;
  const int fk = ((r8 >> 1) & 1) | (((r8 >> 3) & 1) << 1);
  const int vsrc = phys ^ fk;
  const int qstr = tya ? 3072 * 2 : F_ * 2;
  unsigned voa = (((r8 & 32) + 2 * (r8 & 15) + ((r8 >> 4) & 1)) * D_ + ((pos ^ (r8 & 7)) * 8)) * 2;
  unsigned vog = r8 * (F_ * 2) + vsrc * 32 + half * 16, voq = r8 * qstr + vsrc * 32 + half * 16;
  unsigned vom = r8 * qstr + (vsrc >> (tya ? 0 : 1)) * 32 + half * 16, vol = lane * 16;
  asm volatile("" : "+v"(voa), "+v"(vog), "+v"(voq), "+v"(vom), "+v"(vol));
  asm volatile("" :: "s"(x), "s"(wqkv), "s"(wg), "s"(wu), "s"(wd), "s"(wo), "s"(out), "s"(ws));
  const char* const ab = (const char*)(x + (size_t)m0 * D_);
  int kz = 0;
  asm volatile("" : "+s"(kz));
  auto stage_a = [&](int buf, int kt, int i, int h) {
    glds16_s(ab + (long)(((i * 64 + (h - 1) * 32) * D_ + kt * 64) * 2), voa, shm_base + buf * G1_STAGE + (wid - 4 + 4 * h) * 1024 + i * 8192);
  };
  if (wid >= 4) {
#pragma unroll
    for (int i = 0; i < 8; ++i) stage_a(0, kz, i >> 1, i & 1);
  } else {
#pragma unroll
    for (int i = 0; i < 4; ++i) glds16_s(x + (size_t)srow * D_ + i * 512, vol, shm_base + G1_STAGE + wid * 4096 + i * 1024);
  }
  const int kt0 = (hg >> 1) * 7, he = hg & 1;
  const char* const gb = (const char*)(wg + (kt0 + (tya ? 5 + he : 2 * he + 1)) * 64);
  const char* const qb = tya ? (const char*)(wqkv + 64 * hg) : gb - 128;
  const long dgu = (const char*)wu - (const char*)wg, dp1 = tya ? 2048 : dgu;
  const char* const mb0 = tya ? qb + 4096 : (const char*)(wg + (kt0 + 4) * 64 + 32 * he);
  const char* mptr = mb0 + (((vsrc & 1) && !tya) ? dgu : 0) + vom;
  auto wload = [&](int p, int rows, unsigned dst) {
    long off = (long)(rows * (p < 3 ? qstr : F_ * 2)) + (p == 1 ? dp1 : (p == 4 ? dgu : 0));
    if (p == 2) { asm volatile("" : "+s"(off)); GLDS_NC(mptr + off, dst); }
    else glds16_s((p < 2 ? qb : gb) + off, p < 2 ? voq : vog, dst);
  };
  const int fr = lane & 15, fq = lane >> 4, q4 = fr >> 2, pp = lane & 3;
  const int ff = ((q4 >> 1) & 1) | ((fq & 1) << 1);
  int aoff[2], boff[2];
#pragma unroll
  for (int ks = 0; ks < 2; ++ks) aoff[ks] = (wm * 64 + fr) * 128 + (((ks * 4 + fq) ^ (fr & 7)) * 16);
#pragma unroll
  for (int j = 0; j < 2; ++j) boff[j] = 32768 + (8 * fq + q4) * 128 + (((2 * wc + j) ^ ff) * 32) + pp * 8;

  f32x4 acc[4][10];
#pragma unroll
  for (int a = 0; a < 4; ++a)
#pragma unroll
    for (int b = 0; b < 10; ++b) acc[a][b] = f32x4{0.f, 0.f, 0.f, 0.f};

  auto stage_part = [&](int buf, int kt, int i, int h) {
    if (i < 5) wload(i, kt * 64 + (h - 1) * 32, shm_base + buf * G1_STAGE + (wid - 4 + 4 * h) * 1024 + 32768 + i * 8192);
    else stage_a(buf, kt, i - 5, h);
  };
  constexpr int NT = D_ / 64;
  constexpr int CNT = AUX + 4096 + 3584;
  if (tid < 8) *(unsigned*)(shm + CNT + tid * 4) = 0u;
  bf16x8 af0[4], af1[4];
  bf16x4 bl[4], bh[4];
#pragma unroll
  for (int i = 0; i < 4; ++i) { af0[i] = bf16x8{0, 0, 0, 0, 0, 0, 0, 0}; af1[i] = af0[i]; bl[i] = bf16x4{0, 0, 0, 0}; bh[i] = bf16x4{0, 0, 0, 0}; }
#define SB0() __builtin_amdgcn_sched_barrier(0)
  auto mm = [&](int g, int t) {
    const int mi = t >> 1, mf = ((g & 1) && g != 9) ? 3 - mi : mi, ks = (t & 1) ^ (mi & 1), s = ((g + 1) & 1) * 2 + ks;
    const bf16x8 b = __builtin_shufflevector(bl[s], bh[s], 0, 1, 2, 3, 4, 5, 6, 7);
    if ((g >> 1) == 2) acc[mf][g] = __builtin_amdgcn_mfma_f32_16x16x32_bf16(ks ? af1[mf] : af0[mf], b, acc[mf][g], 0, 0, 0);
    else acc[mf][g] = __builtin_amdgcn_mfma_f32_16x16x32_bf16(b, ks ? af1[mf] : af0[mf], acc[mf][g], 0, 0, 0);
  };
  typedef __attribute__((address_space(3))) unsigned lds_u32;
  lds_u32* const cnt = (lds_u32*)(shm3 + CNT);
  bool cok;
  constexpr int NY = 28;
  int role, mt2, nt2;
  struct WS { unsigned vb[2][2]; const char* wb[2]; int sh[2]; };
  auto wsetup = [&](const u16* w, int crow0, int l) {
    WS r;
    const unsigned vr = ((l >> 5) & 1) * 16 + (wm & 1) * 8 + ((l >> 3) & 3), vc = (((l >> 1) & 3) ^ ((l >> 4) & 1)) * 32 + (l & 1) * 16;
    u64 cp = (u64)(wcopy + (nt2 >> 2) * 589824 + crow0 * 128);
    asm volatile("" : "+s"(cp));
#pragma unroll
    for (int i = 0; i < 2; ++i) {
      u64 dp = (u64)(w + nt2 * 128 + i * 64);
      asm volatile("" : "+s"(dp));
      const bool slow = cok & ((((unsigned)dp >> 7) & 7) == 3);
      r.sh[i] = slow ? 7 : 11;
      r.wb[i] = (const char*)(slow ? cp : dp);
      r.vb[i][0] = (vr << r.sh[i]) + vc; r.vb[i][1] = ((vr + 4) << r.sh[i]) + (vc ^ 64);
    }
    asm volatile("" : "+v"(r.vb[0][0]), "+v"(r.vb[0][1]), "+v"(r.vb[1][0]), "+v"(r.vb[1][1]), "+s"(r.wb[0]), "+s"(r.wb[1]), "+s"(r.sh[0]), "+s"(r.sh[1]));
    return r;
  };
  WS wy;
  unsigned vg;
  int st = 0;
  unsigned vxo;
  const char *sa, *sw[2];
  auto wsel = [&]() {
    const unsigned r = __builtin_amdgcn_readlane(vg, st);
    sw[0] = wy.wb[0] + (r << wy.sh[0]); sw[1] = wy.wb[1] + (r << wy.sh[1]);
  };
  auto setup2 = [&](bool ld) {
    int l = threadIdx.x & 63;
    int b = bid;
    asm volatile("" : "+v"(l));
    asm volatile("" : "+s"(b));
    role = 1 - ((b >> 2) & 1);
    mt2 = ((role ? b : b ^ 5) & 7) * 4 + ((b >> 3) & 3), nt2 = b >> 5;
    cok = __builtin_amdgcn_readfirstlane(cnt[1] & cnt[2]) != 0u;
    sa = (const char*)abuf + (size_t)(mt2 * 72 + role * NY + (ld ? 3 : 0)) * 16384 + wm * 4096;
    asm volatile("" : "+s"(sa));
    if (!ld) return;
    vxo = ((mt2 * 128 + (wm >> 1) * 64 + role * 32 + 2 * (l & 15)) * D_ + nt2 * 128 + (wm & 1) * 64) * 2 + ((l >> 4) & 1) * 32 + (l >> 5) * 16;
    static_assert(DLT == 1, "");
    const int t = min(l, NY - 1), w = wm >> 1;
    int o1 = __umul24(t, 11) >> 5, o2 = (t - 12) >> 2, u1 = t - __umul24(o1, 3), u2 = 3 + (t & 3);
    asm volatile("" : "+v"(o1), "+v"(o2), "+v"(u1), "+v"(u2));
    const int o = t < 12 ? o1 : o2, u = t < 12 ? u1 : u2;
    vg = (__umul24((l < NY ? role : 1 - role) * 4 + (((o & 1) << 1) | (o >> 1)), 14) + ((0xb85140a >> (4 * u)) & 15) + (u == 5 ? w : 2 * w)) * 32;
    wy = wsetup(wd, 0, l);
    wsel();
    asm volatile("" : "+v"(vg), "+v"(vxo), "+s"(sw[0]), "+s"(sw[1]));
    SB0();
  };
  auto kloop = [&](auto wmc) {
    constexpr bool LD = decltype(wmc)::value != 0;
    const int WID = wid;
    constexpr int DMP = 2 * G1_STAGE;
    const char* pa = nullptr;
    unsigned vob = 0;
    f32x4 ca = {0.f, 0.f, 0.f, 0.f}, cb = {0.f, 0.f, 0.f, 0.f};
    bf16x8 sel = {0, 0, 0, 0, 0, 0, 0, 0};
    int tro = 0;
    const int psh = (wid == 2 && !tya) ? 1 : 0, pstr = wid < 3 ? qstr : F_ * 2;
    const char* const pbase = wid == 0 ? qb : (wid == 1 ? qb + dp1 : (wid == 2 ? mb0 : gb));
    if (!LD) {
      const int rs = mtile * 8 + (lane >> 3), vs = ((lane & 7) >> 1) ^ (((rs >> 1) & 1) | ((mtile & 1) << 1));
      pa = pbase + ((psh & vs) ? dgu : 0) + (rs * pstr + (vs >> psh) * 32 + (lane & 1) * 16);
      vob = rs * (F_ * 2) + vs * 32 + (lane & 1) * 16;
      const short one = fr == fq ? 0x3f80 : 0;
      sel = bf16x8{one, one, one, one, one, one, one, one};
      tro = q4 * 128 + ((fq ^ ((q4 >> 1) | ((mtile & 1) << 1))) * 32) + pp * 8;
      asm volatile("" : "+v"(sel), "+v"(tro), "+v"(ca), "+v"(cb));
    }
    auto share = [&](int t, unsigned dsta, unsigned dstb) {
      long off = (long)(t * 64 * pstr);
      asm volatile("" : "+s"(off));
      GLDS_NC(pa + off, shm_base + dsta);
      if (wid == 0) glds16_s(gb + dgu + (long)(t * 64 * F_ * 2), vob, shm_base + dstb);
    };
    auto accum = [&](int oa, int ob) {
      ca = __builtin_amdgcn_mfma_f32_16x16x32_bf16(tr_frag(shm3 + tro + oa), sel, ca, 0, 0, 0);
      if (wid == 0) cb = __builtin_amdgcn_mfma_f32_16x16x32_bf16(tr_frag(shm3 + tro + ob), sel, cb, 0, 0, 0);
    };
    if (LD) {
#pragma unroll
      for (int i = 0; i < 10; ++i) {
        stage_part(0, kz, i >> 1, i & 1);
        asm volatile("" : "+v"(acc[0][i]), "+v"(acc[1][i]), "+v"(acc[2][i]), "+v"(acc[3][i]));
      }
    }
    unsigned st0 = 0, st1 = 0;
    if (!LD) {
#pragma unroll
      for (int a = 0; a < 4; ++a)
        asm volatile("" : "+v"(acc[a][0]), "+v"(acc[a][1]), "+v"(acc[a][2]), "+v"(acc[a][3]), "+v"(acc[a][4]), "+v"(acc[a][5]), "+v"(acc[a][6]),
                     "+v"(acc[a][7]), "+v"(acc[a][8]), "+v"(acc[a][9]));
      WAIT_V(0);
      const char* const xr = shm + G1_STAGE + WID * 4096 + (lane >> 5) * 2048 + (lane & 31) * 16;
      const u32x4 xv0 = *(const u32x4*)xr, xv1 = *(const u32x4*)(xr + 512), xv2 = *(const u32x4*)(xr + 1024), xv3 = *(const u32x4*)(xr + 1536);
      f32x2 f[16];
#pragma unroll
      for (int i = 0; i < 4; ++i) {
        f[i] = f32x2{lo_f(xv0[i]), hi_f(xv0[i])}; f[4 + i] = f32x2{lo_f(xv1[i]), hi_f(xv1[i])};
        f[8 + i] = f32x2{lo_f(xv2[i]), hi_f(xv2[i])}; f[12 + i] = f32x2{lo_f(xv3[i]), hi_f(xv3[i])};
      }
      f32x2 sa = f[0], sb = f[1];
#pragma unroll
      for (int i = 2; i < 16; i += 2) { sa += f[i]; sb += f[i + 1]; }
      sa += sb;
      const float mean = half_sum(sa[0] + sa[1]) * (1.0f / D_);
      f32x2 qa = {0.f, 0.f}, qb = {0.f, 0.f};
#pragma unroll
      for (int i = 0; i < 16; i += 2) {
        const f32x2 d = f[i] - f32x2{mean, mean}, e = f[i + 1] - f32x2{mean, mean};
        qa = __builtin_elementwise_fma(d, d, qa); qb = __builtin_elementwise_fma(e, e, qb);
      }
      qa += qb;
      const unsigned ps = pack_stats(mean, half_sum(qa[0] + qa[1]) * (1.0f / D_));
      st0 = __builtin_amdgcn_readlane(ps, 0);
      st1 = __builtin_amdgcn_readlane(ps, 32);
    }
    asm volatile("" : "+v"(aoff[0]), "+v"(aoff[1]), "+v"(boff[0]), "+v"(boff[1]));
    TS1(0);
#pragma clang loop unroll(disable)
    for (int kt = 0; kt < NT; ++kt) {
      const int cur = kt & 1;
      if (LD) WAIT_V(0);
      BARL();
      lds_cptr sb = shm3;
      if (LD && kt + 1 < NT) {
#pragma unroll
        for (int i = 0; i < 18; ++i) stage_part(cur ^ 1, kt + 1, i >> 1, i & 1);
      } else if (LD) {
        setup2(true);
      }
      auto rdb = [&](int g, int i) {
        lds_cptr p = sb + boff[g & 1] + (g >> 1) * 8192 + (i >> 1) * 4096;
        const int s = ((g + 1) & 1) * 2 + (i >> 1);
        if (i & 1) bh[s] = tr_read(p + 512); else bl[s] = tr_read(p);
      };
      auto rda = [&](int ks, int mf) { (ks ? af1 : af0)[mf] = *(const __attribute__((address_space(3))) bf16x8*)(sb + aoff[ks] + mf * 2048); };
      SB0();
      __builtin_amdgcn_s_waitcnt(0xC07F);
#pragma unroll
      for (int gg = 0; gg < 10; ++gg) {
        const int g = (gg + 9) % 10;
#pragma unroll
        for (int t = 0; t < 8; ++t) {
          SB0();
          mm(g, t);
          SB0();
          if (g == 9) {
            if (t == 0 || t == 2) { rdb(0, t); rdb(0, t + 1); }
            if (t & 1) { rda((t >> 1) & 1, t >> 1); rda(((t >> 1) & 1) ^ 1, t >> 1); }
          } else if (t < 4) rdb(g + 1, t);
        }
      }
      SB0();
      if (!LD && kt == NT - 1) break;
      {
        const int d = cur ? -G1_STAGE : G1_STAGE;
        aoff[0] += d; aoff[1] += d; boff[0] += d; boff[1] += d;
        asm volatile("" : "+v"(aoff[0]), "+v"(aoff[1]), "+v"(boff[0]), "+v"(boff[1]));
      }
      if (!LD) {
        int lane = threadIdx.x & 63;
        asm volatile("" : "+v"(lane));
        const unsigned vol = lane * 16;
        if (kt >= 1 && kt <= 12) { WAIT_V(0); accum(DMP + wid * 1024, DMP + 4096); WAIT_L(0); }
        if (kt <= 1) share(kt + 2, DMP + 7168, DMP + 7168);
        if (kt <= 11) share(kt + 4, DMP + wid * 1024, DMP + 4096);
        if (kt <= 3) {
          const int sp = cur * G1_STAGE + 32768 + mtile * 1024;
          accum(sp + wid * 8192, sp + 4 * 8192);
        }
        if (kt == 12 && (lane & 15) < 4) {
          const __amdgpu_buffer_rsrc_t rc = __builtin_amdgcn_make_buffer_rsrc((void*)(csum + ntile * 5 * 512 + mtile * 64), 0, 5 * 2048, 0x00020000);
          const int co = (lane & 15) * 64 + (lane >> 4) * 16;
          __builtin_amdgcn_raw_buffer_store_b128(__builtin_bit_cast(u32x4, ca), rc, co + wid * 2048, 0, 16);
          if (wid == 0) __builtin_amdgcn_raw_buffer_store_b128(__builtin_bit_cast(u32x4, cb), rc, co + 4 * 2048, 0, 16);
        }
        if (kt == 13) {
          WAIT_V(0);
          u64 now;
          asm volatile("s_memrealtime %0\n\ts_waitcnt lgkmcnt(0)" : "=s"(now) :: "memory");
          if (lane == 0) __hip_atomic_store(&cstamp[(ntile * 4 + wid) * 8 + mtile], now, RLX_AGENT);
        }
        if (kt >= CP_K0 && kt < CP_K0 + 3) {
          int ln = threadIdx.x & 63;
          asm volatile("" : "+v"(ln));
          const int q1 = bid * 4 + WID + (kt - CP_K0) * 1024, q0 = q1 - 1024;
          if (q0 >= 0 && q0 < 1152) {
            const __amdgpu_buffer_rsrc_t rc = __builtin_amdgcn_make_buffer_rsrc((void*)(wcopy + (q0 & 1) * 589824 + (q0 >> 1) * 1024), 0, 1024, 0x00020000);
            __builtin_amdgcn_raw_buffer_store_b128(*(const u32x4*)(shm + AUX + WID * 1024 + ln * 16), rc, ln * 16, 0, 16);
            WAIT_L(0);
          }
          if (q1 < 1152) {
            const char* src = (const char*)((q1 >> 1) < 448 ? wd + (size_t)(q1 >> 1) * 8 * D_ : wo + (size_t)((q1 >> 1) - 448) * 8 * D_);
            src += ((3 - ((unsigned)(uintptr_t)src >> 7)) & 7) * 128 + (q1 & 1) * 1024;
            GLDS(src + (ln >> 3) * 2048 + (ln & 7) * 16, shm_base + AUX + WID * 1024);
          }
        }
        if (WID == 0 && kt == CP_K0 + 4) {
          u64 now;
          asm volatile("s_memrealtime %0\n\ts_waitcnt lgkmcnt(0)" : "=s"(now) :: "memory");
          if (lane == 0) __hip_atomic_store(&donec[bid], now, RLX_AGENT);
        }
        if (kt >= PF_K0 && kt < PF_K0 + 5) {
#pragma unroll
          for (int i = 0; i < 2; ++i) {
            const int c = (bid * 4 + wid) * 10 + (kt - PF_K0) * 2 + i;
            if (c < 9216) {
              const u16* src = (c < 7168 ? wd + (size_t)c * 512 : wo + (size_t)(c - 7168) * 512) + lane * 8;
              GLDS_NC(src, shm_base + DMP + 7168);
            }
          }
        }
        if (kt == 3) {
          const __amdgpu_buffer_rsrc_t rs = __builtin_amdgcn_make_buffer_rsrc((void*)(slab + (size_t)bid * 16384 + wid * 4096), 0, 4096, 0x00020000);
#pragma unroll
          for (int i = 0; i < 4; ++i) __builtin_amdgcn_raw_buffer_store_b128(u32x4{BLANK, BLANK, BLANK, BLANK}, rs, vol + i * 1024, 0, 16);
          const __amdgpu_buffer_rsrc_t rh = __builtin_amdgcn_make_buffer_rsrc((void*)((char*)halo + (mtile * 32 + ntile) * 4096 + wid * 2048), 0, tya ? 2048 : 0, 0x00020000);
#pragma unroll
          for (int i = 0; i < 2; ++i) __builtin_amdgcn_raw_buffer_store_b128(u32x4{BLANK, BLANK, BLANK, BLANK}, rh, vol + i * 1024, 0, 16);
        }
        if (kt == PUB_K) {
          u64 now;
          asm volatile("s_memrealtime %0\n\ts_waitcnt lgkmcnt(0)" : "=s"(now) :: "memory");
          if (lane == 0) {
            const __amdgpu_buffer_rsrc_t rs = __builtin_amdgcn_make_buffer_rsrc((void*)(rstat + srow), 0, 32, 0x00020000);
            __builtin_amdgcn_raw_buffer_store_b128(u32x4{st0, (unsigned)now, st0, (unsigned)(now >> 32)}, rs, 0, 0, 16);
            __builtin_amdgcn_raw_buffer_store_b128(u32x4{st1, (unsigned)now, st1, (unsigned)(now >> 32)}, rs, 16, 0, 16);
          }
        }
        if (kt == NT - 3) GLDS_SC1(rstat + m0 + wid * 64 + (lane & 32) + 2 * (lane & 15) + ((lane >> 4) & 1), shm_base + AUX + wid * 1024);
        if (WID == 2 && kt == 8) GLDS_SC1((const char*)(done1 + (bid & ~1) * 64) + lane * 16, shm_base + DMP + 7168);
        if (WID >= 2 && kt == NT - 3) GLDS_SC1((const char*)donec + (WID - 2) * 1024 + lane * 16, shm_base + DMP + WID * 1024);
        if (kt == NT - 2) {
          WAIT_V(0);
          const __amdgpu_buffer_rsrc_t rc = __builtin_amdgcn_make_buffer_rsrc((void*)(csum + ntile * 5 * 512), 0, 5 * 2048, 0x00020000);
          u64 tg, cs;
          u32x2 cp[8];
          auto cgather = [&]() {
            asm volatile("s_memrealtime %0\n\ts_waitcnt lgkmcnt(0)" : "=s"(tg) :: "memory");
#pragma unroll
            for (int r = 0; r < 8; ++r) cp[r] = __builtin_bit_cast(u32x2, __builtin_amdgcn_raw_buffer_load_b64(rc, (lane < 32 ? wid * 2048 : 8192 - 256) + lane * 8 + r * 256, 0, 16));
            cs = __hip_atomic_load(cstamp + (ntile * 4 + wid) * 8 + (lane & 7), RLX_AGENT);
          };
          cgather();
          SB0();
          int ln = threadIdx.x;
          asm volatile("" : "+v"(ln));
          if (WID >= 2) {
            const u32x4 c4 = *(const u32x4*)(shm + DMP + ln * 16);
            const bool ok = __all(fresh(((u64)c4[1] << 32) | c4[0], t_own) & fresh(((u64)c4[3] << 32) | c4[2], t_own));
            *(unsigned*)(shm + CNT - 4 + WID * 4) = ok ? 1u : 0u;
          }
          u32x4 gr = *(const u32x4*)(shm + AUX + ln * 16);
          if (__builtin_expect(!__all((gr[0] == gr[2]) & fresh(((u64)gr[3] << 32) | gr[1], t_own)), 0)) {
            const __amdgpu_buffer_rsrc_t rs = __builtin_amdgcn_make_buffer_rsrc((void*)(rstat + m0), 0, 4096, 0x00020000);
            const int prow = (ln & 0xe0) + 2 * (ln & 15) + ((ln >> 4) & 1);
            for (;;) {
              gr = __builtin_bit_cast(u32x4, __builtin_amdgcn_raw_buffer_load_b128(rs, prow * 16, 0, 16));
              if (__all((gr[0] == gr[2]) & fresh(((u64)gr[3] << 32) | gr[1], t_own))) break;
              if (__builtin_amdgcn_s_memrealtime() - t_own > TIMEOUT_) { gr[0] = row_stats_serial(x + (size_t)(m0 + prow) * D_); break; }
              __builtin_amdgcn_s_sleep(2);
            }
          }
          const float nm = -lo_f(gr[0]), dn = hi_f(gr[0]), r = __builtin_amdgcn_rcpf(dn);
          const f32x4 fc = {nm, -2.3022081986f * r, -0.10294324f * r * r * r, dn * dn};
          *(f32x4*)(shm + AUX + ln * 16) = fc;
          float* const tw = (float*)(shm + 2 * G1_STAGE + 4096 + WID * 768) + ln;
#pragma unroll
          for (int i = 0; i < 4; ++i) tw[i * 64] = fc[i];
          ((float*)(shm + AUX + 4096))[384 + ln] = r;
          SB0();
          bool late = false;
          while (__builtin_expect(!__all(cs - t_own - 100ull < tg - t_own - 200ull), 0)) {
            late = __builtin_amdgcn_s_memrealtime() - t_own > TIMEOUT_;
            if (late) break;
            __builtin_amdgcn_s_sleep(2);
            cgather();
          }
          float t0 = __uint_as_float(cp[0][0]), t1 = __uint_as_float(cp[0][1]);
#pragma unroll
          for (int i = 1; i < 8; ++i)
            asm("v_add_f32 %0, %0, %2\n\tv_add_f32 %1, %1, %3" : "+v"(t0), "+v"(t1) : "v"(cp[i][0]), "v"(cp[i][1]));
          if (__builtin_expect(late, 0)) {
            const int gq = (lane >> 3) & 3;
            const char* pc = lane < 32 ? pbase + ((psh & gq) ? dgu : 0) + (gq >> psh) * 32 + (lane & 7) * 4 : gb + dgu + (lane & 31) * 4;
            t0 = 0.f; t1 = 0.f;
#pragma unroll 1
            for (int k = 0; k < D_; ++k) { const unsigned w = *(const unsigned*)(pc + (size_t)k * (lane < 32 ? pstr : F_ * 2)); t0 += lo_f(w); t1 += hi_f(w); }
          }
          if (lane < 32 || wid == 0)
            *(f32x2*)((float*)(shm + AUX + 4096) + ((lane >> 4) & 1) * 192 + (lane < 32 ? wid : 4) * 32 + (lane & 15) * 2) = f32x2{t0, t1};
          asm volatile("" : "=v"(ca), "=v"(cb), "=v"(sel), "=v"(tro), "=v"(pa), "=v"(vob));
          __builtin_amdgcn_s_setprio(2);
        }
      }
      SB0();
    }
#pragma unroll
    for (int t = 0; t < 8; ++t) mm(9, t);
    SB0();
    TS1(3);
    if (!LD) setup2(false);
  };
  if (wid < 4) kloop(std::integral_constant<int, 0>{});
  else kloop(std::integral_constant<int, 1>{});
  TS1(1);
  __builtin_amdgcn_sched_barrier(0);
  int tz = threadIdx.x;
  asm volatile("" : "+v"(tz));
  __builtin_amdgcn_sched_barrier(0);
  const int li = tz & 15, lq = (tz >> 4) & 3, ln = tz & 63;
  const __amdgpu_buffer_rsrc_t ra = __builtin_amdgcn_make_buffer_rsrc((void*)abuf, 0, 16 * 72 * 16384, 0x00020000);
  float* const cfin = (float*)(shm + AUX + 4096);
  float* const svr = cfin + 384;
  float* const tb = (float*)(shm + 2 * G1_STAGE + 4096 + wm * 1024);
  auto signal = [&](unsigned v) {
    asm volatile("" ::: "memory");
    if (ln == 0) __hip_atomic_fetch_add(cnt, v, __ATOMIC_RELAXED, __HIP_MEMORY_SCOPE_WORKGROUP);
    asm volatile("" ::: "memory");
  };
  auto await = [&](unsigned mask, unsigned val) {
    while ((__builtin_amdgcn_readfirstlane(__hip_atomic_load(cnt, __ATOMIC_RELAXED, __HIP_MEMORY_SCOPE_WORKGROUP)) & mask) != val) __builtin_amdgcn_s_sleep(1);
    asm volatile("" ::: "memory");
  };
  auto client = [&](int l) { const int m128 = mtile * 2 + ((l >> 4) & 1); return (((l & 7) * 4 + (m128 & 3)) * 8) + ((m128 >> 2) ^ ((l & 8) ? 5 : 0)); };
  auto notify = [&](int l0, int nl, int early = 0) {
    u64 now;
    asm volatile("s_memrealtime %0\n\ts_waitcnt lgkmcnt(0)" : "=s"(now) :: "memory");
    int l = threadIdx.x & 63;
    asm volatile("" : "+v"(l));
    if (l < nl) __hip_atomic_store(&done1[client(l0 + l) * 64 + early + ntile], now, RLX_AGENT);
  };
  const int xo = ((xcd & 1) << 1) | ((xcd >> 1) & 1), te = (xcd >> 2) * 28 + 3 * xo, tl = (xcd >> 2) * 28 + 12 + 4 * xo;
  auto early = [&]() {
    WAIT_V(0);
    unsigned old = 0;
    if (ln == 0) old = __hip_atomic_fetch_add(cnt + 4 + (wm >> 1), 1u, __ATOMIC_RELAXED, __HIP_MEMORY_SCOPE_WORKGROUP);
    if (__builtin_amdgcn_readfirstlane(old) == 1u) notify((wm >> 1) * 16 + 8 - (xcd & 4) * 2, 8, 32);
  };
  signal(1u << (8 * wc));
  const int p = wid & 3;
  unsigned sl = G2_STAGE;
#define G2_W "s_add_u32 m0, %[l], %[ow]\n\ts_nop 0\n\tglobal_load_lds_dwordx4 %[v0], %[w0] sc1\n\t" \
             "s_add_u32 m0, m0, 1024\n\ts_nop 0\n\tglobal_load_lds_dwordx4 %[v1], %[w0] sc1\n\t" \
             "s_add_u32 m0, m0, 7168\n\ts_nop 0\n\tglobal_load_lds_dwordx4 %[v2], %[w1] sc1\n\t" \
             "s_add_u32 m0, m0, 1024\n\ts_nop 0\n\tglobal_load_lds_dwordx4 %[v3], %[w1] sc1"
#define G2_WOP(w) [ow] "s"(shm_base + 16384 + p * 2048), [w0] "s"(sw[0]), [w1] "s"(sw[1]), [v0] "v"(w.vb[0][0]), [v1] "v"(w.vb[0][1]), [v2] "v"(w.vb[1][0]), [v3] "v"(w.vb[1][1])
  if (wc) {
    await(0xffffu, 0x404u);
#pragma unroll
    for (int i = 0; i < 3; ++i) {
      asm volatile(G2_W :: [l] "s"(sl), G2_WOP(wy) : "memory", "scc", "m0");
      ++st;
      sl = (sl + G2_STAGE) & (4 * G2_STAGE - 1);
      wsel();
    }
    asm volatile("s_mov_b32 m0, %[l]\n\ts_nop 0\n\tglobal_load_lds_dwordx4 %[vx], %[x]\n\tglobal_load_lds_dwordx4 %[vx], %[x] offset:2048\n\t"
                 "s_add_u32 m0, m0, 960\n\ts_nop 0\n\tglobal_load_lds_dwordx4 %[vx], %[x] offset:64\n\tglobal_load_lds_dwordx4 %[vx], %[x] offset:2112"
                 :: [l] "s"(shm_base + 4 * G2_STAGE + p * 4096), [x] "s"(x), [vx] "v"(vxo) : "memory", "scc", "m0");
    __builtin_amdgcn_s_setprio(1);
  }
  auto landed = [&]() { if (wc) { WAIT_V(4); signal(1u << 24); } };
  TS1(4);
  f32x4 rf[4];
#pragma unroll
  for (int mf = 0; mf < 4; ++mf) rf[mf] = *(const f32x4*)(shm + AUX + (wm * 64 + mf * 16 + li) * 16);
  const float* const cl = cfin + wc * 192 + lq * 4;
  const int sbase = ((mtile * 2 + (wm >> 1)) * 72 * 8 + (wm & 1) * 4) * 2048;
  auto putf = [&](int kg, int mf, u32x4 v) {
    __builtin_amdgcn_raw_buffer_store_b128(v, ra, ln * 16 + sbase + ((kg >> 1) * 8 + mf) * 2048 + (kg & 1) * 1024, 0, 16);
  };
  auto gelp = [&](f32x2 g, f32x2 u, f32x2 cg, f32x2 cu, f32x2 m2, f32x2 k12, f32x2 k22, f32x2 d2) {
    const f32x2 tg = __builtin_elementwise_fma(m2, cg, g), tu = __builtin_elementwise_fma(m2, cu, u);
    const f32x2 arg = tg * __builtin_elementwise_fma(tg * tg, k22, k12);
    const f32x2 dn = __builtin_elementwise_fma(f32x2{__builtin_amdgcn_exp2f(arg[0]), __builtin_amdgcn_exp2f(arg[1])}, d2, d2);
    const f32x2 r = (tg * tu) * f32x2{__builtin_amdgcn_rcpf(dn[0]), __builtin_amdgcn_rcpf(dn[1])};
    return pack2(r[0], r[1]);
  };
  auto gel2 = [&](int p, int kg, int mf0 = 0, int mf1 = 4) {
    f32x4 cg[2], cu[2];
#pragma unroll
    for (int j = 0; j < 2; ++j) { cg[j] = *(const f32x4*)(cl + p * 32 + j * 16); cu[j] = *(const f32x4*)(cl + p * 32 + 32 + j * 16); }
#pragma unroll
    for (int mf = mf0; mf < mf1; ++mf) {
      const f32x2 m2 = {rf[mf][0], rf[mf][0]}, k12 = {rf[mf][1], rf[mf][1]}, k22 = {rf[mf][2], rf[mf][2]}, d2 = {rf[mf][3], rf[mf][3]};
      u32x4 o;
#pragma unroll
      for (int i = 0; i < 4; ++i) {
        const int j = i >> 1, e = (i & 1) * 2;
        const f32x4 g = acc[mf][p * 2 + j], u = acc[mf][p * 2 + 2 + j];
        o[i] = gelp(f32x2{g[e], g[e + 1]}, f32x2{u[e], u[e + 1]}, f32x2{cg[j][e], cg[j][e + 1]}, f32x2{cu[j][e], cu[j][e + 1]}, m2, k12, k22, d2);
      }
      putf(kg, mf, o);
    }
  };
  int tsel = ntile & 1;
  asm volatile("" : "+s"(tsel));
  if (tsel != 0) {
    const int tq = (wc ? tl : te + 1) + he;
    gel2(0, 2 * tq);
    landed();
    gel2(3, 2 * tq + 1);
    TS1(5);
    const float cg = cfin[wc * 192 + 64 + li], cu = cfin[wc * 192 + 80 + li];
#pragma unroll
    for (int mf = 0; mf < 4; ++mf) {
      f32x4 t[4];
#pragma unroll
      for (int i = 0; i < 4; ++i) t[i] = *(const f32x4*)(tb + i * 64 + mf * 16 + lq * 4);
      const f32x4 g = acc[mf][4], u = acc[mf][5];
      u32x2 o;
#pragma unroll
      for (int e = 0; e < 4; e += 2)
        o[e >> 1] = gelp(f32x2{g[e], g[e + 1]}, f32x2{u[e], u[e + 1]}, f32x2{cg, cg}, f32x2{cu, cu}, f32x2{t[0][e], t[0][e + 1]},
                         f32x2{t[1][e], t[1][e + 1]}, f32x2{t[2][e], t[2][e + 1]}, f32x2{t[3][e], t[3][e + 1]});
      *(u32x2*)(shm + wm * 4096 + mf * 1024 + wc * 512 + li * 32 + lq * 8) = o;
    }
    if (wc == 0) {
      signal(0x10000u << wid);
      early();
    } else {
      await(0x1000u << wid, 0x1000u << wid);
#pragma unroll
      for (int mf = 0; mf < 4; ++mf) putf(2 * (tl + 2) + he, mf, __builtin_bit_cast(u32x4, tr_frag(shm3 + wm * 4096 + mf * 1024 + ln * 8)));
    }
    TS1(6);
  } else {
    float rd[4];
#pragma unroll
    for (int mf = 0; mf < 4; ++mf) rd[mf] = svr[wm * 64 + mf * 16 + li];
    auto cvq = [&](int p, int mf, const f32x4& c0, const f32x4& c1) {
      u32x4 o;
#pragma unroll
      for (int j = 0; j < 2; ++j) {
        const f32x4 a = acc[mf][p * 2 + j], c4 = j ? c1 : c0;
        const f32x2 m2 = {rf[mf][0], rf[mf][0]}, r2 = {rd[mf], rd[mf]};
        f32x2 t0 = __builtin_elementwise_fma(m2, f32x2{c4[0], c4[1]}, f32x2{a[0], a[1]});
        f32x2 t1 = __builtin_elementwise_fma(m2, f32x2{c4[2], c4[3]}, f32x2{a[2], a[3]});
        if (p) { t0 *= r2; t1 *= r2; }
        o[2 * j] = pack2(t0[0], t0[1]); o[2 * j + 1] = pack2(t1[0], t1[1]);
      }
      return o;
    };
    const float cv0 = cfin[wc * 192 + 64 + li], cv1 = cfin[wc * 192 + 80 + li];
    auto cvv = [&](int mf) {
      const f32x4 n4 = *(const f32x4*)(tb + mf * 16 + lq * 4), r4 = *(const f32x4*)(svr + wm * 64 + mf * 16 + lq * 4);
      u32x4 o;
#pragma unroll
      for (int j = 0; j < 2; ++j) {
        const f32x4 a = acc[mf][4 + j];
        const f32x2 c2 = {j ? cv1 : cv0, j ? cv1 : cv0};
        const f32x2 t0 = __builtin_elementwise_fma(f32x2{n4[0], n4[1]}, c2, f32x2{a[0], a[1]}) * f32x2{r4[0], r4[1]};
        const f32x2 t1 = __builtin_elementwise_fma(f32x2{n4[2], n4[3]}, c2, f32x2{a[2], a[3]}) * f32x2{r4[2], r4[3]};
        o[2 * j] = pack2(t0[0], t0[1]); o[2 * j + 1] = pack2(t1[0], t1[1]);
      }
      return o;
    };
    u32x4 kx[4], vx[4], qx[4], hx[4];
    const bf16x8 ones = {0x3f80, 0x3f80, 0x3f80, 0x3f80, 0x3f80, 0x3f80, 0x3f80, 0x3f80};
    const f32x4 ck0 = *(const f32x4*)(cl + 32), ck1 = *(const f32x4*)(cl + 48);
    kx[2] = cvq(1, 2, ck0, ck1); kx[3] = cvq(1, 3, ck0, ck1);
    vx[2] = cvv(2); vx[3] = cvv(3);
    const bool needh = wm == 0 && mtile > 0;
    const __amdgpu_buffer_rsrc_t rh = __builtin_amdgcn_make_buffer_rsrc((void*)((char*)halo + ((needh ? mtile - 1 : mtile) * 32 + ntile + wc) * 4096), 0,
                                                                        needh || wm == 3 ? 4096 : 0, 0x00020000);
    if (wm < 3) {
      char* const ex = shm + wid * 4096 + ln * 16;
      *(u32x4*)ex = kx[2]; *(u32x4*)(ex + 1024) = kx[3]; *(u32x4*)(ex + 2048) = vx[2]; *(u32x4*)(ex + 3072) = vx[3];
      signal(0x10000u << wid);
    } else {
#pragma unroll
      for (int r = 0; r < 4; ++r) __builtin_amdgcn_raw_buffer_store_b128(r < 2 ? kx[2 + r] : vx[r], rh, ln * 16 + r * 1024, 0, 16);
    }
    auto hload = [&]() {
#pragma unroll
      for (int r = 0; r < 4; ++r) hx[r] = __builtin_bit_cast(u32x4, __builtin_amdgcn_raw_buffer_load_b128(rh, ln * 16 + r * 1024, 0, 16));
    };
    SB0();
    gel2(3, 2 * (wc ? tl + 3 : te) + he);
    landed();
    SB0();
    if (wm == 0) hload();
    SB0();
    kx[0] = cvq(1, 0, ck0, ck1); kx[1] = cvq(1, 1, ck0, ck1);
    vx[0] = cvv(0); vx[1] = cvv(1);
    {
      const f32x4 cq0 = *(const f32x4*)(cl), cq1 = *(const f32x4*)(cl + 16);
#pragma unroll
      for (int mf = 0; mf < 4; ++mf) qx[mf] = cvq(0, mf, cq0, cq1);
    }
    f32x4 mk0, mk1;
#pragma unroll
    for (int j = 0; j < 4; ++j) {
      const int k0 = 4 * lq + j, k1 = 16 + 4 * lq + j;
      mk0[j] = (k0 >= li + 9 && k0 <= li + 16) ? 0.f : -1.0e30f;
      mk1[j] = (k1 >= li + 9 && k1 <= li + 16) ? 0.f : -1.0e30f;
    }
    auto max3 = [](float a, float b, float c) { float r; asm("v_max3_f32 %0, %1, %2, %3" : "=v"(r) : "v"(a), "v"(b), "v"(c)); return r; };
    auto max2 = [](float a, float b) { float r; asm("v_max_f32 %0, %1, %2" : "=v"(r) : "v"(a), "v"(b)); return r; };
    TS1(5);
    asm volatile("" : "+v"(kx[0]), "+v"(kx[1]), "+v"(vx[0]), "+v"(vx[1]), "+v"(qx[0]), "+v"(qx[1]), "+v"(qx[2]), "+v"(qx[3]), "+v"(mk0), "+v"(mk1));
    if (wc == 0) early();
    if (wm > 0) {
      await(0x8000u << wid, 0x8000u << wid);
#pragma unroll
      for (int i = 0; i < 4; ++i) hx[i] = *(const u32x4*)(shm + (wid - 1) * 4096 + i * 1024 + ln * 16);
    } else if (__builtin_expect(cok || !needh, 1)) {
      while (__builtin_expect(blank(hx), 0)) {
        __builtin_amdgcn_s_sleep(1);
        asm volatile("" ::: "memory");
        hload();
      }
    } else {
      char* const fb = shm + (wid + 3) * 4096 + ln * 16;
#pragma unroll 1
      for (int i = 0; i < 32; ++i) {
        const int rec = i >> 3, e = i & 7;
        const int row = m0 - 32 + 2 * (rec < 2 ? li : lq * 4 + (e & 3)) + (rec & 1);
        const int col = (rec < 2 ? D_ + lq * 4 + (e & 3) : 2 * D_ + li) + (ntile + wc) * 32 + (e >> 2) * 16;
        const u16* xr = x + (size_t)row * D_;
        const unsigned stt = row_stats_serial(xr);
        const float mb = lo_f(stt);
        float a = 0.f;
#pragma unroll 1
        for (int k = 0; k < D_; ++k) a += (bf2f(xr[k]) - mb) * bf2f(wqkv[(size_t)k * 3072 + col]);
        *(u16*)(fb + rec * 1024 + e * 2) = f2bf(a / hi_f(stt));
      }
#pragma unroll
      for (int i = 0; i < 4; ++i) hx[i] = *(const u32x4*)(fb + i * 1024);
    }
    TS1(6);
    {
      const f32x4 z = {0.f, 0.f, 0.f, 0.f};
      const float mz = mtile == 0 && wm == 0 ? -1.0e30f : 0.f;
      const f32x4 mkf = {fminf(mk0[0], mz), fminf(mk0[1], mz), fminf(mk0[2], mz), fminf(mk0[3], mz)};
      f32x4 s0[4], s1[4];
#pragma unroll
      for (int i = 0; i < 4; ++i) {
        const int n = i ^ 2;
        const bf16x8 qf = __builtin_bit_cast(bf16x8, qx[n]);
        s1[n] = __builtin_amdgcn_mfma_f32_16x16x32_bf16(__builtin_bit_cast(bf16x8, kx[n]), qf, mk1, 0, 0, 0);
        s0[n] = __builtin_amdgcn_mfma_f32_16x16x32_bf16(__builtin_bit_cast(bf16x8, n < 2 ? hx[n] : kx[n - 2]), qf, n < 2 ? mkf : mk0, 0, 0, 0);
      }
      SB0();
      asm volatile("s_nop 7\n\ts_nop 3" : "+v"(s0[0]), "+v"(s1[0]), "+v"(s0[1]), "+v"(s1[1]), "+v"(s0[2]), "+v"(s1[2]), "+v"(s0[3]), "+v"(s1[3]));
      float mx[4];
#pragma unroll
      for (int n = 0; n < 4; ++n) mx[n] = max3(max3(s0[n][0], s0[n][1], s0[n][2]), max3(s0[n][3], s1[n][0], s1[n][1]), max2(s1[n][2], s1[n][3]));
#pragma unroll
      for (int n = 0; n < 4; ++n) { auto r = __builtin_amdgcn_permlane16_swap(__float_as_uint(mx[n]), __float_as_uint(mx[n]), false, false);
        mx[n] = max2(__uint_as_float(r[0]), __uint_as_float(r[1])); }
#pragma unroll
      for (int n = 0; n < 4; ++n) { auto r = __builtin_amdgcn_permlane32_swap(__float_as_uint(mx[n]), __float_as_uint(mx[n]), false, false);
        mx[n] = max2(__uint_as_float(r[0]), __uint_as_float(r[1])); }
      SB0();
      constexpr float cs2 = 0.17677669529663687f * 1.4426950408889634f;
      f32x4 os[4], o0[4], o1[4];
#pragma unroll
      for (int n = 0; n < 4; ++n) {
        const float c = cs2 * rd[n], nm = -mx[n] * c;
        float e8[8];
#pragma unroll
        for (int j = 0; j < 4; ++j) {
          e8[j] = __builtin_amdgcn_exp2f(__builtin_fmaf(s0[n][j], c, nm));
          e8[4 + j] = __builtin_amdgcn_exp2f(__builtin_fmaf(s1[n][j], c, nm));
        }
        u32x4 pk;
#pragma unroll
        for (int j = 0; j < 4; ++j) pk[j] = pack2(e8[2 * j], e8[2 * j + 1]);
        const bf16x8 pf = __builtin_bit_cast(bf16x8, pk);
        const u32x4 vp = n < 2 ? hx[2 + n] : vx[n - 2];
        const u32x4 va = {vp[0], vp[1], vx[n][0], vx[n][1]}, vb = {vp[2], vp[3], vx[n][2], vx[n][3]};
        os[n] = __builtin_amdgcn_mfma_f32_16x16x32_bf16(ones, pf, z, 0, 0, 0);
        o0[n] = __builtin_amdgcn_mfma_f32_16x16x32_bf16(__builtin_bit_cast(bf16x8, va), pf, z, 0, 0, 0);
        o1[n] = __builtin_amdgcn_mfma_f32_16x16x32_bf16(__builtin_bit_cast(bf16x8, vb), pf, z, 0, 0, 0);
      }
#pragma unroll
      for (int n = 0; n < 4; ++n) {
        const float inv = __builtin_amdgcn_rcpf(os[n][0]);
        const f32x4 n0 = o0[n] * inv, n1 = o1[n] * inv;
        putf(112 + ntile + wc, n, u32x4{pack2(n0[0], n0[1]), pack2(n0[2], n0[3]), pack2(n1[0], n1[1]), pack2(n1[2], n1[3])});
      }
    }
  }
  TS1(2);
  TSFLUSH(1);
  const bool tya1 = tya;
  {
  __builtin_amdgcn_sched_barrier(0);
  int tid = threadIdx.x;
  asm volatile("" : "+v"(tid));
  __builtin_amdgcn_sched_barrier(0);
  const int lane = tid & 63;
  const int wm = wid >> 1, wn = wid & 1;
  const int m0 = mt2 * 128, n0 = nt2 * 128;
  constexpr int PD = G2_NSTAGE;
  const int ny = (xcd & 1) ? NY - DLT : NY + DLT;
  TS(0);
  const unsigned va = lane * 16;
  auto stage_y = [&]() {
    asm volatile("s_add_u32 m0, %[l], %[oa]\n\ts_nop 0\n\tglobal_load_lds_dwordx4 %[va], %[a] sc1\n\tglobal_load_lds_dwordx4 %[va], %[a] offset:1024 sc1\n\t"
                 "global_load_lds_dwordx4 %[va], %[a] offset:2048 sc1\n\tglobal_load_lds_dwordx4 %[va], %[a] offset:3072 sc1\n\t" G2_W
                 :: [l] "s"(sl), [oa] "s"(shm_base + (p ^ role) * 4096), [a] "s"(sa), [va] "v"(va), G2_WOP(wy) : "memory", "scc", "m0");
    ++st;
    sa += st == NY ? (long)((1 - 2 * role) * NY * 16384) : 16384l;
    sl = (sl + G2_STAGE) & (4 * G2_STAGE - 1);
    wsel();
  };
  u64 ready = 0;
  const u64* const mbox = &done1[bid * 64 + lane];
  auto seen = [&](u64 v) { const u64 r = __ballot(fresh(v, t_own)); ready |= r | (r << 32); };
  auto need = [&](u64 mask, bool renew) {
    while ((ready & mask) != mask) {
      seen(__hip_atomic_load(mbox, RLX_AGENT));
      if ((ready & mask) != mask) {
        __builtin_amdgcn_s_sleep(PSLEEP);
        if (renew && !cok) notify(0, 32);
      }
    }
    asm volatile("" ::: "memory");
  };
  if (wid >= 4) {
    u64 fl = 0;
    auto peek = [&]() { asm volatile("global_load_dwordx2 %0, %1, off sc1" : "+v"(fl) : "v"(mbox) : "memory"); };
    auto check = [&](u64 mask) {
      asm volatile("" : "+v"(fl));
      seen(fl);
      if (__builtin_expect((ready & mask) != mask, 0)) need(mask, true);
    };
    TS(4);
    __builtin_amdgcn_s_setprio(0);
    BARL();
    stage_y();
    peek();
    stage_y();
    BAR();
    stage_y();
    WAIT_V(16);
    BAR();
    check(0xf0f0ull << (32 + 16 * role));
    stage_y();
    if (p == 0) notify(0, 32);
    WS wz = wsetup(wo, F_, lane);
    const char* sz[3] = {(const char*)abuf + (size_t)(mt2 * 72 + 56) * 16384 + (p >> 1) * 8192 + role * 4096 + (p & 1) * 2048,
                         wz.wb[0] + ((p >> 1) * 32 << wz.sh[0]), wz.wb[1] + ((p >> 1) * 32 << wz.sh[1])};
    int ws0 = 64 << wz.sh[0], ws1 = 64 << wz.sh[1];
    asm volatile("" : "+s"(sz[0]), "+s"(sz[1]), "+s"(sz[2]), "+s"(ws0), "+s"(ws1));
    u64 cm = 0x0f0full << (16 * role);
#pragma unroll 1
    for (int g = 3; g < ny - PD; ++g) {
      WAIT_V(16);
      BAR();
      if ((g & 7) == 0) { check(cm); cm = (0xffffull << (16 * role)) | 0x55555555ull; }
      if ((g & 7) == 6) peek();
      stage_y();
    }
    WAIT_V(16);
    BAR();
    sa = sz[0]; sw[0] = sz[1]; sw[1] = sz[2];
    auto stage_s = [&]() {
      asm volatile("s_add_u32 m0, %[l], %[oa]\n\ts_nop 0\n\tglobal_load_lds_dwordx4 %[va], %[a] sc1\n\tglobal_load_lds_dwordx4 %[va], %[a] offset:1024 sc1\n\t" G2_W
                   :: [l] "s"(sl), [oa] "s"(shm_base + (p >> 1) * 8192 + (p & 1) * 2048), [a] "s"(sa), [va] "v"(va), G2_WOP(wz) : "memory", "scc", "m0");
      sa += 16384; sw[0] += ws0; sw[1] += ws1; sl = (sl + G2_STAGE) & (4 * G2_STAGE - 1);
    };
    stage_s();
#pragma unroll
    for (int j = -3; j < 15; ++j) {
      WAIT_V((j == -3 ? 2 : 0) + (j < 13 ? 12 : (14 - j) * 6) + (j > GETJ && j < GETJ + 4 ? 2 : 0) + (j > GETJ + 1 && j < GETJ + 5 ? 2 : 0));
      BAR();
      if (j < 12) stage_s();
      if (j == GETJ || j == GETJ + 1)
        asm volatile("s_mov_b32 m0, %[l]\n\ts_nop 0\n\tglobal_load_lds_dwordx4 %[va], %[a] offset:%[o] sc1\n\tglobal_load_lds_dwordx4 %[va], %[a] offset:%[o]+1024 sc1"
                     :: [l] "s"(shm_base + 4 * G2_STAGE + 16384 + p * 4096), [a] "s"((const char*)slab + (size_t)(bid ^ 5) * 16384 + p * 4096), [va] "v"(va),
                        [o] "n"((j - GETJ) * 2048) : "memory", "m0");
    }
    asm volatile("" : "+v"(fl));
    TS(5);
    if (tid < 258) __hip_atomic_store(lane ? done2 + bid * 8 : &acks[bid], __builtin_amdgcn_s_memrealtime(), RLX_AGENT);
    if (__builtin_expect(!cok, 0) && p == 0) {
      const bool mine = lane == 32 || (lane < 32 && (tya1 || ((lane >> 3) & 1) != (xcd >> 2)));
      const u64* const pa = &acks[lane < 32 ? client(lane) : bid ^ 5];
      while (!__all(!mine || fresh(__hip_atomic_load(pa, RLX_AGENT), t_own))) {
        notify(0, 32);
        if (lane == 0) __hip_atomic_store(done2 + bid * 8, __builtin_amdgcn_s_memrealtime(), RLX_AGENT);
        __builtin_amdgcn_s_sleep(32);
      }
    }
  } else {
    __builtin_amdgcn_s_setprio(0);
    TS(6);
    f32x4 acc[4][4], sacc[2][4];
    const int fr = lane & 15, fq = lane >> 4, q4 = fr >> 2, pp = lane & 3;
    const int ff = ((q4 >> 1) & 1) | ((fq & 1) << 1);
    int aof = wm * 8192 + lane * 16;
    int boff[4];
#pragma unroll
    for (int j = 0; j < 4; ++j) boff[j] = 16384 + wn * 8192 + (8 * fq + q4) * 128 + ((j ^ ff) * 32) + pp * 8;
    int vo = wid * 4096 + lane * 16;
    int vx = ((m0 + wm * 64 + role * 32 + 2 * fr) * D_ + n0 + wn * 64) * 2 + (fq & 1) * 32 + (fq >> 1) * 16;
    lds_cptr px = shm3 + 4 * G2_STAGE + wid * 4096 + (fq >> 1) * 512 + fr * 16 + (fq & 1) * 8;
    asm volatile("" : "+v"(vo), "+v"(vx), "+v"(px));
#define SB() __builtin_amdgcn_sched_barrier(0)
    struct Fr { bf16x8 a[4]; bf16x4 bl[4], bh[4]; };
    Fr fa[2], fb[2];
    auto rd = [&](Fr* f, int odd, int t) {
      constexpr int ord[12] = {0, 1, 8, 2, 3, 4, 5, 6, 7, 9, 10, 11};
      constexpr int seq[24] = {0, 1, 2, 12, 13, 14, 15, 16, 3, 4, 5, 6, 17, 18, 19, 20, 7, 8, 9, 21, 10, 22, 11, 23};
      lds_cptr sb = shm3 + odd * G2_STAGE;
      const int ks = seq[t] / 12, o = ord[seq[t] % 12];
      if (o >= 8) f[ks].a[o - 8] = *(const __attribute__((address_space(3))) bf16x8*)(sb + aof + (o - 8) * 2048 + ks * 1024);
      else if (o & 1) f[ks].bh[o >> 1] = tr_read(sb + boff[o >> 1] + ks * 4096 + 512);
      else f[ks].bl[o >> 1] = tr_read(sb + boff[o >> 1] + ks * 4096);
    };
    auto bfr = [&](const Fr& c, int j) { return __builtin_shufflevector(c.bl[j], c.bh[j], 0, 1, 2, 3, 4, 5, 6, 7); };
    auto mf1 = [](f32x4& c, bf16x8 b, bf16x8 a, bool z = false) {
      if (z) asm volatile("v_mfma_f32_16x16x32_bf16 %0, %1, %2, 0" : "=&v"(c) : "v"(b), "v"(a));
      else asm volatile("v_mfma_f32_16x16x32_bf16 %0, %1, %2, %0" : "+v"(c) : "v"(b), "v"(a));
    };
    auto mf2 = [](f32x4& c, bf16x8 b, bf16x8 a, bf16x8 b2, bf16x8 a2, bool z) {
      if (z) asm volatile("v_mfma_f32_16x16x32_bf16 %0, %1, %2, 0\n\tv_mfma_f32_16x16x32_bf16 %0, %3, %4, %0" : "=&v"(c) : "v"(b), "v"(a), "v"(b2), "v"(a2));
      else asm volatile("v_mfma_f32_16x16x32_bf16 %0, %1, %2, %0\n\tv_mfma_f32_16x16x32_bf16 %0, %3, %4, %0" : "+v"(c) : "v"(b), "v"(a), "v"(b2), "v"(a2));
    };
    auto pairs = [&](f32x4 (*ac)[4], const Fr* c, int nq, int mx, int nf, auto fill, bool z = false) {
#pragma unroll
      for (int q = 0; q < nq; ++q) {
        const int mf = (q >> 2) ^ mx, j = (q & 4) ? 3 - (q & 3) : q & 3, ks = q & 1;
        SB();
        if (2 * q >= nf) mf2(ac[mf][j], bfr(c[ks], j), c[ks].a[mf], bfr(c[ks ^ 1], j), c[ks ^ 1].a[mf], z);
        else {
          mf1(ac[mf][j], bfr(c[ks], j), c[ks].a[mf], z);
          SB();
          fill(2 * q);
          SB();
          mf1(ac[mf][j], bfr(c[ks ^ 1], j), c[ks ^ 1].a[mf]);
          SB();
          fill(2 * q + 1);
        }
      }
      SB();
    };
    auto flip = [&]() {
      aof ^= 2 * G2_STAGE; boff[0] ^= 2 * G2_STAGE; boff[1] ^= 2 * G2_STAGE; boff[2] ^= 2 * G2_STAGE; boff[3] ^= 2 * G2_STAGE;
      asm volatile("" : "+v"(aof), "+v"(boff[0]), "+v"(boff[1]), "+v"(boff[2]), "+v"(boff[3]));
    };
    auto bar = [&]() { __builtin_amdgcn_s_waitcnt(0xC07F); BAR(); };
    auto ystg = [&](const Fr* c, Fr* n, int odd, bool z = false) { pairs(acc, c, 16, 0, 24, [&](int t) { rd(n, odd, t); }, z); };
    auto stg = [&](const Fr* c, Fr* n, int odd, bool z = false) { pairs(sacc, c, 8, 0, 10, [&](int t) { rd(n, odd, 2 * t); rd(n, odd, 2 * t + 1); }, z); };
    asm volatile("" : "+v"(aof), "+v"(boff[0]), "+v"(boff[1]), "+v"(boff[2]), "+v"(boff[3]));
    await(0xffffu, 0x404u);
    need(0x0f0full << (32 + 16 * role), false);
    TS(4);
#pragma unroll
    for (int i = 0; i < 3; ++i)
      asm volatile("s_mov_b32 m0, %[l]\n\ts_nop 0\n\tglobal_load_lds_dwordx4 %[va], %[a] sc1\n\tglobal_load_lds_dwordx4 %[va], %[a] offset:1024 sc1\n\t"
                   "global_load_lds_dwordx4 %[va], %[a] offset:2048 sc1\n\tglobal_load_lds_dwordx4 %[va], %[a] offset:3072 sc1"
                   :: [l] "s"(shm_base + (i + 1) * G2_STAGE + (p ^ role) * 4096), [a] "s"(sa + i * 16384), [va] "v"(va) : "memory", "m0");
    WAIT_V(8);
    signal(1u << 24);
    await(0xff000000u, 8u << 24);
    __builtin_amdgcn_s_waitcnt(0xC07F);
    SB();
#pragma unroll
    for (int t = 0; t < 24; ++t) rd(fa, 1, t);
    flip();
    WAIT_V(4);
    bar();
    TS(7);
    ystg(fa, fb, 0, true);
    WAIT_V(0);
    bar();
    ystg(fb, fa, 1);
    static_assert((NY - DLT) % 2 == 1, "");
#pragma unroll 1
    for (int g = 2; g < ny - 1; g += 2) {
      flip();
      bar();
      ystg(fa, fb, 0);
      bar();
      ystg(fb, fa, 1);
    }
    flip();
    bar();
    {
      const __amdgpu_buffer_rsrc_t rs = __builtin_amdgcn_make_buffer_rsrc((void*)((char*)slab + (size_t)bid * 16384), 0, 16384, 0x00020000);
      u32x4 pk;
      pairs(acc, fa, 16, 2, 32, [&](int t) {
        if (t < 20) rd(fb, 0, t);
        else {
          const int f = (t - 20) / 3, e = (t - 20) % 3;
          if (e < 2) { const f32x4 a = acc[2 + (f >> 1)][(f & 1) * 2 + e]; pk[2 * e] = pack2(a[0], a[1]); pk[2 * e + 1] = pack2(a[2], a[3]); }
          else __builtin_amdgcn_raw_buffer_store_b128(pk, rs, vo + f * 1024, 0, 16);
        }
      });
    }
    TS(2);
    static_assert(GETJ % 2 == 1 && GETJ >= 3 && GETJ <= 7, "");
    f32x4 xf[2][4];
#pragma unroll
    for (int j = 0; j < GETJ + 5; j += 2) {
      bar();
      stg(fb, fa, 1, j == 0);
      flip();
      bar();
      u32x2 xv[2];
      pairs(sacc, fa, 8, 0, 12, [&](int t) {
        if (t < 10) { rd(fb, 0, 2 * t); rd(fb, 0, 2 * t + 1); }
        else if (j < 8) xv[t - 10] = *(const __attribute__((address_space(3))) u32x2*)(px + (j >> 1) * 1024 + (t - 10) * 256);
      });
      if (j < 8) {
#pragma unroll
        for (int e = 0; e < 2; ++e) xf[j >> 2][(j & 2) + e] = f32x4{lo_f(xv[e][0]), hi_f(xv[e][0]), lo_f(xv[e][1]), hi_f(xv[e][1])};
        asm volatile("" : "+v"(xf[j >> 2][j & 2]), "+v"(xf[j >> 2][(j & 2) + 1]));
        SB();
      }
    }
    WAIT_V(0);
    bar();
    u32x4 pv[4];
    {
      lds_cptr pp_ = shm3 + 4 * G2_STAGE + 16384 + vo;
      pairs(sacc, fb, 8, 0, 12, [&](int t) {
        if (t < 2) {
#pragma unroll
          for (int f = 2 * t; f < 2 * t + 2; ++f) pv[f] = *(const __attribute__((address_space(3))) u32x4*)(pp_ + f * 1024);
        } else { rd(fa, 1, 2 * t - 4); rd(fa, 1, 2 * t - 3); }
      });
      if (__builtin_expect(blank(pv) || !cok, 0)) {
        const __amdgpu_buffer_rsrc_t rp = __builtin_amdgcn_make_buffer_rsrc((void*)((char*)slab + (size_t)(bid ^ 5) * 16384), 0, 16384, 0x00020000);
        while (!cok && !__all(fresh(__hip_atomic_load(done2 + (bid ^ 5) * 8, RLX_AGENT), t_own))) {
          notify(0, 32);
          if (lane == 0) __hip_atomic_store(done2 + bid * 8, __builtin_amdgcn_s_memrealtime(), RLX_AGENT);
          __builtin_amdgcn_s_sleep(8);
        }
        do {
          asm volatile("" ::: "memory");
#pragma unroll
          for (int f = 0; f < 4; ++f) pv[f] = __builtin_bit_cast(u32x4, __builtin_amdgcn_raw_buffer_load_b128(rp, vo + f * 1024, 0, 16));
        } while (blank(pv));
      }
    }
    auto silu = [&](int f0, int f1) {
#pragma unroll
      for (int f = f0; f < f1; ++f) {
        const int mf = f >> 2, j = f & 3;
        const u32x4 pw = pv[mf * 2 + (j >> 1)];
        const f32x4 ya = acc[mf][j] + f32x4{lo_f(pw[2 * (j & 1)]), hi_f(pw[2 * (j & 1)]), lo_f(pw[2 * (j & 1) + 1]), hi_f(pw[2 * (j & 1) + 1])};
#pragma unroll
        for (int e = 0; e < 2; ++e) {
          const f32x2 y2 = {ya[2 * e], ya[2 * e + 1]};
          const f32x2 ar = y2 * f32x2{-1.4426950408889634f, -1.4426950408889634f};
          const f32x2 dn = f32x2{__builtin_amdgcn_exp2f(ar[0]), __builtin_amdgcn_exp2f(ar[1])} + f32x2{1.0f, 1.0f};
          const f32x2 t = y2 * f32x2{__builtin_amdgcn_rcpf(dn[0]), __builtin_amdgcn_rcpf(dn[1])};
          acc[mf][j][2 * e] = t[0]; acc[mf][j][2 * e + 1] = t[1];
        }
        asm volatile("" : "+v"(acc[mf][j]));
      }
      SB();
    };
#define SPT 0, 1, 2, 3, 5, 7, 8, 8
    constexpr int spt[] = {SPT};
    silu(0, spt[1]);
#pragma unroll
    for (int j = GETJ + 6; j < 15; ++j) {
      if (j & 1) flip();
      bar();
      if (j & 1) stg(fa, fb, 0); else stg(fb, fa, 1);
      silu(spt[j - GETJ - 5], j == 14 ? 8 : spt[j - GETJ - 4]);
    }
    pairs(sacc, fa, 8, 0, 0, [](int) {});
    asm volatile("s_nop 7\n\ts_nop 3" : "+v"(sacc[1][0]), "+v"(sacc[1][1]), "+v"(sacc[1][2]), "+v"(sacc[1][3]));
    SB();
    TS(1);
    {
      const __amdgpu_buffer_rsrc_t ro = __builtin_amdgcn_make_buffer_rsrc((void*)out, 0, S_ * D_ * 2, 0x00020000);
#pragma unroll
      for (int f = 0; f < 4; ++f) {
        const int mf = f >> 1, j = (f & 1) * 2;
        const f32x4 a = __builtin_elementwise_fma(sacc[mf][j], acc[mf][j], xf[mf][j]), b = __builtin_elementwise_fma(sacc[mf][j + 1], acc[mf][j + 1], xf[mf][j + 1]);
        const auto r0 = __builtin_amdgcn_permlane16_swap(pack2(a[0], a[1]), pack2(b[0], b[1]), false, false);
        const auto r1 = __builtin_amdgcn_permlane16_swap(pack2(a[2], a[3]), pack2(b[2], b[3]), false, false);
        __builtin_amdgcn_raw_buffer_store_b128(u32x4{r0[0], r1[0], r0[1], r1[1]}, ro, vx + mf * 2048 + j * 32, 0, 2);
      }
    }
    TS(3);
  }
  TSFLUSH(2);
  }
  asm volatile(".Lcode_end:");
}

extern "C" void kernel_launch(void* const* d_in, const int* in_sizes, int n_in, void* d_out, int out_size, void* d_ws,
                              size_t ws_size, hipStream_t stream) {
  const u16* A_x = (const u16*)d_in[0];
  const u16* A_wqkv = (const u16*)d_in[1];
  const u16* A_wo = (const u16*)d_in[2];
  const u16* B_wg = (const u16*)d_in[4];
  const u16* B_wu = (const u16*)d_in[5];
  const u16* B_wd = (const u16*)d_in[6];
  u16* out = (u16*)d_out;
  fused_kernel<<<256, 512, 0, stream>>>(A_x, A_wqkv, B_wg, B_wu, B_wd, A_wo, out, (char*)d_ws);
}
```

```cpp
#include <hip/hip_runtime.h>
#include <hip/hip_bf16.h>
#include <stdint.h>
#include <type_traits>

typedef unsigned short u16;
using bf16x8 = __attribute__((ext_vector_type(8))) short;
using bf16x4 = __attribute__((ext_vector_type(4))) short;
using f32x4 = __attribute__((ext_vector_type(4))) float;
using u32x2 = __attribute__((ext_vector_type(2))) unsigned;
using u32x4 = __attribute__((ext_vector_type(4))) unsigned;
typedef __attribute__((address_space(3))) const char* lds_cptr;

constexpr int S_ = 2048, D_ = 1024, F_ = 3584;

__device__ __forceinline__ float bf2f(u16 h) { return __uint_as_float((unsigned)h << 16); }
__device__ __forceinline__ u16 f2bf(float f) {
  unsigned u = __float_as_uint(f);
  u += 0x7fffu + ((u >> 16) & 1u);
  return (u16)(u >> 16);
}
__device__ __forceinline__ float rbf(float f) { return bf2f(f2bf(f)); }
typedef __attribute__((ext_vector_type(2))) __bf16 bf16x2_t;
typedef __attribute__((ext_vector_type(2))) float f32x2;
__device__ __forceinline__ unsigned pack2(float a, float b) {
  bf16x2_t r = __builtin_convertvector((f32x2){a, b}, bf16x2_t);
  return __builtin_bit_cast(unsigned, r);
}
__device__ __forceinline__ float lo_f(unsigned p) { return __uint_as_float(p << 16); }
__device__ __forceinline__ float hi_f(unsigned p) { return __uint_as_float(p & 0xffff0000u); }

#define TSDECL
#define TSS(i) do {} while (0)
#define TSFLUSH() do {} while (0)
#define TS1(i) TSS(i)
#define TS(i) TSS(8 + (i))
#define PIN_S(v) asm volatile("" : "+s"(v))
#define WAIT_V(n) asm volatile("s_waitcnt vmcnt(%0)" ::"n"(n) : "memory")
#define WAIT_L(n) asm volatile("s_waitcnt lgkmcnt(%0)" ::"n"(n) : "memory")
#define BARL() do { WAIT_L(0); BAR(); } while (0)
#define BAR() do { asm volatile("" ::: "memory"); __builtin_amdgcn_s_barrier(); asm volatile("" ::: "memory"); } while (0)

__device__ __forceinline__ bf16x4 tr_read(lds_cptr p) {
  return __builtin_amdgcn_ds_read_tr16_b64_v4i16((__attribute__((address_space(3))) bf16x4*)p);
}
__device__ __forceinline__ bf16x8 tr_frag(lds_cptr p) {
  bf16x4 lo = tr_read(p), hi = tr_read(p + 512);
  bf16x8 r;
  r[0] = lo[0]; r[1] = lo[1]; r[2] = lo[2]; r[3] = lo[3];
  r[4] = hi[0]; r[5] = hi[1]; r[6] = hi[2]; r[7] = hi[3];
  return r;
}
__device__ __forceinline__ void glds16(const void* gsrc, unsigned lds_dst) {
  unsigned keep;
  asm volatile("s_mov_b32 %0, m0\n\ts_mov_b32 m0, %2\n\ts_nop 0\n\tglobal_load_lds_dwordx4 %1, off\n\ts_mov_b32 m0, %0"
               : "=&s"(keep) : "v"(gsrc), "s"(lds_dst) : "memory");
}
#define GLDS(gptr, laddr) glds16((const void*)(gptr), (unsigned)(laddr))
__device__ __forceinline__ void glds16_sc1(const void* gsrc, unsigned lds_dst) {
  unsigned keep;
  asm volatile("s_mov_b32 %0, m0\n\ts_mov_b32 m0, %2\n\ts_nop 0\n\tglobal_load_lds_dwordx4 %1, off sc1\n\ts_mov_b32 m0, %0"
               : "=&s"(keep) : "v"(gsrc), "s"(lds_dst) : "memory");
}
#define GLDS_SC1(gptr, laddr) glds16_sc1((const void*)(gptr), (unsigned)(laddr))
__device__ __forceinline__ void glds16_nc(const void* gsrc, unsigned lds_dst) {
  unsigned keep;
  asm volatile("s_mov_b32 %0, m0\n\ts_mov_b32 m0, %2\n\ts_nop 0\n\tglobal_load_lds_dwordx4 %1, off\n\ts_mov_b32 m0, %0"
               : "=&s"(keep) : "v"(gsrc), "s"(lds_dst));
}
#define GLDS_NC(gptr, laddr) glds16_nc((const void*)(gptr), (unsigned)(laddr))
__device__ __forceinline__ void glds16_s(const void* sbase, unsigned voff, unsigned lds_dst) {
  asm volatile("s_mov_b32 m0, %2\n\ts_nop 0\n\tglobal_load_lds_dwordx4 %0, %1" :: "v"(voff), "s"(sbase), "s"(lds_dst) : "m0");
}

typedef unsigned long long u64;
__device__ __forceinline__ bool fresh(u64 stamp, u64 t_own) {
  const u64 b = t_own + 100ull;
  const unsigned lo = (unsigned)stamp - (unsigned)b, hi = (unsigned)(stamp >> 32) - (unsigned)(b >> 32) - ((unsigned)stamp < (unsigned)b ? 1u : 0u);
  return (hi == 0u) & (lo < (unsigned)(__builtin_amdgcn_s_memrealtime() - t_own));
}
constexpr u64 TIMEOUT_ = 200000;
__device__ __forceinline__ float half_sum(float v) {
  v += __builtin_bit_cast(float, __builtin_amdgcn_update_dpp(0, __builtin_bit_cast(int, v), 0xB1, 0xf, 0xf, false));
  v += __builtin_bit_cast(float, __builtin_amdgcn_update_dpp(0, __builtin_bit_cast(int, v), 0x4E, 0xf, 0xf, false));
  v += __builtin_bit_cast(float, __builtin_amdgcn_update_dpp(0, __builtin_bit_cast(int, v), 0x141, 0xf, 0xf, false));
  v += __builtin_bit_cast(float, __builtin_amdgcn_update_dpp(0, __builtin_bit_cast(int, v), 0x140, 0xf, 0xf, false));
  { auto r = __builtin_amdgcn_permlane16_swap(__float_as_uint(v), __float_as_uint(v), false, false); v = __uint_as_float(r[0]) + __uint_as_float(r[1]); }
  return v;
}
__device__ __forceinline__ unsigned pack_stats(float mean, float var) {
  return (unsigned)f2bf(mean) | ((unsigned)f2bf(sqrtf(rbf(rbf(var) + 1e-5f))) << 16);
}
__device__ __forceinline__ unsigned row_stats_serial(const u16* xr) {
  float s = 0.f, q = 0.f;
#pragma unroll 1
  for (int k = 0; k < D_; ++k) s += bf2f(xr[k]);
  const float mean = s * (1.0f / D_);
#pragma unroll 1
  for (int k = 0; k < D_; ++k) { const float d = bf2f(xr[k]) - mean; q += d * d; }
  return pack_stats(mean, q * (1.0f / D_));
}
constexpr int G1_STAGE = 32768 + 5 * 8192;
#define SH1 0
#define PF_K0 4
#define CP_K0 5
#define DLT 1
#define PSLEEP 2
#define GETJ 3
#define PUB_K 9

constexpr unsigned BLANK = 0x7f817f81u;
__device__ __forceinline__ bool blank(const u32x4* pv) {
  u64 m;
#define CX(i) "v_cmpx_ne_u32 vcc, %1, %" #i "\n\t"
  asm volatile(CX(2) CX(3) CX(4) CX(5) CX(6) CX(7) CX(8) CX(9) CX(10) CX(11) CX(12) CX(13) CX(14) CX(15) CX(16) CX(17)
               "s_mov_b64 %0, exec\n\ts_mov_b64 exec, -1\n\ts_nop 3" : "=s"(m) : "s"(BLANK), "v"(pv[0][0]), "v"(pv[0][1]), "v"(pv[0][2]), "v"(pv[0][3]),
               "v"(pv[1][0]), "v"(pv[1][1]), "v"(pv[1][2]), "v"(pv[1][3]), "v"(pv[2][0]), "v"(pv[2][1]), "v"(pv[2][2]), "v"(pv[2][3]),
               "v"(pv[3][0]), "v"(pv[3][1]), "v"(pv[3][2]), "v"(pv[3][3]) : "vcc");
  return m != ~0ull;
}
#define RLX_AGENT __ATOMIC_RELAXED, __HIP_MEMORY_SCOPE_AGENT

constexpr int G2_A = 65536, G2_X = 131072;
constexpr int G2_NSTAGE = 4;
constexpr size_t MB_ = 1024 * 1024;
__global__ __launch_bounds__(512, 2) void fused_kernel(const u16* __restrict__ x, const u16* __restrict__ wqkv,
                                                       const u16* __restrict__ wg, const u16* __restrict__ wu,
                                                       const u16* __restrict__ wd, const u16* __restrict__ wo,
                                                       u16* __restrict__ out, char* __restrict__ ws) {
  u16* const halo = (u16*)(ws + 4 * MB_);
  u16* const abuf = (u16*)(ws + 16 * MB_);
  char* const slab = ws + 64 * MB_;
  unsigned* const flags = (unsigned*)(ws + 96 * MB_);
  u32x4* const rstat = (u32x4*)(ws + 97 * MB_);
  char* const wcopy = ws + 98 * MB_;
  u64* const done1 = (u64*)(ws + 102 * MB_);
  u64* const done2 = (u64*)(ws + 100 * MB_ + 65536);
  u64* const donec = (u64*)(ws + 100 * MB_ + 131072);
  u64* const acks = (u64*)(ws + 100 * MB_ + 196608);
  float* const csum = (float*)(ws + 101 * MB_);
  __shared__ __attribute__((aligned(1024))) char shm[163840];
  constexpr int AUX = 2 * G1_STAGE + 8192;
  {
    u64 pc; int len;
    asm volatile("s_getpc_b64 %0\n.Lcode_beg:\n\ts_mov_b32 %1, .Lcode_end-.Lcode_beg" : "=s"(pc), "=s"(len));
    const int ol = ((blockIdx.x >> 3) + 32 * (threadIdx.x >> 6)) * 1024 + (threadIdx.x & 63) * 16;
    asm volatile("s_mov_b32 m0, %2\n\ts_nop 0\n\tglobal_load_lds_dwordx4 %0, %1"
                 :: "v"(ol < len - 16 ? ol : 0), "s"(pc & ~15ull), "s"((unsigned)(uintptr_t)(lds_cptr)shm + 2 * G1_STAGE + 7168) : "m0");
    __builtin_amdgcn_sched_barrier(0);
  }
  const u64 t_own = __builtin_amdgcn_s_memrealtime();
  TSDECL;
  TS1(7);
  const int tid = threadIdx.x, lane = tid & 63;
  const int wid = __builtin_amdgcn_readfirstlane(tid >> 6);
  const int wm = wid & 3, wc = wid >> 2;
  const int bid = blockIdx.x, xcd = bid & 7, idx = bid >> 3;
  const int ntile = xcd * 4 + (idx & 3), mtile = idx >> 2;
  const int m0 = mtile * 256;
  const int hg = ntile >> 1;
  const bool tya = (ntile & 1) == 0;
  const int srow = m0 + ntile * 8 + wm * 2;
  lds_cptr shm3 = (lds_cptr)shm;
  const unsigned shm_base = (unsigned)(uintptr_t)shm3;
  const int r8 = tid >> 3, pos = tid & 7;
  const int phys = pos >> 1, half = pos & 1;
  const int fk = ((r8 >> 1) & 1) | (((r8 >> 3) & 1) << 1);
  const int vsrc = phys ^ fk;
  const int qstr = tya ? 3072 * 2 : F_ * 2;
  unsigned voa = (((r8 & 32) + 2 * (r8 & 15) + ((r8 >> 4) & 1)) * D_ + ((pos ^ (r8 & 7)) * 8)) * 2;
  unsigned vog = r8 * (F_ * 2) + vsrc * 32 + half * 16, voq = r8 * qstr + vsrc * 32 + half * 16;
  unsigned vom = r8 * qstr + (vsrc >> (tya ? 0 : 1)) * 32 + half * 16;
  asm volatile("" : "+v"(voa), "+v"(vog), "+v"(voq), "+v"(vom));
  __builtin_amdgcn_sched_barrier(0);
  asm volatile("" :: "s"(x), "s"(wqkv), "s"(wg), "s"(wu), "s"(wd), "s"(wo), "s"(out), "s"(ws));
  TS1(1);
  const char* const ab = (const char*)(x + (size_t)m0 * D_);
  int kz = 0;
  asm volatile("" : "+s"(kz));
#pragma unroll
  for (int i = 0; i < 4; ++i) glds16_s(ab + i * 64 * D_ * 2, voa, shm_base + wid * 1024 + i * 8192);
  const int kt0 = (hg >> 1) * 7, he = hg & 1;
  const char* const gb = (const char*)(wg + (kt0 + (tya ? 5 + he : 2 * he + 1)) * 64);
  const long dgu = (const char*)wu - (const char*)wg, dp1 = tya ? 2048 : dgu;
  glds16_s(gb, vog, shm_base + wid * 1024 + 32768 + 3 * 8192);
  glds16_s(gb + dgu, vog, shm_base + wid * 1024 + 32768 + 4 * 8192);
  u64 qa = (u64)(wqkv + 64 * hg), ma = (u64)(wg + (kt0 + 4) * 64 + 32 * he);
  PIN_S(qa); PIN_S(ma);
  const char* const qb = tya ? (const char*)qa : gb - 128;
  glds16_s(qb, voq, shm_base + wid * 1024 + 32768);
  glds16_s(qb + dp1, voq, shm_base + wid * 1024 + 32768 + 8192);
  const char* const mb0 = (const char*)(tya ? qa + 4096 : ma);
  const char* mptr = mb0 + (((vsrc & 1) && !tya) ? dgu : 0) + vom;
  GLDS_NC(mptr, shm_base + wid * 1024 + 32768 + 2 * 8192);
  __builtin_amdgcn_sched_barrier(0);
  TS1(4);
  f32x4 acc[4][10];

  constexpr int NT = D_ / 64;
  constexpr int CNT = AUX + 4096 + 3584;
  if (tid < 8) *(unsigned*)(shm + CNT + tid * 4) = 0u;
  bf16x8 af0[4], af1[4];
  bf16x4 bl[4], bh[4];
  auto clear = [&]() __attribute__((always_inline)) {
    f32x4 z = {0.f, 0.f, 0.f, 0.f};
    asm volatile("" : "+v"(z));
#pragma unroll
    for (int a = 0; a < 4; ++a) {
#pragma unroll
      for (int b = 0; b < 10; ++b) { acc[a][b] = z; asm volatile("" : "+v"(acc[a][b])); }
      af0[a] = __builtin_bit_cast(bf16x8, z); af1[a] = af0[a];
      asm volatile("" : "+v"(af0[a]));
      asm volatile("" : "+v"(af1[a]));
      bl[a] = bf16x4{0, 0, 0, 0}; bh[a] = bf16x4{0, 0, 0, 0};
    }
  };
#define SB0() __builtin_amdgcn_sched_barrier(0)
  auto mm = [&](int g, int t, bool two) {
    const int mi = t >> 1, mf = ((g & 1) && g != 9) ? 3 - mi : mi, ks = (t & 1) ^ (mi & 1), s = ((g + 1) & 1) * 2;
    const bf16x8 b0 = __builtin_shufflevector(bl[s + ks], bh[s + ks], 0, 1, 2, 3, 4, 5, 6, 7), a0 = ks ? af1[mf] : af0[mf];
    const bf16x8 b1 = __builtin_shufflevector(bl[s + 1 - ks], bh[s + 1 - ks], 0, 1, 2, 3, 4, 5, 6, 7), a1 = ks ? af0[mf] : af1[mf];
    const bool tr = (g >> 1) == 2;
    if (two) asm volatile("v_mfma_f32_16x16x32_bf16 %0, %1, %2, %0\n\tv_mfma_f32_16x16x32_bf16 %0, %3, %4, %0" : "+v"(acc[mf][g])
                          : "v"(tr ? a0 : b0), "v"(tr ? b0 : a0), "v"(tr ? a1 : b1), "v"(tr ? b1 : a1));
    else asm volatile("v_mfma_f32_16x16x32_bf16 %0, %1, %2, %0" : "+v"(acc[mf][g]) : "v"(tr ? a0 : b0), "v"(tr ? b0 : a0));
  };
  typedef __attribute__((address_space(3))) unsigned lds_u32;
  lds_u32* const cnt = (lds_u32*)(shm3 + CNT);
  bool cok;
  constexpr int NY = 28;
  int role, mt2, nt2;
  struct WS { unsigned vb[2][2]; const char* wb[2]; int sh[2]; };
  auto wsetup = [&](const u16* w, int crow0, int l, bool ck) __attribute__((always_inline)) {
    WS r;
    const unsigned vr = ((l >> 5) & 1) * 16 + (wm & 1) * 8 + ((l >> 3) & 3), vc = (((l >> 1) & 3) ^ ((l >> 4) & 1)) * 32 + (l & 1) * 16;
    u64 cp = (u64)(wcopy + (nt2 >> 2) * 589824 + crow0 * 128);
    asm volatile("" : "+s"(cp));
#pragma unroll
    for (int i = 0; i < 2; ++i) {
      u64 dp = (u64)(w + nt2 * 128 + i * 64);
      asm volatile("" : "+s"(dp));
      const bool slow = ck & ((((unsigned)dp >> 7) & 7) == 3);
      r.sh[i] = slow ? 7 : 11;
      r.wb[i] = (const char*)(slow ? cp : dp) + (crow0 ? (wm >> 1) * 32 << r.sh[i] : 0);
      r.vb[i][0] = (vr << r.sh[i]) + vc; r.vb[i][1] = ((vr + 4) << r.sh[i]) + (vc ^ 64);
    }
    asm volatile("" : "+v"(r.vb[0][0]), "+v"(r.vb[0][1]), "+v"(r.vb[1][0]), "+v"(r.vb[1][1]));
    PIN_S(r.wb[0]); PIN_S(r.wb[1]); PIN_S(r.sh[0]); PIN_S(r.sh[1]);
    return r;
  };
  WS wy, wz;
  int vxq;
  u32x4 xq[4];
  unsigned vg;
  int st = 0;
  const char *sa, *sw[2];
  auto wsel = [&]() {
    const unsigned r = __builtin_amdgcn_readlane(vg, st);
    sw[0] = wy.wb[0] + (r << wy.sh[0]); sw[1] = wy.wb[1] + (r << wy.sh[1]);
  };
  const int p = wid & 3;
  unsigned sl = 16384;
#define G2_W "s_add_u32 m0, %[l], %[ow]\n\ts_nop 0\n\tglobal_load_lds_dwordx4 %[v0], %[w0] sc1\n\t" \
             "s_add_u32 m0, m0, 1024\n\ts_nop 0\n\tglobal_load_lds_dwordx4 %[v1], %[w0] sc1\n\t" \
             "s_add_u32 m0, m0, 7168\n\ts_nop 0\n\tglobal_load_lds_dwordx4 %[v2], %[w1] sc1\n\t" \
             "s_add_u32 m0, m0, 1024\n\ts_nop 0\n\tglobal_load_lds_dwordx4 %[v3], %[w1] sc1"
#define G2_WOP(w) [ow] "s"(shm_base + p * 2048), [w0] "s"(sw[0]), [w1] "s"(sw[1]), [v0] "v"(w.vb[0][0]), [v1] "v"(w.vb[0][1]), [v2] "v"(w.vb[1][0]), [v3] "v"(w.vb[1][1])
  auto tile2 = [&](int t0) __attribute__((always_inline)) {
    int b = bid;
    asm volatile("" : "+s"(b));
    role = 1 - ((b >> 2) & 1);
    mt2 = ((role ? b : b ^ 5) & 7) * 4 + ((b >> 3) & 3), nt2 = b >> 5;
    sa = (const char*)abuf + (size_t)(mt2 * 72 + role * NY + t0) * 16384 + wm * 4096;
    asm volatile("" : "+s"(sa));
  };
  auto prep = [&](bool first) __attribute__((always_inline)) {
    int l = threadIdx.x;
    asm volatile("" : "+v"(l));
    l &= 63;
    if (first) {
      tile2(3);
      vxq = ((mt2 * 128 + (wm >> 1) * 64 + role * 32 + 2 * (l & 15)) * D_ + nt2 * 128 + (wm & 1) * 64) * 2 + ((l >> 4) & 1) * 32 + (l >> 5) * 16;
      static_assert(DLT == 1, "");
      const int t = min(l, NY - 1), w = wm >> 1;
      const int o = (0xffaa5500fae504ull >> (2 * t)) & 3;
      int u1 = (0x990990 >> ((2 * t) & 31)) & 3, u2 = 3 + (t & 3);
      asm volatile("" : "+v"(u1), "+v"(u2));
      const int u = t < 12 ? u1 : u2;
      vg = (__umul24((l < NY ? role : 1 - role) * 4 + (((o & 1) << 1) | (o >> 1)), 14) + ((0xb85140a >> (4 * u)) & 15) + (u == 5 ? w : 2 * w)) * 32;
      asm volatile("" : "+v"(vg), "+v"(vxq));
    }
    wy = wsetup(wd, 0, l, first);
    wz = wsetup(wo, F_, l, first);
    SB0();
  };
  auto start2 = [&]() __attribute__((always_inline)) {
    const __amdgpu_buffer_rsrc_t rx = __builtin_amdgcn_make_buffer_rsrc((void*)x, 0, S_ * D_ * 2, 0x00020000);
#pragma unroll
    for (int f = 0; f < 4; ++f) xq[f] = __builtin_bit_cast(u32x4, __builtin_amdgcn_raw_buffer_load_b128(rx, vxq + (f >> 1) * 2048 + (f & 1) * 64, 0, 0));
    cok = __builtin_amdgcn_readfirstlane(cnt[1] & cnt[2]) != 0u;
    if (__builtin_expect(!cok, 0)) prep(false);
    wsel();
#pragma unroll
    for (int i = 0; i < 3; ++i) {
      asm volatile(G2_W :: [l] "s"(sl), G2_WOP(wy) : "memory", "scc", "m0");
      ++st;
      sl = (sl + 16384) & 65535;
      wsel();
    }
    PIN_S(sw[0]); PIN_S(sw[1]);
    SB0();
  };
  auto kloop = [&](auto wmc) {
    constexpr bool LD = decltype(wmc)::value != 0;
    const int WID = wid;
    constexpr int DMP = 2 * G1_STAGE;
    const char* pa = nullptr;
    unsigned vob = 0;
    f32x4 ca = {0.f, 0.f, 0.f, 0.f}, cb = {0.f, 0.f, 0.f, 0.f}, cd = ca;
    bf16x8 sel = {0, 0, 0, 0, 0, 0, 0, 0};
    int tro = 0;
    const int psh = (wid == 2 && !tya) ? 1 : 0, pstr = wid < 3 ? qstr : F_ * 2;
    const char* const pbase = wid == 0 ? qb : (wid == 1 ? qb + dp1 : (wid == 2 ? mb0 : gb));
    int lane = threadIdx.x;
    asm volatile("; %1" : "+v"(lane) : "n"(LD ? 1 : 0));
    lane &= 63;
    const int fr = lane & 15, fq = lane >> 4, q4 = fr >> 2, pp = lane & 3;
    const int ff = ((q4 >> 1) & 1) | ((fq & 1) << 1);
    int aoff[2], boff[2];
#pragma unroll
    for (int ks = 0; ks < 2; ++ks) aoff[ks] = (wm * 64 + fr) * 128 + (((ks * 4 + fq) ^ (fr & 7)) * 16);
#pragma unroll
    for (int j = 0; j < 2; ++j) boff[j] = 32768 + (8 * fq + q4) * 128 + (((2 * wc + j) ^ ff) * 32) + pp * 8;
    if (!LD) {
      const int rs = mtile * 8 + (lane >> 3), vs = ((lane & 7) >> 1) ^ (((rs >> 1) & 1) | ((mtile & 1) << 1));
      pa = pbase + ((psh & vs) ? dgu : 0) + (rs * pstr + (vs >> psh) * 32 + (lane & 1) * 16);
      vob = rs * (F_ * 2) + vs * 32 + (lane & 1) * 16;
      tro = q4 * 128 + ((fq ^ ((q4 >> 1) | ((mtile & 1) << 1))) * 32) + pp * 8;
      asm volatile("" : "+v"(tro));
    }
    auto share = [&](int t, unsigned dsta, unsigned dstb) {
      long off = (long)(t * 64 * pstr);
      asm volatile("" : "+s"(off));
      GLDS_NC(pa + off, shm_base + dsta);
      if (wid == 0) glds16_s(gb + dgu + (long)(t * 64 * F_ * 2), vob, shm_base + dstb);
    };
    auto accum = [&](int oa, int ob) {
      ca = __builtin_amdgcn_mfma_f32_16x16x32_bf16(tr_frag(shm3 + tro + oa), sel, ca, 0, 0, 0);
      if (wid == 0) cb = __builtin_amdgcn_mfma_f32_16x16x32_bf16(tr_frag(shm3 + tro + ob), sel, cb, 0, 0, 0);
    };
    const int q32 = 32 * qstr;
    const char *sq0 = qb + q32, *sq1 = sq0 + dp1, *sg0 = gb + 32 * F_ * 2, *sg1 = sg0 + dgu, *sx = ab + (long)(kz + 128 - 32 * D_ * 2);
    u32x2 mp[2] = {__builtin_bit_cast(u32x2, mptr + q32), __builtin_bit_cast(u32x2, mptr + 2 * q32)};
    u64 cy[2];
    unsigned dst = shm_base + G1_STAGE + (wid - 4) * 1024;
    int dbuf = G1_STAGE;
    auto stage = [&](int i0, int i1) __attribute__((always_inline)) {
#pragma unroll
        for (int i = i0; i < i1; ++i) {
          const int q = i >> 1, h = i & 1;
          const unsigned ld = dst + h * 4096 + (q < 5 ? 32768 + q * 8192 : (q - 5) * 8192);
          if (q < 2) glds16_s((q ? sq1 : sq0) + h * q32, voq, ld);
          else if (q == 2) asm volatile("s_mov_b32 m0, %1\n\ts_nop 0\n\tglobal_load_lds_dwordx4 %0, off" :: "v"(mp[h]), "s"(ld) : "m0");
          else if (q < 5) glds16_s((q == 4 ? sg1 : sg0) + h * 32 * F_ * 2, vog, ld);
          else glds16_s(sx + ((q - 5) * 64 + h * 32) * D_ * 2, voa, ld);
        }
    };
    unsigned ktv = 0;
    if (!LD) {
      clear();
      asm volatile("global_load_dwordx4 %0, %4, %5\n\tglobal_load_dwordx4 %1, %4, %5 offset:512\n\t"
                   "global_load_dwordx4 %2, %4, %5 offset:1024\n\tglobal_load_dwordx4 %3, %4, %5 offset:1536"
                   : "+v"(ca), "+v"(cb), "+v"(sel), "+v"(cd) : "v"((lane >> 5) * 2048 + (lane & 31) * 16), "s"(x + (size_t)srow * D_) : "memory");
      WAIT_V(4);
      TS1(6);
    }
    asm volatile("" : "+v"(aoff[0]), "+v"(aoff[1]), "+v"(boff[0]), "+v"(boff[1]));
    auto body = [&](bool upd) __attribute__((always_inline)) {
      lds_cptr sb = shm3;
      auto rdb = [&](int g, int i) {
        lds_cptr p = sb + boff[g & 1] + (g >> 1) * 8192 + (i >> 1) * 4096;
        const int s = ((g + 1) & 1) * 2 + (i >> 1);
        if (i & 1) bh[s] = tr_read(p + 512); else bl[s] = tr_read(p);
      };
      auto rda = [&](int ks, int mf) { (ks ? af1 : af0)[mf] = *(const __attribute__((address_space(3))) bf16x8*)(sb + aoff[ks] + mf * 2048); };
      SB0();
      __builtin_amdgcn_s_waitcnt(0xC07F);
#pragma unroll
      for (int gg = 0; gg < 10; ++gg) {
        const int g = (gg + 9) % 10;
#pragma unroll
        for (int t = 0; t < 8; ++t) {
          const bool two = t >= 4 && !(upd && g >= 5 && g <= 8);
          SB0();
          if (!two || (t & 1)) mm(g, two ? t - 1 : t, two);
          SB0();
          if (g == 9) {
            if (t == 0 || t == 2) { rdb(0, t); rdb(0, t + 1); }
            if (t & 1) { rda((t >> 1) & 1, t >> 1); rda(((t >> 1) & 1) ^ 1, t >> 1); }
          } else if (t < 4) rdb(g + 1, t);
          else if (upd) {
            if (g == 5 && t == 4) { sq0 += 2 * q32; PIN_S(sq0); }
            if (g == 5 && t == 5) { sq1 += 2 * q32; PIN_S(sq1); }
            if (g == 5 && t == 6) { sg0 += 64 * F_ * 2; PIN_S(sg0); }
            if (g == 6 && t == 4) { sg1 += 64 * F_ * 2; PIN_S(sg1); }
            if (g == 6 && t == 5) { sx += 128; PIN_S(sx); }
            if (g == 6 && t == 6) { dst -= dbuf; PIN_S(dst); }
            if (g == 7 && t == 4) asm volatile("v_add_co_u32 %0, %2, %4, %0\n\tv_add_co_u32 %1, %3, %4, %1" : "+v"(mp[0][0]), "+v"(mp[1][0]), "=&s"(cy[0]), "=&s"(cy[1]) : "s"(2 * q32));
            if (g == 7 && t == 6) asm volatile("v_addc_co_u32 %0, vcc, 0, %0, %2\n\tv_addc_co_u32 %1, vcc, 0, %1, %3" : "+v"(mp[0][1]), "+v"(mp[1][1]) : "s"(cy[0]), "s"(cy[1]) : "vcc");
            if (g == 8 && t == 4) { aoff[0] += dbuf; aoff[1] += dbuf; asm volatile("" : "+v"(aoff[0]), "+v"(aoff[1])); }
            if (g == 8 && t == 5) { boff[0] += dbuf; boff[1] += dbuf; asm volatile("" : "+v"(boff[0]), "+v"(boff[1])); }
            if (g == 8 && t == 6) { dbuf = -dbuf; PIN_S(dbuf); }
          }
        }
      }
      SB0();
    };
    TS1(0);
#define PRE1 0
    if (LD) {
      stage(0, PRE1);
      if (PRE1 == 0) clear();
      WAIT_V(PRE1);
      TS1(5);
      BARL();
      stage(PRE1, 18);
      if (PRE1) clear();
    }
#pragma clang loop unroll(disable)
    for (int kt = 0;; ++kt) {
      const int cur = kt & 1;
      if (!LD) BARL();
      body(LD);
      if (!LD && kt == NT - 1) break;
      if (LD) {
        WAIT_V(0);
        BARL();
        if (__builtin_expect(kt == 4 + wid, 0)) prep(true);
        asm volatile("" ::: "memory");
        if (kt == NT - 2) break;
        stage(0, 18);
      } else {
        const int d = cur ? -G1_STAGE : G1_STAGE;
        aoff[0] += d; aoff[1] += d; boff[0] += d; boff[1] += d;
        asm volatile("" : "+v"(aoff[0]), "+v"(aoff[1]), "+v"(boff[0]), "+v"(boff[1]));
      }
      if (!LD) {
        int lane = threadIdx.x;
        asm volatile("" : "+v"(lane), "+v"(tro));
        lane &= 63;
        const unsigned vol = lane * 16;
        unsigned bw = BLANK;
        PIN_S(bw);
        const u32x4 bk = {bw, bw, bw, bw};
        if (kt == 0) {
          WAIT_V(0);
          float sm = 0.f, sq = 0.f;
          const u32x4 w[4] = {__builtin_bit_cast(u32x4, ca), __builtin_bit_cast(u32x4, cb), __builtin_bit_cast(u32x4, sel), __builtin_bit_cast(u32x4, cd)};
#pragma unroll
          for (int i = 0; i < 4; ++i)
            asm volatile("v_dot2_f32_bf16 %0, %2, %6, %0\n\tv_dot2_f32_bf16 %1, %2, %2, %1\n\tv_dot2_f32_bf16 %0, %3, %6, %0\n\tv_dot2_f32_bf16 %1, %3, %3, %1\n\t"
                         "v_dot2_f32_bf16 %0, %4, %6, %0\n\tv_dot2_f32_bf16 %1, %4, %4, %1\n\tv_dot2_f32_bf16 %0, %5, %6, %0\n\tv_dot2_f32_bf16 %1, %5, %5, %1\n\ts_nop 2"
                         : "+v"(sm), "+v"(sq) : "v"(w[i][0]), "v"(w[i][1]), "v"(w[i][2]), "v"(w[i][3]), "s"(0x3f803f80u));
          const float ml = sm * (1.0f / 32), mean = half_sum(sm) * (1.0f / D_), dm = ml - mean;
          cd[0] = mean; cd[1] = __builtin_fmaf(dm * 32.0f, dm, __builtin_fmaf(-ml, sm, sq));
          ca = f32x4{0.f, 0.f, 0.f, 0.f}; cb = ca;
          const short one = (lane & 15) == (lane >> 4) ? 0x3f80 : 0;
          sel = bf16x8{one, one, one, one, one, one, one, one};
        }
        if (kt >= 1 && kt <= 12) { WAIT_V(0); accum(DMP + wid * 1024, DMP + 4096); WAIT_L(0); }
        if (kt <= 1) share(kt + 2, DMP + 7168, DMP + 7168);
        if (kt <= 11) share(kt + 4, DMP + wid * 1024, DMP + 4096);
        if (kt <= 3) {
          const int sp = cur * G1_STAGE + 32768 + mtile * 1024;
          accum(sp + wid * 8192, sp + 4 * 8192);
        }
        if ((kt == 12 || kt == 2) && (lane & 15) < 4) {
          const __amdgpu_buffer_rsrc_t rc = __builtin_amdgcn_make_buffer_rsrc((void*)(csum + ntile * 5 * 512 + mtile * 64), 0, 5 * 2048, 0x00020000);
          const int co = (lane & 15) * 64 + (lane >> 4) * 16;
          __builtin_amdgcn_raw_buffer_store_b128(kt == 2 ? bk : __builtin_bit_cast(u32x4, ca), rc, co + wid * 2048, 0, 16);
          if (wid == 0) __builtin_amdgcn_raw_buffer_store_b128(kt == 2 ? bk : __builtin_bit_cast(u32x4, cb), rc, co + 4 * 2048, 0, 16);
        }
        if (kt >= CP_K0 && kt < CP_K0 + 3) {
          const int ln = lane;
          const int q1 = bid * 4 + WID + (kt - CP_K0) * 1024, q0 = q1 - 1024;
          if (q0 >= 0 && q0 < 1152) {
            const __amdgpu_buffer_rsrc_t rc = __builtin_amdgcn_make_buffer_rsrc((void*)(wcopy + (q0 & 1) * 589824 + (q0 >> 1) * 1024), 0, 1024, 0x00020000);
            __builtin_amdgcn_raw_buffer_store_b128(*(const u32x4*)(shm + AUX + WID * 1024 + ln * 16), rc, ln * 16, 0, 16);
            WAIT_L(0);
          }
          if (q1 < 1152) {
            const char* src = (const char*)((q1 >> 1) < 448 ? wd + (size_t)(q1 >> 1) * 8 * D_ : wo + (size_t)((q1 >> 1) - 448) * 8 * D_);
            src += ((3 - ((unsigned)(uintptr_t)src >> 7)) & 7) * 128 + (q1 & 1) * 1024;
            GLDS(src + (ln >> 3) * 2048 + (ln & 7) * 16, shm_base + AUX + WID * 1024);
          }
        }
        if (WID == 0 && kt == CP_K0 + 4) {
          u64 now;
          asm volatile("s_memrealtime %0\n\ts_waitcnt lgkmcnt(0)" : "=s"(now) :: "memory");
          if (lane == 0) __hip_atomic_store(&donec[bid], now, RLX_AGENT);
        }
        if (kt >= PF_K0 && kt < PF_K0 + 5) {
#pragma unroll
          for (int i = 0; i < 2; ++i) {
            const int c = (bid * 4 + wid) * 10 + (kt - PF_K0) * 2 + i;
            if (c < 9216) {
              const u16* src = (c < 7168 ? wd + (size_t)c * 512 : wo + (size_t)(c - 7168) * 512) + lane * 8;
              GLDS_NC(src, shm_base + DMP + 7168);
            }
          }
        }
#define BLK 3
        if (kt == BLK) {
          const __amdgpu_buffer_rsrc_t rs = __builtin_amdgcn_make_buffer_rsrc((void*)(slab + (size_t)bid * 16384 + wid * 4096), 0, 4096, 0x00020000);
#pragma unroll
          for (int i = 0; i < 4; ++i) __builtin_amdgcn_raw_buffer_store_b128(bk, rs, vol + i * 1024, 0, 16);
          const __amdgpu_buffer_rsrc_t rh = __builtin_amdgcn_make_buffer_rsrc((void*)((char*)halo + (mtile * 32 + ntile) * 4096 + wid * 2048), 0, tya ? 2048 : 0, 0x00020000);
#pragma unroll
          for (int i = 0; i < 2; ++i) __builtin_amdgcn_raw_buffer_store_b128(bk, rh, vol + i * 1024, 0, 16);
        }
        if (kt == PUB_K) {
          const unsigned ps = pack_stats(cd[0], fmaxf(half_sum(cd[1]) * (1.0f / D_), 0.f));
          u64 now;
          asm volatile("s_memrealtime %0\n\ts_waitcnt lgkmcnt(0)" : "=s"(now) :: "memory");
          const __amdgpu_buffer_rsrc_t rs = __builtin_amdgcn_make_buffer_rsrc((void*)(rstat + srow), 0, 32, 0x00020000);
          if ((lane & 31) == 0) __builtin_amdgcn_raw_buffer_store_b128(u32x4{ps, (unsigned)now, ps, (unsigned)(now >> 32)}, rs, lane >> 1, 0, 16);
        }
        if (WID == 2 && kt == 8) GLDS_SC1((const char*)(done1 + (bid & ~1) * 64) + lane * 16, shm_base + DMP + 7168);
        if (WID >= 2 && kt == 11) GLDS_SC1((const char*)donec + (WID - 2) * 1024 + lane * 16, shm_base + DMP + 3072 + WID * 1024);
        if (kt == 12) {
          GLDS_SC1(rstat + m0 + wid * 64 + (lane & 32) + 2 * (lane & 15) + ((lane >> 4) & 1), shm_base + AUX + wid * 1024);
          if (WID >= 2) {
            u32x4 c4 = *(const u32x4*)(shm + DMP + 3072 + WID * 1024 + vol);
            bool ok = __all(fresh(((u64)c4[1] << 32) | c4[0], t_own) & fresh(((u64)c4[3] << 32) | c4[2], t_own));
            while (__builtin_expect(!ok, 0) && __builtin_amdgcn_s_memrealtime() - t_own < TIMEOUT_ / 10) {
              const __amdgpu_buffer_rsrc_t rs = __builtin_amdgcn_make_buffer_rsrc((void*)donec, 0, 2048, 0x00020000);
              asm volatile("" ::: "memory");
              c4 = __builtin_bit_cast(u32x4, __builtin_amdgcn_raw_buffer_load_b128(rs, (WID - 2) * 1024 + vol, 0, 16));
              ok = __all(fresh(((u64)c4[1] << 32) | c4[0], t_own) & fresh(((u64)c4[3] << 32) | c4[2], t_own));
            }
            *(unsigned*)(shm + CNT - 4 + WID * 4) = ok ? 1u : 0u;
          }
        }
        const unsigned vcs = (lane < 32 ? wid * 2048 : 8192) + ((lane >> 4) & 1) * 1024 + (lane & 15) * 16;
        auto cgather = [&]() {
          asm volatile("global_load_dwordx4 %0, %4, %5 sc1\n\tglobal_load_dwordx4 %1, %4, %5 offset:256 sc1\n\t"
                       "global_load_dwordx4 %2, %4, %5 offset:512 sc1\n\tglobal_load_dwordx4 %3, %4, %5 offset:768 sc1"
                       : "+v"(ca), "+v"(cb), "+v"(sel), "+v"(cd) : "v"(vcs), "s"(csum + ntile * 5 * 512) : "memory");
        };
        if (kt == 13) {
          WAIT_V(0);
          cgather();
          int ln = threadIdx.x;
          asm volatile("" : "+v"(ln));
          u32x4 gr = *(const u32x4*)(shm + AUX + ln * 16);
          if (__builtin_expect(!__all((gr[0] == gr[2]) & fresh(((u64)gr[3] << 32) | gr[1], t_own)), 0)) {
            const __amdgpu_buffer_rsrc_t rs = __builtin_amdgcn_make_buffer_rsrc((void*)(rstat + m0), 0, 4096, 0x00020000);
            const int prow = (ln & 0xe0) + 2 * (ln & 15) + ((ln >> 4) & 1);
            for (;;) {
              gr = __builtin_bit_cast(u32x4, __builtin_amdgcn_raw_buffer_load_b128(rs, prow * 16, 0, 16));
              if (__all((gr[0] == gr[2]) & fresh(((u64)gr[3] << 32) | gr[1], t_own))) break;
              if (__builtin_amdgcn_s_memrealtime() - t_own > TIMEOUT_) { gr[0] = row_stats_serial(x + (size_t)(m0 + prow) * D_); break; }
              __builtin_amdgcn_s_sleep(2);
            }
          }
          const float nm = -lo_f(gr[0]), dn = hi_f(gr[0]), r = __builtin_amdgcn_rcpf(dn);
          const f32x4 fc = {nm, -2.3022081986f * r, -0.10294324f * r * r * r, dn * dn};
          *(f32x4*)(shm + AUX + ln * 16) = fc;
          float* const tw = (float*)(shm + 2 * G1_STAGE + 4096 + WID * 768) + ln;
#pragma unroll
          for (int i = 0; i < 4; ++i) tw[i * 64] = fc[i];
          ((float*)(shm + AUX + 4096))[384 + ln] = r;
        }
        if (kt == 14) {
          WAIT_V(0);
          f32x4 t;
          auto csum4 = [&](const u32x4* w) {
#pragma unroll
            for (int i = 0; i < 4; ++i) {
              float a, b;
              asm volatile("v_add_f32 %0, %2, %3\n\tv_add_f32 %1, %4, %5\n\tv_add_f32 %0, %0, %1" : "=&v"(a), "=&v"(b) : "v"(w[0][i]), "v"(w[1][i]), "v"(w[2][i]), "v"(w[3][i]));
              const auto r2 = __builtin_amdgcn_permlane16_swap(__float_as_uint(a), __float_as_uint(a), false, false);
              asm("v_add_f32 %0, %1, %2" : "=v"(t[i]) : "v"(r2[0]), "v"(r2[1]));
            }
            return blank(w);
          };
          const u32x4 w0[4] = {__builtin_bit_cast(u32x4, ca), __builtin_bit_cast(u32x4, cb), __builtin_bit_cast(u32x4, sel), __builtin_bit_cast(u32x4, cd)};
          const bool bl0 = csum4(w0);
          const bool ck = __builtin_amdgcn_readfirstlane(cnt[1] & cnt[2]) != 0u;
          if (__builtin_expect(bl0 && ck, 0)) {
            const __amdgpu_buffer_rsrc_t rc = __builtin_amdgcn_make_buffer_rsrc((void*)(csum + ntile * 5 * 512), 0, 5 * 2048, 0x00020000);
            u32x4 w[4];
            do {
              __builtin_amdgcn_s_sleep(2);
              asm volatile("" ::: "memory");
#pragma unroll
              for (int i = 0; i < 4; ++i) w[i] = __builtin_bit_cast(u32x4, __builtin_amdgcn_raw_buffer_load_b128(rc, vcs + i * 256, 0, 16));
            } while (csum4(w));
          }
          if (__builtin_expect(!ck, 0)) {
            const int gq = (lane >> 2) & 3;
            const char* pc = lane < 32 ? pbase + ((psh & gq) ? dgu : 0) + (gq >> psh) * 32 + (lane & 3) * 8 : gb + dgu + (lane & 15) * 8;
            t = f32x4{0.f, 0.f, 0.f, 0.f};
#pragma unroll 1
            for (int k = 0; k < D_; ++k) { const u32x2 w = *(const u32x2*)(pc + (size_t)k * (lane < 32 ? pstr : F_ * 2)); t += f32x4{lo_f(w[0]), hi_f(w[0]), lo_f(w[1]), hi_f(w[1])}; }
          }
          if (lane < 32 || wid == 0)
            *(f32x4*)((float*)(shm + AUX + 4096) + ((lane >> 3) & 1) * 192 + (lane < 32 ? wid : 4) * 32 + (lane & 7) * 4) = t;
          __builtin_amdgcn_s_setprio(2);
        }
      }
      SB0();
    }
    if (LD) {
      start2();
      body(false);
    }
#pragma unroll
    for (int t = 0; t < 8; t += 2) mm(9, t, true);
    asm volatile("s_nop 7\n\ts_nop 3");
    SB0();
    TS1(3);
    if (!LD) {
      tile2(0);
      cok = __builtin_amdgcn_readfirstlane(cnt[1] & cnt[2]) != 0u;
    } else {
      __builtin_amdgcn_s_setprio(3);
      WAIT_V(0);
    }
  };
  if (wid < 4) kloop(std::integral_constant<int, 0>{});
  else kloop(std::integral_constant<int, 1>{});
  __builtin_amdgcn_sched_barrier(0);
  int tz = threadIdx.x;
  asm volatile("" : "+v"(tz));
  __builtin_amdgcn_sched_barrier(0);
  const int li = tz & 15, lq = (tz >> 4) & 3, ln = tz & 63;
  const __amdgpu_buffer_rsrc_t ra = __builtin_amdgcn_make_buffer_rsrc((void*)abuf, 0, 16 * 72 * 16384, 0x00020000);
  float* const cfin = (float*)(shm + AUX + 4096);
  float* const svr = cfin + 384;
  float* const tb = (float*)(shm + 2 * G1_STAGE + 4096 + wm * 1024);
  auto signal = [&](unsigned v) {
    asm volatile("" ::: "memory");
    if (ln == 0) __hip_atomic_fetch_add(cnt, v, __ATOMIC_RELAXED, __HIP_MEMORY_SCOPE_WORKGROUP);
    asm volatile("" ::: "memory");
  };
  auto await = [&](unsigned mask, unsigned val) {
    while ((__builtin_amdgcn_readfirstlane(__hip_atomic_load(cnt, __ATOMIC_RELAXED, __HIP_MEMORY_SCOPE_WORKGROUP)) & mask) != val) __builtin_amdgcn_s_sleep(1);
    asm volatile("" ::: "memory");
  };
  auto client = [&](int l) { const int m128 = mtile * 2 + ((l >> 4) & 1); return (((l & 7) * 4 + (m128 & 3)) * 8) + ((m128 >> 2) ^ ((l & 8) ? 5 : 0)); };
  auto notify = [&](int l0, int nl, int early = 0) {
    u64 now;
    asm volatile("s_memrealtime %0\n\ts_waitcnt lgkmcnt(0)" : "=s"(now) :: "memory");
    int l = threadIdx.x;
    asm volatile("" : "+v"(l));
    l &= 63;
    const __amdgpu_buffer_rsrc_t rd1 = __builtin_amdgcn_make_buffer_rsrc((void*)(done1 + early + ntile), 0, 256 * 512, 0x00020000);
    if (l < nl) __builtin_amdgcn_raw_buffer_store_b64(u32x2{(unsigned)now, (unsigned)(now >> 32)}, rd1, client(l0 + l) * 512, 0, 16);
  };
  const int xo = ((xcd & 1) << 1) | ((xcd >> 1) & 1), te = (xcd >> 2) * 28 + 6 * (xo >> 1) + (xo & 1), tl = (xcd >> 2) * 28 + 12 + 4 * xo;
  auto early = [&]() {
    unsigned old = 0;
    if (ln == 0) old = __hip_atomic_fetch_add(cnt + 4 + (wm >> 1), 1u, __ATOMIC_RELAXED, __HIP_MEMORY_SCOPE_WORKGROUP);
    if (__builtin_amdgcn_readfirstlane(old) == 1u) notify((wm >> 1) * 16 + 8 - (xcd & 4) * 2, 8, 32);
  };
  signal(1u << (8 * wc));
  if (wc) {
    await(0xffffu, 0x404u);
#pragma unroll
    for (int f = 0; f < 4; ++f) *(u32x4*)(shm + G2_X + wm * 4096 + f * 1024 + ln * 16) = xq[f];
    SB0();
    __builtin_amdgcn_s_setprio(1);
  }
  f32x4 rf[4];
#pragma unroll
  for (int mf = 0; mf < 4; ++mf) rf[mf] = *(const f32x4*)(shm + AUX + (wm * 64 + mf * 16 + li) * 16);
  const float* const cl = cfin + wc * 192 + lq * 4;
  const int sbase = ((mtile * 2 + (wm >> 1)) * 72 * 8 + (wm & 1) * 4) * 2048;
  auto putf = [&](int kg, int mf, u32x4 v) {
    __builtin_amdgcn_raw_buffer_store_b128(v, ra, ln * 16 + sbase + ((kg >> 1) * 8 + mf) * 2048 + (kg & 1) * 1024, 0, 16);
  };
  auto gelp = [&](f32x2 g, f32x2 u, f32x2 cg, f32x2 cu, f32x2 m2, f32x2 k12, f32x2 k22, f32x2 d2) {
    const f32x2 tg = __builtin_elementwise_fma(m2, cg, g), tu = __builtin_elementwise_fma(m2, cu, u);
    const f32x2 arg = tg * __builtin_elementwise_fma(tg * tg, k22, k12);
    const f32x2 dn = __builtin_elementwise_fma(f32x2{__builtin_amdgcn_exp2f(arg[0]), __builtin_amdgcn_exp2f(arg[1])}, d2, d2);
    const f32x2 r = (tg * tu) * f32x2{__builtin_amdgcn_rcpf(dn[0]), __builtin_amdgcn_rcpf(dn[1])};
    return pack2(r[0], r[1]);
  };
  auto gel2 = [&](int p, int kg, int mf0 = 0, int mf1 = 4) {
    f32x4 cg[2], cu[2];
#pragma unroll
    for (int j = 0; j < 2; ++j) { cg[j] = *(const f32x4*)(cl + p * 32 + j * 16); cu[j] = *(const f32x4*)(cl + p * 32 + 32 + j * 16); }
#pragma unroll
    for (int mf = mf0; mf < mf1; ++mf) {
      const f32x2 m2 = {rf[mf][0], rf[mf][0]}, k12 = {rf[mf][1], rf[mf][1]}, k22 = {rf[mf][2], rf[mf][2]}, d2 = {rf[mf][3], rf[mf][3]};
      u32x4 o;
#pragma unroll
      for (int i = 0; i < 4; ++i) {
        const int j = i >> 1, e = (i & 1) * 2;
        const f32x4 g = acc[mf][p * 2 + j], u = acc[mf][p * 2 + 2 + j];
        o[i] = gelp(f32x2{g[e], g[e + 1]}, f32x2{u[e], u[e + 1]}, f32x2{cg[j][e], cg[j][e + 1]}, f32x2{cu[j][e], cu[j][e + 1]}, m2, k12, k22, d2);
      }
      putf(kg, mf, o);
    }
  };
  int tsel = ntile & 1;
  asm volatile("" : "+s"(tsel));
  if (tsel != 0) {
    const int tq = (wc ? tl : te + 2 + (xo & 1)) + he;
    gel2(0, 2 * tq);
    gel2(3, 2 * tq + 1);
    const float cg = cfin[wc * 192 + 64 + li], cu = cfin[wc * 192 + 80 + li];
#pragma unroll
    for (int mf = 0; mf < 4; ++mf) {
      f32x4 t[4];
#pragma unroll
      for (int i = 0; i < 4; ++i) t[i] = *(const f32x4*)(tb + i * 64 + mf * 16 + lq * 4);
      const f32x4 g = acc[mf][4], u = acc[mf][5];
      u32x2 o;
#pragma unroll
      for (int e = 0; e < 4; e += 2)
        o[e >> 1] = gelp(f32x2{g[e], g[e + 1]}, f32x2{u[e], u[e + 1]}, f32x2{cg, cg}, f32x2{cu, cu}, f32x2{t[0][e], t[0][e + 1]},
                         f32x2{t[1][e], t[1][e + 1]}, f32x2{t[2][e], t[2][e + 1]}, f32x2{t[3][e], t[3][e + 1]});
      *(u32x2*)(shm + wm * 4096 + mf * 1024 + wc * 512 + li * 32 + lq * 8) = o;
    }
    if (wc == 0) {
      signal(0x10000u << wid);
      WAIT_V(0);
      early();
    } else {
      await(0x1000u << wid, 0x1000u << wid);
#pragma unroll
      for (int mf = 0; mf < 4; ++mf) putf(2 * (tl + 2) + he, mf, __builtin_bit_cast(u32x4, tr_frag(shm3 + wm * 4096 + mf * 1024 + ln * 8)));
    }
  } else {
    float rd[4];
#pragma unroll
    for (int mf = 0; mf < 4; ++mf) rd[mf] = svr[wm * 64 + mf * 16 + li];
    auto cvq = [&](int p, int mf, const f32x4& c0, const f32x4& c1) {
      u32x4 o;
#pragma unroll
      for (int j = 0; j < 2; ++j) {
        const f32x4 a = acc[mf][p * 2 + j], c4 = j ? c1 : c0;
        const f32x2 m2 = {rf[mf][0], rf[mf][0]}, r2 = {rd[mf], rd[mf]};
        f32x2 t0 = __builtin_elementwise_fma(m2, f32x2{c4[0], c4[1]}, f32x2{a[0], a[1]});
        f32x2 t1 = __builtin_elementwise_fma(m2, f32x2{c4[2], c4[3]}, f32x2{a[2], a[3]});
        if (p) { t0 *= r2; t1 *= r2; }
        o[2 * j] = pack2(t0[0], t0[1]); o[2 * j + 1] = pack2(t1[0], t1[1]);
      }
      return o;
    };
    const float cv0 = cfin[wc * 192 + 64 + li], cv1 = cfin[wc * 192 + 80 + li];
    auto cvv = [&](int mf) {
      const f32x4 n4 = *(const f32x4*)(tb + mf * 16 + lq * 4), r4 = *(const f32x4*)(svr + wm * 64 + mf * 16 + lq * 4);
      u32x4 o;
#pragma unroll
      for (int j = 0; j < 2; ++j) {
        const f32x4 a = acc[mf][4 + j];
        const f32x2 c2 = {j ? cv1 : cv0, j ? cv1 : cv0};
        const f32x2 t0 = __builtin_elementwise_fma(f32x2{n4[0], n4[1]}, c2, f32x2{a[0], a[1]}) * f32x2{r4[0], r4[1]};
        const f32x2 t1 = __builtin_elementwise_fma(f32x2{n4[2], n4[3]}, c2, f32x2{a[2], a[3]}) * f32x2{r4[2], r4[3]};
        o[2 * j] = pack2(t0[0], t0[1]); o[2 * j + 1] = pack2(t1[0], t1[1]);
      }
      return o;
    };
    u32x4 kx[4], vx[4], qx[4], hx[4];
    const bf16x8 ones = {0x3f80, 0x3f80, 0x3f80, 0x3f80, 0x3f80, 0x3f80, 0x3f80, 0x3f80};
    const f32x4 ck0 = *(const f32x4*)(cl + 32), ck1 = *(const f32x4*)(cl + 48);
    kx[2] = cvq(1, 2, ck0, ck1); kx[3] = cvq(1, 3, ck0, ck1);
    vx[2] = cvv(2); vx[3] = cvv(3);
    auto exo = [](int e) { return e * 4096 + (e >> 2) * (G2_A - 16384); };
    const bool needh = wm == 0 && mtile > 0;
    const __amdgpu_buffer_rsrc_t rh = __builtin_amdgcn_make_buffer_rsrc((void*)((char*)halo + ((needh ? mtile - 1 : mtile) * 32 + ntile + wc) * 4096), 0,
                                                                        needh || wm == 3 ? 4096 : 0, 0x00020000);
    if (wm < 3) {
      char* const ex = shm + exo(wc * 3 + wm) + ln * 16;
      *(u32x4*)ex = kx[2]; *(u32x4*)(ex + 1024) = kx[3]; *(u32x4*)(ex + 2048) = vx[2]; *(u32x4*)(ex + 3072) = vx[3];
      signal(0x10000u << wid);
    } else {
#pragma unroll
      for (int r = 0; r < 4; ++r) __builtin_amdgcn_raw_buffer_store_b128(r < 2 ? kx[2 + r] : vx[r], rh, ln * 16 + r * 1024, 0, 16);
    }
    auto hload = [&]() {
#pragma unroll
      for (int r = 0; r < 4; ++r) hx[r] = __builtin_bit_cast(u32x4, __builtin_amdgcn_raw_buffer_load_b128(rh, ln * 16 + r * 1024, 0, 16));
    };
    SB0();
    gel2(3, 2 * (wc ? tl + 3 : te) + he);
    SB0();
    if (wm == 0) hload();
    SB0();
    kx[0] = cvq(1, 0, ck0, ck1); kx[1] = cvq(1, 1, ck0, ck1);
    vx[0] = cvv(0); vx[1] = cvv(1);
    {
      const f32x4 cq0 = *(const f32x4*)(cl), cq1 = *(const f32x4*)(cl + 16);
#pragma unroll
      for (int mf = 0; mf < 4; ++mf) qx[mf] = cvq(0, mf, cq0, cq1);
    }
    f32x4 mk0, mk1;
#pragma unroll
    for (int j = 0; j < 4; ++j) {
      const int k0 = 4 * lq + j, k1 = 16 + 4 * lq + j;
      mk0[j] = (k0 >= li + 9 && k0 <= li + 16) ? 0.f : -1.0e30f;
      mk1[j] = (k1 >= li + 9 && k1 <= li + 16) ? 0.f : -1.0e30f;
    }
    auto max3 = [](float a, float b, float c) { float r; asm("v_max3_f32 %0, %1, %2, %3" : "=v"(r) : "v"(a), "v"(b), "v"(c)); return r; };
    auto max2 = [](float a, float b) { float r; asm("v_max_f32 %0, %1, %2" : "=v"(r) : "v"(a), "v"(b)); return r; };
    asm volatile("" : "+v"(kx[0]), "+v"(kx[1]), "+v"(vx[0]), "+v"(vx[1]), "+v"(qx[0]), "+v"(qx[1]), "+v"(qx[2]), "+v"(qx[3]), "+v"(mk0), "+v"(mk1));
    if (wc == 0) {
      if (wm == 0) WAIT_V(4); else WAIT_V(0);
      early();
    }
    if (wm > 0) {
      await(0x8000u << wid, 0x8000u << wid);
#pragma unroll
      for (int i = 0; i < 4; ++i) hx[i] = *(const u32x4*)(shm + exo(wc * 3 + wm - 1) + i * 1024 + ln * 16);
    } else if (__builtin_expect(cok || !needh, 1)) {
      while (__builtin_expect(blank(hx), 0)) {
        __builtin_amdgcn_s_sleep(1);
        asm volatile("" ::: "memory");
        hload();
      }
    } else {
      await(0xffffu, 0x404u);
      char* const fb = shm + exo(6 + wc) + ln * 16;
#pragma unroll 1
      for (int i = 0; i < 32; ++i) {
        const int rec = i >> 3, e = i & 7;
        const int row = m0 - 32 + 2 * (rec < 2 ? li : lq * 4 + (e & 3)) + (rec & 1);
        const int col = (rec < 2 ? D_ + lq * 4 + (e & 3) : 2 * D_ + li) + (ntile + wc) * 32 + (e >> 2) * 16;
        const u16* xr = x + (size_t)row * D_;
        const unsigned stt = row_stats_serial(xr);
        const float mb = lo_f(stt);
        float a = 0.f;
#pragma unroll 1
        for (int k = 0; k < D_; ++k) a += (bf2f(xr[k]) - mb) * bf2f(wqkv[(size_t)k * 3072 + col]);
        *(u16*)(fb + rec * 1024 + e * 2) = f2bf(a / hi_f(stt));
      }
#pragma unroll
      for (int i = 0; i < 4; ++i) hx[i] = *(const u32x4*)(fb + i * 1024);
    }
    {
      const f32x4 z = {0.f, 0.f, 0.f, 0.f};
      const float mz = mtile == 0 && wm == 0 ? -1.0e30f : 0.f;
      const f32x4 mkf = {fminf(mk0[0], mz), fminf(mk0[1], mz), fminf(mk0[2], mz), fminf(mk0[3], mz)};
      f32x4 s0[4], s1[4];
#pragma unroll
      for (int i = 0; i < 4; ++i) {
        const int n = i ^ 2;
        const bf16x8 qf = __builtin_bit_cast(bf16x8, qx[n]);
        s1[n] = __builtin_amdgcn_mfma_f32_16x16x32_bf16(__builtin_bit_cast(bf16x8, kx[n]), qf, mk1, 0, 0, 0);
        s0[n] = __builtin_amdgcn_mfma_f32_16x16x32_bf16(__builtin_bit_cast(bf16x8, n < 2 ? hx[n] : kx[n - 2]), qf, n < 2 ? mkf : mk0, 0, 0, 0);
      }
      SB0();
      asm volatile("s_nop 7\n\ts_nop 3" : "+v"(s0[0]), "+v"(s1[0]), "+v"(s0[1]), "+v"(s1[1]), "+v"(s0[2]), "+v"(s1[2]), "+v"(s0[3]), "+v"(s1[3]));
      float mx[4];
#pragma unroll
      for (int n = 0; n < 4; ++n) mx[n] = max3(max3(s0[n][0], s0[n][1], s0[n][2]), max3(s0[n][3], s1[n][0], s1[n][1]), max2(s1[n][2], s1[n][3]));
#pragma unroll
      for (int n = 0; n < 4; ++n) { auto r = __builtin_amdgcn_permlane16_swap(__float_as_uint(mx[n]), __float_as_uint(mx[n]), false, false);
        mx[n] = max2(__uint_as_float(r[0]), __uint_as_float(r[1])); }
#pragma unroll
      for (int n = 0; n < 4; ++n) { auto r = __builtin_amdgcn_permlane32_swap(__float_as_uint(mx[n]), __float_as_uint(mx[n]), false, false);
        mx[n] = max2(__uint_as_float(r[0]), __uint_as_float(r[1])); }
      SB0();
      constexpr float cs2 = 0.17677669529663687f * 1.4426950408889634f;
      f32x4 os[4], o0[4], o1[4];
#pragma unroll
      for (int n = 0; n < 4; ++n) {
        const float c = cs2 * rd[n], nm = -mx[n] * c;
        float e8[8];
#pragma unroll
        for (int j = 0; j < 4; ++j) {
          e8[j] = __builtin_amdgcn_exp2f(__builtin_fmaf(s0[n][j], c, nm));
          e8[4 + j] = __builtin_amdgcn_exp2f(__builtin_fmaf(s1[n][j], c, nm));
        }
        u32x4 pk;
#pragma unroll
        for (int j = 0; j < 4; ++j) pk[j] = pack2(e8[2 * j], e8[2 * j + 1]);
        const bf16x8 pf = __builtin_bit_cast(bf16x8, pk);
        const u32x4 vp = n < 2 ? hx[2 + n] : vx[n - 2];
        const u32x4 va = {vp[0], vp[1], vx[n][0], vx[n][1]}, vb = {vp[2], vp[3], vx[n][2], vx[n][3]};
        os[n] = __builtin_amdgcn_mfma_f32_16x16x32_bf16(ones, pf, z, 0, 0, 0);
        o0[n] = __builtin_amdgcn_mfma_f32_16x16x32_bf16(__builtin_bit_cast(bf16x8, va), pf, z, 0, 0, 0);
        o1[n] = __builtin_amdgcn_mfma_f32_16x16x32_bf16(__builtin_bit_cast(bf16x8, vb), pf, z, 0, 0, 0);
      }
#pragma unroll
      for (int n = 0; n < 4; ++n) {
        const float inv = __builtin_amdgcn_rcpf(os[n][0]);
        const f32x4 n0 = o0[n] * inv, n1 = o1[n] * inv;
        putf(112 + ntile + wc, n, u32x4{pack2(n0[0], n0[1]), pack2(n0[2], n0[3]), pack2(n1[0], n1[1]), pack2(n1[2], n1[3])});
      }
    }
  }
  TS1(2);
  const bool tya1 = tya;
  {
  __builtin_amdgcn_sched_barrier(0);
  int tid = threadIdx.x;
  asm volatile("" : "+v"(tid));
  __builtin_amdgcn_sched_barrier(0);
  const int lane = tid & 63;
  const int wm = wid >> 1, wn = wid & 1;
  const int m0 = mt2 * 128, n0 = nt2 * 128;
  constexpr int PD = G2_NSTAGE;
  const int ny = (xcd & 1) ? NY - DLT : NY + DLT;
  TS(0);
  const unsigned va = lane * 16;
  auto stage_y = [&]() {
    asm volatile("s_add_u32 m0, %[l], %[oa]\n\ts_nop 0\n\tglobal_load_lds_dwordx4 %[va], %[a] sc1\n\tglobal_load_lds_dwordx4 %[va], %[a] offset:1024 sc1\n\t"
                 "global_load_lds_dwordx4 %[va], %[a] offset:2048 sc1\n\tglobal_load_lds_dwordx4 %[va], %[a] offset:3072 sc1\n\t" G2_W
                 :: [l] "s"(sl), [oa] "s"(shm_base + G2_A + (p ^ role) * 4096), [a] "s"(sa), [va] "v"(va), G2_WOP(wy) : "memory", "scc", "m0");
    ++st;
    sa += st == NY ? (long)((1 - 2 * role) * NY * 16384) : 16384l;
    sl = (sl + 16384) & 65535;
    wsel();
  };
  u64 ready = 0;
  const u64* const mbox = &done1[bid * 64 + lane];
  auto seen = [&](u64 v) { const u64 r = __ballot(fresh(v, t_own)); ready |= r | (r << 32); };
  auto need = [&](u64 mask, bool renew) {
    while ((ready & mask) != mask) {
      seen(__hip_atomic_load(mbox, RLX_AGENT));
      if ((ready & mask) != mask) {
        __builtin_amdgcn_s_sleep(PSLEEP);
        if (renew && !cok) notify(0, 32);
      }
    }
    asm volatile("" ::: "memory");
  };
  u64 fl = 0;
  auto peek = [&]() { asm volatile("global_load_dwordx2 %0, %1, %2 sc1" : "+v"(fl) : "v"(va >> 1), "s"(done1 + bid * 64) : "memory"); };
  auto check = [&](u64 mask, bool renew) {
    asm volatile("" : "+v"(fl));
    seen(fl);
    if (__builtin_expect((ready & mask) != mask, 0)) need(mask, renew);
  };
  if (wid >= 4) {
    TS(4);
    __builtin_amdgcn_s_setprio(0);
    BARL();
    stage_y();
    stage_y();
    BAR();
    if (p == 1) peek();
    stage_y();
    WAIT_V(16);
    BAR();
    if (p == 0) notify(0, 32);
    stage_y();
    const char* sz;
    int ws0, ws1;
    u64 cm = 0xa0a0ull << (32 + 16 * role), cn = 0x0f0full << (16 * role);
    const unsigned evc = p == 1 ? 0x8088u : 0u, evp = p == 1 ? 0x2020u : 0u;
#pragma unroll 1
    for (int g = 3; g < ny - PD; ++g) {
      WAIT_V(16);
      BAR();
      if ((evc >> g) & 1) { check(cm, true); cm = cn; cn = (0xffffull << (16 * role)) | 0x55555555ull; }
      if ((evp >> g) & 1) peek();
      if (g == 9 + p) {
        sz = (const char*)abuf + (size_t)(mt2 * 72 + 56) * 16384 + (p >> 1) * 8192 + role * 4096 + (p & 1) * 2048;
        ws0 = __builtin_amdgcn_readfirstlane((wz.vb[0][1] ^ 64) - wz.vb[0][0]) * 16; ws1 = __builtin_amdgcn_readfirstlane((wz.vb[1][1] ^ 64) - wz.vb[1][0]) * 16;
        PIN_S(sz); PIN_S(ws0); PIN_S(ws1); PIN_S(wz.wb[0]); PIN_S(wz.wb[1]);
      }
      stage_y();
    }
    WAIT_V(16);
    BAR();
    sa = sz; sw[0] = wz.wb[0]; sw[1] = wz.wb[1];
    auto stage_s = [&]() {
      asm volatile("s_add_u32 m0, %[l], %[oa]\n\ts_nop 0\n\tglobal_load_lds_dwordx4 %[va], %[a] sc1\n\tglobal_load_lds_dwordx4 %[va], %[a] offset:1024 sc1\n\t" G2_W
                   :: [l] "s"(sl), [oa] "s"(shm_base + G2_A + (p >> 1) * 8192 + (p & 1) * 2048), [a] "s"(sa), [va] "v"(va), G2_WOP(wz) : "memory", "scc", "m0");
      sa += 16384; sw[0] += ws0; sw[1] += ws1; sl = (sl + 16384) & 65535;
    };
    stage_s();
#pragma unroll
    for (int j = -3; j < 15; ++j) {
      WAIT_V((j == -3 ? 2 : 0) + (j < 13 ? 12 : (14 - j) * 6) + (j > GETJ && j < GETJ + 4 ? 2 : 0) + (j > GETJ + 1 && j < GETJ + 5 ? 2 : 0));
      BAR();
      if (j < 12) stage_s();
      if (j == GETJ || j == GETJ + 1)
        asm volatile("s_mov_b32 m0, %[l]\n\ts_nop 0\n\tglobal_load_lds_dwordx4 %[va], %[a] offset:%[o] sc1\n\tglobal_load_lds_dwordx4 %[va], %[a] offset:%[o]+1024 sc1"
                     :: [l] "s"(shm_base + G2_X + 16384 + p * 4096), [a] "s"((const char*)slab + (size_t)(bid ^ 5) * 16384 + p * 4096), [va] "v"(va),
                        [o] "n"((j - GETJ) * 2048) : "memory", "m0");
    }
    asm volatile("" : "+v"(fl));
    TS(5);
    if (tid < 258) __hip_atomic_store(lane ? done2 + bid * 8 : &acks[bid], __builtin_amdgcn_s_memrealtime(), RLX_AGENT);
    if (__builtin_expect(!cok, 0) && p == 0) {
      const bool mine = lane == 32 || (lane < 32 && (tya1 || ((lane >> 3) & 1) != (xcd >> 2)));
      const u64* const pa = &acks[lane < 32 ? client(lane) : bid ^ 5];
      while (!__all(!mine || fresh(__hip_atomic_load(pa, RLX_AGENT), t_own))) {
        notify(0, 32);
        if (lane == 0) __hip_atomic_store(done2 + bid * 8, __builtin_amdgcn_s_memrealtime(), RLX_AGENT);
        __builtin_amdgcn_s_sleep(32);
      }
    }
  } else {
    TS(6);
    f32x4 acc[4][4], sacc[2][4];
    const int fr = lane & 15, fq = lane >> 4, q4 = fr >> 2, pp = lane & 3;
    const int ff = ((q4 >> 1) & 1) | ((fq & 1) << 1);
    int aof = G2_A + wm * 8192 + lane * 16;
    int boff[4];
#pragma unroll
    for (int j = 0; j < 4; ++j) boff[j] = wn * 8192 + (8 * fq + q4) * 128 + ((j ^ ff) * 32) + pp * 8;
    int vo = wid * 4096 + lane * 16;
    int vx = ((m0 + wm * 64 + role * 32 + 2 * fr) * D_ + n0 + wn * 64) * 2 + (fq & 1) * 32 + (fq >> 1) * 16;
    lds_cptr px = shm3 + G2_X + wid * 4096 + (fq >> 1) * 512 + fr * 16 + (fq & 1) * 8;
    asm volatile("" : "+v"(vo), "+v"(vx), "+v"(px));
#define SB() __builtin_amdgcn_sched_barrier(0)
    struct Fr { bf16x8 a[4]; bf16x4 bl[4], bh[4]; };
    Fr fa[2], fb[2];
    auto rd = [&](Fr* f, int odd, int t) {
      constexpr int ord[12] = {0, 1, 8, 2, 3, 4, 5, 6, 7, 9, 10, 11};
      constexpr int seq[24] = {0, 1, 2, 12, 13, 14, 15, 16, 3, 4, 5, 6, 17, 18, 19, 20, 7, 8, 9, 21, 10, 22, 11, 23};
      lds_cptr sb = shm3 + odd * 16384;
      const int ks = seq[t] / 12, o = ord[seq[t] % 12];
      if (o >= 8) f[ks].a[o - 8] = *(const __attribute__((address_space(3))) bf16x8*)(sb + aof + (o - 8) * 2048 + ks * 1024);
      else if (o & 1) f[ks].bh[o >> 1] = tr_read(sb + boff[o >> 1] + ks * 4096 + 512);
      else f[ks].bl[o >> 1] = tr_read(sb + boff[o >> 1] + ks * 4096);
    };
    auto bfr = [&](const Fr& c, int j) { return __builtin_shufflevector(c.bl[j], c.bh[j], 0, 1, 2, 3, 4, 5, 6, 7); };
    auto mf1 = [](f32x4& c, bf16x8 b, bf16x8 a, bool z = false) {
      if (z) asm volatile("v_mfma_f32_16x16x32_bf16 %0, %1, %2, 0" : "=&v"(c) : "v"(b), "v"(a));
      else asm volatile("v_mfma_f32_16x16x32_bf16 %0, %1, %2, %0" : "+v"(c) : "v"(b), "v"(a));
    };
    auto mf2 = [](f32x4& c, bf16x8 b, bf16x8 a, bf16x8 b2, bf16x8 a2, bool z) {
      if (z) asm volatile("v_mfma_f32_16x16x32_bf16 %0, %1, %2, 0\n\tv_mfma_f32_16x16x32_bf16 %0, %3, %4, %0" : "=&v"(c) : "v"(b), "v"(a), "v"(b2), "v"(a2));
      else asm volatile("v_mfma_f32_16x16x32_bf16 %0, %1, %2, %0\n\tv_mfma_f32_16x16x32_bf16 %0, %3, %4, %0" : "+v"(c) : "v"(b), "v"(a), "v"(b2), "v"(a2));
    };
    auto pairs = [&](f32x4 (*ac)[4], const Fr* c, int nq, int mx, int nf, auto fill, bool z = false) {
#pragma unroll
      for (int q = 0; q < nq; ++q) {
        const int mf = (q >> 2) ^ mx, j = (q & 4) ? 3 - (q & 3) : q & 3, ks = q & 1;
        SB();
        if (2 * q >= nf) mf2(ac[mf][j], bfr(c[ks], j), c[ks].a[mf], bfr(c[ks ^ 1], j), c[ks ^ 1].a[mf], z);
        else {
          mf1(ac[mf][j], bfr(c[ks], j), c[ks].a[mf], z);
          SB();
          fill(2 * q);
          SB();
          mf1(ac[mf][j], bfr(c[ks ^ 1], j), c[ks ^ 1].a[mf]);
          SB();
          fill(2 * q + 1);
        }
      }
      SB();
    };
    auto flip = [&]() {
      aof ^= 32768; boff[0] ^= 32768; boff[1] ^= 32768; boff[2] ^= 32768; boff[3] ^= 32768;
      asm volatile("" : "+v"(aof), "+v"(boff[0]), "+v"(boff[1]), "+v"(boff[2]), "+v"(boff[3]));
    };
    auto bar = [&]() { __builtin_amdgcn_s_waitcnt(0xC07F); BAR(); };
    auto ystg = [&](const Fr* c, Fr* n, int odd, bool z = false) { pairs(acc, c, 16, 0, 24, [&](int t) { rd(n, odd, t); }, z); };
    auto stg = [&](const Fr* c, Fr* n, int odd, bool z = false) { pairs(sacc, c, 8, 0, 10, [&](int t) { rd(n, odd, 2 * t); rd(n, odd, 2 * t + 1); }, z); };
    asm volatile("" : "+v"(aof), "+v"(boff[0]), "+v"(boff[1]), "+v"(boff[2]), "+v"(boff[3]));
    await(0xffffu, 0x404u);
    auto lda = [&](int i) {
      asm volatile("s_mov_b32 m0, %[l]\n\ts_nop 0\n\tglobal_load_lds_dwordx4 %[va], %[a] sc1\n\tglobal_load_lds_dwordx4 %[va], %[a] offset:1024 sc1\n\t"
                   "global_load_lds_dwordx4 %[va], %[a] offset:2048 sc1\n\tglobal_load_lds_dwordx4 %[va], %[a] offset:3072 sc1"
                   :: [l] "s"(shm_base + G2_A + (i + 1) * 16384 + (p ^ role) * 4096), [a] "s"(sa + i * 16384), [va] "v"(va) : "memory", "m0");
    };
    need(0x0505ull << (32 + 16 * role), false);
    TS(4);
    lda(0);
    lda(1);
    need(0x0f0full << (32 + 16 * role), false);
    lda(2);
    WAIT_V(8);
    signal(1u << 24);
    await(0xff000000u, 4u << 24);
    __builtin_amdgcn_s_waitcnt(0xC07F);
    SB();
#pragma unroll
    for (int t = 0; t < 24; ++t) rd(fa, 1, t);
    flip();
#define CPRIO 0
    __builtin_amdgcn_s_setprio(CPRIO);
    WAIT_V(4);
    TS(5);
    peek();
    bar();
    TS(7);
    ystg(fa, fb, 0, true);
    WAIT_V(1);
    bar();
    ystg(fb, fa, 1);
    WAIT_V(0);
    check(0x5050ull << (32 + 16 * role), false);
    static_assert((NY - DLT) % 2 == 1, "");
#pragma unroll 1
    for (int g = 2; g < ny - 1; g += 2) {
      flip();
      bar();
      ystg(fa, fb, 0);
      bar();
      ystg(fb, fa, 1);
    }
    flip();
    bar();
    {
      const __amdgpu_buffer_rsrc_t rs = __builtin_amdgcn_make_buffer_rsrc((void*)((char*)slab + (size_t)bid * 16384), 0, 16384, 0x00020000);
      u32x4 pk;
      pairs(acc, fa, 16, 2, 32, [&](int t) {
        if (t < 20) rd(fb, 0, t);
        else {
          const int f = (t - 20) / 3, e = (t - 20) % 3;
          if (e < 2) { const f32x4 a = acc[2 + (f >> 1)][(f & 1) * 2 + e]; pk[2 * e] = pack2(a[0], a[1]); pk[2 * e + 1] = pack2(a[2], a[3]); }
          else __builtin_amdgcn_raw_buffer_store_b128(pk, rs, vo + f * 1024, 0, 16);
        }
      });
    }
    TS(2);
    static_assert(GETJ % 2 == 1 && GETJ >= 3 && GETJ <= 7, "");
    f32x4 xf[2][4];
#pragma unroll
    for (int j = 0; j < GETJ + 5; j += 2) {
      bar();
      stg(fb, fa, 1, j == 0);
      flip();
      bar();
      u32x2 xv[2];
      pairs(sacc, fa, 8, 0, 12, [&](int t) {
        if (t < 10) { rd(fb, 0, 2 * t); rd(fb, 0, 2 * t + 1); }
        else if (j < 8) xv[t - 10] = *(const __attribute__((address_space(3))) u32x2*)(px + (j >> 1) * 1024 + (t - 10) * 256);
      });
      if (j < 8) {
#pragma unroll
        for (int e = 0; e < 2; ++e) xf[j >> 2][(j & 2) + e] = f32x4{lo_f(xv[e][0]), hi_f(xv[e][0]), lo_f(xv[e][1]), hi_f(xv[e][1])};
        asm volatile("" : "+v"(xf[j >> 2][j & 2]), "+v"(xf[j >> 2][(j & 2) + 1]));
        SB();
      }
    }
    WAIT_V(0);
    bar();
    u32x4 pv[4];
    {
      lds_cptr pp_ = shm3 + G2_X + 16384 + vo;
      pairs(sacc, fb, 8, 0, 12, [&](int t) {
        if (t < 2) {
#pragma unroll
          for (int f = 2 * t; f < 2 * t + 2; ++f) pv[f] = *(const __attribute__((address_space(3))) u32x4*)(pp_ + f * 1024);
        } else { rd(fa, 1, 2 * t - 4); rd(fa, 1, 2 * t - 3); }
      });
      if (__builtin_expect(blank(pv) || !cok, 0)) {
        const __amdgpu_buffer_rsrc_t rp = __builtin_amdgcn_make_buffer_rsrc((void*)((char*)slab + (size_t)(bid ^ 5) * 16384), 0, 16384, 0x00020000);
        while (!cok && !__all(fresh(__hip_atomic_load(done2 + (bid ^ 5) * 8, RLX_AGENT), t_own))) {
          notify(0, 32);
          if (lane == 0) __hip_atomic_store(done2 + bid * 8, __builtin_amdgcn_s_memrealtime(), RLX_AGENT);
          __builtin_amdgcn_s_sleep(8);
        }
        do {
          asm volatile("" ::: "memory");
#pragma unroll
          for (int f = 0; f < 4; ++f) pv[f] = __builtin_bit_cast(u32x4, __builtin_amdgcn_raw_buffer_load_b128(rp, vo + f * 1024, 0, 16));
        } while (blank(pv));
      }
    }
    auto silu = [&](int f0, int f1) {
#pragma unroll
      for (int f = f0; f < f1; ++f) {
        const int mf = f >> 2, j = f & 3;
        const u32x4 pw = pv[mf * 2 + (j >> 1)];
        const f32x4 ya = acc[mf][j] + f32x4{lo_f(pw[2 * (j & 1)]), hi_f(pw[2 * (j & 1)]), lo_f(pw[2 * (j & 1) + 1]), hi_f(pw[2 * (j & 1) + 1])};
#pragma unroll
        for (int e = 0; e < 2; ++e) {
          const f32x2 y2 = {ya[2 * e], ya[2 * e + 1]};
          const f32x2 ar = y2 * f32x2{-1.4426950408889634f, -1.4426950408889634f};
          const f32x2 dn = f32x2{__builtin_amdgcn_exp2f(ar[0]), __builtin_amdgcn_exp2f(ar[1])} + f32x2{1.0f, 1.0f};
          const f32x2 t = y2 * f32x2{__builtin_amdgcn_rcpf(dn[0]), __builtin_amdgcn_rcpf(dn[1])};
          acc[mf][j][2 * e] = t[0]; acc[mf][j][2 * e + 1] = t[1];
        }
        asm volatile("" : "+v"(acc[mf][j]));
      }
      SB();
    };
#define SPT 0, 1, 2, 3, 5, 7, 8, 8
    constexpr int spt[] = {SPT};
    silu(0, spt[1]);
#pragma unroll
    for (int j = GETJ + 6; j < 15; ++j) {
      if (j & 1) flip();
      bar();
      if (j & 1) stg(fa, fb, 0); else stg(fb, fa, 1);
      silu(spt[j - GETJ - 5], j == 14 ? 8 : spt[j - GETJ - 4]);
    }
    pairs(sacc, fa, 8, 0, 0, [](int) {});
    asm volatile("s_nop 7\n\ts_nop 3" : "+v"(sacc[1][0]), "+v"(sacc[1][1]), "+v"(sacc[1][2]), "+v"(sacc[1][3]));
    SB();
    TS(1);
    {
      const __amdgpu_buffer_rsrc_t ro = __builtin_amdgcn_make_buffer_rsrc((void*)out, 0, S_ * D_ * 2, 0x00020000);
#pragma unroll
      for (int f = 0; f < 4; ++f) {
        const int mf = f >> 1, j = (f & 1) * 2;
        const f32x4 a = __builtin_elementwise_fma(sacc[mf][j], acc[mf][j], xf[mf][j]), b = __builtin_elementwise_fma(sacc[mf][j + 1], acc[mf][j + 1], xf[mf][j + 1]);
        const auto r0 = __builtin_amdgcn_permlane16_swap(pack2(a[0], a[1]), pack2(b[0], b[1]), false, false);
        const auto r1 = __builtin_amdgcn_permlane16_swap(pack2(a[2], a[3]), pack2(b[2], b[3]), false, false);
        __builtin_amdgcn_raw_buffer_store_b128(u32x4{r0[0], r1[0], r0[1], r1[1]}, ro, vx + mf * 2048 + j * 32, 0, 2);
      }
    }
    TS(3);
  }
  TSFLUSH();
  }
  asm volatile(".Lcode_end:");
}

extern "C" void kernel_launch(void* const* d_in, const int* in_sizes, int n_in, void* d_out, int out_size, void* d_ws,
                              size_t ws_size, hipStream_t stream) {
  const u16* A_x = (const u16*)d_in[0];
  const u16* A_wqkv = (const u16*)d_in[1];
  const u16* A_wo = (const u16*)d_in[2];
  const u16* B_wg = (const u16*)d_in[4];
  const u16* B_wu = (const u16*)d_in[5];
  const u16* B_wd = (const u16*)d_in[6];
  u16* out = (u16*)d_out;
  fused_kernel<<<256, 512, 0, stream>>>(A_x, A_wqkv, B_wg, B_wu, B_wd, A_wo, out, (char*)d_ws);
}
```

```cpp
#define ALL_TS 1
#define TSMASK 160
#include <hip/hip_runtime.h>
#include <hip/hip_bf16.h>
#include <stdint.h>
#include <type_traits>

typedef unsigned short u16;
using bf16x8 = __attribute__((ext_vector_type(8))) short;
using bf16x4 = __attribute__((ext_vector_type(4))) short;
using f32x4 = __attribute__((ext_vector_type(4))) float;
using u32x2 = __attribute__((ext_vector_type(2))) unsigned;
using u32x4 = __attribute__((ext_vector_type(4))) unsigned;
typedef __attribute__((address_space(3))) const char* lds_cptr;

constexpr int S_ = 2048, D_ = 1024, F_ = 3584;

__device__ __forceinline__ float bf2f(u16 h) { return __uint_as_float((unsigned)h << 16); }
__device__ __forceinline__ u16 f2bf(float f) {
  unsigned u = __float_as_uint(f);
  u += 0x7fffu + ((u >> 16) & 1u);
  return (u16)(u >> 16);
}
__device__ __forceinline__ float rbf(float f) { return bf2f(f2bf(f)); }
typedef __attribute__((ext_vector_type(2))) __bf16 bf16x2_t;
typedef __attribute__((ext_vector_type(2))) float f32x2;
__device__ __forceinline__ unsigned pack2(float a, float b) {
  bf16x2_t r = __builtin_convertvector((f32x2){a, b}, bf16x2_t);
  return __builtin_bit_cast(unsigned, r);
}
__device__ __forceinline__ float lo_f(unsigned p) { return __uint_as_float(p << 16); }
__device__ __forceinline__ float hi_f(unsigned p) { return __uint_as_float(p & 0xffff0000u); }

#define TSDECL unsigned ts_r[16] = {0, 0, 0, 0, 0, 0, 0, 0, 0, 0, 0, 0, 0, 0, 0, 0}
#define TSS(i) do { if ((TSMASK >> (i)) & 1) { unsigned long long t_; asm volatile("s_memrealtime %0\n\ts_waitcnt lgkmcnt(0)" : "=s"(t_)); ts_r[i] = (unsigned)t_; } } while (0)
#define TSFLUSH() do { if ((threadIdx.x & 63) == 0) { for (int i_ = 0; i_ < 16; ++i_) flags[(blockIdx.x * 8 + (threadIdx.x >> 6)) * 16 + i_] = ts_r[i_]; } } while (0)
#define TS1(i) TSS(i)
#define TS(i) TSS(8 + (i))
#define PIN_S(v) asm volatile("" : "+s"(v))
#define WAIT_V(n) asm volatile("s_waitcnt vmcnt(%0)" ::"n"(n) : "memory")
#define WAIT_L(n) asm volatile("s_waitcnt lgkmcnt(%0)" ::"n"(n) : "memory")
#define BARL() do { WAIT_L(0); BAR(); } while (0)
#define BAR() do { asm volatile("" ::: "memory"); __builtin_amdgcn_s_barrier(); asm volatile("" ::: "memory"); } while (0)

__device__ __forceinline__ bf16x4 tr_read(lds_cptr p) {
  return __builtin_amdgcn_ds_read_tr16_b64_v4i16((__attribute__((address_space(3))) bf16x4*)p);
}
__device__ __forceinline__ bf16x8 tr_frag(lds_cptr p) {
  bf16x4 lo = tr_read(p), hi = tr_read(p + 512);
  bf16x8 r;
  r[0] = lo[0]; r[1] = lo[1]; r[2] = lo[2]; r[3] = lo[3];
  r[4] = hi[0]; r[5] = hi[1]; r[6] = hi[2]; r[7] = hi[3];
  return r;
}
__device__ __forceinline__ void glds16(const void* gsrc, unsigned lds_dst) {
  unsigned keep;
  asm volatile("s_mov_b32 %0, m0\n\ts_mov_b32 m0, %2\n\ts_nop 0\n\tglobal_load_lds_dwordx4 %1, off\n\ts_mov_b32 m0, %0"
               : "=&s"(keep) : "v"(gsrc), "s"(lds_dst) : "memory");
}
#define GLDS(gptr, laddr) glds16((const void*)(gptr), (unsigned)(laddr))
__device__ __forceinline__ void glds16_sc1(const void* gsrc, unsigned lds_dst) {
  unsigned keep;
  asm volatile("s_mov_b32 %0, m0\n\ts_mov_b32 m0, %2\n\ts_nop 0\n\tglobal_load_lds_dwordx4 %1, off sc1\n\ts_mov_b32 m0, %0"
               : "=&s"(keep) : "v"(gsrc), "s"(lds_dst) : "memory");
}
#define GLDS_SC1(gptr, laddr) glds16_sc1((const void*)(gptr), (unsigned)(laddr))
__device__ __forceinline__ void glds16_nc(const void* gsrc, unsigned lds_dst) {
  unsigned keep;
  asm volatile("s_mov_b32 %0, m0\n\ts_mov_b32 m0, %2\n\ts_nop 0\n\tglobal_load_lds_dwordx4 %1, off\n\ts_mov_b32 m0, %0"
               : "=&s"(keep) : "v"(gsrc), "s"(lds_dst));
}
#define GLDS_NC(gptr, laddr) glds16_nc((const void*)(gptr), (unsigned)(laddr))
__device__ __forceinline__ void glds16_s(const void* sbase, unsigned voff, unsigned lds_dst) {
  asm volatile("s_mov_b32 m0, %2\n\ts_nop 0\n\tglobal_load_lds_dwordx4 %0, %1" :: "v"(voff), "s"(sbase), "s"(lds_dst) : "m0");
}

typedef unsigned long long u64;
__device__ __forceinline__ bool fresh(u64 stamp, u64 t_own) {
  const u64 b = t_own + 100ull;
  const unsigned lo = (unsigned)stamp - (unsigned)b, hi = (unsigned)(stamp >> 32) - (unsigned)(b >> 32) - ((unsigned)stamp < (unsigned)b ? 1u : 0u);
  return (hi == 0u) & (lo < (unsigned)(__builtin_amdgcn_s_memrealtime() - t_own));
}
constexpr u64 TIMEOUT_ = 200000;
__device__ __forceinline__ float half_sum(float v) {
  v += __builtin_bit_cast(float, __builtin_amdgcn_update_dpp(0, __builtin_bit_cast(int, v), 0xB1, 0xf, 0xf, false));
  v += __builtin_bit_cast(float, __builtin_amdgcn_update_dpp(0, __builtin_bit_cast(int, v), 0x4E, 0xf, 0xf, false));
  v += __builtin_bit_cast(float, __builtin_amdgcn_update_dpp(0, __builtin_bit_cast(int, v), 0x141, 0xf, 0xf, false));
  v += __builtin_bit_cast(float, __builtin_amdgcn_update_dpp(0, __builtin_bit_cast(int, v), 0x140, 0xf, 0xf, false));
  { auto r = __builtin_amdgcn_permlane16_swap(__float_as_uint(v), __float_as_uint(v), false, false); v = __uint_as_float(r[0]) + __uint_as_float(r[1]); }
  return v;
}
__device__ __forceinline__ unsigned pack_stats(float mean, float var) {
  return (unsigned)f2bf(mean) | ((unsigned)f2bf(sqrtf(rbf(rbf(var) + 1e-5f))) << 16);
}
__device__ __forceinline__ unsigned row_stats_serial(const u16* xr) {
  float s = 0.f, q = 0.f;
#pragma unroll 1
  for (int k = 0; k < D_; ++k) s += bf2f(xr[k]);
  const float mean = s * (1.0f / D_);
#pragma unroll 1
  for (int k = 0; k < D_; ++k) { const float d = bf2f(xr[k]) - mean; q += d * d; }
  return pack_stats(mean, q * (1.0f / D_));
}
constexpr int G1_STAGE = 32768 + 5 * 8192;
#define SH1 0
#define PF_K0 4
#define CP_K0 5
#define DLT 1
#define PSLEEP 2
#define GETJ 3
#define PUB_K 9

constexpr unsigned BLANK = 0x7f817f81u;
__device__ __forceinline__ bool blank(const u32x4* pv) {
  u64 m;
#define CX(i) "v_cmpx_ne_u32 vcc, %1, %" #i "\n\t"
  asm volatile(CX(2) CX(3) CX(4) CX(5) CX(6) CX(7) CX(8) CX(9) CX(10) CX(11) CX(12) CX(13) CX(14) CX(15) CX(16) CX(17)
               "s_mov_b64 %0, exec\n\ts_mov_b64 exec, -1\n\ts_nop 3" : "=s"(m) : "s"(BLANK), "v"(pv[0][0]), "v"(pv[0][1]), "v"(pv[0][2]), "v"(pv[0][3]),
               "v"(pv[1][0]), "v"(pv[1][1]), "v"(pv[1][2]), "v"(pv[1][3]), "v"(pv[2][0]), "v"(pv[2][1]), "v"(pv[2][2]), "v"(pv[2][3]),
               "v"(pv[3][0]), "v"(pv[3][1]), "v"(pv[3][2]), "v"(pv[3][3]) : "vcc");
  return m != ~0ull;
}
#define RLX_AGENT __ATOMIC_RELAXED, __HIP_MEMORY_SCOPE_AGENT

constexpr int G2_A = 65536, G2_X = 131072;
constexpr int G2_NSTAGE = 4;
constexpr size_t MB_ = 1024 * 1024;
__global__ __launch_bounds__(512, 2) void fused_kernel(const u16* __restrict__ x, const u16* __restrict__ wqkv,
                                                       const u16* __restrict__ wg, const u16* __restrict__ wu,
                                                       const u16* __restrict__ wd, const u16* __restrict__ wo,
                                                       u16* __restrict__ out, char* __restrict__ ws) {
  u16* const halo = (u16*)(ws + 4 * MB_);
  u16* const abuf = (u16*)(ws + 16 * MB_);
  char* const slab = ws + 64 * MB_;
  unsigned* const flags = (unsigned*)(ws + 96 * MB_);
  u32x4* const rstat = (u32x4*)(ws + 97 * MB_);
  char* const wcopy = ws + 98 * MB_;
  u64* const done1 = (u64*)(ws + 102 * MB_);
  u64* const done2 = (u64*)(ws + 100 * MB_ + 65536);
  u64* const donec = (u64*)(ws + 100 * MB_ + 131072);
  u64* const acks = (u64*)(ws + 100 * MB_ + 196608);
  float* const csum = (float*)(ws + 101 * MB_);
  __shared__ __attribute__((aligned(1024))) char shm[163840];
  constexpr int AUX = 2 * G1_STAGE + 8192;
  {
    u64 pc; int len;
    asm volatile("s_getpc_b64 %0\n.Lcode_beg:\n\ts_mov_b32 %1, .Lcode_end-.Lcode_beg" : "=s"(pc), "=s"(len));
    const int ol = ((blockIdx.x >> 3) + 32 * (threadIdx.x >> 6)) * 1024 + (threadIdx.x & 63) * 16;
    asm volatile("s_mov_b32 m0, %2\n\ts_nop 0\n\tglobal_load_lds_dwordx4 %0, %1"
                 :: "v"(ol < len - 16 ? ol : 0), "s"(pc & ~15ull), "s"((unsigned)(uintptr_t)(lds_cptr)shm + 2 * G1_STAGE + 7168) : "m0");
    __builtin_amdgcn_sched_barrier(0);
  }
  const u64 t_own = __builtin_amdgcn_s_memrealtime();
  TSDECL;
  TS1(7);
  const int tid = threadIdx.x, lane = tid & 63;
  const int wid = __builtin_amdgcn_readfirstlane(tid >> 6);
  const int wm = wid & 3, wc = wid >> 2;
  const int bid = blockIdx.x, xcd = bid & 7, idx = bid >> 3;
  const int ntile = xcd * 4 + (idx & 3), mtile = idx >> 2;
  const int m0 = mtile * 256;
  const int hg = ntile >> 1;
  const bool tya = (ntile & 1) == 0;
  const int srow = m0 + ntile * 8 + wm * 2;
  lds_cptr shm3 = (lds_cptr)shm;
  const unsigned shm_base = (unsigned)(uintptr_t)shm3;
  const int r8 = tid >> 3, pos = tid & 7;
  const int phys = pos >> 1, half = pos & 1;
  const int fk = ((r8 >> 1) & 1) | (((r8 >> 3) & 1) << 1);
  const int vsrc = phys ^ fk;
  const int qstr = tya ? 3072 * 2 : F_ * 2;
  unsigned voa = (((r8 & 32) + 2 * (r8 & 15) + ((r8 >> 4) & 1)) * D_ + ((pos ^ (r8 & 7)) * 8)) * 2;
  unsigned vog = r8 * (F_ * 2) + vsrc * 32 + half * 16, voq = r8 * qstr + vsrc * 32 + half * 16;
  unsigned vom = r8 * qstr + (vsrc >> (tya ? 0 : 1)) * 32 + half * 16;
  asm volatile("" : "+v"(voa), "+v"(vog), "+v"(voq), "+v"(vom));
  __builtin_amdgcn_sched_barrier(0);
  asm volatile("" :: "s"(x), "s"(wqkv), "s"(wg), "s"(wu), "s"(wd), "s"(wo), "s"(out), "s"(ws));
  TS1(1);
  const char* const ab = (const char*)(x + (size_t)m0 * D_);
  int kz = 0;
  asm volatile("" : "+s"(kz));
#pragma unroll
  for (int i = 0; i < 4; ++i) glds16_s(ab + i * 64 * D_ * 2, voa, shm_base + wid * 1024 + i * 8192);
  const int kt0 = (hg >> 1) * 7, he = hg & 1;
  const char* const gb = (const char*)(wg + (kt0 + (tya ? 5 + he : 2 * he + 1)) * 64);
  const long dgu = (const char*)wu - (const char*)wg, dp1 = tya ? 2048 : dgu;
  glds16_s(gb, vog, shm_base + wid * 1024 + 32768 + 3 * 8192);
  glds16_s(gb + dgu, vog, shm_base + wid * 1024 + 32768 + 4 * 8192);
  u64 qa = (u64)(wqkv + 64 * hg), ma = (u64)(wg + (kt0 + 4) * 64 + 32 * he);
  PIN_S(qa); PIN_S(ma);
  const char* const qb = tya ? (const char*)qa : gb - 128;
  glds16_s(qb, voq, shm_base + wid * 1024 + 32768);
  glds16_s(qb + dp1, voq, shm_base + wid * 1024 + 32768 + 8192);
  const char* const mb0 = (const char*)(tya ? qa + 4096 : ma);
  const char* mptr = mb0 + (((vsrc & 1) && !tya) ? dgu : 0) + vom;
  GLDS_NC(mptr, shm_base + wid * 1024 + 32768 + 2 * 8192);
  __builtin_amdgcn_sched_barrier(0);
  TS1(4);
  f32x4 acc[4][10];

  constexpr int NT = D_ / 64;
  constexpr int CNT = AUX + 4096 + 3584;
  if (tid < 8) *(unsigned*)(shm + CNT + tid * 4) = 0u;
  bf16x8 af0[4], af1[4];
  bf16x4 bl[4], bh[4];
  auto clear = [&]() __attribute__((always_inline)) {
    f32x4 z = {0.f, 0.f, 0.f, 0.f};
    asm volatile("" : "+v"(z));
#pragma unroll
    for (int a = 0; a < 4; ++a) {
#pragma unroll
      for (int b = 0; b < 10; ++b) { acc[a][b] = z; asm volatile("" : "+v"(acc[a][b])); }
      af0[a] = __builtin_bit_cast(bf16x8, z); af1[a] = af0[a];
      asm volatile("" : "+v"(af0[a]));
      asm volatile("" : "+v"(af1[a]));
      bl[a] = bf16x4{0, 0, 0, 0}; bh[a] = bf16x4{0, 0, 0, 0};
    }
  };
#define SB0() __builtin_amdgcn_sched_barrier(0)
  auto mm = [&](int g, int t, bool two) {
    const int mi = t >> 1, mf = ((g & 1) && g != 9) ? 3 - mi : mi, ks = (t & 1) ^ (mi & 1), s = ((g + 1) & 1) * 2;
    const bf16x8 b0 = __builtin_shufflevector(bl[s + ks], bh[s + ks], 0, 1, 2, 3, 4, 5, 6, 7), a0 = ks ? af1[mf] : af0[mf];
    const bf16x8 b1 = __builtin_shufflevector(bl[s + 1 - ks], bh[s + 1 - ks], 0, 1, 2, 3, 4, 5, 6, 7), a1 = ks ? af0[mf] : af1[mf];
    const bool tr = (g >> 1) == 2;
    if (two) asm volatile("v_mfma_f32_16x16x32_bf16 %0, %1, %2, %0\n\tv_mfma_f32_16x16x32_bf16 %0, %3, %4, %0" : "+v"(acc[mf][g])
                          : "v"(tr ? a0 : b0), "v"(tr ? b0 : a0), "v"(tr ? a1 : b1), "v"(tr ? b1 : a1));
    else asm volatile("v_mfma_f32_16x16x32_bf16 %0, %1, %2, %0" : "+v"(acc[mf][g]) : "v"(tr ? a0 : b0), "v"(tr ? b0 : a0));
  };
  typedef __attribute__((address_space(3))) unsigned lds_u32;
  lds_u32* const cnt = (lds_u32*)(shm3 + CNT);
  bool cok;
  constexpr int NY = 28;
  int role, mt2, nt2;
  struct WS { unsigned vb[2][2]; const char* wb[2]; int sh[2]; };
  auto wsetup = [&](const u16* w, int crow0, int l, bool ck) __attribute__((always_inline)) {
    WS r;
    const unsigned vr = ((l >> 5) & 1) * 16 + (wm & 1) * 8 + ((l >> 3) & 3), vc = (((l >> 1) & 3) ^ ((l >> 4) & 1)) * 32 + (l & 1) * 16;
    u64 cp = (u64)(wcopy + (nt2 >> 2) * 589824 + crow0 * 128);
    asm volatile("" : "+s"(cp));
#pragma unroll
    for (int i = 0; i < 2; ++i) {
      u64 dp = (u64)(w + nt2 * 128 + i * 64);
      asm volatile("" : "+s"(dp));
      const bool slow = ck & ((((unsigned)dp >> 7) & 7) == 3);
      r.sh[i] = slow ? 7 : 11;
      r.wb[i] = (const char*)(slow ? cp : dp) + (crow0 ? (wm >> 1) * 32 << r.sh[i] : 0);
      r.vb[i][0] = (vr << r.sh[i]) + vc; r.vb[i][1] = ((vr + 4) << r.sh[i]) + (vc ^ 64);
    }
    asm volatile("" : "+v"(r.vb[0][0]), "+v"(r.vb[0][1]), "+v"(r.vb[1][0]), "+v"(r.vb[1][1]));
    PIN_S(r.wb[0]); PIN_S(r.wb[1]); PIN_S(r.sh[0]); PIN_S(r.sh[1]);
    return r;
  };
  WS wy, wz;
  int vxq;
  u32x4 xq[4];
  unsigned vg;
  int st = 0;
  const char *sa, *sw[2];
  auto wsel = [&]() {
    const unsigned r = __builtin_amdgcn_readlane(vg, st);
    sw[0] = wy.wb[0] + (r << wy.sh[0]); sw[1] = wy.wb[1] + (r << wy.sh[1]);
  };
  const int p = wid & 3;
  unsigned sl = 16384;
#define G2_W "s_add_u32 m0, %[l], %[ow]\n\ts_nop 0\n\tglobal_load_lds_dwordx4 %[v0], %[w0] sc1\n\t" \
             "s_add_u32 m0, m0, 1024\n\ts_nop 0\n\tglobal_load_lds_dwordx4 %[v1], %[w0] sc1\n\t" \
             "s_add_u32 m0, m0, 7168\n\ts_nop 0\n\tglobal_load_lds_dwordx4 %[v2], %[w1] sc1\n\t" \
             "s_add_u32 m0, m0, 1024\n\ts_nop 0\n\tglobal_load_lds_dwordx4 %[v3], %[w1] sc1"
#define G2_WOP(w) [ow] "s"(shm_base + p * 2048), [w0] "s"(sw[0]), [w1] "s"(sw[1]), [v0] "v"(w.vb[0][0]), [v1] "v"(w.vb[0][1]), [v2] "v"(w.vb[1][0]), [v3] "v"(w.vb[1][1])
  auto tile2 = [&](int t0) __attribute__((always_inline)) {
    int b = bid;
    asm volatile("" : "+s"(b));
    role = 1 - ((b >> 2) & 1);
    mt2 = ((role ? b : b ^ 5) & 7) * 4 + ((b >> 3) & 3), nt2 = b >> 5;
    sa = (const char*)abuf + (size_t)(mt2 * 72 + role * NY + t0) * 16384 + wm * 4096;
    asm volatile("" : "+s"(sa));
  };
  auto prep = [&](bool first) __attribute__((always_inline)) {
    int l = threadIdx.x;
    asm volatile("" : "+v"(l));
    l &= 63;
    if (first) {
      tile2(3);
      vxq = ((mt2 * 128 + (wm >> 1) * 64 + role * 32 + 2 * (l & 15)) * D_ + nt2 * 128 + (wm & 1) * 64) * 2 + ((l >> 4) & 1) * 32 + (l >> 5) * 16;
      static_assert(DLT == 1, "");
      const int t = min(l, NY - 1), w = wm >> 1;
      const int o = (0xffaa5500fae504ull >> (2 * t)) & 3;
      int u1 = (0x990990 >> ((2 * t) & 31)) & 3, u2 = 3 + (t & 3);
      asm volatile("" : "+v"(u1), "+v"(u2));
      const int u = t < 12 ? u1 : u2;
      vg = (__umul24((l < NY ? role : 1 - role) * 4 + (((o & 1) << 1) | (o >> 1)), 14) + ((0xb85140a >> (4 * u)) & 15) + (u == 5 ? w : 2 * w)) * 32;
      asm volatile("" : "+v"(vg), "+v"(vxq));
    }
    wy = wsetup(wd, 0, l, first);
    wz = wsetup(wo, F_, l, first);
    SB0();
  };
  auto start2 = [&]() __attribute__((always_inline)) {
    const __amdgpu_buffer_rsrc_t rx = __builtin_amdgcn_make_buffer_rsrc((void*)x, 0, S_ * D_ * 2, 0x00020000);
#pragma unroll
    for (int f = 0; f < 4; ++f) xq[f] = __builtin_bit_cast(u32x4, __builtin_amdgcn_raw_buffer_load_b128(rx, vxq + (f >> 1) * 2048 + (f & 1) * 64, 0, 0));
    cok = __builtin_amdgcn_readfirstlane(cnt[1] & cnt[2]) != 0u;
    if (__builtin_expect(!cok, 0)) prep(false);
    wsel();
#pragma unroll
    for (int i = 0; i < 3; ++i) {
      asm volatile(G2_W :: [l] "s"(sl), G2_WOP(wy) : "memory", "scc", "m0");
      ++st;
      sl = (sl + 16384) & 65535;
      wsel();
    }
    PIN_S(sw[0]); PIN_S(sw[1]);
    SB0();
  };
  auto kloop = [&](auto wmc) {
    constexpr bool LD = decltype(wmc)::value != 0;
    const int WID = wid;
    constexpr int DMP = 2 * G1_STAGE;
    const char* pa = nullptr;
    unsigned vob = 0;
    f32x4 ca = {0.f, 0.f, 0.f, 0.f}, cb = {0.f, 0.f, 0.f, 0.f}, cd = ca;
    bf16x8 sel = {0, 0, 0, 0, 0, 0, 0, 0};
    int tro = 0;
    const int psh = (wid == 2 && !tya) ? 1 : 0, pstr = wid < 3 ? qstr : F_ * 2;
    const char* const pbase = wid == 0 ? qb : (wid == 1 ? qb + dp1 : (wid == 2 ? mb0 : gb));
    int lane = threadIdx.x;
    asm volatile("; %1" : "+v"(lane) : "n"(LD ? 1 : 0));
    lane &= 63;
    const int fr = lane & 15, fq = lane >> 4, q4 = fr >> 2, pp = lane & 3;
    const int ff = ((q4 >> 1) & 1) | ((fq & 1) << 1);
    int aoff[2], boff[2];
#pragma unroll
    for (int ks = 0; ks < 2; ++ks) aoff[ks] = (wm * 64 + fr) * 128 + (((ks * 4 + fq) ^ (fr & 7)) * 16);
#pragma unroll
    for (int j = 0; j < 2; ++j) boff[j] = 32768 + (8 * fq + q4) * 128 + (((2 * wc + j) ^ ff) * 32) + pp * 8;
    if (!LD) {
      const int rs = mtile * 8 + (lane >> 3), vs = ((lane & 7) >> 1) ^ (((rs >> 1) & 1) | ((mtile & 1) << 1));
      pa = pbase + ((psh & vs) ? dgu : 0) + (rs * pstr + (vs >> psh) * 32 + (lane & 1) * 16);
      vob = rs * (F_ * 2) + vs * 32 + (lane & 1) * 16;
      tro = q4 * 128 + ((fq ^ ((q4 >> 1) | ((mtile & 1) << 1))) * 32) + pp * 8;
      asm volatile("" : "+v"(tro));
    }
    auto share = [&](int t, unsigned dsta, unsigned dstb) {
      long off = (long)(t * 64 * pstr);
      asm volatile("" : "+s"(off));
      GLDS_NC(pa + off, shm_base + dsta);
      if (wid == 0) glds16_s(gb + dgu + (long)(t * 64 * F_ * 2), vob, shm_base + dstb);
    };
    auto accum = [&](int oa, int ob) {
      ca = __builtin_amdgcn_mfma_f32_16x16x32_bf16(tr_frag(shm3 + tro + oa), sel, ca, 0, 0, 0);
      if (wid == 0) cb = __builtin_amdgcn_mfma_f32_16x16x32_bf16(tr_frag(shm3 + tro + ob), sel, cb, 0, 0, 0);
    };
    const int q32 = 32 * qstr;
    const char *sq0 = qb + q32, *sq1 = sq0 + dp1, *sg0 = gb + 32 * F_ * 2, *sg1 = sg0 + dgu, *sx = ab + (long)(kz + 128 - 32 * D_ * 2);
    u32x2 mp[2] = {__builtin_bit_cast(u32x2, mptr + q32), __builtin_bit_cast(u32x2, mptr + 2 * q32)};
    u64 cy[2];
    unsigned dst = shm_base + G1_STAGE + (wid - 4) * 1024;
    int dbuf = G1_STAGE;
    auto stage = [&](int i0, int i1) __attribute__((always_inline)) {
#pragma unroll
        for (int i = i0; i < i1; ++i) {
          const int q = i >> 1, h = i & 1;
          const unsigned ld = dst + h * 4096 + (q < 5 ? 32768 + q * 8192 : (q - 5) * 8192);
          if (q < 2) glds16_s((q ? sq1 : sq0) + h * q32, voq, ld);
          else if (q == 2) asm volatile("s_mov_b32 m0, %1\n\ts_nop 0\n\tglobal_load_lds_dwordx4 %0, off" :: "v"(mp[h]), "s"(ld) : "m0");
          else if (q < 5) glds16_s((q == 4 ? sg1 : sg0) + h * 32 * F_ * 2, vog, ld);
          else glds16_s(sx + ((q - 5) * 64 + h * 32) * D_ * 2, voa, ld);
        }
    };
    unsigned ktv = 0;
    if (!LD) {
      clear();
      asm volatile("global_load_dwordx4 %0, %4, %5\n\tglobal_load_dwordx4 %1, %4, %5 offset:512\n\t"
                   "global_load_dwordx4 %2, %4, %5 offset:1024\n\tglobal_load_dwordx4 %3, %4, %5 offset:1536"
                   : "+v"(ca), "+v"(cb), "+v"(sel), "+v"(cd) : "v"((lane >> 5) * 2048 + (lane & 31) * 16), "s"(x + (size_t)srow * D_) : "memory");
      WAIT_V(4);
      TS1(6);
    }
    asm volatile("" : "+v"(aoff[0]), "+v"(aoff[1]), "+v"(boff[0]), "+v"(boff[1]));
    auto body = [&](bool upd) __attribute__((always_inline)) {
      lds_cptr sb = shm3;
      auto rdb = [&](int g, int i) {
        lds_cptr p = sb + boff[g & 1] + (g >> 1) * 8192 + (i >> 1) * 4096;
        const int s = ((g + 1) & 1) * 2 + (i >> 1);
        if (i & 1) bh[s] = tr_read(p + 512); else bl[s] = tr_read(p);
      };
      auto rda = [&](int ks, int mf) { (ks ? af1 : af0)[mf] = *(const __attribute__((address_space(3))) bf16x8*)(sb + aoff[ks] + mf * 2048); };
      SB0();
      __builtin_amdgcn_s_waitcnt(0xC07F);
#pragma unroll
      for (int gg = 0; gg < 10; ++gg) {
        const int g = (gg + 9) % 10;
#pragma unroll
        for (int t = 0; t < 8; ++t) {
          const bool two = t >= 4 && !(upd && g >= 5 && g <= 8);
          SB0();
          if (!two || (t & 1)) mm(g, two ? t - 1 : t, two);
          SB0();
          if (g == 9) {
            if (t == 0 || t == 2) { rdb(0, t); rdb(0, t + 1); }
            if (t & 1) { rda((t >> 1) & 1, t >> 1); rda(((t >> 1) & 1) ^ 1, t >> 1); }
          } else if (t < 4) rdb(g + 1, t);
          else if (upd) {
            if (g == 5 && t == 4) { sq0 += 2 * q32; PIN_S(sq0); }
            if (g == 5 && t == 5) { sq1 += 2 * q32; PIN_S(sq1); }
            if (g == 5 && t == 6) { sg0 += 64 * F_ * 2; PIN_S(sg0); }
            if (g == 6 && t == 4) { sg1 += 64 * F_ * 2; PIN_S(sg1); }
            if (g == 6 && t == 5) { sx += 128; PIN_S(sx); }
            if (g == 6 && t == 6) { dst -= dbuf; PIN_S(dst); }
            if (g == 7 && t == 4) asm volatile("v_add_co_u32 %0, %2, %4, %0\n\tv_add_co_u32 %1, %3, %4, %1" : "+v"(mp[0][0]), "+v"(mp[1][0]), "=&s"(cy[0]), "=&s"(cy[1]) : "s"(2 * q32));
            if (g == 7 && t == 6) asm volatile("v_addc_co_u32 %0, vcc, 0, %0, %2\n\tv_addc_co_u32 %1, vcc, 0, %1, %3" : "+v"(mp[0][1]), "+v"(mp[1][1]) : "s"(cy[0]), "s"(cy[1]) : "vcc");
            if (g == 8 && t == 4) { aoff[0] += dbuf; aoff[1] += dbuf; asm volatile("" : "+v"(aoff[0]), "+v"(aoff[1])); }
            if (g == 8 && t == 5) { boff[0] += dbuf; boff[1] += dbuf; asm volatile("" : "+v"(boff[0]), "+v"(boff[1])); }
            if (g == 8 && t == 6) { dbuf = -dbuf; PIN_S(dbuf); }
          }
        }
      }
      SB0();
    };
    TS1(0);
#define PRE1 0
    if (LD) {
      stage(0, PRE1);
      if (PRE1 == 0) clear();
      WAIT_V(PRE1);
      TS1(5);
      BARL();
      stage(PRE1, 18);
      if (PRE1) clear();
    }
#pragma clang loop unroll(disable)
    for (int kt = 0;; ++kt) {
      const int cur = kt & 1;
      if (!LD) BARL();
      body(LD);
      if (!LD && kt == NT - 1) break;
      if (LD) {
        WAIT_V(0);
        BARL();
        if (__builtin_expect(kt == 4 + wid, 0)) prep(true);
        asm volatile("" ::: "memory");
        if (kt == NT - 2) break;
        stage(0, 18);
      } else {
        const int d = cur ? -G1_STAGE : G1_STAGE;
        aoff[0] += d; aoff[1] += d; boff[0] += d; boff[1] += d;
        asm volatile("" : "+v"(aoff[0]), "+v"(aoff[1]), "+v"(boff[0]), "+v"(boff[1]));
      }
      if (!LD) {
        int lane = threadIdx.x;
        asm volatile("" : "+v"(lane), "+v"(tro));
        lane &= 63;
        const unsigned vol = lane * 16;
        unsigned bw = BLANK;
        PIN_S(bw);
        const u32x4 bk = {bw, bw, bw, bw};
        if (kt == 0) {
          WAIT_V(0);
          float sm = 0.f, sq = 0.f;
          const u32x4 w[4] = {__builtin_bit_cast(u32x4, ca), __builtin_bit_cast(u32x4, cb), __builtin_bit_cast(u32x4, sel), __builtin_bit_cast(u32x4, cd)};
#pragma unroll
          for (int i = 0; i < 4; ++i)
            asm volatile("v_dot2_f32_bf16 %0, %2, %6, %0\n\tv_dot2_f32_bf16 %1, %2, %2, %1\n\tv_dot2_f32_bf16 %0, %3, %6, %0\n\tv_dot2_f32_bf16 %1, %3, %3, %1\n\t"
                         "v_dot2_f32_bf16 %0, %4, %6, %0\n\tv_dot2_f32_bf16 %1, %4, %4, %1\n\tv_dot2_f32_bf16 %0, %5, %6, %0\n\tv_dot2_f32_bf16 %1, %5, %5, %1\n\ts_nop 2"
                         : "+v"(sm), "+v"(sq) : "v"(w[i][0]), "v"(w[i][1]), "v"(w[i][2]), "v"(w[i][3]), "s"(0x3f803f80u));
          const float ml = sm * (1.0f / 32), mean = half_sum(sm) * (1.0f / D_), dm = ml - mean;
          cd[0] = mean; cd[1] = __builtin_fmaf(dm * 32.0f, dm, __builtin_fmaf(-ml, sm, sq));
          ca = f32x4{0.f, 0.f, 0.f, 0.f}; cb = ca;
          const short one = (lane & 15) == (lane >> 4) ? 0x3f80 : 0;
          sel = bf16x8{one, one, one, one, one, one, one, one};
        }
        if (kt >= 1 && kt <= 12) { WAIT_V(0); accum(DMP + wid * 1024, DMP + 4096); WAIT_L(0); }
        if (kt <= 1) share(kt + 2, DMP + 7168, DMP + 7168);
        if (kt <= 11) share(kt + 4, DMP + wid * 1024, DMP + 4096);
        if (kt <= 3) {
          const int sp = cur * G1_STAGE + 32768 + mtile * 1024;
          accum(sp + wid * 8192, sp + 4 * 8192);
        }
        if ((kt == 12 || kt == 2) && (lane & 15) < 4) {
          const __amdgpu_buffer_rsrc_t rc = __builtin_amdgcn_make_buffer_rsrc((void*)(csum + ntile * 5 * 512 + mtile * 64), 0, 5 * 2048, 0x00020000);
          const int co = (lane & 15) * 64 + (lane >> 4) * 16;
          __builtin_amdgcn_raw_buffer_store_b128(kt == 2 ? bk : __builtin_bit_cast(u32x4, ca), rc, co + wid * 2048, 0, 16);
          if (wid == 0) __builtin_amdgcn_raw_buffer_store_b128(kt == 2 ? bk : __builtin_bit_cast(u32x4, cb), rc, co + 4 * 2048, 0, 16);
        }
        if (kt >= CP_K0 && kt < CP_K0 + 3) {
          const int ln = lane;
          const int q1 = bid * 4 + WID + (kt - CP_K0) * 1024, q0 = q1 - 1024;
          if (q0 >= 0 && q0 < 1152) {
            const __amdgpu_buffer_rsrc_t rc = __builtin_amdgcn_make_buffer_rsrc((void*)(wcopy + (q0 & 1) * 589824 + (q0 >> 1) * 1024), 0, 1024, 0x00020000);
            __builtin_amdgcn_raw_buffer_store_b128(*(const u32x4*)(shm + AUX + WID * 1024 + ln * 16), rc, ln * 16, 0, 16);
            WAIT_L(0);
          }
          if (q1 < 1152) {
            const char* src = (const char*)((q1 >> 1) < 448 ? wd + (size_t)(q1 >> 1) * 8 * D_ : wo + (size_t)((q1 >> 1) - 448) * 8 * D_);
            src += ((3 - ((unsigned)(uintptr_t)src >> 7)) & 7) * 128 + (q1 & 1) * 1024;
            GLDS(src + (ln >> 3) * 2048 + (ln & 7) * 16, shm_base + AUX + WID * 1024);
          }
        }
        if (WID == 0 && kt == CP_K0 + 4) {
          u64 now;
          asm volatile("s_memrealtime %0\n\ts_waitcnt lgkmcnt(0)" : "=s"(now) :: "memory");
          if (lane == 0) __hip_atomic_store(&donec[bid], now, RLX_AGENT);
        }
        if (kt >= PF_K0 && kt < PF_K0 + 5) {
#pragma unroll
          for (int i = 0; i < 2; ++i) {
            const int c = (bid * 4 + wid) * 10 + (kt - PF_K0) * 2 + i;
            if (c < 9216) {
              const u16* src = (c < 7168 ? wd + (size_t)c * 512 : wo + (size_t)(c - 7168) * 512) + lane * 8;
              GLDS_NC(src, shm_base + DMP + 7168);
            }
          }
        }
#define BLK 3
        if (kt == BLK) {
          const __amdgpu_buffer_rsrc_t rs = __builtin_amdgcn_make_buffer_rsrc((void*)(slab + (size_t)bid * 16384 + wid * 4096), 0, 4096, 0x00020000);
#pragma unroll
          for (int i = 0; i < 4; ++i) __builtin_amdgcn_raw_buffer_store_b128(bk, rs, vol + i * 1024, 0, 16);
          const __amdgpu_buffer_rsrc_t rh = __builtin_amdgcn_make_buffer_rsrc((void*)((char*)halo + (mtile * 32 + ntile) * 4096 + wid * 2048), 0, tya ? 2048 : 0, 0x00020000);
#pragma unroll
          for (int i = 0; i < 2; ++i) __builtin_amdgcn_raw_buffer_store_b128(bk, rh, vol + i * 1024, 0, 16);
        }
        if (kt == PUB_K) {
          const unsigned ps = pack_stats(cd[0], fmaxf(half_sum(cd[1]) * (1.0f / D_), 0.f));
          u64 now;
          asm volatile("s_memrealtime %0\n\ts_waitcnt lgkmcnt(0)" : "=s"(now) :: "memory");
          const __amdgpu_buffer_rsrc_t rs = __builtin_amdgcn_make_buffer_rsrc((void*)(rstat + srow), 0, 32, 0x00020000);
          if ((lane & 31) == 0) __builtin_amdgcn_raw_buffer_store_b128(u32x4{ps, (unsigned)now, ps, (unsigned)(now >> 32)}, rs, lane >> 1, 0, 16);
        }
        if (WID == 2 && kt == 8) GLDS_SC1((const char*)(done1 + (bid & ~1) * 64) + lane * 16, shm_base + DMP + 7168);
        if (WID >= 2 && kt == 11) GLDS_SC1((const char*)donec + (WID - 2) * 1024 + lane * 16, shm_base + DMP + 3072 + WID * 1024);
        if (kt == 12) {
          GLDS_SC1(rstat + m0 + wid * 64 + (lane & 32) + 2 * (lane & 15) + ((lane >> 4) & 1), shm_base + AUX + wid * 1024);
          if (WID >= 2) {
            u32x4 c4 = *(const u32x4*)(shm + DMP + 3072 + WID * 1024 + vol);
            bool ok = __all(fresh(((u64)c4[1] << 32) | c4[0], t_own) & fresh(((u64)c4[3] << 32) | c4[2], t_own));
            while (__builtin_expect(!ok, 0) && __builtin_amdgcn_s_memrealtime() - t_own < TIMEOUT_ / 10) {
              const __amdgpu_buffer_rsrc_t rs = __builtin_amdgcn_make_buffer_rsrc((void*)donec, 0, 2048, 0x00020000);
              asm volatile("" ::: "memory");
              c4 = __builtin_bit_cast(u32x4, __builtin_amdgcn_raw_buffer_load_b128(rs, (WID - 2) * 1024 + vol, 0, 16));
              ok = __all(fresh(((u64)c4[1] << 32) | c4[0], t_own) & fresh(((u64)c4[3] << 32) | c4[2], t_own));
            }
            *(unsigned*)(shm + CNT - 4 + WID * 4) = ok ? 1u : 0u;
          }
        }
        const unsigned vcs = (lane < 32 ? wid * 2048 : 8192) + ((lane >> 4) & 1) * 1024 + (lane & 15) * 16;
        auto cgather = [&]() {
          asm volatile("global_load_dwordx4 %0, %4, %5 sc1\n\tglobal_load_dwordx4 %1, %4, %5 offset:256 sc1\n\t"
                       "global_load_dwordx4 %2, %4, %5 offset:512 sc1\n\tglobal_load_dwordx4 %3, %4, %5 offset:768 sc1"
                       : "+v"(ca), "+v"(cb), "+v"(sel), "+v"(cd) : "v"(vcs), "s"(csum + ntile * 5 * 512) : "memory");
        };
        if (kt == 13) {
          WAIT_V(0);
          cgather();
          int ln = threadIdx.x;
          asm volatile("" : "+v"(ln));
          u32x4 gr = *(const u32x4*)(shm + AUX + ln * 16);
          if (__builtin_expect(!__all((gr[0] == gr[2]) & fresh(((u64)gr[3] << 32) | gr[1], t_own)), 0)) {
            const __amdgpu_buffer_rsrc_t rs = __builtin_amdgcn_make_buffer_rsrc((void*)(rstat + m0), 0, 4096, 0x00020000);
            const int prow = (ln & 0xe0) + 2 * (ln & 15) + ((ln >> 4) & 1);
            for (;;) {
              gr = __builtin_bit_cast(u32x4, __builtin_amdgcn_raw_buffer_load_b128(rs, prow * 16, 0, 16));
              if (__all((gr[0] == gr[2]) & fresh(((u64)gr[3] << 32) | gr[1], t_own))) break;
              if (__builtin_amdgcn_s_memrealtime() - t_own > TIMEOUT_) { gr[0] = row_stats_serial(x + (size_t)(m0 + prow) * D_); break; }
              __builtin_amdgcn_s_sleep(2);
            }
          }
          const float nm = -lo_f(gr[0]), dn = hi_f(gr[0]), r = __builtin_amdgcn_rcpf(dn);
          const f32x4 fc = {nm, -2.3022081986f * r, -0.10294324f * r * r * r, dn * dn};
          *(f32x4*)(shm + AUX + ln * 16) = fc;
          float* const tw = (float*)(shm + 2 * G1_STAGE + 4096 + WID * 768) + ln;
#pragma unroll
          for (int i = 0; i < 4; ++i) tw[i * 64] = fc[i];
          ((float*)(shm + AUX + 4096))[384 + ln] = r;
        }
        if (kt == 14) {
          WAIT_V(0);
          f32x4 t;
          auto csum4 = [&](const u32x4* w) {
#pragma unroll
            for (int i = 0; i < 4; ++i) {
              float a, b;
              asm volatile("v_add_f32 %0, %2, %3\n\tv_add_f32 %1, %4, %5\n\tv_add_f32 %0, %0, %1" : "=&v"(a), "=&v"(b) : "v"(w[0][i]), "v"(w[1][i]), "v"(w[2][i]), "v"(w[3][i]));
              const auto r2 = __builtin_amdgcn_permlane16_swap(__float_as_uint(a), __float_as_uint(a), false, false);
              asm("v_add_f32 %0, %1, %2" : "=v"(t[i]) : "v"(r2[0]), "v"(r2[1]));
            }
            return blank(w);
          };
          const u32x4 w0[4] = {__builtin_bit_cast(u32x4, ca), __builtin_bit_cast(u32x4, cb), __builtin_bit_cast(u32x4, sel), __builtin_bit_cast(u32x4, cd)};
          const bool bl0 = csum4(w0);
          const bool ck = __builtin_amdgcn_readfirstlane(cnt[1] & cnt[2]) != 0u;
          if (__builtin_expect(bl0 && ck, 0)) {
            const __amdgpu_buffer_rsrc_t rc = __builtin_amdgcn_make_buffer_rsrc((void*)(csum + ntile * 5 * 512), 0, 5 * 2048, 0x00020000);
            u32x4 w[4];
            do {
              __builtin_amdgcn_s_sleep(2);
              asm volatile("" ::: "memory");
#pragma unroll
              for (int i = 0; i < 4; ++i) w[i] = __builtin_bit_cast(u32x4, __builtin_amdgcn_raw_buffer_load_b128(rc, vcs + i * 256, 0, 16));
            } while (csum4(w));
          }
          if (__builtin_expect(!ck, 0)) {
            const int gq = (lane >> 2) & 3;
            const char* pc = lane < 32 ? pbase + ((psh & gq) ? dgu : 0) + (gq >> psh) * 32 + (lane & 3) * 8 : gb + dgu + (lane & 15) * 8;
            t = f32x4{0.f, 0.f, 0.f, 0.f};
#pragma unroll 1
            for (int k = 0; k < D_; ++k) { const u32x2 w = *(const u32x2*)(pc + (size_t)k * (lane < 32 ? pstr : F_ * 2)); t += f32x4{lo_f(w[0]), hi_f(w[0]), lo_f(w[1]), hi_f(w[1])}; }
          }
          if (lane < 32 || wid == 0)
            *(f32x4*)((float*)(shm + AUX + 4096) + ((lane >> 3) & 1) * 192 + (lane < 32 ? wid : 4) * 32 + (lane & 7) * 4) = t;
          __builtin_amdgcn_s_setprio(2);
        }
      }
      SB0();
    }
    if (LD) {
      start2();
      body(false);
    }
#pragma unroll
    for (int t = 0; t < 8; t += 2) mm(9, t, true);
    asm volatile("s_nop 7\n\ts_nop 3");
    SB0();
    TS1(3);
    if (!LD) {
      tile2(0);
      cok = __builtin_amdgcn_readfirstlane(cnt[1] & cnt[2]) != 0u;
    } else {
      __builtin_amdgcn_s_setprio(3);
      WAIT_V(0);
    }
  };
  if (wid < 4) kloop(std::integral_constant<int, 0>{});
  else kloop(std::integral_constant<int, 1>{});
  __builtin_amdgcn_sched_barrier(0);
  int tz = threadIdx.x;
  asm volatile("" : "+v"(tz));
  __builtin_amdgcn_sched_barrier(0);
  const int li = tz & 15, lq = (tz >> 4) & 3, ln = tz & 63;
  const __amdgpu_buffer_rsrc_t ra = __builtin_amdgcn_make_buffer_rsrc((void*)abuf, 0, 16 * 72 * 16384, 0x00020000);
  float* const cfin = (float*)(shm + AUX + 4096);
  float* const svr = cfin + 384;
  float* const tb = (float*)(shm + 2 * G1_STAGE + 4096 + wm * 1024);
  auto signal = [&](unsigned v) {
    asm volatile("" ::: "memory");
    if (ln == 0) __hip_atomic_fetch_add(cnt, v, __ATOMIC_RELAXED, __HIP_MEMORY_SCOPE_WORKGROUP);
    asm volatile("" ::: "memory");
  };
  auto await = [&](unsigned mask, unsigned val) {
    while ((__builtin_amdgcn_readfirstlane(__hip_atomic_load(cnt, __ATOMIC_RELAXED, __HIP_MEMORY_SCOPE_WORKGROUP)) & mask) != val) __builtin_amdgcn_s_sleep(1);
    asm volatile("" ::: "memory");
  };
  auto client = [&](int l) { const int m128 = mtile * 2 + ((l >> 4) & 1); return (((l & 7) * 4 + (m128 & 3)) * 8) + ((m128 >> 2) ^ ((l & 8) ? 5 : 0)); };
  auto notify = [&](int l0, int nl, int early = 0) {
    u64 now;
    asm volatile("s_memrealtime %0\n\ts_waitcnt lgkmcnt(0)" : "=s"(now) :: "memory");
    int l = threadIdx.x;
    asm volatile("" : "+v"(l));
    l &= 63;
    const __amdgpu_buffer_rsrc_t rd1 = __builtin_amdgcn_make_buffer_rsrc((void*)(done1 + early + ntile), 0, 256 * 512, 0x00020000);
    if (l < nl) __builtin_amdgcn_raw_buffer_store_b64(u32x2{(unsigned)now, (unsigned)(now >> 32)}, rd1, client(l0 + l) * 512, 0, 16);
  };
  const int xo = ((xcd & 1) << 1) | ((xcd >> 1) & 1), te = (xcd >> 2) * 28 + 6 * (xo >> 1) + (xo & 1), tl = (xcd >> 2) * 28 + 12 + 4 * xo;
  auto early = [&]() {
    unsigned old = 0;
    if (ln == 0) old = __hip_atomic_fetch_add(cnt + 4 + (wm >> 1), 1u, __ATOMIC_RELAXED, __HIP_MEMORY_SCOPE_WORKGROUP);
    if (__builtin_amdgcn_readfirstlane(old) == 1u) notify((wm >> 1) * 16 + 8 - (xcd & 4) * 2, 8, 32);
  };
  signal(1u << (8 * wc));
  if (wc) {
    await(0xffffu, 0x404u);
#pragma unroll
    for (int f = 0; f < 4; ++f) *(u32x4*)(shm + G2_X + wm * 4096 + f * 1024 + ln * 16) = xq[f];
    SB0();
    __builtin_amdgcn_s_setprio(1);
  }
  f32x4 rf[4];
#pragma unroll
  for (int mf = 0; mf < 4; ++mf) rf[mf] = *(const f32x4*)(shm + AUX + (wm * 64 + mf * 16 + li) * 16);
  const float* const cl = cfin + wc * 192 + lq * 4;
  const int sbase = ((mtile * 2 + (wm >> 1)) * 72 * 8 + (wm & 1) * 4) * 2048;
  auto putf = [&](int kg, int mf, u32x4 v) {
    __builtin_amdgcn_raw_buffer_store_b128(v, ra, ln * 16 + sbase + ((kg >> 1) * 8 + mf) * 2048 + (kg & 1) * 1024, 0, 16);
  };
  auto gelp = [&](f32x2 g, f32x2 u, f32x2 cg, f32x2 cu, f32x2 m2, f32x2 k12, f32x2 k22, f32x2 d2) {
    const f32x2 tg = __builtin_elementwise_fma(m2, cg, g), tu = __builtin_elementwise_fma(m2, cu, u);
    const f32x2 arg = tg * __builtin_elementwise_fma(tg * tg, k22, k12);
    const f32x2 dn = __builtin_elementwise_fma(f32x2{__builtin_amdgcn_exp2f(arg[0]), __builtin_amdgcn_exp2f(arg[1])}, d2, d2);
    const f32x2 r = (tg * tu) * f32x2{__builtin_amdgcn_rcpf(dn[0]), __builtin_amdgcn_rcpf(dn[1])};
    return pack2(r[0], r[1]);
  };
  auto gel2 = [&](int p, int kg, int mf0 = 0, int mf1 = 4) {
    f32x4 cg[2], cu[2];
#pragma unroll
    for (int j = 0; j < 2; ++j) { cg[j] = *(const f32x4*)(cl + p * 32 + j * 16); cu[j] = *(const f32x4*)(cl + p * 32 + 32 + j * 16); }
#pragma unroll
    for (int mf = mf0; mf < mf1; ++mf) {
      const f32x2 m2 = {rf[mf][0], rf[mf][0]}, k12 = {rf[mf][1], rf[mf][1]}, k22 = {rf[mf][2], rf[mf][2]}, d2 = {rf[mf][3], rf[mf][3]};
      u32x4 o;
#pragma unroll
      for (int i = 0; i < 4; ++i) {
        const int j = i >> 1, e = (i & 1) * 2;
        const f32x4 g = acc[mf][p * 2 + j], u = acc[mf][p * 2 + 2 + j];
        o[i] = gelp(f32x2{g[e], g[e + 1]}, f32x2{u[e], u[e + 1]}, f32x2{cg[j][e], cg[j][e + 1]}, f32x2{cu[j][e], cu[j][e + 1]}, m2, k12, k22, d2);
      }
      putf(kg, mf, o);
    }
  };
  int tsel = ntile & 1;
  asm volatile("" : "+s"(tsel));
  if (tsel != 0) {
    const int tq = (wc ? tl : te + 2 + (xo & 1)) + he;
    gel2(0, 2 * tq);
    gel2(3, 2 * tq + 1);
    const float cg = cfin[wc * 192 + 64 + li], cu = cfin[wc * 192 + 80 + li];
#pragma unroll
    for (int mf = 0; mf < 4; ++mf) {
      f32x4 t[4];
#pragma unroll
      for (int i = 0; i < 4; ++i) t[i] = *(const f32x4*)(tb + i * 64 + mf * 16 + lq * 4);
      const f32x4 g = acc[mf][4], u = acc[mf][5];
      u32x2 o;
#pragma unroll
      for (int e = 0; e < 4; e += 2)
        o[e >> 1] = gelp(f32x2{g[e], g[e + 1]}, f32x2{u[e], u[e + 1]}, f32x2{cg, cg}, f32x2{cu, cu}, f32x2{t[0][e], t[0][e + 1]},
                         f32x2{t[1][e], t[1][e + 1]}, f32x2{t[2][e], t[2][e + 1]}, f32x2{t[3][e], t[3][e + 1]});
      *(u32x2*)(shm + wm * 4096 + mf * 1024 + wc * 512 + li * 32 + lq * 8) = o;
    }
    if (wc == 0) {
      signal(0x10000u << wid);
      WAIT_V(0);
      early();
    } else {
      await(0x1000u << wid, 0x1000u << wid);
#pragma unroll
      for (int mf = 0; mf < 4; ++mf) putf(2 * (tl + 2) + he, mf, __builtin_bit_cast(u32x4, tr_frag(shm3 + wm * 4096 + mf * 1024 + ln * 8)));
    }
  } else {
    float rd[4];
#pragma unroll
    for (int mf = 0; mf < 4; ++mf) rd[mf] = svr[wm * 64 + mf * 16 + li];
    auto cvq = [&](int p, int mf, const f32x4& c0, const f32x4& c1) {
      u32x4 o;
#pragma unroll
      for (int j = 0; j < 2; ++j) {
        const f32x4 a = acc[mf][p * 2 + j], c4 = j ? c1 : c0;
        const f32x2 m2 = {rf[mf][0], rf[mf][0]}, r2 = {rd[mf], rd[mf]};
        f32x2 t0 = __builtin_elementwise_fma(m2, f32x2{c4[0], c4[1]}, f32x2{a[0], a[1]});
        f32x2 t1 = __builtin_elementwise_fma(m2, f32x2{c4[2], c4[3]}, f32x2{a[2], a[3]});
        if (p) { t0 *= r2; t1 *= r2; }
        o[2 * j] = pack2(t0[0], t0[1]); o[2 * j + 1] = pack2(t1[0], t1[1]);
      }
      return o;
    };
    const float cv0 = cfin[wc * 192 + 64 + li], cv1 = cfin[wc * 192 + 80 + li];
    auto cvv = [&](int mf) {
      const f32x4 n4 = *(const f32x4*)(tb + mf * 16 + lq * 4), r4 = *(const f32x4*)(svr + wm * 64 + mf * 16 + lq * 4);
      u32x4 o;
#pragma unroll
      for (int j = 0; j < 2; ++j) {
        const f32x4 a = acc[mf][4 + j];
        const f32x2 c2 = {j ? cv1 : cv0, j ? cv1 : cv0};
        const f32x2 t0 = __builtin_elementwise_fma(f32x2{n4[0], n4[1]}, c2, f32x2{a[0], a[1]}) * f32x2{r4[0], r4[1]};
        const f32x2 t1 = __builtin_elementwise_fma(f32x2{n4[2], n4[3]}, c2, f32x2{a[2], a[3]}) * f32x2{r4[2], r4[3]};
        o[2 * j] = pack2(t0[0], t0[1]); o[2 * j + 1] = pack2(t1[0], t1[1]);
      }
      return o;
    };
    u32x4 kx[4], vx[4], qx[4], hx[4];
    const bf16x8 ones = {0x3f80, 0x3f80, 0x3f80, 0x3f80, 0x3f80, 0x3f80, 0x3f80, 0x3f80};
    const f32x4 ck0 = *(const f32x4*)(cl + 32), ck1 = *(const f32x4*)(cl + 48);
    kx[2] = cvq(1, 2, ck0, ck1); kx[3] = cvq(1, 3, ck0, ck1);
    vx[2] = cvv(2); vx[3] = cvv(3);
    auto exo = [](int e) { return e * 4096 + (e >> 2) * (G2_A - 16384); };
    const bool needh = wm == 0 && mtile > 0;
    const __amdgpu_buffer_rsrc_t rh = __builtin_amdgcn_make_buffer_rsrc((void*)((char*)halo + ((needh ? mtile - 1 : mtile) * 32 + ntile + wc) * 4096), 0,
                                                                        needh || wm == 3 ? 4096 : 0, 0x00020000);
    if (wm < 3) {
      char* const ex = shm + exo(wc * 3 + wm) + ln * 16;
      *(u32x4*)ex = kx[2]; *(u32x4*)(ex + 1024) = kx[3]; *(u32x4*)(ex + 2048) = vx[2]; *(u32x4*)(ex + 3072) = vx[3];
      signal(0x10000u << wid);
    } else {
#pragma unroll
      for (int r = 0; r < 4; ++r) __builtin_amdgcn_raw_buffer_store_b128(r < 2 ? kx[2 + r] : vx[r], rh, ln * 16 + r * 1024, 0, 16);
    }
    auto hload = [&]() {
#pragma unroll
      for (int r = 0; r < 4; ++r) hx[r] = __builtin_bit_cast(u32x4, __builtin_amdgcn_raw_buffer_load_b128(rh, ln * 16 + r * 1024, 0, 16));
    };
    SB0();
    gel2(3, 2 * (wc ? tl + 3 : te) + he);
    SB0();
    if (wm == 0) hload();
    SB0();
    kx[0] = cvq(1, 0, ck0, ck1); kx[1] = cvq(1, 1, ck0, ck1);
    vx[0] = cvv(0); vx[1] = cvv(1);
    {
      const f32x4 cq0 = *(const f32x4*)(cl), cq1 = *(const f32x4*)(cl + 16);
#pragma unroll
      for (int mf = 0; mf < 4; ++mf) qx[mf] = cvq(0, mf, cq0, cq1);
    }
    f32x4 mk0, mk1;
#pragma unroll
    for (int j = 0; j < 4; ++j) {
      const int k0 = 4 * lq + j, k1 = 16 + 4 * lq + j;
      mk0[j] = (k0 >= li + 9 && k0 <= li + 16) ? 0.f : -1.0e30f;
      mk1[j] = (k1 >= li + 9 && k1 <= li + 16) ? 0.f : -1.0e30f;
    }
    auto max3 = [](float a, float b, float c) { float r; asm("v_max3_f32 %0, %1, %2, %3" : "=v"(r) : "v"(a), "v"(b), "v"(c)); return r; };
    auto max2 = [](float a, float b) { float r; asm("v_max_f32 %0, %1, %2" : "=v"(r) : "v"(a), "v"(b)); return r; };
    asm volatile("" : "+v"(kx[0]), "+v"(kx[1]), "+v"(vx[0]), "+v"(vx[1]), "+v"(qx[0]), "+v"(qx[1]), "+v"(qx[2]), "+v"(qx[3]), "+v"(mk0), "+v"(mk1));
    if (wc == 0) {
      if (wm == 0) WAIT_V(4); else WAIT_V(0);
      early();
    }
    if (wm > 0) {
      await(0x8000u << wid, 0x8000u << wid);
#pragma unroll
      for (int i = 0; i < 4; ++i) hx[i] = *(const u32x4*)(shm + exo(wc * 3 + wm - 1) + i * 1024 + ln * 16);
    } else if (__builtin_expect(cok || !needh, 1)) {
      while (__builtin_expect(blank(hx), 0)) {
        __builtin_amdgcn_s_sleep(1);
        asm volatile("" ::: "memory");
        hload();
      }
    } else {
      await(0xffffu, 0x404u);
      char* const fb = shm + exo(6 + wc) + ln * 16;
#pragma unroll 1
      for (int i = 0; i < 32; ++i) {
        const int rec = i >> 3, e = i & 7;
        const int row = m0 - 32 + 2 * (rec < 2 ? li : lq * 4 + (e & 3)) + (rec & 1);
        const int col = (rec < 2 ? D_ + lq * 4 + (e & 3) : 2 * D_ + li) + (ntile + wc) * 32 + (e >> 2) * 16;
        const u16* xr = x + (size_t)row * D_;
        const unsigned stt = row_stats_serial(xr);
        const float mb = lo_f(stt);
        float a = 0.f;
#pragma unroll 1
        for (int k = 0; k < D_; ++k) a += (bf2f(xr[k]) - mb) * bf2f(wqkv[(size_t)k * 3072 + col]);
        *(u16*)(fb + rec * 1024 + e * 2) = f2bf(a / hi_f(stt));
      }
#pragma unroll
      for (int i = 0; i < 4; ++i) hx[i] = *(const u32x4*)(fb + i * 1024);
    }
    {
      const f32x4 z = {0.f, 0.f, 0.f, 0.f};
      const float mz = mtile == 0 && wm == 0 ? -1.0e30f : 0.f;
      const f32x4 mkf = {fminf(mk0[0], mz), fminf(mk0[1], mz), fminf(mk0[2], mz), fminf(mk0[3], mz)};
      f32x4 s0[4], s1[4];
#pragma unroll
      for (int i = 0; i < 4; ++i) {
        const int n = i ^ 2;
        const bf16x8 qf = __builtin_bit_cast(bf16x8, qx[n]);
        s1[n] = __builtin_amdgcn_mfma_f32_16x16x32_bf16(__builtin_bit_cast(bf16x8, kx[n]), qf, mk1, 0, 0, 0);
        s0[n] = __builtin_amdgcn_mfma_f32_16x16x32_bf16(__builtin_bit_cast(bf16x8, n < 2 ? hx[n] : kx[n - 2]), qf, n < 2 ? mkf : mk0, 0, 0, 0);
      }
      SB0();
      asm volatile("s_nop 7\n\ts_nop 3" : "+v"(s0[0]), "+v"(s1[0]), "+v"(s0[1]), "+v"(s1[1]), "+v"(s0[2]), "+v"(s1[2]), "+v"(s0[3]), "+v"(s1[3]));
      float mx[4];
#pragma unroll
      for (int n = 0; n < 4; ++n) mx[n] = max3(max3(s0[n][0], s0[n][1], s0[n][2]), max3(s0[n][3], s1[n][0], s1[n][1]), max2(s1[n][2], s1[n][3]));
#pragma unroll
      for (int n = 0; n < 4; ++n) { auto r = __builtin_amdgcn_permlane16_swap(__float_as_uint(mx[n]), __float_as_uint(mx[n]), false, false);
        mx[n] = max2(__uint_as_float(r[0]), __uint_as_float(r[1])); }
#pragma unroll
      for (int n = 0; n < 4; ++n) { auto r = __builtin_amdgcn_permlane32_swap(__float_as_uint(mx[n]), __float_as_uint(mx[n]), false, false);
        mx[n] = max2(__uint_as_float(r[0]), __uint_as_float(r[1])); }
      SB0();
      constexpr float cs2 = 0.17677669529663687f * 1.4426950408889634f;
      f32x4 os[4], o0[4], o1[4];
#pragma unroll
      for (int n = 0; n < 4; ++n) {
        const float c = cs2 * rd[n], nm = -mx[n] * c;
        float e8[8];
#pragma unroll
        for (int j = 0; j < 4; ++j) {
          e8[j] = __builtin_amdgcn_exp2f(__builtin_fmaf(s0[n][j], c, nm));
          e8[4 + j] = __builtin_amdgcn_exp2f(__builtin_fmaf(s1[n][j], c, nm));
        }
        u32x4 pk;
#pragma unroll
        for (int j = 0; j < 4; ++j) pk[j] = pack2(e8[2 * j], e8[2 * j + 1]);
        const bf16x8 pf = __builtin_bit_cast(bf16x8, pk);
        const u32x4 vp = n < 2 ? hx[2 + n] : vx[n - 2];
        const u32x4 va = {vp[0], vp[1], vx[n][0], vx[n][1]}, vb = {vp[2], vp[3], vx[n][2], vx[n][3]};
        os[n] = __builtin_amdgcn_mfma_f32_16x16x32_bf16(ones, pf, z, 0, 0, 0);
        o0[n] = __builtin_amdgcn_mfma_f32_16x16x32_bf16(__builtin_bit_cast(bf16x8, va), pf, z, 0, 0, 0);
        o1[n] = __builtin_amdgcn_mfma_f32_16x16x32_bf16(__builtin_bit_cast(bf16x8, vb), pf, z, 0, 0, 0);
      }
#pragma unroll
      for (int n = 0; n < 4; ++n) {
        const float inv = __builtin_amdgcn_rcpf(os[n][0]);
        const f32x4 n0 = o0[n] * inv, n1 = o1[n] * inv;
        putf(112 + ntile + wc, n, u32x4{pack2(n0[0], n0[1]), pack2(n0[2], n0[3]), pack2(n1[0], n1[1]), pack2(n1[2], n1[3])});
      }
    }
  }
  TS1(2);
  const bool tya1 = tya;
  {
  __builtin_amdgcn_sched_barrier(0);
  int tid = threadIdx.x;
  asm volatile("" : "+v"(tid));
  __builtin_amdgcn_sched_barrier(0);
  const int lane = tid & 63;
  const int wm = wid >> 1, wn = wid & 1;
  const int m0 = mt2 * 128, n0 = nt2 * 128;
  constexpr int PD = G2_NSTAGE;
  const int ny = (xcd & 1) ? NY - DLT : NY + DLT;
  TS(0);
  const unsigned va = lane * 16;
  auto stage_y = [&]() {
    asm volatile("s_add_u32 m0, %[l], %[oa]\n\ts_nop 0\n\tglobal_load_lds_dwordx4 %[va], %[a] sc1\n\tglobal_load_lds_dwordx4 %[va], %[a] offset:1024 sc1\n\t"
                 "global_load_lds_dwordx4 %[va], %[a] offset:2048 sc1\n\tglobal_load_lds_dwordx4 %[va], %[a] offset:3072 sc1\n\t" G2_W
                 :: [l] "s"(sl), [oa] "s"(shm_base + G2_A + (p ^ role) * 4096), [a] "s"(sa), [va] "v"(va), G2_WOP(wy) : "memory", "scc", "m0");
    ++st;
    sa += st == NY ? (long)((1 - 2 * role) * NY * 16384) : 16384l;
    sl = (sl + 16384) & 65535;
    wsel();
  };
  u64 ready = 0;
  const u64* const mbox = &done1[bid * 64 + lane];
  auto seen = [&](u64 v) { const u64 r = __ballot(fresh(v, t_own)); ready |= r | (r << 32); };
  auto need = [&](u64 mask, bool renew) {
    while ((ready & mask) != mask) {
      seen(__hip_atomic_load(mbox, RLX_AGENT));
      if ((ready & mask) != mask) {
        __builtin_amdgcn_s_sleep(PSLEEP);
        if (renew && !cok) notify(0, 32);
      }
    }
    asm volatile("" ::: "memory");
  };
  u64 fl = 0;
  auto peek = [&]() { asm volatile("global_load_dwordx2 %0, %1, %2 sc1" : "+v"(fl) : "v"(va >> 1), "s"(done1 + bid * 64) : "memory"); };
  auto check = [&](u64 mask, bool renew) {
    asm volatile("" : "+v"(fl));
    seen(fl);
    if (__builtin_expect((ready & mask) != mask, 0)) need(mask, renew);
  };
  if (wid >= 4) {
    TS(4);
    __builtin_amdgcn_s_setprio(0);
    BARL();
    stage_y();
    stage_y();
    BAR();
    if (p == 1) peek();
    stage_y();
    WAIT_V(16);
    BAR();
    if (p == 0) notify(0, 32);
    stage_y();
    const char* sz;
    int ws0, ws1;
    u64 cm = 0xa0a0ull << (32 + 16 * role), cn = 0x0f0full << (16 * role);
    const unsigned evc = p == 1 ? 0x8088u : 0u, evp = p == 1 ? 0x2020u : 0u;
#pragma unroll 1
    for (int g = 3; g < ny - PD; ++g) {
      WAIT_V(16);
      BAR();
      if ((evc >> g) & 1) { check(cm, true); cm = cn; cn = (0xffffull << (16 * role)) | 0x55555555ull; }
      if ((evp >> g) & 1) peek();
      if (g == 9 + p) {
        sz = (const char*)abuf + (size_t)(mt2 * 72 + 56) * 16384 + (p >> 1) * 8192 + role * 4096 + (p & 1) * 2048;
        ws0 = __builtin_amdgcn_readfirstlane((wz.vb[0][1] ^ 64) - wz.vb[0][0]) * 16; ws1 = __builtin_amdgcn_readfirstlane((wz.vb[1][1] ^ 64) - wz.vb[1][0]) * 16;
        PIN_S(sz); PIN_S(ws0); PIN_S(ws1); PIN_S(wz.wb[0]); PIN_S(wz.wb[1]);
      }
      stage_y();
    }
    WAIT_V(16);
    BAR();
    sa = sz; sw[0] = wz.wb[0]; sw[1] = wz.wb[1];
    auto stage_s = [&]() {
      asm volatile("s_add_u32 m0, %[l], %[oa]\n\ts_nop 0\n\tglobal_load_lds_dwordx4 %[va], %[a] sc1\n\tglobal_load_lds_dwordx4 %[va], %[a] offset:1024 sc1\n\t" G2_W
                   :: [l] "s"(sl), [oa] "s"(shm_base + G2_A + (p >> 1) * 8192 + (p & 1) * 2048), [a] "s"(sa), [va] "v"(va), G2_WOP(wz) : "memory", "scc", "m0");
      sa += 16384; sw[0] += ws0; sw[1] += ws1; sl = (sl + 16384) & 65535;
    };
    stage_s();
#pragma unroll
    for (int j = -3; j < 15; ++j) {
      WAIT_V((j == -3 ? 2 : 0) + (j < 13 ? 12 : (14 - j) * 6) + (j > GETJ && j < GETJ + 4 ? 2 : 0) + (j > GETJ + 1 && j < GETJ + 5 ? 2 : 0));
      BAR();
      if (j < 12) stage_s();
      if (j == GETJ || j == GETJ + 1)
        asm volatile("s_mov_b32 m0, %[l]\n\ts_nop 0\n\tglobal_load_lds_dwordx4 %[va], %[a] offset:%[o] sc1\n\tglobal_load_lds_dwordx4 %[va], %[a] offset:%[o]+1024 sc1"
                     :: [l] "s"(shm_base + G2_X + 16384 + p * 4096), [a] "s"((const char*)slab + (size_t)(bid ^ 5) * 16384 + p * 4096), [va] "v"(va),
                        [o] "n"((j - GETJ) * 2048) : "memory", "m0");
    }
    asm volatile("" : "+v"(fl));
    TS(5);
    if (tid < 258) __hip_atomic_store(lane ? done2 + bid * 8 : &acks[bid], __builtin_amdgcn_s_memrealtime(), RLX_AGENT);
    if (__builtin_expect(!cok, 0) && p == 0) {
      const bool mine = lane == 32 || (lane < 32 && (tya1 || ((lane >> 3) & 1) != (xcd >> 2)));
      const u64* const pa = &acks[lane < 32 ? client(lane) : bid ^ 5];
      while (!__all(!mine || fresh(__hip_atomic_load(pa, RLX_AGENT), t_own))) {
        notify(0, 32);
        if (lane == 0) __hip_atomic_store(done2 + bid * 8, __builtin_amdgcn_s_memrealtime(), RLX_AGENT);
        __builtin_amdgcn_s_sleep(32);
      }
    }
  } else {
    TS(6);
    f32x4 acc[4][4], sacc[2][4];
    const int fr = lane & 15, fq = lane >> 4, q4 = fr >> 2, pp = lane & 3;
    const int ff = ((q4 >> 1) & 1) | ((fq & 1) << 1);
    int aof = G2_A + wm * 8192 + lane * 16;
    int boff[4];
#pragma unroll
    for (int j = 0; j < 4; ++j) boff[j] = wn * 8192 + (8 * fq + q4) * 128 + ((j ^ ff) * 32) + pp * 8;
    int vo = wid * 4096 + lane * 16;
    int vx = ((m0 + wm * 64 + role * 32 + 2 * fr) * D_ + n0 + wn * 64) * 2 + (fq & 1) * 32 + (fq >> 1) * 16;
    lds_cptr px = shm3 + G2_X + wid * 4096 + (fq >> 1) * 512 + fr * 16 + (fq & 1) * 8;
    asm volatile("" : "+v"(vo), "+v"(vx), "+v"(px));
#define SB() __builtin_amdgcn_sched_barrier(0)
    struct Fr { bf16x8 a[4]; bf16x4 bl[4], bh[4]; };
    Fr fa[2], fb[2];
    auto rd = [&](Fr* f, int odd, int t) {
      constexpr int ord[12] = {0, 1, 8, 2, 3, 4, 5, 6, 7, 9, 10, 11};
      constexpr int seq[24] = {0, 1, 2, 12, 13, 14, 15, 16, 3, 4, 5, 6, 17, 18, 19, 20, 7, 8, 9, 21, 10, 22, 11, 23};
      lds_cptr sb = shm3 + odd * 16384;
      const int ks = seq[t] / 12, o = ord[seq[t] % 12];
      if (o >= 8) f[ks].a[o - 8] = *(const __attribute__((address_space(3))) bf16x8*)(sb + aof + (o - 8) * 2048 + ks * 1024);
      else if (o & 1) f[ks].bh[o >> 1] = tr_read(sb + boff[o >> 1] + ks * 4096 + 512);
      else f[ks].bl[o >> 1] = tr_read(sb + boff[o >> 1] + ks * 4096);
    };
    auto bfr = [&](const Fr& c, int j) { return __builtin_shufflevector(c.bl[j], c.bh[j], 0, 1, 2, 3, 4, 5, 6, 7); };
    auto mf1 = [](f32x4& c, bf16x8 b, bf16x8 a, bool z = false) {
      if (z) asm volatile("v_mfma_f32_16x16x32_bf16 %0, %1, %2, 0" : "=&v"(c) : "v"(b), "v"(a));
      else asm volatile("v_mfma_f32_16x16x32_bf16 %0, %1, %2, %0" : "+v"(c) : "v"(b), "v"(a));
    };
    auto mf2 = [](f32x4& c, bf16x8 b, bf16x8 a, bf16x8 b2, bf16x8 a2, bool z) {
      if (z) asm volatile("v_mfma_f32_16x16x32_bf16 %0, %1, %2, 0\n\tv_mfma_f32_16x16x32_bf16 %0, %3, %4, %0" : "=&v"(c) : "v"(b), "v"(a), "v"(b2), "v"(a2));
      else asm volatile("v_mfma_f32_16x16x32_bf16 %0, %1, %2, %0\n\tv_mfma_f32_16x16x32_bf16 %0, %3, %4, %0" : "+v"(c) : "v"(b), "v"(a), "v"(b2), "v"(a2));
    };
    auto pairs = [&](f32x4 (*ac)[4], const Fr* c, int nq, int mx, int nf, auto fill, bool z = false) {
#pragma unroll
      for (int q = 0; q < nq; ++q) {
        const int mf = (q >> 2) ^ mx, j = (q & 4) ? 3 - (q & 3) : q & 3, ks = q & 1;
        SB();
        if (2 * q >= nf) mf2(ac[mf][j], bfr(c[ks], j), c[ks].a[mf], bfr(c[ks ^ 1], j), c[ks ^ 1].a[mf], z);
        else {
          mf1(ac[mf][j], bfr(c[ks], j), c[ks].a[mf], z);
          SB();
          fill(2 * q);
          SB();
          mf1(ac[mf][j], bfr(c[ks ^ 1], j), c[ks ^ 1].a[mf]);
          SB();
          fill(2 * q + 1);
        }
      }
      SB();
    };
    auto flip = [&]() {
      aof ^= 32768; boff[0] ^= 32768; boff[1] ^= 32768; boff[2] ^= 32768; boff[3] ^= 32768;
      asm volatile("" : "+v"(aof), "+v"(boff[0]), "+v"(boff[1]), "+v"(boff[2]), "+v"(boff[3]));
    };
    auto bar = [&]() { __builtin_amdgcn_s_waitcnt(0xC07F); BAR(); };
    auto ystg = [&](const Fr* c, Fr* n, int odd, bool z = false) { pairs(acc, c, 16, 0, 24, [&](int t) { rd(n, odd, t); }, z); };
    auto stg = [&](const Fr* c, Fr* n, int odd, bool z = false) { pairs(sacc, c, 8, 0, 10, [&](int t) { rd(n, odd, 2 * t); rd(n, odd, 2 * t + 1); }, z); };
    asm volatile("" : "+v"(aof), "+v"(boff[0]), "+v"(boff[1]), "+v"(boff[2]), "+v"(boff[3]));
    await(0xffffu, 0x404u);
    auto lda = [&](int i) {
      asm volatile("s_mov_b32 m0, %[l]\n\ts_nop 0\n\tglobal_load_lds_dwordx4 %[va], %[a] sc1\n\tglobal_load_lds_dwordx4 %[va], %[a] offset:1024 sc1\n\t"
                   "global_load_lds_dwordx4 %[va], %[a] offset:2048 sc1\n\tglobal_load_lds_dwordx4 %[va], %[a] offset:3072 sc1"
                   :: [l] "s"(shm_base + G2_A + (i + 1) * 16384 + (p ^ role) * 4096), [a] "s"(sa + i * 16384), [va] "v"(va) : "memory", "m0");
    };
    need(0x0505ull << (32 + 16 * role), false);
    TS(4);
    lda(0);
    lda(1);
    need(0x0f0full << (32 + 16 * role), false);
    lda(2);
    WAIT_V(8);
    signal(1u << 24);
    await(0xff000000u, 4u << 24);
    __builtin_amdgcn_s_waitcnt(0xC07F);
    SB();
#pragma unroll
    for (int t = 0; t < 24; ++t) rd(fa, 1, t);
    flip();
#define CPRIO 0
    __builtin_amdgcn_s_setprio(CPRIO);
    WAIT_V(4);
    TS(5);
    peek();
    bar();
    TS(7);
    ystg(fa, fb, 0, true);
    WAIT_V(1);
    bar();
    ystg(fb, fa, 1);
    WAIT_V(0);
    check(0x5050ull << (32 + 16 * role), false);
    static_assert((NY - DLT) % 2 == 1, "");
#pragma unroll 1
    for (int g = 2; g < ny - 1; g += 2) {
      flip();
      bar();
      ystg(fa, fb, 0);
      bar();
      ystg(fb, fa, 1);
    }
    flip();
    bar();
    {
      const __amdgpu_buffer_rsrc_t rs = __builtin_amdgcn_make_buffer_rsrc((void*)((char*)slab + (size_t)bid * 16384), 0, 16384, 0x00020000);
      u32x4 pk;
      pairs(acc, fa, 16, 2, 32, [&](int t) {
        if (t < 20) rd(fb, 0, t);
        else {
          const int f = (t - 20) / 3, e = (t - 20) % 3;
          if (e < 2) { const f32x4 a = acc[2 + (f >> 1)][(f & 1) * 2 + e]; pk[2 * e] = pack2(a[0], a[1]); pk[2 * e + 1] = pack2(a[2], a[3]); }
          else __builtin_amdgcn_raw_buffer_store_b128(pk, rs, vo + f * 1024, 0, 16);
        }
      });
    }
    TS(2);
    static_assert(GETJ % 2 == 1 && GETJ >= 3 && GETJ <= 7, "");
    f32x4 xf[2][4];
#pragma unroll
    for (int j = 0; j < GETJ + 5; j += 2) {
      bar();
      stg(fb, fa, 1, j == 0);
      flip();
      bar();
      u32x2 xv[2];
      pairs(sacc, fa, 8, 0, 12, [&](int t) {
        if (t < 10) { rd(fb, 0, 2 * t); rd(fb, 0, 2 * t + 1); }
        else if (j < 8) xv[t - 10] = *(const __attribute__((address_space(3))) u32x2*)(px + (j >> 1) * 1024 + (t - 10) * 256);
      });
      if (j < 8) {
#pragma unroll
        for (int e = 0; e < 2; ++e) xf[j >> 2][(j & 2) + e] = f32x4{lo_f(xv[e][0]), hi_f(xv[e][0]), lo_f(xv[e][1]), hi_f(xv[e][1])};
        asm volatile("" : "+v"(xf[j >> 2][j & 2]), "+v"(xf[j >> 2][(j & 2) + 1]));
        SB();
      }
    }
    WAIT_V(0);
    bar();
    u32x4 pv[4];
    {
      lds_cptr pp_ = shm3 + G2_X + 16384 + vo;
      pairs(sacc, fb, 8, 0, 12, [&](int t) {
        if (t < 2) {
#pragma unroll
          for (int f = 2 * t; f < 2 * t + 2; ++f) pv[f] = *(const __attribute__((address_space(3))) u32x4*)(pp_ + f * 1024);
        } else { rd(fa, 1, 2 * t - 4); rd(fa, 1, 2 * t - 3); }
      });
      if (__builtin_expect(blank(pv) || !cok, 0)) {
        const __amdgpu_buffer_rsrc_t rp = __builtin_amdgcn_make_buffer_rsrc((void*)((char*)slab + (size_t)(bid ^ 5) * 16384), 0, 16384, 0x00020000);
        while (!cok && !__all(fresh(__hip_atomic_load(done2 + (bid ^ 5) * 8, RLX_AGENT), t_own))) {
          notify(0, 32);
          if (lane == 0) __hip_atomic_store(done2 + bid * 8, __builtin_amdgcn_s_memrealtime(), RLX_AGENT);
          __builtin_amdgcn_s_sleep(8);
        }
        do {
          asm volatile("" ::: "memory");
#pragma unroll
          for (int f = 0; f < 4; ++f) pv[f] = __builtin_bit_cast(u32x4, __builtin_amdgcn_raw_buffer_load_b128(rp, vo + f * 1024, 0, 16));
        } while (blank(pv));
      }
    }
    auto silu = [&](int f0, int f1) {
#pragma unroll
      for (int f = f0; f < f1; ++f) {
        const int mf = f >> 2, j = f & 3;
        const u32x4 pw = pv[mf * 2 + (j >> 1)];
        const f32x4 ya = acc[mf][j] + f32x4{lo_f(pw[2 * (j & 1)]), hi_f(pw[2 * (j & 1)]), lo_f(pw[2 * (j & 1) + 1]), hi_f(pw[2 * (j & 1) + 1])};
#pragma unroll
        for (int e = 0; e < 2; ++e) {
          const f32x2 y2 = {ya[2 * e], ya[2 * e + 1]};
          const f32x2 ar = y2 * f32x2{-1.4426950408889634f, -1.4426950408889634f};
          const f32x2 dn = f32x2{__builtin_amdgcn_exp2f(ar[0]), __builtin_amdgcn_exp2f(ar[1])} + f32x2{1.0f, 1.0f};
          const f32x2 t = y2 * f32x2{__builtin_amdgcn_rcpf(dn[0]), __builtin_amdgcn_rcpf(dn[1])};
          acc[mf][j][2 * e] = t[0]; acc[mf][j][2 * e + 1] = t[1];
        }
        asm volatile("" : "+v"(acc[mf][j]));
      }
      SB();
    };
#define SPT 0, 1, 2, 3, 5, 7, 8, 8
    constexpr int spt[] = {SPT};
    silu(0, spt[1]);
#pragma unroll
    for (int j = GETJ + 6; j < 15; ++j) {
      if (j & 1) flip();
      bar();
      if (j & 1) stg(fa, fb, 0); else stg(fb, fa, 1);
      silu(spt[j - GETJ - 5], j == 14 ? 8 : spt[j - GETJ - 4]);
    }
    pairs(sacc, fa, 8, 0, 0, [](int) {});
    asm volatile("s_nop 7\n\ts_nop 3" : "+v"(sacc[1][0]), "+v"(sacc[1][1]), "+v"(sacc[1][2]), "+v"(sacc[1][3]));
    SB();
    TS(1);
    {
      const __amdgpu_buffer_rsrc_t ro = __builtin_amdgcn_make_buffer_rsrc((void*)out, 0, S_ * D_ * 2, 0x00020000);
#pragma unroll
      for (int f = 0; f < 4; ++f) {
        const int mf = f >> 1, j = (f & 1) * 2;
        const f32x4 a = __builtin_elementwise_fma(sacc[mf][j], acc[mf][j], xf[mf][j]), b = __builtin_elementwise_fma(sacc[mf][j + 1], acc[mf][j + 1], xf[mf][j + 1]);
        const auto r0 = __builtin_amdgcn_permlane16_swap(pack2(a[0], a[1]), pack2(b[0], b[1]), false, false);
        const auto r1 = __builtin_amdgcn_permlane16_swap(pack2(a[2], a[3]), pack2(b[2], b[3]), false, false);
        __builtin_amdgcn_raw_buffer_store_b128(u32x4{r0[0], r1[0], r0[1], r1[1]}, ro, vx + mf * 2048 + j * 32, 0, 2);
      }
    }
    TS(3);
  }
  if (bid == 73 && wid == 4) { unsigned d_ = ts_r[5] - ts_r[7]; if (d_ > 6000u) d_ = 0; while (__builtin_amdgcn_s_memrealtime() - t_own < 6000ull + d_) {} }
  }
  asm volatile(".Lcode_end:");
}

extern "C" void kernel_launch(void* const* d_in, const int* in_sizes, int n_in, void* d_out, int out_size, void* d_ws,
                              size_t ws_size, hipStream_t stream) {
  const u16* A_x = (const u16*)d_in[0];
  const u16* A_wqkv = (const u16*)d_in[1];
  const u16* A_wo = (const u16*)d_in[2];
  const u16* B_wg = (const u16*)d_in[4];
  const u16* B_wu = (const u16*)d_in[5];
  const u16* B_wd = (const u16*)d_in[6];
  u16* out = (u16*)d_out;
  fused_kernel<<<256, 512, 0, stream>>>(A_x, A_wqkv, B_wg, B_wu, B_wd, A_wo, out, (char*)d_ws);
}
```

```cpp
#include <hip/hip_runtime.h>
#include <hip/hip_bf16.h>
#include <stdint.h>
#include <type_traits>

typedef unsigned short u16;
using bf16x8 = __attribute__((ext_vector_type(8))) short;
using bf16x4 = __attribute__((ext_vector_type(4))) short;
using f32x4 = __attribute__((ext_vector_type(4))) float;
using u32x2 = __attribute__((ext_vector_type(2))) unsigned;
using u32x4 = __attribute__((ext_vector_type(4))) unsigned;
typedef __attribute__((address_space(3))) const char* lds_cptr;

constexpr int S_ = 2048, D_ = 1024, F_ = 3584;

__device__ __forceinline__ float bf2f(u16 h) { return __uint_as_float((unsigned)h << 16); }
__device__ __forceinline__ u16 f2bf(float f) {
  unsigned u = __float_as_uint(f);
  u += 0x7fffu + ((u >> 16) & 1u);
  return (u16)(u >> 16);
}
__device__ __forceinline__ float rbf(float f) { return bf2f(f2bf(f)); }
typedef __attribute__((ext_vector_type(2))) __bf16 bf16x2_t;
typedef __attribute__((ext_vector_type(2))) float f32x2;
__device__ __forceinline__ unsigned pack2(float a, float b) {
  bf16x2_t r = __builtin_convertvector((f32x2){a, b}, bf16x2_t);
  return __builtin_bit_cast(unsigned, r);
}
__device__ __forceinline__ float lo_f(unsigned p) { return __uint_as_float(p << 16); }
__device__ __forceinline__ float hi_f(unsigned p) { return __uint_as_float(p & 0xffff0000u); }

#define TSDECL
#define TSS(i) do {} while (0)
#define TSFLUSH() do {} while (0)
#define TS1(i) TSS(i)
#define TS(i) TSS(8 + (i))
#define PIN_S(v) asm volatile("" : "+s"(v))
#define WAIT_V(n) asm volatile("s_waitcnt vmcnt(%0)" ::"n"(n) : "memory")
#define WAIT_L(n) asm volatile("s_waitcnt lgkmcnt(%0)" ::"n"(n) : "memory")
#define BARL() do { WAIT_L(0); BAR(); } while (0)
#define BAR() do { asm volatile("" ::: "memory"); __builtin_amdgcn_s_barrier(); asm volatile("" ::: "memory"); } while (0)

__device__ __forceinline__ bf16x4 tr_read(lds_cptr p) {
  return __builtin_amdgcn_ds_read_tr16_b64_v4i16((__attribute__((address_space(3))) bf16x4*)p);
}
__device__ __forceinline__ bf16x8 tr_frag(lds_cptr p) {
  bf16x4 lo = tr_read(p), hi = tr_read(p + 512);
  bf16x8 r;
  r[0] = lo[0]; r[1] = lo[1]; r[2] = lo[2]; r[3] = lo[3];
  r[4] = hi[0]; r[5] = hi[1]; r[6] = hi[2]; r[7] = hi[3];
  return r;
}
__device__ __forceinline__ void glds16(const void* gsrc, unsigned lds_dst) {
  unsigned keep;
  asm volatile("s_mov_b32 %0, m0\n\ts_mov_b32 m0, %2\n\ts_nop 0\n\tglobal_load_lds_dwordx4 %1, off\n\ts_mov_b32 m0, %0"
               : "=&s"(keep) : "v"(gsrc), "s"(lds_dst) : "memory");
}
#define GLDS(gptr, laddr) glds16((const void*)(gptr), (unsigned)(laddr))
__device__ __forceinline__ void glds16_sc1(const void* gsrc, unsigned lds_dst) {
  unsigned keep;
  asm volatile("s_mov_b32 %0, m0\n\ts_mov_b32 m0, %2\n\ts_nop 0\n\tglobal_load_lds_dwordx4 %1, off sc1\n\ts_mov_b32 m0, %0"
               : "=&s"(keep) : "v"(gsrc), "s"(lds_dst) : "memory");
}
#define GLDS_SC1(gptr, laddr) glds16_sc1((const void*)(gptr), (unsigned)(laddr))
__device__ __forceinline__ void glds16_nc(const void* gsrc, unsigned lds_dst) {
  unsigned keep;
  asm volatile("s_mov_b32 %0, m0\n\ts_mov_b32 m0, %2\n\ts_nop 0\n\tglobal_load_lds_dwordx4 %1, off\n\ts_mov_b32 m0, %0"
               : "=&s"(keep) : "v"(gsrc), "s"(lds_dst));
}
#define GLDS_NC(gptr, laddr) glds16_nc((const void*)(gptr), (unsigned)(laddr))
__device__ __forceinline__ void glds16_s(const void* sbase, unsigned voff, unsigned lds_dst) {
  asm volatile("s_mov_b32 m0, %2\n\ts_nop 0\n\tglobal_load_lds_dwordx4 %0, %1" :: "v"(voff), "s"(sbase), "s"(lds_dst) : "m0");
}

__device__ __forceinline__ void glds16_s1(const void* sbase, unsigned voff, unsigned lds_dst) {
  asm volatile("s_mov_b32 m0, %2\n\ts_nop 0\n\tglobal_load_lds_dwordx4 %0, %1 sc1" :: "v"(voff), "s"(sbase), "s"(lds_dst) : "memory", "m0");
}
__device__ __forceinline__ void glds16_v(const void* p, unsigned lds_dst) {
  asm volatile("s_mov_b32 m0, %1\n\ts_nop 0\n\tglobal_load_lds_dwordx4 %0, off" :: "v"(p), "s"(lds_dst) : "m0");
}
template <int OFF = 0>
__device__ __forceinline__ void gst16(const void* sbase, unsigned voff, u32x4 v) {
  asm volatile("global_store_dwordx4 %0, %1, %2 offset:%3 sc1\n\ts_nop 1" :: "v"(voff), "v"(v), "s"(sbase), "n"(OFF) : "memory");
}

typedef unsigned long long u64;
__device__ __forceinline__ bool fresh(u64 stamp, u64 t_own) {
  const u64 b = t_own + 100ull;
  const unsigned lo = (unsigned)stamp - (unsigned)b, hi = (unsigned)(stamp >> 32) - (unsigned)(b >> 32) - ((unsigned)stamp < (unsigned)b ? 1u : 0u);
  return (hi == 0u) & (lo < (unsigned)(__builtin_amdgcn_s_memrealtime() - t_own));
}
constexpr u64 TIMEOUT_ = 200000;
__device__ __forceinline__ float half_sum(float v) {
  v += __builtin_bit_cast(float, __builtin_amdgcn_update_dpp(0, __builtin_bit_cast(int, v), 0xB1, 0xf, 0xf, false));
  v += __builtin_bit_cast(float, __builtin_amdgcn_update_dpp(0, __builtin_bit_cast(int, v), 0x4E, 0xf, 0xf, false));
  v += __builtin_bit_cast(float, __builtin_amdgcn_update_dpp(0, __builtin_bit_cast(int, v), 0x141, 0xf, 0xf, false));
  v += __builtin_bit_cast(float, __builtin_amdgcn_update_dpp(0, __builtin_bit_cast(int, v), 0x140, 0xf, 0xf, false));
  { auto r = __builtin_amdgcn_permlane16_swap(__float_as_uint(v), __float_as_uint(v), false, false); v = __uint_as_float(r[0]) + __uint_as_float(r[1]); }
  return v;
}
__device__ __forceinline__ unsigned pack_stats(float mean, float var) {
  return (unsigned)f2bf(mean) | ((unsigned)f2bf(sqrtf(rbf(rbf(var) + 1e-5f))) << 16);
}
__device__ __forceinline__ unsigned row_stats_serial(const u16* xr) {
  float s = 0.f, q = 0.f;
#pragma unroll 1
  for (int k = 0; k < D_; ++k) s += bf2f(xr[k]);
  const float mean = s * (1.0f / D_);
#pragma unroll 1
  for (int k = 0; k < D_; ++k) { const float d = bf2f(xr[k]) - mean; q += d * d; }
  return pack_stats(mean, q * (1.0f / D_));
}
constexpr int G1_STAGE = 32768 + 5 * 8192;
#define NG 0
#define NOCP 0
#define NOMB 0
#define NOPF 0
#define NP 0
#define SH1 0
#define PF_K0 4
#define CP_K0 5
#define DLT 1
#define PSLEEP 2
#define GETJ 3
#define PUB_K 9

constexpr unsigned BLANK = 0x7f817f81u;
__device__ __forceinline__ bool blank(const u32x4* pv) {
  u64 m;
#define CX(i) "v_cmpx_ne_u32 vcc, %1, %" #i "\n\t"
  asm volatile(CX(2) CX(3) CX(4) CX(5) CX(6) CX(7) CX(8) CX(9) CX(10) CX(11) CX(12) CX(13) CX(14) CX(15) CX(16) CX(17)
               "s_mov_b64 %0, exec\n\ts_mov_b64 exec, -1\n\ts_nop 3" : "=s"(m) : "s"(BLANK), "v"(pv[0][0]), "v"(pv[0][1]), "v"(pv[0][2]), "v"(pv[0][3]),
               "v"(pv[1][0]), "v"(pv[1][1]), "v"(pv[1][2]), "v"(pv[1][3]), "v"(pv[2][0]), "v"(pv[2][1]), "v"(pv[2][2]), "v"(pv[2][3]),
               "v"(pv[3][0]), "v"(pv[3][1]), "v"(pv[3][2]), "v"(pv[3][3]) : "vcc");
  return m != ~0ull;
}
#define RLX_AGENT __ATOMIC_RELAXED, __HIP_MEMORY_SCOPE_AGENT

constexpr int G2_A = 65536, G2_X = 131072;
constexpr int G2_NSTAGE = 4;
constexpr size_t MB_ = 1024 * 1024;
__global__ __launch_bounds__(512, 2) void fused_kernel(const u16* __restrict__ x, const u16* __restrict__ wqkv,
                                                       const u16* __restrict__ wg, const u16* __restrict__ wu,
                                                       const u16* __restrict__ wd, const u16* __restrict__ wo,
                                                       u16* __restrict__ out, char* __restrict__ ws) {
  u16* const halo = (u16*)(ws + 4 * MB_);
  u16* const abuf = (u16*)(ws + 16 * MB_);
  char* const slab = ws + 64 * MB_;
  unsigned* const flags = (unsigned*)(ws + 96 * MB_);
  u32x4* const rstat = (u32x4*)(ws + 97 * MB_);
  char* const wcopy = ws + 98 * MB_;
  u64* const done1 = (u64*)(ws + 102 * MB_);
  u64* const done2 = (u64*)(ws + 100 * MB_ + 65536);
  u64* const donec = (u64*)(ws + 100 * MB_ + 131072);
  u64* const acks = (u64*)(ws + 100 * MB_ + 196608);
  float* const csum = (float*)(ws + 101 * MB_);
  __shared__ __attribute__((aligned(1024))) char shm[163840];
  constexpr int AUX = 2 * G1_STAGE + 8192;
  {
    u64 pc; int len;
    asm volatile("s_getpc_b64 %0\n.Lcode_beg:\n\ts_mov_b32 %1, .Lcode_end-.Lcode_beg" : "=s"(pc), "=s"(len));
    const int ol = ((blockIdx.x >> 3) + 32 * (threadIdx.x >> 6)) * 1024 + (threadIdx.x & 63) * 16;
    asm volatile("s_mov_b32 m0, %2\n\ts_nop 0\n\tglobal_load_lds_dwordx4 %0, %1"
                 :: "v"(ol < len - 16 ? ol : 0), "s"(pc & ~15ull), "s"((unsigned)(uintptr_t)(lds_cptr)shm + 2 * G1_STAGE + 7168) : "m0");
    __builtin_amdgcn_sched_barrier(0);
  }
  const u64 t_own = __builtin_amdgcn_s_memrealtime();
  TSDECL;
  TS1(7);
  const int tid = threadIdx.x, lane = tid & 63;
  const int wid = __builtin_amdgcn_readfirstlane(tid >> 6);
  const int wm = wid & 3, wc = wid >> 2;
  const int bid = blockIdx.x, xcd = bid & 7, idx = bid >> 3;
  const int ntile = xcd * 4 + (idx & 3), mtile = idx >> 2;
  const int m0 = mtile * 256;
  const int hg = ntile >> 1;
  const bool tya = (ntile & 1) == 0;
  const int srow = m0 + ntile * 8 + wm * 2;
  lds_cptr shm3 = (lds_cptr)shm;
  const unsigned shm_base = (unsigned)(uintptr_t)shm3;
  const int r8 = tid >> 3, pos = tid & 7;
  const int phys = pos >> 1, half = pos & 1;
  const int fk = ((r8 >> 1) & 1) | (((r8 >> 3) & 1) << 1);
  const int vsrc = phys ^ fk;
  const int qstr = tya ? 3072 * 2 : F_ * 2;
  unsigned voa = (((r8 & 32) + 2 * (r8 & 15) + ((r8 >> 4) & 1)) * D_ + ((pos ^ (r8 & 7)) * 8)) * 2;
  unsigned vog = r8 * (F_ * 2) + vsrc * 32 + half * 16, voq = r8 * qstr + vsrc * 32 + half * 16;
  unsigned vom = r8 * qstr + (vsrc >> (tya ? 0 : 1)) * 32 + half * 16;
  asm volatile("" : "+v"(voa), "+v"(vog), "+v"(voq), "+v"(vom));
  __builtin_amdgcn_sched_barrier(0);
  asm volatile("" :: "s"(x), "s"(wqkv), "s"(wg), "s"(wu), "s"(wd), "s"(wo), "s"(out), "s"(ws));
  TS1(1);
  const char* const ab = (const char*)(x + (size_t)m0 * D_);
  int kz = 0;
  asm volatile("" : "+s"(kz));
#pragma unroll
  for (int i = 0; i < 4; ++i) glds16_s(ab + i * 64 * D_ * 2, voa, shm_base + wid * 1024 + i * 8192);
  const int kt0 = (hg >> 1) * 7, he = hg & 1;
  const char* const gb = (const char*)(wg + (kt0 + (tya ? 5 + he : 2 * he + 1)) * 64);
  const long dgu = (const char*)wu - (const char*)wg, dp1 = tya ? 2048 : dgu;
  glds16_s(gb, vog, shm_base + wid * 1024 + 32768 + 3 * 8192);
  glds16_s(gb + dgu, vog, shm_base + wid * 1024 + 32768 + 4 * 8192);
  u64 qa = (u64)(wqkv + 64 * hg), ma = (u64)(wg + (kt0 + 4) * 64 + 32 * he);
  PIN_S(qa); PIN_S(ma);
  const char* const qb = tya ? (const char*)qa : gb - 128;
  glds16_s(qb, voq, shm_base + wid * 1024 + 32768);
  glds16_s(qb + dp1, voq, shm_base + wid * 1024 + 32768 + 8192);
  const char* const mb0 = (const char*)(tya ? qa + 4096 : ma);
  const char* mptr = mb0 + (((vsrc & 1) && !tya) ? dgu : 0) + vom;
  GLDS_NC(mptr, shm_base + wid * 1024 + 32768 + 2 * 8192);
  __builtin_amdgcn_sched_barrier(0);
  TS1(4);
  f32x4 acc[4][10];

  constexpr int NT = D_ / 64;
  constexpr int CNT = AUX + 4096 + 3584;
  if (tid < 8) *(unsigned*)(shm + CNT + tid * 4) = 0u;
  bf16x8 af0[4], af1[4];
  bf16x4 bl[4], bh[4];
  auto clear = [&]() __attribute__((always_inline)) {
    f32x4 z = {0.f, 0.f, 0.f, 0.f};
    asm volatile("" : "+v"(z));
#pragma unroll
    for (int a = 0; a < 4; ++a) {
#pragma unroll
      for (int b = 0; b < 10; ++b) { acc[a][b] = z; asm volatile("" : "+v"(acc[a][b])); }
      af0[a] = __builtin_bit_cast(bf16x8, z); af1[a] = af0[a];
      asm volatile("" : "+v"(af0[a]));
      asm volatile("" : "+v"(af1[a]));
      bl[a] = bf16x4{0, 0, 0, 0}; bh[a] = bf16x4{0, 0, 0, 0};
    }
  };
#define SB0() __builtin_amdgcn_sched_barrier(0)
  auto mm = [&](int g, int t, bool two) {
    const int mi = t >> 1, mf = ((g & 1) && g != 9) ? 3 - mi : mi, ks = (t & 1) ^ (mi & 1), s = ((g + 1) & 1) * 2;
    const bf16x8 b0 = __builtin_shufflevector(bl[s + ks], bh[s + ks], 0, 1, 2, 3, 4, 5, 6, 7), a0 = ks ? af1[mf] : af0[mf];
    const bf16x8 b1 = __builtin_shufflevector(bl[s + 1 - ks], bh[s + 1 - ks], 0, 1, 2, 3, 4, 5, 6, 7), a1 = ks ? af0[mf] : af1[mf];
    const bool tr = (g >> 1) == 2;
    if (two) asm volatile("v_mfma_f32_16x16x32_bf16 %0, %1, %2, %0\n\tv_mfma_f32_16x16x32_bf16 %0, %3, %4, %0" : "+v"(acc[mf][g])
                          : "v"(tr ? a0 : b0), "v"(tr ? b0 : a0), "v"(tr ? a1 : b1), "v"(tr ? b1 : a1));
    else asm volatile("v_mfma_f32_16x16x32_bf16 %0, %1, %2, %0" : "+v"(acc[mf][g]) : "v"(tr ? a0 : b0), "v"(tr ? b0 : a0));
  };
  typedef __attribute__((address_space(3))) unsigned lds_u32;
  lds_u32* const cnt = (lds_u32*)(shm3 + CNT);
  bool cok;
  constexpr int NY = 28;
  int role, mt2, nt2;
  struct WS { unsigned vb[2][2]; const char* wb[2]; int sh[2]; };
  auto wsetup = [&](const u16* w, int crow0, int l, bool ck) __attribute__((always_inline)) {
    WS r;
    const unsigned vr = ((l >> 5) & 1) * 16 + (wm & 1) * 8 + ((l >> 3) & 3), vc = (((l >> 1) & 3) ^ ((l >> 4) & 1)) * 32 + (l & 1) * 16;
    u64 cp = (u64)(wcopy + (nt2 >> 2) * 589824 + crow0 * 128);
    asm volatile("" : "+s"(cp));
#pragma unroll
    for (int i = 0; i < 2; ++i) {
      u64 dp = (u64)(w + nt2 * 128 + i * 64);
      asm volatile("" : "+s"(dp));
      const bool slow = !NOCP && ck & ((((unsigned)dp >> 7) & 7) == 3);
      r.sh[i] = slow ? 7 : 11;
      r.wb[i] = (const char*)(slow ? cp : dp) + (crow0 ? (wm >> 1) * 32 << r.sh[i] : 0);
      r.vb[i][0] = (vr << r.sh[i]) + vc; r.vb[i][1] = ((vr + 4) << r.sh[i]) + (vc ^ 64);
    }
    asm volatile("" : "+v"(r.vb[0][0]), "+v"(r.vb[0][1]), "+v"(r.vb[1][0]), "+v"(r.vb[1][1]));
    PIN_S(r.wb[0]); PIN_S(r.wb[1]); PIN_S(r.sh[0]); PIN_S(r.sh[1]);
    return r;
  };
  WS wy, wz;
  int vxq;
  u32x4 xq[4];
  unsigned vg;
  int st = 0;
  const char *sa, *sw[2];
  auto wsel = [&]() {
    const unsigned r = __builtin_amdgcn_readlane(vg, st);
    sw[0] = wy.wb[0] + (r << wy.sh[0]); sw[1] = wy.wb[1] + (r << wy.sh[1]);
  };
  const int p = wid & 3;
  unsigned sl = 16384;
#define G2_W "s_add_u32 m0, %[l], %[ow]\n\ts_nop 0\n\tglobal_load_lds_dwordx4 %[v0], %[w0] sc1\n\t" \
             "s_add_u32 m0, m0, 1024\n\ts_nop 0\n\tglobal_load_lds_dwordx4 %[v1], %[w0] sc1\n\t" \
             "s_add_u32 m0, m0, 7168\n\ts_nop 0\n\tglobal_load_lds_dwordx4 %[v2], %[w1] sc1\n\t" \
             "s_add_u32 m0, m0, 1024\n\ts_nop 0\n\tglobal_load_lds_dwordx4 %[v3], %[w1] sc1"
#define G2_WOP(w) [ow] "s"(shm_base + p * 2048), [w0] "s"(sw[0]), [w1] "s"(sw[1]), [v0] "v"(w.vb[0][0]), [v1] "v"(w.vb[0][1]), [v2] "v"(w.vb[1][0]), [v3] "v"(w.vb[1][1])
  auto tile2 = [&](int t0) __attribute__((always_inline)) {
    int b = bid;
    asm volatile("" : "+s"(b));
    role = 1 - ((b >> 2) & 1);
    mt2 = ((role ? b : b ^ 5) & 7) * 4 + ((b >> 3) & 3), nt2 = b >> 5;
    sa = (const char*)abuf + (size_t)(mt2 * 72 + role * NY + t0) * 16384 + wm * 4096;
    asm volatile("" : "+s"(sa));
  };
  auto prep = [&](bool first) __attribute__((always_inline)) {
    int l = threadIdx.x;
    asm volatile("" : "+v"(l));
    l &= 63;
    if (first) {
      tile2(3);
      vxq = ((mt2 * 128 + (wm >> 1) * 64 + role * 32 + 2 * (l & 15)) * D_ + nt2 * 128 + (wm & 1) * 64) * 2 + ((l >> 4) & 1) * 32 + (l >> 5) * 16;
      static_assert(DLT == 1, "");
      const int t = min(l, NY - 1), w = wm >> 1;
      const int o = (0xffaa5500fae504ull >> (2 * t)) & 3;
      int u1 = (0x990990 >> ((2 * t) & 31)) & 3, u2 = 3 + (t & 3);
      asm volatile("" : "+v"(u1), "+v"(u2));
      const int u = t < 12 ? u1 : u2;
      vg = (__umul24((l < NY ? role : 1 - role) * 4 + (((o & 1) << 1) | (o >> 1)), 14) + ((0xb85140a >> (4 * u)) & 15) + (u == 5 ? w : 2 * w)) * 32;
      asm volatile("" : "+v"(vg), "+v"(vxq));
    }
    wy = wsetup(wd, 0, l, first);
    wz = wsetup(wo, F_, l, first);
    SB0();
  };
  auto start2 = [&]() __attribute__((always_inline)) {
    const __amdgpu_buffer_rsrc_t rx = __builtin_amdgcn_make_buffer_rsrc((void*)x, 0, S_ * D_ * 2, 0x00020000);
#pragma unroll
    for (int f = 0; f < 4; ++f) xq[f] = __builtin_bit_cast(u32x4, __builtin_amdgcn_raw_buffer_load_b128(rx, vxq + (f >> 1) * 2048 + (f & 1) * 64, 0, 0));
    cok = __builtin_amdgcn_readfirstlane(cnt[1] & cnt[2]) != 0u;
    if (__builtin_expect(!cok, 0)) prep(false);
    wsel();
#pragma unroll
    for (int i = 0; i < 3; ++i) {
      asm volatile(G2_W :: [l] "s"(sl), G2_WOP(wy) : "memory", "scc", "m0");
      ++st;
      sl = (sl + 16384) & 65535;
      wsel();
    }
    PIN_S(sw[0]); PIN_S(sw[1]);
    SB0();
  };
  auto kloop = [&](auto wmc) {
    constexpr bool LD = decltype(wmc)::value != 0;
    constexpr int DMP = 2 * G1_STAGE;
    const char* pa = nullptr;
    unsigned vob = 0;
    f32x4 ca = {0.f, 0.f, 0.f, 0.f}, cb = {0.f, 0.f, 0.f, 0.f}, cd = ca;
    bf16x8 sel = {0, 0, 0, 0, 0, 0, 0, 0};
    int tro = 0;
    const int psh = (wid == 2 && !tya) ? 1 : 0, pstr = wid < 3 ? qstr : F_ * 2;
    const char* const pbase = wid == 0 ? qb : (wid == 1 ? qb + dp1 : (wid == 2 ? mb0 : gb));
    int lane = threadIdx.x;
    asm volatile("; %1" : "+v"(lane) : "n"(LD ? 1 : 0));
    lane &= 63;
    const int fr = lane & 15, fq = lane >> 4, q4 = fr >> 2, pp = lane & 3;
    const int ff = ((q4 >> 1) & 1) | ((fq & 1) << 1);
    int aoff[2], boff[2];
#pragma unroll
    for (int ks = 0; ks < 2; ++ks) aoff[ks] = (wm * 64 + fr) * 128 + (((ks * 4 + fq) ^ (fr & 7)) * 16);
#pragma unroll
    for (int j = 0; j < 2; ++j) boff[j] = 32768 + (8 * fq + q4) * 128 + (((2 * wc + j) ^ ff) * 32) + pp * 8;
    unsigned vol = lane * 16;
    float sm = 0.f, sq = 0.f;
    const char* pf = (const char*)wd + (bid * 4 + wid) * 7168;
    long pstep = 64 * pstr;
    if (!LD) {
      const int rs = 256 + mtile * 8 + (lane >> 3), vs = ((lane & 7) >> 1) ^ (((rs >> 1) & 1) | ((mtile & 1) << 1));
      pa = pbase + ((psh & vs) ? dgu : 0) + (rs * pstr + (vs >> psh) * 32 + (lane & 1) * 16);
      vob = rs * (F_ * 2) + vs * 32 + (lane & 1) * 16;
      tro = DMP + wid * 1024 + q4 * 128 + ((fq ^ ((q4 >> 1) | ((mtile & 1) << 1))) * 32) + pp * 8;
      const short one = fr == fq ? 0x3f80 : 0;
      sel = bf16x8{one, one, one, one, one, one, one, one};
      asm volatile("" : "+v"(tro), "+v"(vol), "+v"(sel), "+v"(ca), "+v"(cb), "+v"(sm), "+v"(sq));
      PIN_S(pf); PIN_S(pstep);
    }
    auto accum = [&](lds_cptr p, int ob) {
      ca = __builtin_amdgcn_mfma_f32_16x16x32_bf16(tr_frag(p), sel, ca, 0, 0, 0);
      if (wid == 0) cb = __builtin_amdgcn_mfma_f32_16x16x32_bf16(tr_frag(p + ob), sel, cb, 0, 0, 0);
    };
    auto xrow = [&](int i) {
      asm volatile("global_load_dwordx4 %0, %1, %2" : "+v"(cd) : "v"(vol + (vol & 512) * 3), "s"(x + (size_t)srow * D_ + i * 256) : "memory");
    };
    const int q32 = 32 * qstr;
    const char *sq0 = qb + (LD ? 1 : 2) * q32, *sq1 = sq0 + dp1, *sg0 = gb + (LD ? 32 : 64) * F_ * 2, *sg1 = sg0 + dgu, *sx = ab + (long)(kz + 128 - 32 * D_ * 2);
    u32x2 mp[2] = {__builtin_bit_cast(u32x2, mptr + q32), __builtin_bit_cast(u32x2, mptr + 2 * q32)};
    u64 cy[2];
    unsigned dst = shm_base + G1_STAGE + wm * 1024;
    int dbuf = G1_STAGE;
    auto stage = [&](int i0, int i1) __attribute__((always_inline)) {
#pragma unroll
        for (int i = i0; i < i1; ++i) {
          const int q = i >> 1, h = i & 1;
          if (h == 0 && ((NP >= 2 && (q == 3 || q == 4)) || (NP >= 4 && q < 2))) continue;
          const unsigned ld = dst + h * 4096 + (q < 5 ? 32768 + q * 8192 : (q - 5) * 8192);
          if (q < 2) glds16_s((q ? sq1 : sq0) + h * q32, voq, ld);
          else if (q == 2) glds16_v(__builtin_bit_cast(const char*, mp[h]), ld);
          else if (q < 5) glds16_s((q == 4 ? sg1 : sg0) + h * 32 * F_ * 2, vog, ld);
          else glds16_s(sx + ((q - 5) * 64 + h * 32) * D_ * 2, voa, ld);
        }
    };
    u64 cy0 = 0, rt0 = 0;
    if (!LD) {
      clear();
      xrow(0);
      WAIT_V(1);
      TS1(6);
    }
    asm volatile("" : "+v"(aoff[0]), "+v"(aoff[1]), "+v"(boff[0]), "+v"(boff[1]));
    auto body = [&](bool upd) __attribute__((always_inline)) {
      lds_cptr sb = shm3;
      auto rdb = [&](int g, int i) {
        lds_cptr p = sb + boff[g & 1] + (g >> 1) * 8192 + (i >> 1) * 4096;
        const int s = ((g + 1) & 1) * 2 + (i >> 1);
        if (i & 1) bh[s] = tr_read(p + 512); else bl[s] = tr_read(p);
      };
      auto rda = [&](int ks, int mf) { (ks ? af1 : af0)[mf] = *(const __attribute__((address_space(3))) bf16x8*)(sb + aoff[ks] + mf * 2048); };
      SB0();
      __builtin_amdgcn_s_waitcnt(0xC07F);
#pragma unroll
      for (int gg = 0; gg < 10; ++gg) {
        const int g = (gg + 9) % 10;
#pragma unroll
        for (int t = 0; t < 8; ++t) {
          const bool two = t >= 4 && !(upd && g >= 5 && g <= 8);
          SB0();
          if (!two || (t & 1)) mm(g, two ? t - 1 : t, two);
          SB0();
          if (g == 9) {
            if (t == 0 || t == 2) { rdb(0, t); rdb(0, t + 1); }
            if (t & 1) { rda((t >> 1) & 1, t >> 1); rda(((t >> 1) & 1) ^ 1, t >> 1); }
          } else if (t < 4) rdb(g + 1, t);
          else if (upd) {
            if (g == 5 && t == 4) { sq0 += 2 * q32; PIN_S(sq0); }
            if (g == 5 && t == 5) { sq1 += 2 * q32; PIN_S(sq1); }
            if (g == 5 && t == 6) { sg0 += 64 * F_ * 2; PIN_S(sg0); }
            if (g == 6 && t == 4) { sg1 += 64 * F_ * 2; PIN_S(sg1); }
            if (g == 6 && t == 5) { sx += 128; PIN_S(sx); }
            if (g == 6 && t == 6) { dst -= dbuf; PIN_S(dst); }
            if (g == 7 && t == 4) asm volatile("v_add_co_u32 %0, %2, %4, %0\n\tv_add_co_u32 %1, %3, %4, %1" : "+v"(mp[0][0]), "+v"(mp[1][0]), "=&s"(cy[0]), "=&s"(cy[1]) : "s"(2 * q32));
            if (g == 7 && t == 6) asm volatile("v_addc_co_u32 %0, vcc, 0, %0, %2\n\tv_addc_co_u32 %1, vcc, 0, %1, %3" : "+v"(mp[0][1]), "+v"(mp[1][1]) : "s"(cy[0]), "s"(cy[1]) : "vcc");
            if (g == 8 && t == 4) { aoff[0] += dbuf; aoff[1] += dbuf; asm volatile("" : "+v"(aoff[0]), "+v"(aoff[1])); }
            if (g == 8 && t == 5) { boff[0] += dbuf; boff[1] += dbuf; asm volatile("" : "+v"(boff[0]), "+v"(boff[1])); }
            if (g == 8 && t == 6) { dbuf = -dbuf; PIN_S(dbuf); }
          }
        }
      }
      SB0();
    };
    TS1(0);
#define ARRP(o, c) do {} while (0)
    if (LD) {
      clear();
      WAIT_V(0);
      TS1(5);
      BARL();
      stage(0, 18);
#pragma clang loop unroll(disable)
      for (int kt = 0;; ++kt) {
        body(true);
        WAIT_V(0);
        BARL();
        if (__builtin_expect(kt == 4 + wid, 0)) prep(true);
        asm volatile("" ::: "memory");
        if (kt == NT - 2) break;
        stage(0, 18);
        SB0();
      }
      start2();
      body(false);
    } else {
      char* wsx;
      auto tile = [&](int kt) __attribute__((always_inline)) {
        SB0();
        ARRP(wid * 64 + kt * 4, true);
        BARL();
        body(false);
        ARRP(320 + kt * 4 + 4, wid == 0);
        WAIT_V(0);
        if (NP >= 2) { glds16_s(sg0, vog, dst + 32768 + 3 * 8192); glds16_s(sg1, vog, dst + 32768 + 4 * 8192); sg0 += 64 * F_ * 2; sg1 += 64 * F_ * 2; }
        if (NP >= 4) { glds16_s(sq0, voq, dst + 32768); glds16_s(sq1, voq, dst + 32768 + 8192); sq0 += 2 * q32; sq1 += 2 * q32; }
        aoff[0] += dbuf; aoff[1] += dbuf; boff[0] += dbuf; boff[1] += dbuf;
        dst -= dbuf;
        dbuf = -dbuf;
        asm volatile("" : "+v"(aoff[0]), "+v"(aoff[1]), "+v"(boff[0]), "+v"(boff[1]), "+v"(vol), "+v"(tro) :: "memory");
        wsx = ws;
        PIN_S(wsx);
      };
      auto share = [&]() {
        WAIT_L(0);
        WAIT_V(0);
        glds16_v(pa, shm_base + DMP + wid * 1024);
        if (wid == 0) glds16_s(gb + dgu, vob, shm_base + DMP + 4096);
      };
      auto cpub = [&](u32x4 a, u32x4 b) {
        if ((vol & 0xc0) == 0) {
          const char* const cs = wsx + 101 * MB_ + ntile * 10240 + mtile * 256;
          const unsigned co = (vol & 0x30) * 4 + (vol >> 8) * 16;
          gst16(cs + wid * 2048, co, a);
          if (wid == 0) gst16(cs + 8192, co, b);
        }
      };
#pragma clang loop unroll(disable)
      for (int kt = 0; kt < 4; ++kt) {
        tile(kt);
        if (kt) accum(shm3 + tro, 4096);
        share();
        if (kt < 2) {
          glds16_v(pa - 2 * pstep, shm_base + DMP + 7168);
          if (wid == 0) glds16_s(gb + dgu, vob - 128 * F_ * 2, shm_base + DMP + 7168);
        }
        pa += pstep; vob += 64 * F_ * 2;
        accum(shm3 + tro + ((kt & 1) * G1_STAGE + 32768 + mtile * 1024 + wid * 7168 - DMP), 4 * 8192);
#pragma unroll
        for (int i = 0; i < 4; ++i) {
          float lo, hi;
          asm volatile("v_lshlrev_b32 %2, 16, %4\n\tv_and_b32 %3, 0xffff0000, %4\n\tv_add_f32 %0, %0, %2\n\tv_fmac_f32 %1, %2, %2\n\t"
                       "v_add_f32 %0, %0, %3\n\tv_fmac_f32 %1, %3, %3" : "+v"(sm), "+v"(sq), "=&v"(lo), "=&v"(hi) : "v"(cd[i]));
        }
        if (kt < 3) xrow(kt + 1);
      }
      glds16_s((const char*)wo + (bid * 4 + wid) * 2048, vol, shm_base + DMP + 7168);
#pragma clang loop unroll(disable)
      for (int kt = 4; kt < 12; ++kt) {
        tile(kt);
        accum(shm3 + tro, 4096);
        share();
        pa += pstep; vob += 64 * F_ * 2;
        glds16_s(pf, vol, shm_base + DMP + 7168);
        pf = kt == 10 ? (const char*)wo + (bid * 4 + wid) * 2048 + 1024 : pf + 1024;
        if (kt < 8) {
          if (kt == 4) {
            u32x4 bk;
            asm volatile("v_mov_b32 %0, %1" : "=v"(bk[0]) : "s"(BLANK));
            bk[1] = bk[0]; bk[2] = bk[0]; bk[3] = bk[0];
            const char* const sb = wsx + 64 * MB_ + bid * 16384 + wid * 4096;
            gst16(sb, vol, bk); gst16<1024>(sb, vol, bk); gst16<2048>(sb, vol, bk); gst16<3072>(sb, vol, bk);
            if (tya) {
              const char* const hb = wsx + 4 * MB_ + (mtile * 32 + ntile) * 4096 + wid * 2048;
              gst16(hb, vol, bk); gst16<1024>(hb, vol, bk);
            }
            cpub(bk, bk);
          }
          const unsigned q1 = bid * 4 + wid + (kt - 5) * 1024, q0 = q1 - 1024;
          if (q0 < 1152) gst16(wsx + 98 * MB_ + (q0 & 1) * 589824 + (q0 >> 1) * 1024, vol, *(const u32x4*)(shm + AUX + wid * 1024 + vol));
          if (q1 < 1152) {
            const char* src = (const char*)((q1 >> 1) < 448 ? wd + (size_t)(q1 >> 1) * 8 * D_ : wo + (size_t)((q1 >> 1) - 448) * 8 * D_);
            src += ((3 - ((unsigned)(uintptr_t)src >> 7)) & 7) * 128 + (q1 & 1) * 1024;
            glds16_s(src, (vol >> 7) * 2048 + (vol & 0x70), shm_base + AUX + wid * 1024);
          }
        } else {
          if (kt == 8) {
            const float ml = sm * (1.0f / 32), mean = half_sum(sm) * (1.0f / D_), dm = ml - mean;
            const float q = __builtin_fmaf(dm * 32.0f, dm, __builtin_fmaf(-ml, sm, sq));
            const unsigned ps = pack_stats(mean, fmaxf(half_sum(q) * (1.0f / D_), 0.f));
            const u64 now = __builtin_amdgcn_s_memrealtime();
            if ((vol & 0x1f0) == 0) gst16(wsx + 97 * MB_ + srow * 16, vol >> 5, u32x4{ps, (unsigned)now, ps, (unsigned)(now >> 32)});
          }
          if (kt == 9) {
            if (wid == 0) {
              u64 now;
              asm volatile("s_memrealtime %0\n\ts_waitcnt lgkmcnt(0)" : "=s"(now) :: "memory");
              if (vol == 0) asm volatile("global_store_dwordx2 %0, %1, %2 sc1" :: "v"(vol), "v"(now), "s"(wsx + 100 * MB_ + 131072 + bid * 8) : "memory");
            }
            if (wid == 2) glds16_s1(wsx + 102 * MB_ + (bid & ~1) * 512, vol, shm_base + DMP + 7168);
          }
        }
      }
      if (wid >= 2) glds16_s1(wsx + 100 * MB_ + 131072 + (wid - 2) * 1024, vol, shm_base + DMP + 3072 + wid * 1024);
      tile(12);
      accum(shm3 + tro, 4096);
      const int lane = vol >> 4;
      WAIT_V(0);
      glds16_s1(wsx + 97 * MB_ + (m0 + wid * 64) * 16, ((lane & 32) + 2 * (lane & 15) + ((lane >> 4) & 1)) * 16, shm_base + AUX + wid * 1024);
      asm volatile("s_nop 7\n\ts_nop 3" : "+v"(ca), "+v"(cb));
      cpub(__builtin_bit_cast(u32x4, ca), __builtin_bit_cast(u32x4, cb));
      if (wid >= 2) {
        u32x4 c4 = *(const u32x4*)(shm + DMP + 3072 + wid * 1024 + vol);
        bool ok = __all(fresh(((u64)c4[1] << 32) | c4[0], t_own) & fresh(((u64)c4[3] << 32) | c4[2], t_own));
        while (__builtin_expect(!ok, 0) && __builtin_amdgcn_s_memrealtime() - t_own < TIMEOUT_ / 10) {
          const __amdgpu_buffer_rsrc_t rs = __builtin_amdgcn_make_buffer_rsrc((void*)(wsx + 100 * MB_ + 131072), 0, 2048, 0x00020000);
          asm volatile("" ::: "memory");
          c4 = __builtin_bit_cast(u32x4, __builtin_amdgcn_raw_buffer_load_b128(rs, (wid - 2) * 1024 + vol, 0, 16));
          ok = __all(fresh(((u64)c4[1] << 32) | c4[0], t_own) & fresh(((u64)c4[3] << 32) | c4[2], t_own));
        }
        *(unsigned*)(shm + CNT - 4 + wid * 4) = ok ? 1u : 0u;
      }
      tile(13);
      u32x4 cw[4];
      auto cgather = [&]() {
        const __amdgpu_buffer_rsrc_t rc = __builtin_amdgcn_make_buffer_rsrc((void*)(wsx + 101 * MB_ + ntile * 10240), 0, 5 * 2048, 0x00020000);
        const unsigned vcs = (vol < 512 ? wid * 2048 : 8192) + (vol & 256) * 4 + (vol & 0xf0);
#pragma unroll
        for (int i = 0; i < 4; ++i) cw[i] = __builtin_bit_cast(u32x4, __builtin_amdgcn_raw_buffer_load_b128(rc, vcs + i * 256, 0, 16));
      };
      {
        const int ln = wid * 64 + (vol >> 4);
        u32x4 gr = *(const u32x4*)(shm + AUX + ln * 16);
        if (__builtin_expect(!__all((gr[0] == gr[2]) & fresh(((u64)gr[3] << 32) | gr[1], t_own)), 0)) {
          const __amdgpu_buffer_rsrc_t rs = __builtin_amdgcn_make_buffer_rsrc((void*)(wsx + 97 * MB_ + m0 * 16), 0, 4096, 0x00020000);
          const int prow = (ln & 0xe0) + 2 * (ln & 15) + ((ln >> 4) & 1);
          for (;;) {
            gr = __builtin_bit_cast(u32x4, __builtin_amdgcn_raw_buffer_load_b128(rs, prow * 16, 0, 16));
            if (__all((gr[0] == gr[2]) & fresh(((u64)gr[3] << 32) | gr[1], t_own))) break;
            if (__builtin_amdgcn_s_memrealtime() - t_own > TIMEOUT_) { gr[0] = row_stats_serial(x + (size_t)(m0 + prow) * D_); break; }
            __builtin_amdgcn_s_sleep(2);
          }
        }
        WAIT_V(0);
        cgather();
        const float nm = -lo_f(gr[0]), dn = hi_f(gr[0]), r = __builtin_amdgcn_rcpf(dn);
        auto mul = [](float a, float b) { float c; asm("v_mul_f32 %0, %1, %2" : "=v"(c) : "v"(a), "v"(b)); return c; };
        const f32x4 fc = {nm, -2.3022081986f * r, mul(mul(mul(-0.10294324f, r), r), r), mul(dn, dn)};
        *(f32x4*)(shm + AUX + ln * 16) = fc;
        float* const tw = (float*)(shm + 2 * G1_STAGE + 4096 + wid * 768) + ln;
#pragma unroll
        for (int i = 0; i < 4; ++i) tw[i * 64] = fc[i];
        ((float*)(shm + AUX + 4096))[384 + ln] = r;
      }
      tile(14);
      {
        cok = __builtin_amdgcn_readfirstlane(cnt[1] & cnt[2]) != 0u;
        while (__builtin_expect(blank(cw) && cok, 0)) {
          __builtin_amdgcn_s_sleep(2);
          asm volatile("" ::: "memory");
          cgather();
        }
        f32x4 t;
#pragma unroll
        for (int i = 0; i < 4; ++i) {
          float a, b;
          asm("v_add_f32 %0, %2, %3\n\tv_add_f32 %1, %4, %5\n\tv_add_f32 %0, %0, %1" : "=&v"(a), "=&v"(b) : "v"(cw[0][i]), "v"(cw[1][i]), "v"(cw[2][i]), "v"(cw[3][i]));
          const auto r2 = __builtin_amdgcn_permlane16_swap(__float_as_uint(a), __float_as_uint(a), false, false);
          asm("v_add_f32 %0, %1, %2" : "=v"(t[i]) : "v"(r2[0]), "v"(r2[1]));
        }
        const int l = vol >> 4;
        if (__builtin_expect(!cok, 0)) {
          const int gq = (l >> 2) & 3;
          const char* pc = l < 32 ? pbase + ((psh & gq) ? dgu : 0) + (gq >> psh) * 32 + (l & 3) * 8 : gb + dgu + (l & 15) * 8;
          t = f32x4{0.f, 0.f, 0.f, 0.f};
#pragma unroll 1
          for (int k = 0; k < D_; ++k) { const u32x2 w = *(const __attribute__((address_space(1))) u32x2*)(pc + (size_t)k * (l < 32 ? pstr : F_ * 2)); t += f32x4{lo_f(w[0]), hi_f(w[0]), lo_f(w[1]), hi_f(w[1])}; }
        }
        if (l < 32 || wid == 0)
          *(f32x4*)((float*)(shm + AUX + 4096) + ((l >> 3) & 1) * 192 + (l < 32 ? wid : 4) * 32 + (l & 7) * 4) = t;
        tile2(0);
        __builtin_amdgcn_s_setprio(2);
      }
      SB0();
      WAIT_V(0);
      ARRP(wid * 64 + 60, true);
      BARL();
      body(false);
    }
#pragma unroll
    for (int t = 0; t < 8; t += 2) mm(9, t, true);
    asm volatile("s_nop 7\n\ts_nop 3");
    SB0();
    TS1(3);
    if (LD) {
      __builtin_amdgcn_s_setprio(3);
      WAIT_V(0);
    }
  };
  if (wid < 4) kloop(std::integral_constant<int, 0>{});
  else kloop(std::integral_constant<int, 1>{});
  __builtin_amdgcn_sched_barrier(0);
  int tz = threadIdx.x;
  asm volatile("" : "+v"(tz));
  __builtin_amdgcn_sched_barrier(0);
  const int li = tz & 15, lq = (tz >> 4) & 3, ln = tz & 63;
  const __amdgpu_buffer_rsrc_t ra = __builtin_amdgcn_make_buffer_rsrc((void*)abuf, 0, 16 * 72 * 16384, 0x00020000);
  float* const cfin = (float*)(shm + AUX + 4096);
  float* const svr = cfin + 384;
  float* const tb = (float*)(shm + 2 * G1_STAGE + 4096 + wm * 1024);
  auto signal = [&](unsigned v) {
    asm volatile("" ::: "memory");
    if (ln == 0) __hip_atomic_fetch_add(cnt, v, __ATOMIC_RELAXED, __HIP_MEMORY_SCOPE_WORKGROUP);
    asm volatile("" ::: "memory");
  };
  auto await = [&](unsigned mask, unsigned val) {
    while ((__builtin_amdgcn_readfirstlane(__hip_atomic_load(cnt, __ATOMIC_RELAXED, __HIP_MEMORY_SCOPE_WORKGROUP)) & mask) != val) __builtin_amdgcn_s_sleep(1);
    asm volatile("" ::: "memory");
  };
  auto client = [&](int l) { const int m128 = mtile * 2 + ((l >> 4) & 1); return (((l & 7) * 4 + (m128 & 3)) * 8) + ((m128 >> 2) ^ ((l & 8) ? 5 : 0)); };
  auto notify = [&](int l0, int nl, int early = 0) {
    u64 now;
    asm volatile("s_memrealtime %0\n\ts_waitcnt lgkmcnt(0)" : "=s"(now) :: "memory");
    int l = threadIdx.x;
    asm volatile("" : "+v"(l));
    l &= 63;
    const __amdgpu_buffer_rsrc_t rd1 = __builtin_amdgcn_make_buffer_rsrc((void*)(done1 + early + ntile), 0, 256 * 512, 0x00020000);
    if (l < nl) __builtin_amdgcn_raw_buffer_store_b64(u32x2{(unsigned)now, (unsigned)(now >> 32)}, rd1, client(l0 + l) * 512, 0, 16);
  };
  const int xo = ((xcd & 1) << 1) | ((xcd >> 1) & 1), te = (xcd >> 2) * 28 + 6 * (xo >> 1) + (xo & 1), tl = (xcd >> 2) * 28 + 12 + 4 * xo;
  auto early = [&]() {
    unsigned old = 0;
    if (ln == 0) old = __hip_atomic_fetch_add(cnt + 4 + (wm >> 1), 1u, __ATOMIC_RELAXED, __HIP_MEMORY_SCOPE_WORKGROUP);
    if (__builtin_amdgcn_readfirstlane(old) == 1u) notify((wm >> 1) * 16 + 8 - (xcd & 4) * 2, 8, 32);
  };
  signal(1u << (8 * wc));
  if (wc) {
    await(0xffffu, 0x404u);
#pragma unroll
    for (int f = 0; f < 4; ++f) *(u32x4*)(shm + G2_X + wm * 4096 + f * 1024 + ln * 16) = xq[f];
    SB0();
    __builtin_amdgcn_s_setprio(1);
  }
  f32x4 rf[4];
#pragma unroll
  for (int mf = 0; mf < 4; ++mf) rf[mf] = *(const f32x4*)(shm + AUX + (wm * 64 + mf * 16 + li) * 16);
  const float* const cl = cfin + wc * 192 + lq * 4;
  const int sbase = ((mtile * 2 + (wm >> 1)) * 72 * 8 + (wm & 1) * 4) * 2048;
  auto putf = [&](int kg, int mf, u32x4 v) {
    __builtin_amdgcn_raw_buffer_store_b128(v, ra, ln * 16 + sbase + ((kg >> 1) * 8 + mf) * 2048 + (kg & 1) * 1024, 0, 16);
  };
  auto gelp = [&](f32x2 g, f32x2 u, f32x2 cg, f32x2 cu, f32x2 m2, f32x2 k12, f32x2 k22, f32x2 d2) {
    const f32x2 tg = __builtin_elementwise_fma(m2, cg, g), tu = __builtin_elementwise_fma(m2, cu, u);
    const f32x2 arg = tg * __builtin_elementwise_fma(tg * tg, k22, k12);
    const f32x2 dn = __builtin_elementwise_fma(f32x2{__builtin_amdgcn_exp2f(arg[0]), __builtin_amdgcn_exp2f(arg[1])}, d2, d2);
    const f32x2 r = (tg * tu) * f32x2{__builtin_amdgcn_rcpf(dn[0]), __builtin_amdgcn_rcpf(dn[1])};
    return pack2(r[0], r[1]);
  };
  auto gel2 = [&](int p, int kg, int mf0 = 0, int mf1 = 4) {
    f32x4 cg[2], cu[2];
#pragma unroll
    for (int j = 0; j < 2; ++j) { cg[j] = *(const f32x4*)(cl + p * 32 + j * 16); cu[j] = *(const f32x4*)(cl + p * 32 + 32 + j * 16); }
#pragma unroll
    for (int mf = mf0; mf < mf1; ++mf) {
      const f32x2 m2 = {rf[mf][0], rf[mf][0]}, k12 = {rf[mf][1], rf[mf][1]}, k22 = {rf[mf][2], rf[mf][2]}, d2 = {rf[mf][3], rf[mf][3]};
      u32x4 o;
#pragma unroll
      for (int i = 0; i < 4; ++i) {
        const int j = i >> 1, e = (i & 1) * 2;
        const f32x4 g = acc[mf][p * 2 + j], u = acc[mf][p * 2 + 2 + j];
        o[i] = gelp(f32x2{g[e], g[e + 1]}, f32x2{u[e], u[e + 1]}, f32x2{cg[j][e], cg[j][e + 1]}, f32x2{cu[j][e], cu[j][e + 1]}, m2, k12, k22, d2);
      }
      putf(kg, mf, o);
    }
  };
  int tsel = ntile & 1;
  asm volatile("" : "+s"(tsel));
  if (tsel != 0) {
    const int tq = (wc ? tl : te + 2 + (xo & 1)) + he;
    gel2(0, 2 * tq);
    gel2(3, 2 * tq + 1);
    const float cg = cfin[wc * 192 + 64 + li], cu = cfin[wc * 192 + 80 + li];
#pragma unroll
    for (int mf = 0; mf < 4; ++mf) {
      f32x4 t[4];
#pragma unroll
      for (int i = 0; i < 4; ++i) t[i] = *(const f32x4*)(tb + i * 64 + mf * 16 + lq * 4);
      const f32x4 g = acc[mf][4], u = acc[mf][5];
      u32x2 o;
#pragma unroll
      for (int e = 0; e < 4; e += 2)
        o[e >> 1] = gelp(f32x2{g[e], g[e + 1]}, f32x2{u[e], u[e + 1]}, f32x2{cg, cg}, f32x2{cu, cu}, f32x2{t[0][e], t[0][e + 1]},
                         f32x2{t[1][e], t[1][e + 1]}, f32x2{t[2][e], t[2][e + 1]}, f32x2{t[3][e], t[3][e + 1]});
      *(u32x2*)(shm + wm * 4096 + mf * 1024 + wc * 512 + li * 32 + lq * 8) = o;
    }
    if (wc == 0) {
      signal(0x10000u << wid);
      WAIT_V(0);
      early();
    } else {
      await(0x1000u << wid, 0x1000u << wid);
#pragma unroll
      for (int mf = 0; mf < 4; ++mf) putf(2 * (tl + 2) + he, mf, __builtin_bit_cast(u32x4, tr_frag(shm3 + wm * 4096 + mf * 1024 + ln * 8)));
    }
  } else {
    float rd[4];
#pragma unroll
    for (int mf = 0; mf < 4; ++mf) rd[mf] = svr[wm * 64 + mf * 16 + li];
    auto cvq = [&](int p, int mf, const f32x4& c0, const f32x4& c1) {
      u32x4 o;
#pragma unroll
      for (int j = 0; j < 2; ++j) {
        const f32x4 a = acc[mf][p * 2 + j], c4 = j ? c1 : c0;
        const f32x2 m2 = {rf[mf][0], rf[mf][0]}, r2 = {rd[mf], rd[mf]};
        f32x2 t0 = __builtin_elementwise_fma(m2, f32x2{c4[0], c4[1]}, f32x2{a[0], a[1]});
        f32x2 t1 = __builtin_elementwise_fma(m2, f32x2{c4[2], c4[3]}, f32x2{a[2], a[3]});
        if (p) { t0 *= r2; t1 *= r2; }
        o[2 * j] = pack2(t0[0], t0[1]); o[2 * j + 1] = pack2(t1[0], t1[1]);
      }
      return o;
    };
    const float cv0 = cfin[wc * 192 + 64 + li], cv1 = cfin[wc * 192 + 80 + li];
    auto cvv = [&](int mf) {
      const f32x4 n4 = *(const f32x4*)(tb + mf * 16 + lq * 4), r4 = *(const f32x4*)(svr + wm * 64 + mf * 16 + lq * 4);
      u32x4 o;
#pragma unroll
      for (int j = 0; j < 2; ++j) {
        const f32x4 a = acc[mf][4 + j];
        const f32x2 c2 = {j ? cv1 : cv0, j ? cv1 : cv0};
        const f32x2 t0 = __builtin_elementwise_fma(f32x2{n4[0], n4[1]}, c2, f32x2{a[0], a[1]}) * f32x2{r4[0], r4[1]};
        const f32x2 t1 = __builtin_elementwise_fma(f32x2{n4[2], n4[3]}, c2, f32x2{a[2], a[3]}) * f32x2{r4[2], r4[3]};
        o[2 * j] = pack2(t0[0], t0[1]); o[2 * j + 1] = pack2(t1[0], t1[1]);
      }
      return o;
    };
    u32x4 kx[4], vx[4], qx[4], hx[4];
    const bf16x8 ones = {0x3f80, 0x3f80, 0x3f80, 0x3f80, 0x3f80, 0x3f80, 0x3f80, 0x3f80};
    const f32x4 ck0 = *(const f32x4*)(cl + 32), ck1 = *(const f32x4*)(cl + 48);
    kx[2] = cvq(1, 2, ck0, ck1); kx[3] = cvq(1, 3, ck0, ck1);
    vx[2] = cvv(2); vx[3] = cvv(3);
    auto exo = [](int e) { return e * 4096 + (e >> 2) * (G2_A - 16384); };
    const bool needh = wm == 0 && mtile > 0;
    const __amdgpu_buffer_rsrc_t rh = __builtin_amdgcn_make_buffer_rsrc((void*)((char*)halo + ((needh ? mtile - 1 : mtile) * 32 + ntile + wc) * 4096), 0,
                                                                        needh || wm == 3 ? 4096 : 0, 0x00020000);
    if (wm < 3) {
      char* const ex = shm + exo(wc * 3 + wm) + ln * 16;
      *(u32x4*)ex = kx[2]; *(u32x4*)(ex + 1024) = kx[3]; *(u32x4*)(ex + 2048) = vx[2]; *(u32x4*)(ex + 3072) = vx[3];
      signal(0x10000u << wid);
    } else {
#pragma unroll
      for (int r = 0; r < 4; ++r) __builtin_amdgcn_raw_buffer_store_b128(r < 2 ? kx[2 + r] : vx[r], rh, ln * 16 + r * 1024, 0, 16);
    }
    auto hload = [&]() {
#pragma unroll
      for (int r = 0; r < 4; ++r) hx[r] = __builtin_bit_cast(u32x4, __builtin_amdgcn_raw_buffer_load_b128(rh, ln * 16 + r * 1024, 0, 16));
    };
    SB0();
    gel2(3, 2 * (wc ? tl + 3 : te) + he);
    SB0();
    if (wm == 0) hload();
    SB0();
    kx[0] = cvq(1, 0, ck0, ck1); kx[1] = cvq(1, 1, ck0, ck1);
    vx[0] = cvv(0); vx[1] = cvv(1);
    {
      const f32x4 cq0 = *(const f32x4*)(cl), cq1 = *(const f32x4*)(cl + 16);
#pragma unroll
      for (int mf = 0; mf < 4; ++mf) qx[mf] = cvq(0, mf, cq0, cq1);
    }
    f32x4 mk0, mk1;
#pragma unroll
    for (int j = 0; j < 4; ++j) {
      const int k0 = 4 * lq + j, k1 = 16 + 4 * lq + j;
      mk0[j] = (k0 >= li + 9 && k0 <= li + 16) ? 0.f : -1.0e30f;
      mk1[j] = (k1 >= li + 9 && k1 <= li + 16) ? 0.f : -1.0e30f;
    }
    auto max3 = [](float a, float b, float c) { float r; asm("v_max3_f32 %0, %1, %2, %3" : "=v"(r) : "v"(a), "v"(b), "v"(c)); return r; };
    auto max2 = [](float a, float b) { float r; asm("v_max_f32 %0, %1, %2" : "=v"(r) : "v"(a), "v"(b)); return r; };
    asm volatile("" : "+v"(kx[0]), "+v"(kx[1]), "+v"(vx[0]), "+v"(vx[1]), "+v"(qx[0]), "+v"(qx[1]), "+v"(qx[2]), "+v"(qx[3]), "+v"(mk0), "+v"(mk1));
    if (wc == 0) {
      if (wm == 0) WAIT_V(4); else WAIT_V(0);
      early();
    }
    if (wm > 0) {
      await(0x8000u << wid, 0x8000u << wid);
#pragma unroll
      for (int i = 0; i < 4; ++i) hx[i] = *(const u32x4*)(shm + exo(wc * 3 + wm - 1) + i * 1024 + ln * 16);
    } else if (__builtin_expect(cok || !needh, 1)) {
      while (__builtin_expect(blank(hx), 0)) {
        __builtin_amdgcn_s_sleep(1);
        asm volatile("" ::: "memory");
        hload();
      }
    } else {
      await(0xffffu, 0x404u);
      char* const fb = shm + exo(6 + wc) + ln * 16;
#pragma unroll 1
      for (int i = 0; i < 32; ++i) {
        const int rec = i >> 3, e = i & 7;
        const int row = m0 - 32 + 2 * (rec < 2 ? li : lq * 4 + (e & 3)) + (rec & 1);
        const int col = (rec < 2 ? D_ + lq * 4 + (e & 3) : 2 * D_ + li) + (ntile + wc) * 32 + (e >> 2) * 16;
        const u16* xr = x + (size_t)row * D_;
        const unsigned stt = row_stats_serial(xr);
        const float mb = lo_f(stt);
        float a = 0.f;
#pragma unroll 1
        for (int k = 0; k < D_; ++k) a += (bf2f(xr[k]) - mb) * bf2f(wqkv[(size_t)k * 3072 + col]);
        *(u16*)(fb + rec * 1024 + e * 2) = f2bf(a / hi_f(stt));
      }
#pragma unroll
      for (int i = 0; i < 4; ++i) hx[i] = *(const u32x4*)(fb + i * 1024);
    }
    {
      const f32x4 z = {0.f, 0.f, 0.f, 0.f};
      const float mz = mtile == 0 && wm == 0 ? -1.0e30f : 0.f;
      const f32x4 mkf = {fminf(mk0[0], mz), fminf(mk0[1], mz), fminf(mk0[2], mz), fminf(mk0[3], mz)};
      f32x4 s0[4], s1[4];
#pragma unroll
      for (int i = 0; i < 4; ++i) {
        const int n = i ^ 2;
        const bf16x8 qf = __builtin_bit_cast(bf16x8, qx[n]);
        s1[n] = __builtin_amdgcn_mfma_f32_16x16x32_bf16(__builtin_bit_cast(bf16x8, kx[n]), qf, mk1, 0, 0, 0);
        s0[n] = __builtin_amdgcn_mfma_f32_16x16x32_bf16(__builtin_bit_cast(bf16x8, n < 2 ? hx[n] : kx[n - 2]), qf, n < 2 ? mkf : mk0, 0, 0, 0);
      }
      SB0();
      asm volatile("s_nop 7\n\ts_nop 3" : "+v"(s0[0]), "+v"(s1[0]), "+v"(s0[1]), "+v"(s1[1]), "+v"(s0[2]), "+v"(s1[2]), "+v"(s0[3]), "+v"(s1[3]));
      float mx[4];
#pragma unroll
      for (int n = 0; n < 4; ++n) mx[n] = max3(max3(s0[n][0], s0[n][1], s0[n][2]), max3(s0[n][3], s1[n][0], s1[n][1]), max2(s1[n][2], s1[n][3]));
#pragma unroll
      for (int n = 0; n < 4; ++n) { auto r = __builtin_amdgcn_permlane16_swap(__float_as_uint(mx[n]), __float_as_uint(mx[n]), false, false);
        mx[n] = max2(__uint_as_float(r[0]), __uint_as_float(r[1])); }
#pragma unroll
      for (int n = 0; n < 4; ++n) { auto r = __builtin_amdgcn_permlane32_swap(__float_as_uint(mx[n]), __float_as_uint(mx[n]), false, false);
        mx[n] = max2(__uint_as_float(r[0]), __uint_as_float(r[1])); }
      SB0();
      constexpr float cs2 = 0.17677669529663687f * 1.4426950408889634f;
      f32x4 os[4], o0[4], o1[4];
#pragma unroll
      for (int n = 0; n < 4; ++n) {
        const float c = cs2 * rd[n], nm = -mx[n] * c;
        float e8[8];
#pragma unroll
        for (int j = 0; j < 4; ++j) {
          e8[j] = __builtin_amdgcn_exp2f(__builtin_fmaf(s0[n][j], c, nm));
          e8[4 + j] = __builtin_amdgcn_exp2f(__builtin_fmaf(s1[n][j], c, nm));
        }
        u32x4 pk;
#pragma unroll
        for (int j = 0; j < 4; ++j) pk[j] = pack2(e8[2 * j], e8[2 * j + 1]);
        const bf16x8 pf = __builtin_bit_cast(bf16x8, pk);
        const u32x4 vp = n < 2 ? hx[2 + n] : vx[n - 2];
        const u32x4 va = {vp[0], vp[1], vx[n][0], vx[n][1]}, vb = {vp[2], vp[3], vx[n][2], vx[n][3]};
        os[n] = __builtin_amdgcn_mfma_f32_16x16x32_bf16(ones, pf, z, 0, 0, 0);
        o0[n] = __builtin_amdgcn_mfma_f32_16x16x32_bf16(__builtin_bit_cast(bf16x8, va), pf, z, 0, 0, 0);
        o1[n] = __builtin_amdgcn_mfma_f32_16x16x32_bf16(__builtin_bit_cast(bf16x8, vb), pf, z, 0, 0, 0);
      }
#pragma unroll
      for (int n = 0; n < 4; ++n) {
        const float inv = __builtin_amdgcn_rcpf(os[n][0]);
        const f32x4 n0 = o0[n] * inv, n1 = o1[n] * inv;
        putf(112 + ntile + wc, n, u32x4{pack2(n0[0], n0[1]), pack2(n0[2], n0[3]), pack2(n1[0], n1[1]), pack2(n1[2], n1[3])});
      }
    }
  }
  TS1(2);
  const bool tya1 = tya;
  {
  __builtin_amdgcn_sched_barrier(0);
  int tid = threadIdx.x;
  asm volatile("" : "+v"(tid));
  __builtin_amdgcn_sched_barrier(0);
  const int lane = tid & 63;
  const int wm = wid >> 1, wn = wid & 1;
  const int m0 = mt2 * 128, n0 = nt2 * 128;
  constexpr int PD = G2_NSTAGE;
  const int ny = (xcd & 1) ? NY - DLT : NY + DLT;
  TS(0);
  const unsigned va = lane * 16;
  auto stage_y = [&]() {
    asm volatile("s_add_u32 m0, %[l], %[oa]\n\ts_nop 0\n\tglobal_load_lds_dwordx4 %[va], %[a] sc1\n\tglobal_load_lds_dwordx4 %[va], %[a] offset:1024 sc1\n\t"
                 "global_load_lds_dwordx4 %[va], %[a] offset:2048 sc1\n\tglobal_load_lds_dwordx4 %[va], %[a] offset:3072 sc1\n\t" G2_W
                 :: [l] "s"(sl), [oa] "s"(shm_base + G2_A + (p ^ role) * 4096), [a] "s"(sa), [va] "v"(va), G2_WOP(wy) : "memory", "scc", "m0");
    ++st;
    sa += st == NY ? (long)((1 - 2 * role) * NY * 16384) : 16384l;
    sl = (sl + 16384) & 65535;
    wsel();
  };
  u64 ready = 0;
  const u64* const mbox = &done1[bid * 64 + lane];
  auto seen = [&](u64 v) { const u64 r = __ballot(fresh(v, t_own)); ready |= r | (r << 32); };
  auto need = [&](u64 mask, bool renew) {
    while ((ready & mask) != mask) {
      seen(__hip_atomic_load(mbox, RLX_AGENT));
      if ((ready & mask) != mask) {
        __builtin_amdgcn_s_sleep(PSLEEP);
        if (renew && !cok) notify(0, 32);
      }
    }
    asm volatile("" ::: "memory");
  };
  u64 fl = 0;
  auto peek = [&]() { asm volatile("global_load_dwordx2 %0, %1, %2 sc1" : "+v"(fl) : "v"(va >> 1), "s"(done1 + bid * 64) : "memory"); };
  auto check = [&](u64 mask, bool renew) {
    asm volatile("" : "+v"(fl));
    seen(fl);
    if (__builtin_expect((ready & mask) != mask, 0)) need(mask, renew);
  };
  if (wid >= 4) {
    TS(4);
    __builtin_amdgcn_s_setprio(0);
    BARL();
    stage_y();
    stage_y();
    BAR();
    if (p == 1) peek();
    stage_y();
    WAIT_V(16);
    BAR();
    if (p == 0) notify(0, 32);
    stage_y();
    const char* sz;
    int ws0, ws1;
    u64 cm = 0xa0a0ull << (32 + 16 * role), cn = 0x0f0full << (16 * role);
    const unsigned evc = p == 1 ? 0x8088u : 0u, evp = p == 1 ? 0x2020u : 0u;
#pragma unroll 1
    for (int g = 3; g < ny - PD; ++g) {
      WAIT_V(16);
      BAR();
      if ((evc >> g) & 1) { check(cm, true); cm = cn; cn = (0xffffull << (16 * role)) | 0x55555555ull; }
      if ((evp >> g) & 1) peek();
      if (g == 9 + p) {
        sz = (const char*)abuf + (size_t)(mt2 * 72 + 56) * 16384 + (p >> 1) * 8192 + role * 4096 + (p & 1) * 2048;
        ws0 = __builtin_amdgcn_readfirstlane((wz.vb[0][1] ^ 64) - wz.vb[0][0]) * 16; ws1 = __builtin_amdgcn_readfirstlane((wz.vb[1][1] ^ 64) - wz.vb[1][0]) * 16;
        PIN_S(sz); PIN_S(ws0); PIN_S(ws1); PIN_S(wz.wb[0]); PIN_S(wz.wb[1]);
      }
      stage_y();
    }
    WAIT_V(16);
    BAR();
    sa = sz; sw[0] = wz.wb[0]; sw[1] = wz.wb[1];
    auto stage_s = [&]() {
      asm volatile("s_add_u32 m0, %[l], %[oa]\n\ts_nop 0\n\tglobal_load_lds_dwordx4 %[va], %[a] sc1\n\tglobal_load_lds_dwordx4 %[va], %[a] offset:1024 sc1\n\t" G2_W
                   :: [l] "s"(sl), [oa] "s"(shm_base + G2_A + (p >> 1) * 8192 + (p & 1) * 2048), [a] "s"(sa), [va] "v"(va), G2_WOP(wz) : "memory", "scc", "m0");
      sa += 16384; sw[0] += ws0; sw[1] += ws1; sl = (sl + 16384) & 65535;
    };
    stage_s();
#pragma unroll
    for (int j = -3; j < 15; ++j) {
      WAIT_V((j == -3 ? 2 : 0) + (j < 13 ? 12 : (14 - j) * 6) + (j > GETJ && j < GETJ + 4 ? 2 : 0) + (j > GETJ + 1 && j < GETJ + 5 ? 2 : 0));
      BAR();
      if (j < 12) stage_s();
      if (j == GETJ || j == GETJ + 1)
        asm volatile("s_mov_b32 m0, %[l]\n\ts_nop 0\n\tglobal_load_lds_dwordx4 %[va], %[a] offset:%[o] sc1\n\tglobal_load_lds_dwordx4 %[va], %[a] offset:%[o]+1024 sc1"
                     :: [l] "s"(shm_base + G2_X + 16384 + p * 4096), [a] "s"((const char*)slab + (size_t)(bid ^ 5) * 16384 + p * 4096), [va] "v"(va),
                        [o] "n"((j - GETJ) * 2048) : "memory", "m0");
    }
    asm volatile("" : "+v"(fl));
    TS(5);
    if (tid < 258) __hip_atomic_store(lane ? done2 + bid * 8 : &acks[bid], __builtin_amdgcn_s_memrealtime(), RLX_AGENT);
    if (__builtin_expect(!cok, 0) && p == 0) {
      const bool mine = lane == 32 || (lane < 32 && (tya1 || ((lane >> 3) & 1) != (xcd >> 2)));
      const u64* const pa = &acks[lane < 32 ? client(lane) : bid ^ 5];
      while (!__all(!mine || fresh(__hip_atomic_load(pa, RLX_AGENT), t_own))) {
        notify(0, 32);
        if (lane == 0) __hip_atomic_store(done2 + bid * 8, __builtin_amdgcn_s_memrealtime(), RLX_AGENT);
        __builtin_amdgcn_s_sleep(32);
      }
    }
  } else {
    TS(6);
    f32x4 acc[4][4], sacc[2][4];
    const int fr = lane & 15, fq = lane >> 4, q4 = fr >> 2, pp = lane & 3;
    const int ff = ((q4 >> 1) & 1) | ((fq & 1) << 1);
    int aof = G2_A + wm * 8192 + lane * 16;
    int boff[4];
#pragma unroll
    for (int j = 0; j < 4; ++j) boff[j] = wn * 8192 + (8 * fq + q4) * 128 + ((j ^ ff) * 32) + pp * 8;
    int vo = wid * 4096 + lane * 16;
    int vx = ((m0 + wm * 64 + role * 32 + 2 * fr) * D_ + n0 + wn * 64) * 2 + (fq & 1) * 32 + (fq >> 1) * 16;
    lds_cptr px = shm3 + G2_X + wid * 4096 + (fq >> 1) * 512 + fr * 16 + (fq & 1) * 8;
    asm volatile("" : "+v"(vo), "+v"(vx), "+v"(px));
#define SB() __builtin_amdgcn_sched_barrier(0)
    struct Fr { bf16x8 a[4]; bf16x4 bl[4], bh[4]; };
    Fr fa[2], fb[2];
    auto rd = [&](Fr* f, int odd, int t) {
      constexpr int ord[12] = {0, 1, 8, 2, 3, 4, 5, 6, 7, 9, 10, 11};
      constexpr int seq[24] = {0, 1, 2, 12, 13, 14, 15, 16, 3, 4, 5, 6, 17, 18, 19, 20, 7, 8, 9, 21, 10, 22, 11, 23};
      lds_cptr sb = shm3 + odd * 16384;
      const int ks = seq[t] / 12, o = ord[seq[t] % 12];
      if (o >= 8) f[ks].a[o - 8] = *(const __attribute__((address_space(3))) bf16x8*)(sb + aof + (o - 8) * 2048 + ks * 1024);
      else if (o & 1) f[ks].bh[o >> 1] = tr_read(sb + boff[o >> 1] + ks * 4096 + 512);
      else f[ks].bl[o >> 1] = tr_read(sb + boff[o >> 1] + ks * 4096);
    };
    auto bfr = [&](const Fr& c, int j) { return __builtin_shufflevector(c.bl[j], c.bh[j], 0, 1, 2, 3, 4, 5, 6, 7); };
    auto mf1 = [](f32x4& c, bf16x8 b, bf16x8 a, bool z = false) {
      if (z) asm volatile("v_mfma_f32_16x16x32_bf16 %0, %1, %2, 0" : "=&v"(c) : "v"(b), "v"(a));
      else asm volatile("v_mfma_f32_16x16x32_bf16 %0, %1, %2, %0" : "+v"(c) : "v"(b), "v"(a));
    };
    auto mf2 = [](f32x4& c, bf16x8 b, bf16x8 a, bf16x8 b2, bf16x8 a2, bool z) {
      if (z) asm volatile("v_mfma_f32_16x16x32_bf16 %0, %1, %2, 0\n\tv_mfma_f32_16x16x32_bf16 %0, %3, %4, %0" : "=&v"(c) : "v"(b), "v"(a), "v"(b2), "v"(a2));
      else asm volatile("v_mfma_f32_16x16x32_bf16 %0, %1, %2, %0\n\tv_mfma_f32_16x16x32_bf16 %0, %3, %4, %0" : "+v"(c) : "v"(b), "v"(a), "v"(b2), "v"(a2));
    };
    auto pairs = [&](f32x4 (*ac)[4], const Fr* c, int nq, int mx, int nf, auto fill, bool z = false) {
#pragma unroll
      for (int q = 0; q < nq; ++q) {
        const int mf = (q >> 2) ^ mx, j = (q & 4) ? 3 - (q & 3) : q & 3, ks = q & 1;
        SB();
        if (2 * q >= nf) mf2(ac[mf][j], bfr(c[ks], j), c[ks].a[mf], bfr(c[ks ^ 1], j), c[ks ^ 1].a[mf], z);
        else {
          mf1(ac[mf][j], bfr(c[ks], j), c[ks].a[mf], z);
          SB();
          fill(2 * q);
          SB();
          mf1(ac[mf][j], bfr(c[ks ^ 1], j), c[ks ^ 1].a[mf]);
          SB();
          fill(2 * q + 1);
        }
      }
      SB();
    };
    auto flip = [&]() {
      aof ^= 32768; boff[0] ^= 32768; boff[1] ^= 32768; boff[2] ^= 32768; boff[3] ^= 32768;
      asm volatile("" : "+v"(aof), "+v"(boff[0]), "+v"(boff[1]), "+v"(boff[2]), "+v"(boff[3]));
    };
    auto bar = [&]() { __builtin_amdgcn_s_waitcnt(0xC07F); BAR(); };
    auto ystg = [&](const Fr* c, Fr* n, int odd, bool z = false) { pairs(acc, c, 16, 0, 24, [&](int t) { rd(n, odd, t); }, z); };
    auto stg = [&](const Fr* c, Fr* n, int odd, bool z = false) { pairs(sacc, c, 8, 0, 10, [&](int t) { rd(n, odd, 2 * t); rd(n, odd, 2 * t + 1); }, z); };
    asm volatile("" : "+v"(aof), "+v"(boff[0]), "+v"(boff[1]), "+v"(boff[2]), "+v"(boff[3]));
    await(0xffffu, 0x404u);
    auto lda = [&](int i) {
      asm volatile("s_mov_b32 m0, %[l]\n\ts_nop 0\n\tglobal_load_lds_dwordx4 %[va], %[a] sc1\n\tglobal_load_lds_dwordx4 %[va], %[a] offset:1024 sc1\n\t"
                   "global_load_lds_dwordx4 %[va], %[a] offset:2048 sc1\n\tglobal_load_lds_dwordx4 %[va], %[a] offset:3072 sc1"
                   :: [l] "s"(shm_base + G2_A + (i + 1) * 16384 + (p ^ role) * 4096), [a] "s"(sa + i * 16384), [va] "v"(va) : "memory", "m0");
    };
    need(0x0505ull << (32 + 16 * role), false);
    TS(4);
    lda(0);
    lda(1);
    need(0x0f0full << (32 + 16 * role), false);
    lda(2);
    WAIT_V(8);
    signal(1u << 24);
    await(0xff000000u, 4u << 24);
    __builtin_amdgcn_s_waitcnt(0xC07F);
    SB();
#pragma unroll
    for (int t = 0; t < 24; ++t) rd(fa, 1, t);
    flip();
#define CPRIO 0
    __builtin_amdgcn_s_setprio(CPRIO);
    WAIT_V(4);
    TS(5);
    peek();
    bar();
    TS(7);
    ystg(fa, fb, 0, true);
    WAIT_V(1);
    bar();
    ystg(fb, fa, 1);
    WAIT_V(0);
    check(0x5050ull << (32 + 16 * role), false);
    static_assert((NY - DLT) % 2 == 1, "");
#pragma unroll 1
    for (int g = 2; g < ny - 1; g += 2) {
      flip();
      bar();
      ystg(fa, fb, 0);
      bar();
      ystg(fb, fa, 1);
    }
    flip();
    bar();
    {
      const __amdgpu_buffer_rsrc_t rs = __builtin_amdgcn_make_buffer_rsrc((void*)((char*)slab + (size_t)bid * 16384), 0, 16384, 0x00020000);
      u32x4 pk;
      pairs(acc, fa, 16, 2, 32, [&](int t) {
        if (t < 20) rd(fb, 0, t);
        else {
          const int f = (t - 20) / 3, e = (t - 20) % 3;
          if (e < 2) { const f32x4 a = acc[2 + (f >> 1)][(f & 1) * 2 + e]; pk[2 * e] = pack2(a[0], a[1]); pk[2 * e + 1] = pack2(a[2], a[3]); }
          else __builtin_amdgcn_raw_buffer_store_b128(pk, rs, vo + f * 1024, 0, 16);
        }
      });
    }
    TS(2);
    static_assert(GETJ % 2 == 1 && GETJ >= 3 && GETJ <= 7, "");
    f32x4 xf[2][4];
#pragma unroll
    for (int j = 0; j < GETJ + 5; j += 2) {
      bar();
      stg(fb, fa, 1, j == 0);
      flip();
      bar();
      u32x2 xv[2];
      pairs(sacc, fa, 8, 0, 12, [&](int t) {
        if (t < 10) { rd(fb, 0, 2 * t); rd(fb, 0, 2 * t + 1); }
        else if (j < 8) xv[t - 10] = *(const __attribute__((address_space(3))) u32x2*)(px + (j >> 1) * 1024 + (t - 10) * 256);
      });
      if (j < 8) {
#pragma unroll
        for (int e = 0; e < 2; ++e) xf[j >> 2][(j & 2) + e] = f32x4{lo_f(xv[e][0]), hi_f(xv[e][0]), lo_f(xv[e][1]), hi_f(xv[e][1])};
        asm volatile("" : "+v"(xf[j >> 2][j & 2]), "+v"(xf[j >> 2][(j & 2) + 1]));
        SB();
      }
    }
    WAIT_V(0);
    bar();
    u32x4 pv[4];
    {
      lds_cptr pp_ = shm3 + G2_X + 16384 + vo;
      pairs(sacc, fb, 8, 0, 12, [&](int t) {
        if (t < 2) {
#pragma unroll
          for (int f = 2 * t; f < 2 * t + 2; ++f) pv[f] = *(const __attribute__((address_space(3))) u32x4*)(pp_ + f * 1024);
        } else { rd(fa, 1, 2 * t - 4); rd(fa, 1, 2 * t - 3); }
      });
      if (__builtin_expect(blank(pv) || !cok, 0)) {
        const __amdgpu_buffer_rsrc_t rp = __builtin_amdgcn_make_buffer_rsrc((void*)((char*)slab + (size_t)(bid ^ 5) * 16384), 0, 16384, 0x00020000);
        while (!cok && !__all(fresh(__hip_atomic_load(done2 + (bid ^ 5) * 8, RLX_AGENT), t_own))) {
          notify(0, 32);
          if (lane == 0) __hip_atomic_store(done2 + bid * 8, __builtin_amdgcn_s_memrealtime(), RLX_AGENT);
          __builtin_amdgcn_s_sleep(8);
        }
        do {
          asm volatile("" ::: "memory");
#pragma unroll
          for (int f = 0; f < 4; ++f) pv[f] = __builtin_bit_cast(u32x4, __builtin_amdgcn_raw_buffer_load_b128(rp, vo + f * 1024, 0, 16));
        } while (blank(pv));
      }
    }
    auto silu = [&](int f0, int f1) {
#pragma unroll
      for (int f = f0; f < f1; ++f) {
        const int mf = f >> 2, j = f & 3;
        const u32x4 pw = pv[mf * 2 + (j >> 1)];
        const f32x4 ya = acc[mf][j] + f32x4{lo_f(pw[2 * (j & 1)]), hi_f(pw[2 * (j & 1)]), lo_f(pw[2 * (j & 1) + 1]), hi_f(pw[2 * (j & 1) + 1])};
#pragma unroll
        for (int e = 0; e < 2; ++e) {
          const f32x2 y2 = {ya[2 * e], ya[2 * e + 1]};
          const f32x2 ar = y2 * f32x2{-1.4426950408889634f, -1.4426950408889634f};
          const f32x2 dn = f32x2{__builtin_amdgcn_exp2f(ar[0]), __builtin_amdgcn_exp2f(ar[1])} + f32x2{1.0f, 1.0f};
          const f32x2 t = y2 * f32x2{__builtin_amdgcn_rcpf(dn[0]), __builtin_amdgcn_rcpf(dn[1])};
          acc[mf][j][2 * e] = t[0]; acc[mf][j][2 * e + 1] = t[1];
        }
        asm volatile("" : "+v"(acc[mf][j]));
      }
      SB();
    };
#define SPT 0, 1, 2, 3, 5, 7, 8, 8
    constexpr int spt[] = {SPT};
    silu(0, spt[1]);
#pragma unroll
    for (int j = GETJ + 6; j < 15; ++j) {
      if (j & 1) flip();
      bar();
      if (j & 1) stg(fa, fb, 0); else stg(fb, fa, 1);
      silu(spt[j - GETJ - 5], j == 14 ? 8 : spt[j - GETJ - 4]);
    }
    pairs(sacc, fa, 8, 0, 0, [](int) {});
    asm volatile("s_nop 7\n\ts_nop 3" : "+v"(sacc[1][0]), "+v"(sacc[1][1]), "+v"(sacc[1][2]), "+v"(sacc[1][3]));
    SB();
    TS(1);
    {
      const __amdgpu_buffer_rsrc_t ro = __builtin_amdgcn_make_buffer_rsrc((void*)out, 0, S_ * D_ * 2, 0x00020000);
#pragma unroll
      for (int f = 0; f < 4; ++f) {
        const int mf = f >> 1, j = (f & 1) * 2;
        const f32x4 a = __builtin_elementwise_fma(sacc[mf][j], acc[mf][j], xf[mf][j]), b = __builtin_elementwise_fma(sacc[mf][j + 1], acc[mf][j + 1], xf[mf][j + 1]);
        const auto r0 = __builtin_amdgcn_permlane16_swap(pack2(a[0], a[1]), pack2(b[0], b[1]), false, false);
        const auto r1 = __builtin_amdgcn_permlane16_swap(pack2(a[2], a[3]), pack2(b[2], b[3]), false, false);
        __builtin_amdgcn_raw_buffer_store_b128(u32x4{r0[0], r1[0], r0[1], r1[1]}, ro, vx + mf * 2048 + j * 32, 0, 2);
      }
    }
    TS(3);
  }
  TSFLUSH();
  }
  asm volatile(".Lcode_end:");
}

extern "C" void kernel_launch(void* const* d_in, const int* in_sizes, int n_in, void* d_out, int out_size, void* d_ws,
                              size_t ws_size, hipStream_t stream) {
  const u16* A_x = (const u16*)d_in[0];
  const u16* A_wqkv = (const u16*)d_in[1];
  const u16* A_wo = (const u16*)d_in[2];
  const u16* B_wg = (const u16*)d_in[4];
  const u16* B_wu = (const u16*)d_in[5];
  const u16* B_wd = (const u16*)d_in[6];
  u16* out = (u16*)d_out;
  fused_kernel<<<256, 512, 0, stream>>>(A_x, A_wqkv, B_wg, B_wu, B_wd, A_wo, out, (char*)d_ws);
}
```

```cpp
#include <hip/hip_runtime.h>
#include <hip/hip_bf16.h>
#include <stdint.h>
#include <type_traits>

typedef unsigned short u16;
using bf16x8 = __attribute__((ext_vector_type(8))) short;
using bf16x4 = __attribute__((ext_vector_type(4))) short;
using f32x4 = __attribute__((ext_vector_type(4))) float;
using u32x2 = __attribute__((ext_vector_type(2))) unsigned;
using u32x4 = __attribute__((ext_vector_type(4))) unsigned;
typedef __attribute__((address_space(3))) const char* lds_cptr;

constexpr int S_ = 2048, D_ = 1024, F_ = 3584;

__device__ __forceinline__ float bf2f(u16 h) { return __uint_as_float((unsigned)h << 16); }
__device__ __forceinline__ u16 f2bf(float f) {
  unsigned u = __float_as_uint(f);
  u += 0x7fffu + ((u >> 16) & 1u);
  return (u16)(u >> 16);
}
__device__ __forceinline__ float rbf(float f) { return bf2f(f2bf(f)); }
typedef __attribute__((ext_vector_type(2))) __bf16 bf16x2_t;
typedef __attribute__((ext_vector_type(2))) float f32x2;
__device__ __forceinline__ unsigned pack2(float a, float b) {
  bf16x2_t r = __builtin_convertvector((f32x2){a, b}, bf16x2_t);
  return __builtin_bit_cast(unsigned, r);
}
__device__ __forceinline__ float lo_f(unsigned p) { return __uint_as_float(p << 16); }
__device__ __forceinline__ float hi_f(unsigned p) { return __uint_as_float(p & 0xffff0000u); }

#define TSDECL
#define TSS(i) do {} while (0)
#define TSFLUSH() do {} while (0)
#define TS1(i) TSS(i)
#define TS(i) TSS(8 + (i))
#define PIN_S(v) asm volatile("" : "+s"(v))
#define WAIT_V(n) asm volatile("s_waitcnt vmcnt(%0)" ::"n"(n) : "memory")
#define WAIT_L(n) asm volatile("s_waitcnt lgkmcnt(%0)" ::"n"(n) : "memory")
#define BARL() do { WAIT_L(0); BAR(); } while (0)
#define BAR() do { asm volatile("" ::: "memory"); __builtin_amdgcn_s_barrier(); asm volatile("" ::: "memory"); } while (0)

__device__ __forceinline__ bf16x4 tr_read(lds_cptr p) {
  return __builtin_amdgcn_ds_read_tr16_b64_v4i16((__attribute__((address_space(3))) bf16x4*)p);
}
__device__ __forceinline__ bf16x8 tr_frag(lds_cptr p) {
  bf16x4 lo = tr_read(p), hi = tr_read(p + 512);
  bf16x8 r;
  r[0] = lo[0]; r[1] = lo[1]; r[2] = lo[2]; r[3] = lo[3];
  r[4] = hi[0]; r[5] = hi[1]; r[6] = hi[2]; r[7] = hi[3];
  return r;
}
__device__ __forceinline__ void glds16(const void* gsrc, unsigned lds_dst) {
  unsigned keep;
  asm volatile("s_mov_b32 %0, m0\n\ts_mov_b32 m0, %2\n\ts_nop 0\n\tglobal_load_lds_dwordx4 %1, off\n\ts_mov_b32 m0, %0"
               : "=&s"(keep) : "v"(gsrc), "s"(lds_dst) : "memory");
}
#define GLDS(gptr, laddr) glds16((const void*)(gptr), (unsigned)(laddr))
__device__ __forceinline__ void glds16_sc1(const void* gsrc, unsigned lds_dst) {
  unsigned keep;
  asm volatile("s_mov_b32 %0, m0\n\ts_mov_b32 m0, %2\n\ts_nop 0\n\tglobal_load_lds_dwordx4 %1, off sc1\n\ts_mov_b32 m0, %0"
               : "=&s"(keep) : "v"(gsrc), "s"(lds_dst) : "memory");
}
#define GLDS_SC1(gptr, laddr) glds16_sc1((const void*)(gptr), (unsigned)(laddr))
__device__ __forceinline__ void glds16_nc(const void* gsrc, unsigned lds_dst) {
  unsigned keep;
  asm volatile("s_mov_b32 %0, m0\n\ts_mov_b32 m0, %2\n\ts_nop 0\n\tglobal_load_lds_dwordx4 %1, off\n\ts_mov_b32 m0, %0"
               : "=&s"(keep) : "v"(gsrc), "s"(lds_dst));
}
#define GLDS_NC(gptr, laddr) glds16_nc((const void*)(gptr), (unsigned)(laddr))
__device__ __forceinline__ void glds16_s(const void* sbase, unsigned voff, unsigned lds_dst) {
  asm volatile("s_mov_b32 m0, %2\n\ts_nop 0\n\tglobal_load_lds_dwordx4 %0, %1" :: "v"(voff), "s"(sbase), "s"(lds_dst) : "m0");
}

__device__ __forceinline__ void glds16_s1(const void* sbase, unsigned voff, unsigned lds_dst) {
  asm volatile("s_mov_b32 m0, %2\n\ts_nop 0\n\tglobal_load_lds_dwordx4 %0, %1 sc1" :: "v"(voff), "s"(sbase), "s"(lds_dst) : "memory", "m0");
}
__device__ __forceinline__ void glds16_v(const void* p, unsigned lds_dst) {
  asm volatile("s_mov_b32 m0, %1\n\ts_nop 0\n\tglobal_load_lds_dwordx4 %0, off" :: "v"(p), "s"(lds_dst) : "m0");
}
template <int OFF = 0>
__device__ __forceinline__ void gst16(const void* sbase, unsigned voff, u32x4 v) {
  asm volatile("global_store_dwordx4 %0, %1, %2 offset:%3 sc1\n\ts_nop 1" :: "v"(voff), "v"(v), "s"(sbase), "n"(OFF) : "memory");
}

typedef unsigned long long u64;
__device__ __forceinline__ bool fresh(u64 stamp, u64 t_own) {
  const u64 b = t_own + 100ull;
  const unsigned lo = (unsigned)stamp - (unsigned)b, hi = (unsigned)(stamp >> 32) - (unsigned)(b >> 32) - ((unsigned)stamp < (unsigned)b ? 1u : 0u);
  return (hi == 0u) & (lo < (unsigned)(__builtin_amdgcn_s_memrealtime() - t_own));
}
constexpr u64 TIMEOUT_ = 200000;
__device__ __forceinline__ float half_sum(float v) {
  v += __builtin_bit_cast(float, __builtin_amdgcn_update_dpp(0, __builtin_bit_cast(int, v), 0xB1, 0xf, 0xf, false));
  v += __builtin_bit_cast(float, __builtin_amdgcn_update_dpp(0, __builtin_bit_cast(int, v), 0x4E, 0xf, 0xf, false));
  v += __builtin_bit_cast(float, __builtin_amdgcn_update_dpp(0, __builtin_bit_cast(int, v), 0x141, 0xf, 0xf, false));
  v += __builtin_bit_cast(float, __builtin_amdgcn_update_dpp(0, __builtin_bit_cast(int, v), 0x140, 0xf, 0xf, false));
  { auto r = __builtin_amdgcn_permlane16_swap(__float_as_uint(v), __float_as_uint(v), false, false); v = __uint_as_float(r[0]) + __uint_as_float(r[1]); }
  return v;
}
__device__ __forceinline__ unsigned pack_stats(float mean, float var) {
  return (unsigned)f2bf(mean) | ((unsigned)f2bf(sqrtf(rbf(rbf(var) + 1e-5f))) << 16);
}
__device__ __forceinline__ unsigned row_stats_serial(const u16* xr) {
  float s = 0.f, q = 0.f;
#pragma unroll 1
  for (int k = 0; k < D_; ++k) s += bf2f(xr[k]);
  const float mean = s * (1.0f / D_);
#pragma unroll 1
  for (int k = 0; k < D_; ++k) { const float d = bf2f(xr[k]) - mean; q += d * d; }
  return pack_stats(mean, q * (1.0f / D_));
}
constexpr int G1_STAGE = 32768 + 5 * 8192;
#define EPOS 2
#define DLT 1
#define PSLEEP 2
#define GETJ 3

constexpr unsigned BLANK = 0x7f817f81u;
__device__ __forceinline__ bool blank(const u32x4* pv) {
  u64 m;
#define CX(i) "v_cmpx_ne_u32 vcc, %1, %" #i "\n\t"
  asm volatile(CX(2) CX(3) CX(4) CX(5) CX(6) CX(7) CX(8) CX(9) CX(10) CX(11) CX(12) CX(13) CX(14) CX(15) CX(16) CX(17)
               "s_mov_b64 %0, exec\n\ts_mov_b64 exec, -1\n\ts_nop 3" : "=s"(m) : "s"(BLANK), "v"(pv[0][0]), "v"(pv[0][1]), "v"(pv[0][2]), "v"(pv[0][3]),
               "v"(pv[1][0]), "v"(pv[1][1]), "v"(pv[1][2]), "v"(pv[1][3]), "v"(pv[2][0]), "v"(pv[2][1]), "v"(pv[2][2]), "v"(pv[2][3]),
               "v"(pv[3][0]), "v"(pv[3][1]), "v"(pv[3][2]), "v"(pv[3][3]) : "vcc");
  return m != ~0ull;
}
#define RLX_AGENT __ATOMIC_RELAXED, __HIP_MEMORY_SCOPE_AGENT

constexpr int G2_A = 65536, G2_X = 131072;
constexpr int G2_NSTAGE = 4;
constexpr size_t MB_ = 1024 * 1024;
__global__ __launch_bounds__(512, 2) void fused_kernel(const u16* __restrict__ x, const u16* __restrict__ wqkv,
                                                       const u16* __restrict__ wg, const u16* __restrict__ wu,
                                                       const u16* __restrict__ wd, const u16* __restrict__ wo,
                                                       u16* __restrict__ out, char* __restrict__ ws) {
  u16* const halo = (u16*)(ws + 4 * MB_);
  u16* const abuf = (u16*)(ws + 16 * MB_);
  char* const slab = ws + 64 * MB_;
  unsigned* const flags = (unsigned*)(ws + 96 * MB_);
  u32x4* const rstat = (u32x4*)(ws + 97 * MB_);
  char* const wcopy = ws + 98 * MB_;
  u64* const done1 = (u64*)(ws + 102 * MB_);
  u64* const done2 = (u64*)(ws + 100 * MB_ + 65536);
  u64* const donec = (u64*)(ws + 100 * MB_ + 131072);
  u64* const acks = (u64*)(ws + 100 * MB_ + 196608);
  float* const csum = (float*)(ws + 101 * MB_);
  __shared__ __attribute__((aligned(1024))) char shm[163840];
  constexpr int AUX = 2 * G1_STAGE + 8192;
  {
    u64 pc; int len;
    asm volatile("s_getpc_b64 %0\n.Lcode_beg:\n\ts_mov_b32 %1, .Lcode_end-.Lcode_beg" : "=s"(pc), "=s"(len));
    const int ol = ((blockIdx.x >> 3) + 32 * (threadIdx.x >> 6)) * 1024 + (threadIdx.x & 63) * 16;
    asm volatile("s_mov_b32 m0, %2\n\ts_nop 0\n\tglobal_load_lds_dwordx4 %0, %1"
                 :: "v"(ol < len - 16 ? ol : 0), "s"(pc & ~15ull), "s"((unsigned)(uintptr_t)(lds_cptr)shm + 2 * G1_STAGE + 7168) : "m0");
    __builtin_amdgcn_sched_barrier(0);
  }
  const u64 t_own = __builtin_amdgcn_s_memrealtime();
  TSDECL;
  TS1(7);
  const int tid = threadIdx.x, lane = tid & 63;
  const int wid = __builtin_amdgcn_readfirstlane(tid >> 6);
  const int wm = wid & 3, wc = wid >> 2;
  const int bid = blockIdx.x, xcd = bid & 7, idx = bid >> 3;
  const int ntile = xcd * 4 + (idx & 3), mtile = idx >> 2;
  const int m0 = mtile * 256;
  const int hg = ntile >> 1;
  const bool tya = (ntile & 1) == 0;
  const int srow = m0 + ntile * 8 + wm * 2;
  lds_cptr shm3 = (lds_cptr)shm;
  const unsigned shm_base = (unsigned)(uintptr_t)shm3;
  const int r8 = tid >> 3, pos = tid & 7;
  const int phys = pos >> 1, half = pos & 1;
  const int fk = ((r8 >> 1) & 1) | (((r8 >> 3) & 1) << 1);
  const int vsrc = phys ^ fk;
  const int qstr = tya ? 3072 * 2 : F_ * 2;
  unsigned voa = (((r8 & 32) + 2 * (r8 & 15) + ((r8 >> 4) & 1)) * D_ + ((pos ^ (r8 & 7)) * 8)) * 2;
  unsigned vog = r8 * (F_ * 2) + vsrc * 32 + half * 16, voq = r8 * qstr + vsrc * 32 + half * 16;
  unsigned vom = r8 * qstr + (vsrc >> (tya ? 0 : 1)) * 32 + half * 16;
  asm volatile("" : "+v"(voa), "+v"(vog), "+v"(voq), "+v"(vom));
  __builtin_amdgcn_sched_barrier(0);
  asm volatile("" :: "s"(x), "s"(wqkv), "s"(wg), "s"(wu), "s"(wd), "s"(wo), "s"(out), "s"(ws));
  const char* const ab = (const char*)(x + (size_t)m0 * D_);
  int kz = 0;
  asm volatile("" : "+s"(kz));
#pragma unroll
  for (int i = 0; i < 4; ++i) glds16_s(ab + i * 64 * D_ * 2, voa, shm_base + wid * 1024 + i * 8192);
  const int kt0 = (hg >> 1) * 7, he = hg & 1;
  const char* const gb = (const char*)(wg + (kt0 + (tya ? 5 + he : 2 * he + 1)) * 64);
  const long dgu = (const char*)wu - (const char*)wg, dp1 = tya ? 2048 : dgu;
  glds16_s(gb, vog, shm_base + wid * 1024 + 32768 + 3 * 8192);
  glds16_s(gb + dgu, vog, shm_base + wid * 1024 + 32768 + 4 * 8192);
  u64 qa = (u64)(wqkv + 64 * hg), ma = (u64)(wg + (kt0 + 4) * 64 + 32 * he);
  PIN_S(qa); PIN_S(ma);
  const char* const qb = tya ? (const char*)qa : gb - 128;
  glds16_s(qb, voq, shm_base + wid * 1024 + 32768);
  glds16_s(qb + dp1, voq, shm_base + wid * 1024 + 32768 + 8192);
  const char* const mb0 = (const char*)(tya ? qa + 4096 : ma);
  const char* mptr = mb0 + (((vsrc & 1) && !tya) ? dgu : 0) + vom;
  GLDS_NC(mptr, shm_base + wid * 1024 + 32768 + 2 * 8192);
  __builtin_amdgcn_sched_barrier(0);
  f32x4 acc[4][10];

  constexpr int NT = D_ / 64;
  constexpr int CNT = AUX + 4096 + 3584;
  if (tid < 8) *(unsigned*)(shm + CNT + tid * 4) = 0u;
  bf16x8 af0[4], af1[4];
  bf16x4 bl[4], bh[4];
  auto clear = [&]() __attribute__((always_inline)) {
    f32x4 z = {0.f, 0.f, 0.f, 0.f};
    asm volatile("" : "+v"(z));
#pragma unroll
    for (int a = 0; a < 4; ++a) {
#pragma unroll
      for (int b = 0; b < 10; ++b) { acc[a][b] = z; asm volatile("" : "+v"(acc[a][b])); }
      af0[a] = __builtin_bit_cast(bf16x8, z); af1[a] = af0[a];
      asm volatile("" : "+v"(af0[a]));
      asm volatile("" : "+v"(af1[a]));
      bl[a] = bf16x4{0, 0, 0, 0}; bh[a] = bf16x4{0, 0, 0, 0};
    }
  };
#define SB0() __builtin_amdgcn_sched_barrier(0)
  auto mm = [&](int g, int t, bool two) {
    const int mi = t >> 1, mf = ((g & 1) && g != 9) ? 3 - mi : mi, ks = (t & 1) ^ (mi & 1), s = ((g + 1) & 1) * 2;
    const bf16x8 b0 = __builtin_shufflevector(bl[s + ks], bh[s + ks], 0, 1, 2, 3, 4, 5, 6, 7), a0 = ks ? af1[mf] : af0[mf];
    const bf16x8 b1 = __builtin_shufflevector(bl[s + 1 - ks], bh[s + 1 - ks], 0, 1, 2, 3, 4, 5, 6, 7), a1 = ks ? af0[mf] : af1[mf];
    const bool tr = (g >> 1) == 2;
    if (two) asm volatile("v_mfma_f32_16x16x32_bf16 %0, %1, %2, %0\n\tv_mfma_f32_16x16x32_bf16 %0, %3, %4, %0" : "+v"(acc[mf][g])
                          : "v"(tr ? a0 : b0), "v"(tr ? b0 : a0), "v"(tr ? a1 : b1), "v"(tr ? b1 : a1));
    else asm volatile("v_mfma_f32_16x16x32_bf16 %0, %1, %2, %0" : "+v"(acc[mf][g]) : "v"(tr ? a0 : b0), "v"(tr ? b0 : a0));
  };
  typedef __attribute__((address_space(3))) unsigned lds_u32;
  lds_u32* const cnt = (lds_u32*)(shm3 + CNT);
  bool cok;
  constexpr int NY = 28;
  int role, mt2, nt2;
  struct WS { unsigned vb[2][2]; const char* wb[2]; int sh[2]; };
  auto wsetup = [&](const u16* w, int crow0, int l, bool ck) __attribute__((always_inline)) {
    WS r;
    const unsigned vr = ((l >> 5) & 1) * 16 + (wm & 1) * 8 + ((l >> 3) & 3), vc = (((l >> 1) & 3) ^ ((l >> 4) & 1)) * 32 + (l & 1) * 16;
    u64 cp = (u64)(wcopy + (nt2 >> 2) * 589824 + crow0 * 128);
    asm volatile("" : "+s"(cp));
#pragma unroll
    for (int i = 0; i < 2; ++i) {
      u64 dp = (u64)(w + nt2 * 128 + i * 64);
      asm volatile("" : "+s"(dp));
      const bool slow = ck & ((((unsigned)dp >> 7) & 7) == 3);
      r.sh[i] = slow ? 7 : 11;
      r.wb[i] = (const char*)(slow ? cp : dp) + (crow0 ? (wm >> 1) * 32 << r.sh[i] : 0);
      r.vb[i][0] = (vr << r.sh[i]) + vc; r.vb[i][1] = ((vr + 4) << r.sh[i]) + (vc ^ 64);
    }
    asm volatile("" : "+v"(r.vb[0][0]), "+v"(r.vb[0][1]), "+v"(r.vb[1][0]), "+v"(r.vb[1][1]));
    PIN_S(r.wb[0]); PIN_S(r.wb[1]); PIN_S(r.sh[0]); PIN_S(r.sh[1]);
    return r;
  };
  WS wy, wz;
  int vxq, ws0, ws1;
  const char* sz;
  u32x4 xq[4];
  unsigned vg;
  int st = 3;
  const char *sa, *sw[2];
  auto wsel = [&]() {
    const unsigned r = __builtin_amdgcn_readlane(vg, st);
    sw[0] = wy.wb[0] + (r << wy.sh[0]); sw[1] = wy.wb[1] + (r << wy.sh[1]);
  };
  const int p = wid & 3;
  unsigned sl = 0;
#define G2_W "s_add_u32 m0, %[l], %[ow]\n\ts_nop 0\n\tglobal_load_lds_dwordx4 %[v0], %[w0] sc1\n\t" \
             "s_add_u32 m0, m0, 1024\n\ts_nop 0\n\tglobal_load_lds_dwordx4 %[v1], %[w0] sc1\n\t" \
             "s_add_u32 m0, m0, 7168\n\ts_nop 0\n\tglobal_load_lds_dwordx4 %[v2], %[w1] sc1\n\t" \
             "s_add_u32 m0, m0, 1024\n\ts_nop 0\n\tglobal_load_lds_dwordx4 %[v3], %[w1] sc1"
#define G2_WOP(w) [ow] "s"(shm_base + p * 2048), [w0] "s"(sw[0]), [w1] "s"(sw[1]), [v0] "v"(w.vb[0][0]), [v1] "v"(w.vb[0][1]), [v2] "v"(w.vb[1][0]), [v3] "v"(w.vb[1][1])
  auto tile2 = [&](int t0) __attribute__((always_inline)) {
    int b = bid;
    asm volatile("" : "+s"(b));
    role = 1 - ((b >> 2) & 1);
    mt2 = ((role ? b : b ^ 5) & 7) * 4 + ((b >> 3) & 3), nt2 = b >> 5;
    sa = (const char*)abuf + (size_t)(mt2 * 72 + role * NY + t0) * 16384 + wm * 4096;
    asm volatile("" : "+s"(sa));
  };
  auto prep = [&](bool first) __attribute__((always_inline)) {
    int l = threadIdx.x;
    asm volatile("" : "+v"(l));
    l &= 63;
    if (first) {
      tile2(3);
      vxq = ((mt2 * 128 + (wm >> 1) * 64 + role * 32 + 2 * (l & 15)) * D_ + nt2 * 128 + (wm & 1) * 64) * 2 + ((l >> 4) & 1) * 32 + (l >> 5) * 16;
      static_assert(DLT == 1, "");
      const int t = min(l, NY - 1), w = wm >> 1;
      const int o = (0xffaa5500fae504ull >> (2 * t)) & 3;
      int u1 = (0x990990 >> ((2 * t) & 31)) & 3, u2 = 3 + (t & 3);
      asm volatile("" : "+v"(u1), "+v"(u2));
      const int u = t < 12 ? u1 : u2;
      vg = (__umul24((l < NY ? role : 1 - role) * 4 + (((o & 1) << 1) | (o >> 1)), 14) + ((0xb85140a >> (4 * u)) & 15) + (u == 5 ? w : 2 * w)) * 32;
      asm volatile("" : "+v"(vg), "+v"(vxq));
      sz = (const char*)abuf + (size_t)(mt2 * 72 + 56) * 16384 + (wm >> 1) * 8192 + role * 4096 + (wm & 1) * 2048;
      PIN_S(sz);
    }
    wy = wsetup(wd, 0, l, first);
    wz = wsetup(wo, F_, l, first);
    ws0 = 64 << wz.sh[0]; PIN_S(ws0);
    ws1 = 64 << wz.sh[1]; PIN_S(ws1);
    SB0();
  };
  auto start2 = [&]() __attribute__((always_inline)) {
    const __amdgpu_buffer_rsrc_t rx = __builtin_amdgcn_make_buffer_rsrc((void*)x, 0, S_ * D_ * 2, 0x00020000);
#pragma unroll
    for (int f = 0; f < 4; ++f) xq[f] = __builtin_bit_cast(u32x4, __builtin_amdgcn_raw_buffer_load_b128(rx, vxq + (f >> 1) * 2048 + (f & 1) * 64, 0, 0));
    cok = __builtin_amdgcn_readfirstlane(cnt[1] & cnt[2]) != 0u;
    if (__builtin_expect(!cok, 0)) prep(false);
    wsel();
    PIN_S(sw[0]); PIN_S(sw[1]);
    SB0();
  };
  auto kloop = [&](auto wmc) {
    constexpr bool LD = decltype(wmc)::value != 0;
    constexpr int DMP = 2 * G1_STAGE;
    const char* pa = nullptr;
    unsigned vob = 0;
    f32x4 ca = {0.f, 0.f, 0.f, 0.f}, cb = {0.f, 0.f, 0.f, 0.f}, cd = ca;
    bf16x8 sel = {0, 0, 0, 0, 0, 0, 0, 0};
    int tro = 0;
    const int psh = (wid == 2 && !tya) ? 1 : 0, pstr = wid < 3 ? qstr : F_ * 2;
    const char* const pbase = wid == 0 ? qb : (wid == 1 ? qb + dp1 : (wid == 2 ? mb0 : gb));
    int lane = threadIdx.x;
    asm volatile("; %1" : "+v"(lane) : "n"(LD ? 1 : 0));
    lane &= 63;
    const int fr = lane & 15, fq = lane >> 4, q4 = fr >> 2, pp = lane & 3;
    const int ff = ((q4 >> 1) & 1) | ((fq & 1) << 1);
    int aoff[2], boff[2];
#pragma unroll
    for (int ks = 0; ks < 2; ++ks) aoff[ks] = (wm * 64 + fr) * 128 + (((ks * 4 + fq) ^ (fr & 7)) * 16);
#pragma unroll
    for (int j = 0; j < 2; ++j) boff[j] = 32768 + (8 * fq + q4) * 128 + (((2 * wc + j) ^ ff) * 32) + pp * 8;
    unsigned vol = lane * 16;
    float sm = 0.f, sq = 0.f;
    const char* pf = (const char*)wd + (bid * 4 + wid) * 7168;
    long pstep = 64 * pstr;
    if (!LD) {
      const int rs = 256 + mtile * 8 + (lane >> 3), vs = ((lane & 7) >> 1) ^ (((rs >> 1) & 1) | ((mtile & 1) << 1));
      pa = pbase + ((psh & vs) ? dgu : 0) + (rs * pstr + (vs >> psh) * 32 + (lane & 1) * 16);
      vob = rs * (F_ * 2) + vs * 32 + (lane & 1) * 16;
      tro = DMP + wid * 1024 + q4 * 128 + ((fq ^ ((q4 >> 1) | ((mtile & 1) << 1))) * 32) + pp * 8;
      const short one = fr == fq ? 0x3f80 : 0;
      sel = bf16x8{one, one, one, one, one, one, one, one};
      asm volatile("" : "+v"(tro), "+v"(vol), "+v"(sel), "+v"(ca), "+v"(cb), "+v"(sm), "+v"(sq));
      PIN_S(pf); PIN_S(pstep);
    }
    auto accum = [&](lds_cptr p, int ob) {
      ca = __builtin_amdgcn_mfma_f32_16x16x32_bf16(tr_frag(p), sel, ca, 0, 0, 0);
      if (wid == 0) cb = __builtin_amdgcn_mfma_f32_16x16x32_bf16(tr_frag(p + ob), sel, cb, 0, 0, 0);
    };
    auto xrow = [&](int i) {
      u64 a = (u64)(x + (size_t)srow * D_ + i * 256), b = (u64)(wo + (bid * 4 + wid) * 1024);
      PIN_S(a); PIN_S(b);
      asm volatile("global_load_dwordx4 %0, %1, %2" : "+v"(cd) : "v"(vol + (vol & (i < 4 ? 512 : 0)) * 3), "s"(i < 4 ? a : b) : "memory");
    };
    const int q32 = 32 * qstr;
    const char *sq0 = qb + q32, *sq1 = sq0 + dp1, *sg0 = gb + 32 * F_ * 2, *sg1 = sg0 + dgu, *sx = ab + (long)(kz + 128 - 32 * D_ * 2);
    u32x2 mp[2] = {__builtin_bit_cast(u32x2, mptr + q32), __builtin_bit_cast(u32x2, mptr + 2 * q32)};
    u64 cy[2];
    unsigned dst = shm_base + G1_STAGE + wm * 1024;
    int dbuf = G1_STAGE;
    auto stage = [&](int i0, int i1) __attribute__((always_inline)) {
#pragma unroll
        for (int i = i0; i < i1; ++i) {
          const int q = i >> 1, h = i & 1;
          const unsigned ld = dst + h * 4096 + (q < 5 ? 32768 + q * 8192 : (q - 5) * 8192);
          if (q < 2) glds16_s((q ? sq1 : sq0) + h * q32, voq, ld);
          else if (q == 2) glds16_v(__builtin_bit_cast(const char*, mp[h]), ld);
          else if (q < 5) glds16_s((q == 4 ? sg1 : sg0) + h * 32 * F_ * 2, vog, ld);
          else glds16_s(sx + ((q - 5) * 64 + h * 32) * D_ * 2, voa, ld);
        }
    };
    u64 cy0 = 0, rt0 = 0;
    if (!LD) {
      clear();
      xrow(0);
      WAIT_V(1);
    }
    asm volatile("" : "+v"(aoff[0]), "+v"(aoff[1]), "+v"(boff[0]), "+v"(boff[1]));
    bf16x8 kp[2];
    auto body = [&](bool upd, bool keep = false) __attribute__((always_inline)) {
      lds_cptr sb = shm3;
      auto rdb = [&](int g, int i) {
        lds_cptr p = sb + boff[g & 1] + (g >> 1) * 8192 + (i >> 1) * 4096;
        const int s = ((g + 1) & 1) * 2 + (i >> 1);
        if (i & 1) bh[s] = tr_read(p + 512); else bl[s] = tr_read(p);
      };
      auto rda = [&](int ks, int mf) { (ks ? af1 : af0)[mf] = *(const __attribute__((address_space(3))) bf16x8*)(sb + aoff[ks] + mf * 2048); };
      SB0();
      __builtin_amdgcn_s_waitcnt(0xC07F);
#pragma unroll
      for (int gg = 0; gg < 10; ++gg) {
        const int g = (gg + 9) % 10;
#pragma unroll
        for (int t = 0; t < 8; ++t) {
          const bool two = t >= 4 && !(upd && g >= 5 && g <= 8);
          SB0();
          if (!two || (t & 1)) mm(g, two ? t - 1 : t, two);
          SB0();
          if (g == 9) {
            if (t == 0 || t == 2) { rdb(0, t); rdb(0, t + 1); }
            if (t & 1) { rda((t >> 1) & 1, t >> 1); rda(((t >> 1) & 1) ^ 1, t >> 1); }
          } else if (t < 4) rdb(g + 1, t);
          else if (keep && g == 7) {
            if (t == 5) { WAIT_V(0); kp[0] = tr_frag(shm3 + tro); }
            if (t == 7) kp[1] = tr_frag(shm3 + tro + 4096);
          } else if (upd) {
            if (g == 5 && t == 4) { sq0 += 2 * q32; PIN_S(sq0); }
            if (g == 5 && t == 5) { sq1 += 2 * q32; PIN_S(sq1); }
            if (g == 5 && t == 6) { sg0 += 64 * F_ * 2; PIN_S(sg0); }
            if (g == 6 && t == 4) { sg1 += 64 * F_ * 2; PIN_S(sg1); }
            if (g == 6 && t == 5) { sx += 128; PIN_S(sx); }
            if (g == 6 && t == 6) { dst -= dbuf; PIN_S(dst); }
            if (g == 7 && t == 4) asm volatile("v_add_co_u32 %0, %2, %4, %0\n\tv_add_co_u32 %1, %3, %4, %1" : "+v"(mp[0][0]), "+v"(mp[1][0]), "=&s"(cy[0]), "=&s"(cy[1]) : "s"(2 * q32));
            if (g == 7 && t == 6) asm volatile("v_addc_co_u32 %0, vcc, 0, %0, %2\n\tv_addc_co_u32 %1, vcc, 0, %1, %3" : "+v"(mp[0][1]), "+v"(mp[1][1]) : "s"(cy[0]), "s"(cy[1]) : "vcc");
            if (g == 8 && t == 4) { aoff[0] += dbuf; aoff[1] += dbuf; asm volatile("" : "+v"(aoff[0]), "+v"(aoff[1])); }
            if (g == 8 && t == 5) { boff[0] += dbuf; boff[1] += dbuf; asm volatile("" : "+v"(boff[0]), "+v"(boff[1])); }
            if (g == 8 && t == 6) { dbuf = -dbuf; PIN_S(dbuf); }
          }
        }
      }
      SB0();
    };
    TS1(0);
#define ARRP(o, c) do {} while (0)
    if (LD) {
#define PRE1 10
      stage(0, PRE1);
      if (PRE1 == 0) clear();
      WAIT_V(PRE1);
      BARL();
      stage(PRE1, 18);
      if (PRE1) clear();
#pragma clang loop unroll(disable)
      for (int kt = 0;; ++kt) {
        body(true);
        WAIT_V(0);
        BARL();
        if (__builtin_expect(kt == 4 + wid, 0)) prep(true);
        asm volatile("" ::: "memory");
        if (kt == NT - 2) break;
        stage(0, 18);
        SB0();
      }
      start2();
      body(false);
    } else {
      char* wsx;
      auto tile = [&](int kt, bool keep) __attribute__((always_inline)) {
        SB0();
        ARRP(wid * 64 + kt * 4, true);
        BARL();
        body(false, keep);
        ARRP(320 + kt * 4 + 4, wid == 0);
        if (keep) asm volatile("" : "+v"(kp[0]), "+v"(kp[1])); else WAIT_V(0);
      };
      auto next = [&]() __attribute__((always_inline)) {
        aoff[0] += dbuf; aoff[1] += dbuf; boff[0] += dbuf; boff[1] += dbuf;
        dbuf = -dbuf;
        asm volatile("" : "+v"(aoff[0]), "+v"(aoff[1]), "+v"(boff[0]), "+v"(boff[1]), "+v"(vol), "+v"(tro) :: "memory");
        wsx = ws;
        PIN_S(wsx);
      };
      auto share = [&]() {
        glds16_v(pa, shm_base + DMP + wid * 1024);
        if (wid == 0) glds16_s(gb + dgu, vob, shm_base + DMP + 4096);
      };
      auto kept = [&]() {
        ca = __builtin_amdgcn_mfma_f32_16x16x32_bf16(kp[0], sel, ca, 0, 0, 0);
        if (wid == 0) cb = __builtin_amdgcn_mfma_f32_16x16x32_bf16(kp[1], sel, cb, 0, 0, 0);
      };
      auto cpub = [&](u32x4 a, u32x4 b) {
        if ((vol & 0xc0) == 0) {
          const char* const cs = wsx + 101 * MB_ + ntile * 10240 + mtile * 256;
          const unsigned co = (vol & 0x30) * 4 + (vol >> 8) * 16;
          gst16(cs + wid * 2048, co, a);
          if (wid == 0) gst16(cs + 8192, co, b);
        }
      };
#pragma clang loop unroll(disable)
      for (int kt = 0; kt < 4; ++kt) {
        tile(kt, true);
        if (kt < 2) {
          glds16_v(pa - 2 * pstep, shm_base + DMP + 7168);
          if (wid == 0) glds16_s(gb + dgu, vob - 128 * F_ * 2, shm_base + DMP + 7168);
        }
        share();
        u32x4 xw;
        asm volatile("v_mov_b32 %0, %4\n\tv_mov_b32 %1, %5\n\tv_mov_b32 %2, %6\n\tv_mov_b32 %3, %7" : "=&v"(xw[0]), "=&v"(xw[1]), "=&v"(xw[2]), "=&v"(xw[3])
                     : "v"(cd[0]), "v"(cd[1]), "v"(cd[2]), "v"(cd[3]));
        xrow(kt + 1);
        next();
        if (kt) kept();
        pa += pstep; vob += 64 * F_ * 2;
        accum(shm3 + tro + ((kt & 1) * G1_STAGE + 32768 + mtile * 1024 + wid * 7168 - DMP), 4 * 8192);
#pragma unroll
        for (int i = 0; i < 4; ++i) {
          float lo, hi;
          asm volatile("v_lshlrev_b32 %2, 16, %4\n\tv_and_b32 %3, 0xffff0000, %4\n\tv_add_f32 %0, %0, %2\n\tv_fmac_f32 %1, %2, %2\n\t"
                       "v_add_f32 %0, %0, %3\n\tv_fmac_f32 %1, %3, %3" : "+v"(sm), "+v"(sq), "=&v"(lo), "=&v"(hi) : "v"(xw[i]));
        }
      }
#pragma clang loop unroll(disable)
      for (int kt = 4; kt < 12; ++kt) {
        tile(kt, true);
        share();
        glds16_s(pf, vol, shm_base + DMP + 7168);
        next();
        kept();
        pa += pstep; vob += 64 * F_ * 2;
        pf = kt == 10 ? (const char*)wo + (bid * 4 + wid) * 2048 + 1024 : pf + 1024;
        if (kt < 8) {
          const unsigned q1 = bid * 4 + wid + (kt - 5) * 1024, q0 = q1 - 1024;
          if (q0 < 1152) gst16(wsx + 98 * MB_ + (q0 & 1) * 589824 + (q0 >> 1) * 1024, vol, *(const u32x4*)(shm + AUX + wid * 1024 + vol));
          if (q1 < 1152) {
            const char* src = (const char*)((q1 >> 1) < 448 ? wd + (size_t)(q1 >> 1) * 8 * D_ : wo + (size_t)((q1 >> 1) - 448) * 8 * D_);
            src += ((3 - ((unsigned)(uintptr_t)src >> 7)) & 7) * 128 + (q1 & 1) * 1024;
            glds16_s(src, (vol >> 7) * 2048 + (vol & 0x70), shm_base + AUX + wid * 1024);
          }
          u32x4 bk;
          asm volatile("v_mov_b32 %0, %1" : "=v"(bk[0]) : "s"(BLANK));
          bk[1] = bk[0]; bk[2] = bk[0]; bk[3] = bk[0];
          gst16(wsx + 64 * MB_ + bid * 16384 + wid * 4096 + (kt - 4) * 1024, vol, bk);
          if (tya && (kt == 4 || kt == 7)) gst16(wsx + 4 * MB_ + (mtile * 32 + ntile) * 4096 + wid * 2048 + (kt & 1) * 1024, vol, bk);
          if (kt == 4) cpub(bk, bk);
        } else {
          if (kt == 8) {
            const float ml = sm * (1.0f / 32), mean = half_sum(sm) * (1.0f / D_), dm = ml - mean;
            const float q = __builtin_fmaf(dm * 32.0f, dm, __builtin_fmaf(-ml, sm, sq));
            const unsigned ps = pack_stats(mean, fmaxf(half_sum(q) * (1.0f / D_), 0.f));
            const u64 now = __builtin_amdgcn_s_memrealtime();
            if ((vol & 0x1f0) == 0) gst16(wsx + 97 * MB_ + srow * 16, vol >> 5, u32x4{ps, (unsigned)now, ps, (unsigned)(now >> 32)});
          }
          if (kt == 9) {
            if (wid == 0) {
              u64 now;
              asm volatile("s_memrealtime %0\n\ts_waitcnt lgkmcnt(0)" : "=s"(now) :: "memory");
              if (vol == 0) asm volatile("global_store_dwordx2 %0, %1, %2 sc1" :: "v"(vol), "v"(now), "s"(wsx + 100 * MB_ + 131072 + bid * 8) : "memory");
            }
            if (wid == 2) glds16_s1(wsx + 102 * MB_ + (bid & ~1) * 512, vol, shm_base + DMP + 7168);
          }
        }
      }
      asm volatile("" :: "v"(cd));
      if (wid >= 2) glds16_s1(wsx + 100 * MB_ + 131072 + (wid - 2) * 1024, vol, shm_base + DMP + 3072 + wid * 1024);
      tile(12, true);
      next();
      kept();
      const int lane = vol >> 4;
      glds16_s1(wsx + 97 * MB_ + (m0 + wid * 64) * 16, ((lane & 32) + 2 * (lane & 15) + ((lane >> 4) & 1)) * 16, shm_base + AUX + wid * 1024);
      asm volatile("s_nop 7\n\ts_nop 3" : "+v"(ca), "+v"(cb));
      cpub(__builtin_bit_cast(u32x4, ca), __builtin_bit_cast(u32x4, cb));
      if (wid >= 2) {
        u32x4 c4 = *(const u32x4*)(shm + DMP + 3072 + wid * 1024 + vol);
        bool ok = __all(fresh(((u64)c4[1] << 32) | c4[0], t_own) & fresh(((u64)c4[3] << 32) | c4[2], t_own));
        while (__builtin_expect(!ok, 0) && __builtin_amdgcn_s_memrealtime() - t_own < TIMEOUT_ / 10) {
          const __amdgpu_buffer_rsrc_t rs = __builtin_amdgcn_make_buffer_rsrc((void*)(wsx + 100 * MB_ + 131072), 0, 2048, 0x00020000);
          asm volatile("" ::: "memory");
          c4 = __builtin_bit_cast(u32x4, __builtin_amdgcn_raw_buffer_load_b128(rs, (wid - 2) * 1024 + vol, 0, 16));
          ok = __all(fresh(((u64)c4[1] << 32) | c4[0], t_own) & fresh(((u64)c4[3] << 32) | c4[2], t_own));
        }
        *(unsigned*)(shm + CNT - 4 + wid * 4) = ok ? 1u : 0u;
      }
      tile(13, false);
      next();
      u32x4 cw[4];
      auto cgather = [&]() {
        const __amdgpu_buffer_rsrc_t rc = __builtin_amdgcn_make_buffer_rsrc((void*)(wsx + 101 * MB_ + ntile * 10240), 0, 5 * 2048, 0x00020000);
        const unsigned vcs = (vol < 512 ? wid * 2048 : 8192) + (vol & 256) * 4 + (vol & 0xf0);
#pragma unroll
        for (int i = 0; i < 4; ++i) cw[i] = __builtin_bit_cast(u32x4, __builtin_amdgcn_raw_buffer_load_b128(rc, vcs + i * 256, 0, 16));
      };
      {
        const int ln = wid * 64 + (vol >> 4);
        u32x4 gr = *(const u32x4*)(shm + AUX + ln * 16);
        if (__builtin_expect(!__all((gr[0] == gr[2]) & fresh(((u64)gr[3] << 32) | gr[1], t_own)), 0)) {
          const __amdgpu_buffer_rsrc_t rs = __builtin_amdgcn_make_buffer_rsrc((void*)(wsx + 97 * MB_ + m0 * 16), 0, 4096, 0x00020000);
          const int prow = (ln & 0xe0) + 2 * (ln & 15) + ((ln >> 4) & 1);
          for (;;) {
            gr = __builtin_bit_cast(u32x4, __builtin_amdgcn_raw_buffer_load_b128(rs, prow * 16, 0, 16));
            if (__all((gr[0] == gr[2]) & fresh(((u64)gr[3] << 32) | gr[1], t_own))) break;
            if (__builtin_amdgcn_s_memrealtime() - t_own > TIMEOUT_) { gr[0] = row_stats_serial(x + (size_t)(m0 + prow) * D_); break; }
            __builtin_amdgcn_s_sleep(2);
          }
        }
        cgather();
        const float nm = -lo_f(gr[0]), dn = hi_f(gr[0]), r = __builtin_amdgcn_rcpf(dn);
        auto mul = [](float a, float b) { float c; asm("v_mul_f32 %0, %1, %2" : "=v"(c) : "v"(a), "v"(b)); return c; };
        const f32x4 fc = {nm, -2.3022081986f * r, mul(mul(mul(-0.10294324f, r), r), r), mul(dn, dn)};
        *(f32x4*)(shm + AUX + ln * 16) = fc;
        float* const tw = (float*)(shm + 2 * G1_STAGE + 4096 + wid * 768) + ln;
#pragma unroll
        for (int i = 0; i < 4; ++i) tw[i * 64] = fc[i];
        ((float*)(shm + AUX + 4096))[384 + ln] = r;
        cok = __builtin_amdgcn_readfirstlane(cnt[1] & cnt[2]) != 0u;
        tile2(0);
        wy = wsetup(wd, 0, vol >> 4, cok);
      }
      tile(14, false);
      next();
      {
        while (__builtin_expect(blank(cw) && cok, 0)) {
          __builtin_amdgcn_s_sleep(2);
          asm volatile("" ::: "memory");
          cgather();
        }
        f32x4 t;
#pragma unroll
        for (int i = 0; i < 4; ++i) {
          float a, b;
          asm("v_add_f32 %0, %2, %3\n\tv_add_f32 %1, %4, %5\n\tv_add_f32 %0, %0, %1" : "=&v"(a), "=&v"(b) : "v"(cw[0][i]), "v"(cw[1][i]), "v"(cw[2][i]), "v"(cw[3][i]));
          const auto r2 = __builtin_amdgcn_permlane16_swap(__float_as_uint(a), __float_as_uint(a), false, false);
          asm("v_add_f32 %0, %1, %2" : "=v"(t[i]) : "v"(r2[0]), "v"(r2[1]));
        }
        const int l = vol >> 4;
        if (__builtin_expect(!cok, 0)) {
          const int gq = (l >> 2) & 3;
          const char* pc = l < 32 ? pbase + ((psh & gq) ? dgu : 0) + (gq >> psh) * 32 + (l & 3) * 8 : gb + dgu + (l & 15) * 8;
          t = f32x4{0.f, 0.f, 0.f, 0.f};
#pragma unroll 1
          for (int k = 0; k < D_; ++k) { const u32x2 w = *(const __attribute__((address_space(1))) u32x2*)(pc + (size_t)k * (l < 32 ? pstr : F_ * 2)); t += f32x4{lo_f(w[0]), hi_f(w[0]), lo_f(w[1]), hi_f(w[1])}; }
        }
        if (l < 32 || wid == 0)
          *(f32x4*)((float*)(shm + AUX + 4096) + ((l >> 3) & 1) * 192 + (l < 32 ? wid : 4) * 32 + (l & 7) * 4) = t;
        __builtin_amdgcn_s_setprio(2);
      }
      SB0();
      ARRP(wid * 64 + 60, true);
      BARL();
      body(false);
    }
#pragma unroll
    for (int t = 0; t < 8; t += 2) mm(9, t, true);
    asm volatile("s_nop 7\n\ts_nop 3");
    SB0();
    TS1(3);
    if (!LD) {
#pragma unroll
      for (int i = 0; i < 3; ++i) {
        const unsigned r = ((role * 4 + (i & 1) * 2) * 14 + (i < 2 ? 10 : 0) + (wm & 2)) * 32;
        sw[0] = wy.wb[0] + (r << wy.sh[0]); sw[1] = wy.wb[1] + (r << wy.sh[1]);
        asm volatile(G2_W :: [l] "s"((i + 1) * 16384), G2_WOP(wy) : "memory", "scc", "m0");
      }
    } else {
      __builtin_amdgcn_s_setprio(3);
      WAIT_V(0);
    }
  };
  if (wid < 4) kloop(std::integral_constant<int, 0>{});
  else kloop(std::integral_constant<int, 1>{});
  __builtin_amdgcn_sched_barrier(0);
  int tz = threadIdx.x;
  asm volatile("" : "+v"(tz));
  __builtin_amdgcn_sched_barrier(0);
  const int li = tz & 15, lq = (tz >> 4) & 3, ln = tz & 63;
  const __amdgpu_buffer_rsrc_t ra = __builtin_amdgcn_make_buffer_rsrc((void*)abuf, 0, 16 * 72 * 16384, 0x00020000);
  float* const cfin = (float*)(shm + AUX + 4096);
  float* const svr = cfin + 384;
  float* const tb = (float*)(shm + 2 * G1_STAGE + 4096 + wm * 1024);
  auto signal = [&](unsigned v) {
    asm volatile("" ::: "memory");
    if (ln == 0) __hip_atomic_fetch_add(cnt, v, __ATOMIC_RELAXED, __HIP_MEMORY_SCOPE_WORKGROUP);
    asm volatile("" ::: "memory");
  };
  auto await = [&](unsigned mask, unsigned val) {
    while ((__builtin_amdgcn_readfirstlane(__hip_atomic_load(cnt, __ATOMIC_RELAXED, __HIP_MEMORY_SCOPE_WORKGROUP)) & mask) != val) __builtin_amdgcn_s_sleep(1);
    asm volatile("" ::: "memory");
  };
  auto client = [&](int l) { const int m128 = mtile * 2 + ((l >> 4) & 1); return (((l & 7) * 4 + (m128 & 3)) * 8) + ((m128 >> 2) ^ ((l & 8) ? 5 : 0)); };
  auto notify = [&](int l0, int nl) {
    u64 now;
    asm volatile("s_memrealtime %0\n\ts_waitcnt lgkmcnt(0)" : "=s"(now) :: "memory");
    int l = threadIdx.x;
    asm volatile("" : "+v"(l));
    l &= 63;
    const __amdgpu_buffer_rsrc_t rd1 = __builtin_amdgcn_make_buffer_rsrc((void*)(done1 + ntile), 0, 256 * 512, 0x00020000);
    if (l < nl) __builtin_amdgcn_raw_buffer_store_b64(u32x2{(unsigned)now, (unsigned)(now >> 32)}, rd1, client(l0 + l) * 512, 0, 16);
  };
  const int xo = ((xcd & 1) << 1) | ((xcd >> 1) & 1), te = (xcd >> 2) * 28 + 6 * (xo >> 1) + (xo & 1), tl = (xcd >> 2) * 28 + 12 + 4 * xo;
  const u64 te0 = __builtin_amdgcn_s_memrealtime();
  const __amdgpu_buffer_rsrc_t re = __builtin_amdgcn_make_buffer_rsrc((void*)(done1 + client((wm >> 1) * 16 + 8 - (xcd & 4) * 2) * 64 + 32 + (ntile & 15) * 2 + (wm & 1)),
                                                                      0, 7 * 16384 + 8, 0x00020000);
  auto early = [&]() {
    __builtin_amdgcn_raw_buffer_store_b64(u32x2{(unsigned)te0, (unsigned)(te0 >> 32)}, re, ln * 16384, 0, 16);
  };
  signal(1u << (8 * wc));
  if (wc) {
    await(0xffffu, 0x404u);
#pragma unroll
    for (int f = 0; f < 4; ++f) *(u32x4*)(shm + G2_X + wm * 4096 + f * 1024 + ln * 16) = xq[f];
    SB0();
    __builtin_amdgcn_s_setprio(1);
  }
  TS1(4);
  f32x4 rf[4];
#pragma unroll
  for (int mf = 0; mf < 4; ++mf) rf[mf] = *(const f32x4*)(shm + AUX + (wm * 64 + mf * 16 + li) * 16);
  const float* const cl = cfin + wc * 192 + lq * 4;
  const int sbase = ((mtile * 2 + (wm >> 1)) * 72 * 8 + (wm & 1) * 4) * 2048;
  auto putf = [&](int kg, int mf, u32x4 v) {
    __builtin_amdgcn_raw_buffer_store_b128(v, ra, ln * 16 + sbase + ((kg >> 1) * 8 + mf) * 2048 + (kg & 1) * 1024, 0, 16);
  };
  auto gelp = [&](f32x2 g, f32x2 u, f32x2 cg, f32x2 cu, f32x2 m2, f32x2 k12, f32x2 k22, f32x2 d2) {
    const f32x2 tg = __builtin_elementwise_fma(m2, cg, g), tu = __builtin_elementwise_fma(m2, cu, u);
    const f32x2 arg = tg * __builtin_elementwise_fma(tg * tg, k22, k12);
    const f32x2 dn = __builtin_elementwise_fma(f32x2{__builtin_amdgcn_exp2f(arg[0]), __builtin_amdgcn_exp2f(arg[1])}, d2, d2);
    const f32x2 r = (tg * tu) * f32x2{__builtin_amdgcn_rcpf(dn[0]), __builtin_amdgcn_rcpf(dn[1])};
    return pack2(r[0], r[1]);
  };
  auto gel2 = [&](int p, int kg, int mf0 = 0, int mf1 = 4) {
    f32x4 cg[2], cu[2];
#pragma unroll
    for (int j = 0; j < 2; ++j) { cg[j] = *(const f32x4*)(cl + p * 32 + j * 16); cu[j] = *(const f32x4*)(cl + p * 32 + 32 + j * 16); }
#pragma unroll
    for (int mf = mf0; mf < mf1; ++mf) {
      const f32x2 m2 = {rf[mf][0], rf[mf][0]}, k12 = {rf[mf][1], rf[mf][1]}, k22 = {rf[mf][2], rf[mf][2]}, d2 = {rf[mf][3], rf[mf][3]};
      u32x4 o;
#pragma unroll
      for (int i = 0; i < 4; ++i) {
        const int j = i >> 1, e = (i & 1) * 2;
        const f32x4 g = acc[mf][p * 2 + j], u = acc[mf][p * 2 + 2 + j];
        o[i] = gelp(f32x2{g[e], g[e + 1]}, f32x2{u[e], u[e + 1]}, f32x2{cg[j][e], cg[j][e + 1]}, f32x2{cu[j][e], cu[j][e + 1]}, m2, k12, k22, d2);
      }
      putf(kg, mf, o);
    }
  };
  int tsel = ntile & 1;
  asm volatile("" : "+s"(tsel));
  if (tsel != 0) {
    const int tq = (wc ? tl : te + 2 + (xo & 1)) + he;
    gel2(0, 2 * tq);
    gel2(3, 2 * tq + 1);
    TS1(5);
    const float cg = cfin[wc * 192 + 64 + li], cu = cfin[wc * 192 + 80 + li];
#pragma unroll
    for (int mf = 0; mf < 4; ++mf) {
      f32x4 t[4];
#pragma unroll
      for (int i = 0; i < 4; ++i) t[i] = *(const f32x4*)(tb + i * 64 + mf * 16 + lq * 4);
      const f32x4 g = acc[mf][4], u = acc[mf][5];
      u32x2 o;
#pragma unroll
      for (int e = 0; e < 4; e += 2)
        o[e >> 1] = gelp(f32x2{g[e], g[e + 1]}, f32x2{u[e], u[e + 1]}, f32x2{cg, cg}, f32x2{cu, cu}, f32x2{t[0][e], t[0][e + 1]},
                         f32x2{t[1][e], t[1][e + 1]}, f32x2{t[2][e], t[2][e + 1]}, f32x2{t[3][e], t[3][e + 1]});
      *(u32x2*)(shm + wm * 4096 + mf * 1024 + wc * 512 + li * 32 + lq * 8) = o;
      if (mf == EPOS && wc == 0) {
        TS1(1);
        WAIT_V(0);
        TS1(6);
        early();
      }
    }
    if (wc == 0) {
      signal(0x10000u << wid);
    } else {
      await(0x1000u << wid, 0x1000u << wid);
#pragma unroll
      for (int mf = 0; mf < 4; ++mf) putf(2 * (tl + 2) + he, mf, __builtin_bit_cast(u32x4, tr_frag(shm3 + wm * 4096 + mf * 1024 + ln * 8)));
    }
  } else {
    float rd[4];
#pragma unroll
    for (int mf = 0; mf < 4; ++mf) rd[mf] = svr[wm * 64 + mf * 16 + li];
    auto cvq = [&](int p, int mf, const f32x4& c0, const f32x4& c1) {
      u32x4 o;
#pragma unroll
      for (int j = 0; j < 2; ++j) {
        const f32x4 a = acc[mf][p * 2 + j], c4 = j ? c1 : c0;
        const f32x2 m2 = {rf[mf][0], rf[mf][0]}, r2 = {rd[mf], rd[mf]};
        f32x2 t0 = __builtin_elementwise_fma(m2, f32x2{c4[0], c4[1]}, f32x2{a[0], a[1]});
        f32x2 t1 = __builtin_elementwise_fma(m2, f32x2{c4[2], c4[3]}, f32x2{a[2], a[3]});
        if (p) { t0 *= r2; t1 *= r2; }
        o[2 * j] = pack2(t0[0], t0[1]); o[2 * j + 1] = pack2(t1[0], t1[1]);
      }
      return o;
    };
    const float cv0 = cfin[wc * 192 + 64 + li], cv1 = cfin[wc * 192 + 80 + li];
    auto cvv = [&](int mf) {
      const f32x4 n4 = *(const f32x4*)(tb + mf * 16 + lq * 4), r4 = *(const f32x4*)(svr + wm * 64 + mf * 16 + lq * 4);
      u32x4 o;
#pragma unroll
      for (int j = 0; j < 2; ++j) {
        const f32x4 a = acc[mf][4 + j];
        const f32x2 c2 = {j ? cv1 : cv0, j ? cv1 : cv0};
        const f32x2 t0 = __builtin_elementwise_fma(f32x2{n4[0], n4[1]}, c2, f32x2{a[0], a[1]}) * f32x2{r4[0], r4[1]};
        const f32x2 t1 = __builtin_elementwise_fma(f32x2{n4[2], n4[3]}, c2, f32x2{a[2], a[3]}) * f32x2{r4[2], r4[3]};
        o[2 * j] = pack2(t0[0], t0[1]); o[2 * j + 1] = pack2(t1[0], t1[1]);
      }
      return o;
    };
    u32x4 kx[4], vx[4], qx[4], hx[4];
    const bf16x8 ones = {0x3f80, 0x3f80, 0x3f80, 0x3f80, 0x3f80, 0x3f80, 0x3f80, 0x3f80};
    const f32x4 ck0 = *(const f32x4*)(cl + 32), ck1 = *(const f32x4*)(cl + 48);
    kx[2] = cvq(1, 2, ck0, ck1); kx[3] = cvq(1, 3, ck0, ck1);
    vx[2] = cvv(2); vx[3] = cvv(3);
    auto exo = [](int e) { return e * 4096 + (e >> 2) * (G2_A - 16384); };
    const bool needh = wm == 0 && mtile > 0;
    const __amdgpu_buffer_rsrc_t rh = __builtin_amdgcn_make_buffer_rsrc((void*)((char*)halo + ((needh ? mtile - 1 : mtile) * 32 + ntile + wc) * 4096), 0,
                                                                        needh || wm == 3 ? 4096 : 0, 0x00020000);
    if (wm < 3) {
      char* const ex = shm + exo(wc * 3 + wm) + ln * 16;
      *(u32x4*)ex = kx[2]; *(u32x4*)(ex + 1024) = kx[3]; *(u32x4*)(ex + 2048) = vx[2]; *(u32x4*)(ex + 3072) = vx[3];
      signal(0x10000u << wid);
    } else {
#pragma unroll
      for (int r = 0; r < 4; ++r) __builtin_amdgcn_raw_buffer_store_b128(r < 2 ? kx[2 + r] : vx[r], rh, ln * 16 + r * 1024, 0, 16);
    }
    auto hload = [&]() {
#pragma unroll
      for (int r = 0; r < 4; ++r) hx[r] = __builtin_bit_cast(u32x4, __builtin_amdgcn_raw_buffer_load_b128(rh, ln * 16 + r * 1024, 0, 16));
    };
    SB0();
    gel2(3, 2 * (wc ? tl + 3 : te) + he);
    SB0();
    TS1(5);
    if (wm == 0) hload();
    SB0();
    kx[0] = cvq(1, 0, ck0, ck1); kx[1] = cvq(1, 1, ck0, ck1);
    vx[0] = cvv(0); vx[1] = cvv(1);
    {
      const f32x4 cq0 = *(const f32x4*)(cl), cq1 = *(const f32x4*)(cl + 16);
#pragma unroll
      for (int mf = 0; mf < 4; ++mf) qx[mf] = cvq(0, mf, cq0, cq1);
    }
    f32x4 mk0, mk1;
#pragma unroll
    for (int j = 0; j < 4; ++j) {
      const int k0 = 4 * lq + j, k1 = 16 + 4 * lq + j;
      mk0[j] = (k0 >= li + 9 && k0 <= li + 16) ? 0.f : -1.0e30f;
      mk1[j] = (k1 >= li + 9 && k1 <= li + 16) ? 0.f : -1.0e30f;
    }
    auto max3 = [](float a, float b, float c) { float r; asm("v_max3_f32 %0, %1, %2, %3" : "=v"(r) : "v"(a), "v"(b), "v"(c)); return r; };
    auto max2 = [](float a, float b) { float r; asm("v_max_f32 %0, %1, %2" : "=v"(r) : "v"(a), "v"(b)); return r; };
    asm volatile("" : "+v"(kx[0]), "+v"(kx[1]), "+v"(vx[0]), "+v"(vx[1]), "+v"(qx[0]), "+v"(qx[1]), "+v"(qx[2]), "+v"(qx[3]), "+v"(mk0), "+v"(mk1));
    TS1(1);
    if (wc == 0) {
      if (wm == 0) WAIT_V(4); else WAIT_V(0);
      early();
    }
    if (wm > 0) {
      await(0x8000u << wid, 0x8000u << wid);
#pragma unroll
      for (int i = 0; i < 4; ++i) hx[i] = *(const u32x4*)(shm + exo(wc * 3 + wm - 1) + i * 1024 + ln * 16);
    } else if (__builtin_expect(cok || !needh, 1)) {
      while (__builtin_expect(blank(hx), 0)) {
        __builtin_amdgcn_s_sleep(1);
        asm volatile("" ::: "memory");
        hload();
      }
    } else {
      await(0xffffu, 0x404u);
      char* const fb = shm + exo(6 + wc) + ln * 16;
#pragma unroll 1
      for (int i = 0; i < 32; ++i) {
        const int rec = i >> 3, e = i & 7;
        const int row = m0 - 32 + 2 * (rec < 2 ? li : lq * 4 + (e & 3)) + (rec & 1);
        const int col = (rec < 2 ? D_ + lq * 4 + (e & 3) : 2 * D_ + li) + (ntile + wc) * 32 + (e >> 2) * 16;
        const u16* xr = x + (size_t)row * D_;
        const unsigned stt = row_stats_serial(xr);
        const float mb = lo_f(stt);
        float a = 0.f;
#pragma unroll 1
        for (int k = 0; k < D_; ++k) a += (bf2f(xr[k]) - mb) * bf2f(wqkv[(size_t)k * 3072 + col]);
        *(u16*)(fb + rec * 1024 + e * 2) = f2bf(a / hi_f(stt));
      }
#pragma unroll
      for (int i = 0; i < 4; ++i) hx[i] = *(const u32x4*)(fb + i * 1024);
    }
    TS1(6);
    {
      const f32x4 z = {0.f, 0.f, 0.f, 0.f};
      const float mz = mtile == 0 && wm == 0 ? -1.0e30f : 0.f;
      const f32x4 mkf = {fminf(mk0[0], mz), fminf(mk0[1], mz), fminf(mk0[2], mz), fminf(mk0[3], mz)};
      f32x4 s0[4], s1[4];
#pragma unroll
      for (int i = 0; i < 4; ++i) {
        const int n = i ^ 2;
        const bf16x8 qf = __builtin_bit_cast(bf16x8, qx[n]);
        s1[n] = __builtin_amdgcn_mfma_f32_16x16x32_bf16(__builtin_bit_cast(bf16x8, kx[n]), qf, mk1, 0, 0, 0);
        s0[n] = __builtin_amdgcn_mfma_f32_16x16x32_bf16(__builtin_bit_cast(bf16x8, n < 2 ? hx[n] : kx[n - 2]), qf, n < 2 ? mkf : mk0, 0, 0, 0);
      }
      SB0();
      asm volatile("s_nop 7\n\ts_nop 3" : "+v"(s0[0]), "+v"(s1[0]), "+v"(s0[1]), "+v"(s1[1]), "+v"(s0[2]), "+v"(s1[2]), "+v"(s0[3]), "+v"(s1[3]));
      float mx[4];
#pragma unroll
      for (int n = 0; n < 4; ++n) mx[n] = max3(max3(s0[n][0], s0[n][1], s0[n][2]), max3(s0[n][3], s1[n][0], s1[n][1]), max2(s1[n][2], s1[n][3]));
#pragma unroll
      for (int n = 0; n < 4; ++n) { auto r = __builtin_amdgcn_permlane16_swap(__float_as_uint(mx[n]), __float_as_uint(mx[n]), false, false);
        mx[n] = max2(__uint_as_float(r[0]), __uint_as_float(r[1])); }
#pragma unroll
      for (int n = 0; n < 4; ++n) { auto r = __builtin_amdgcn_permlane32_swap(__float_as_uint(mx[n]), __float_as_uint(mx[n]), false, false);
        mx[n] = max2(__uint_as_float(r[0]), __uint_as_float(r[1])); }
      SB0();
      constexpr float cs2 = 0.17677669529663687f * 1.4426950408889634f;
      f32x4 os[4], o0[4], o1[4];
#pragma unroll
      for (int n = 0; n < 4; ++n) {
        const float c = cs2 * rd[n], nm = -mx[n] * c;
        float e8[8];
#pragma unroll
        for (int j = 0; j < 4; ++j) {
          e8[j] = __builtin_amdgcn_exp2f(__builtin_fmaf(s0[n][j], c, nm));
          e8[4 + j] = __builtin_amdgcn_exp2f(__builtin_fmaf(s1[n][j], c, nm));
        }
        u32x4 pk;
#pragma unroll
        for (int j = 0; j < 4; ++j) pk[j] = pack2(e8[2 * j], e8[2 * j + 1]);
        const bf16x8 pf = __builtin_bit_cast(bf16x8, pk);
        const u32x4 vp = n < 2 ? hx[2 + n] : vx[n - 2];
        const u32x4 va = {vp[0], vp[1], vx[n][0], vx[n][1]}, vb = {vp[2], vp[3], vx[n][2], vx[n][3]};
        os[n] = __builtin_amdgcn_mfma_f32_16x16x32_bf16(ones, pf, z, 0, 0, 0);
        o0[n] = __builtin_amdgcn_mfma_f32_16x16x32_bf16(__builtin_bit_cast(bf16x8, va), pf, z, 0, 0, 0);
        o1[n] = __builtin_amdgcn_mfma_f32_16x16x32_bf16(__builtin_bit_cast(bf16x8, vb), pf, z, 0, 0, 0);
      }
#pragma unroll
      for (int n = 0; n < 4; ++n) {
        const float inv = __builtin_amdgcn_rcpf(os[n][0]);
        const f32x4 n0 = o0[n] * inv, n1 = o1[n] * inv;
        putf(112 + ntile + wc, n, u32x4{pack2(n0[0], n0[1]), pack2(n0[2], n0[3]), pack2(n1[0], n1[1]), pack2(n1[2], n1[3])});
      }
    }
  }
  TS1(2);
  const bool tya1 = tya;
  {
  __builtin_amdgcn_sched_barrier(0);
  int tid = threadIdx.x;
  asm volatile("" : "+v"(tid));
  __builtin_amdgcn_sched_barrier(0);
  const int lane = tid & 63;
  const int wm = wid >> 1, wn = wid & 1;
  const int m0 = mt2 * 128, n0 = nt2 * 128;
  constexpr int PD = G2_NSTAGE;
  const int ny = (xcd & 1) ? NY - DLT : NY + DLT;
  const unsigned va = lane * 16;
  auto stage_y = [&]() {
    asm volatile("s_add_u32 m0, %[l], %[oa]\n\ts_nop 0\n\tglobal_load_lds_dwordx4 %[va], %[a] sc1\n\tglobal_load_lds_dwordx4 %[va], %[a] offset:1024 sc1\n\t"
                 "global_load_lds_dwordx4 %[va], %[a] offset:2048 sc1\n\tglobal_load_lds_dwordx4 %[va], %[a] offset:3072 sc1\n\t" G2_W
                 :: [l] "s"(sl), [oa] "s"(shm_base + G2_A + (p ^ role) * 4096), [a] "s"(sa), [va] "v"(va), G2_WOP(wy) : "memory", "scc", "m0");
    ++st;
    sa += 16384;
    sl = (sl + 16384) & 65535;
    wsel();
  };
  u64 ready = 0;
  const u64* const mbox = &done1[bid * 64 + lane];
  auto seen = [&](u64 v) {
    const u64 r = __ballot(fresh(v, t_own));
    u64 d;
    asm("s_bitreplicate_b64_b32 %0, %1" : "=s"(d) : "s"((unsigned)r >> (16 * role)));
    ready |= r | (d << 32);
  };
  auto need = [&](u64 mask, bool renew) {
    while ((ready & mask) != mask) {
      seen(__hip_atomic_load(mbox, RLX_AGENT));
      if ((ready & mask) != mask) {
        __builtin_amdgcn_s_sleep(PSLEEP);
        if (renew && !cok) notify(0, 32);
      }
    }
    asm volatile("" ::: "memory");
  };
  u64 fl = 0;
  auto peek = [&]() { asm volatile("global_load_dwordx2 %0, %1, %2 sc1" : "+v"(fl) : "v"(va >> 1), "s"(done1 + bid * 64) : "memory"); };
  auto check = [&](u64 mask, bool renew) {
    asm volatile("" : "+v"(fl));
    seen(fl);
    if (__builtin_expect((ready & mask) != mask, 0)) need(mask, renew);
  };
  if (wid >= 4) {
    TS(4);
    __builtin_amdgcn_s_setprio(0);
    BARL();
    stage_y();
    stage_y();
    BAR();
    if (p == 1) peek();
    stage_y();
    WAIT_V(16);
    BAR();
    if (p == 0) notify(0, 32);
#define G2_A2 "global_load_lds_dwordx4 %[va], %[a] sc1\n\tglobal_load_lds_dwordx4 %[va], %[a] offset:1024 sc1"
#define G2_A4 G2_A2 "\n\tglobal_load_lds_dwordx4 %[va], %[a] offset:2048 sc1\n\tglobal_load_lds_dwordx4 %[va], %[a] offset:3072 sc1"
#define G2_SYNC "\n\ts_mov_b32 m0, %[la]\n\ts_waitcnt vmcnt(%[n])\n\ts_barrier\n\t"
    const unsigned oy = shm_base + G2_A + (p ^ role) * 4096, os = shm_base + G2_A + (p >> 1) * 8192 + (p & 1) * 2048;
    asm volatile("s_mov_b32 m0, %[la]\n\ts_nop 0\n\t" G2_A4 :: [la] "s"(sl + oy), [a] "s"(sa), [va] "v"(va) : "memory", "m0");
    auto step = [&](long adv = 16384) __attribute__((always_inline)) {
      wsel();
      ++st;
      sa += adv;
      const unsigned sn = (sl + 16384) & 65535;
      asm volatile(G2_W G2_SYNC G2_A4 :: [l] "s"(sl), [la] "s"(sn + oy), [n] "n"(16), [a] "s"(sa), [va] "v"(va), G2_WOP(wy) : "memory", "scc", "m0");
      sl = sn;
    };
    auto chk1 = [&](u64 mask) __attribute__((always_inline)) { if (p == 1) { WAIT_V(16); check(mask, true); } };
    step();
    chk1(0xcc00cc00ull << 32);
    step(); step();
    if (p == 1) peek();
    step(); step();
    chk1(0x0f0fu << (16 * role));
#pragma unroll 1
    for (int g = 7; g < 13; ++g) step();
    if (p == 1) peek();
    step(); step();
    chk1((0xffffu << (16 * role)) | 0x55555555u);
#pragma unroll 1
    for (int g = 15; g < NY - 6; ++g) step();
    if (ny > NY) { step(); step((1 - 2 * role) * NY * 16384); }
    wsel();
    sa = sz;
    {
      const unsigned sn = (sl + 16384) & 65535;
      asm volatile(G2_W G2_SYNC G2_A2 :: [l] "s"(sl), [la] "s"(sn + os), [n] "n"(16), [a] "s"(sa), [va] "v"(va), G2_WOP(wy) : "memory", "scc", "m0");
      sl = sn;
    }
    sw[0] = wz.wb[0]; sw[1] = wz.wb[1];
#pragma unroll
    for (int j = -3; j < 13; ++j) {
      if (j == GETJ + 1 || j == GETJ + 2)
        asm volatile("s_mov_b32 m0, %[l]\n\ts_nop 0\n\tglobal_load_lds_dwordx4 %[va], %[a] offset:%[o] sc1\n\tglobal_load_lds_dwordx4 %[va], %[a] offset:%[o]+1024 sc1"
                     :: [l] "s"(shm_base + G2_X + 16384 + p * 4096), [a] "s"((const char*)slab + (size_t)(bid ^ 5) * 16384 + p * 4096), [va] "v"(va),
                        [o] "n"((j - GETJ - 1) * 2048) : "memory", "m0");
      sa += 16384;
      const unsigned sn = (sl + 16384) & 65535;
      constexpr int G = GETJ;
      if (j < 12) asm volatile(G2_W G2_SYNC G2_A2 :: [l] "s"(sl), [la] "s"(sn + os), [n] "n"((j == -3 ? 14 : 12) + (j > G && j < G + 3 ? 2 : 0) + (j > G + 1 && j < G + 4 ? 2 : 0)),
                               [a] "s"(sa), [va] "v"(va), G2_WOP(wz) : "memory", "scc", "m0");
      else asm volatile(G2_W :: [l] "s"(sl), G2_WOP(wz) : "memory", "scc", "m0");
      sl = sn;
      sw[0] += ws0; sw[1] += ws1;
    }
    WAIT_V(12);
    BAR();
    WAIT_V(6);
    BAR();
    WAIT_V(0);
    BAR();
    asm volatile("" : "+v"(fl));
    TS(5);
    if (tid < 258) __hip_atomic_store(lane ? done2 + bid * 8 : &acks[bid], __builtin_amdgcn_s_memrealtime(), RLX_AGENT);
    if (__builtin_expect(!cok, 0) && p == 0) {
      const bool mine = lane == 32 || (lane < 32 && (tya1 || ((lane >> 3) & 1) != (xcd >> 2)));
      const u64* const pa = &acks[lane < 32 ? client(lane) : bid ^ 5];
      while (!__all(!mine || fresh(__hip_atomic_load(pa, RLX_AGENT), t_own))) {
        notify(0, 32);
        if (lane == 0) __hip_atomic_store(done2 + bid * 8, __builtin_amdgcn_s_memrealtime(), RLX_AGENT);
        __builtin_amdgcn_s_sleep(32);
      }
    }
  } else {
    TS(6);
    f32x4 acc[4][4], sacc[2][4];
    const int fr = lane & 15, fq = lane >> 4, q4 = fr >> 2, pp = lane & 3;
    const int ff = ((q4 >> 1) & 1) | ((fq & 1) << 1);
    int aof = G2_A + wm * 8192 + lane * 16;
    int boff[4];
#pragma unroll
    for (int j = 0; j < 4; ++j) boff[j] = wn * 8192 + (8 * fq + q4) * 128 + ((j ^ ff) * 32) + pp * 8;
    int vo = wid * 4096 + lane * 16;
    int vx = ((m0 + wm * 64 + role * 32 + 2 * fr) * D_ + n0 + wn * 64) * 2 + (fq & 1) * 32 + (fq >> 1) * 16;
    lds_cptr px = shm3 + G2_X + wid * 4096 + (fq >> 1) * 512 + fr * 16 + (fq & 1) * 8;
    asm volatile("" : "+v"(vo), "+v"(vx), "+v"(px));
#define SB() __builtin_amdgcn_sched_barrier(0)
    struct Fr { bf16x8 a[4]; bf16x4 bl[4], bh[4]; };
    Fr fa[2], fb[2];
    auto rd = [&](Fr* f, int odd, int t) {
      constexpr int ord[12] = {0, 1, 8, 2, 3, 4, 5, 6, 7, 9, 10, 11};
      constexpr int seq[24] = {0, 1, 2, 12, 13, 14, 15, 16, 3, 4, 5, 6, 17, 18, 19, 20, 7, 8, 9, 21, 10, 22, 11, 23};
      lds_cptr sb = shm3 + odd * 16384;
      const int ks = seq[t] / 12, o = ord[seq[t] % 12];
      if (o >= 8) f[ks].a[o - 8] = *(const __attribute__((address_space(3))) bf16x8*)(sb + aof + (o - 8) * 2048 + ks * 1024);
      else if (o & 1) f[ks].bh[o >> 1] = tr_read(sb + boff[o >> 1] + ks * 4096 + 512);
      else f[ks].bl[o >> 1] = tr_read(sb + boff[o >> 1] + ks * 4096);
    };
    auto bfr = [&](const Fr& c, int j) { return __builtin_shufflevector(c.bl[j], c.bh[j], 0, 1, 2, 3, 4, 5, 6, 7); };
    auto mf1 = [](f32x4& c, bf16x8 b, bf16x8 a, bool z = false) {
      if (z) asm volatile("v_mfma_f32_16x16x32_bf16 %0, %1, %2, 0" : "=&v"(c) : "v"(b), "v"(a));
      else asm volatile("v_mfma_f32_16x16x32_bf16 %0, %1, %2, %0" : "+v"(c) : "v"(b), "v"(a));
    };
    auto mf2 = [](f32x4& c, bf16x8 b, bf16x8 a, bf16x8 b2, bf16x8 a2, bool z) {
      if (z) asm volatile("v_mfma_f32_16x16x32_bf16 %0, %1, %2, 0\n\tv_mfma_f32_16x16x32_bf16 %0, %3, %4, %0" : "=&v"(c) : "v"(b), "v"(a), "v"(b2), "v"(a2));
      else asm volatile("v_mfma_f32_16x16x32_bf16 %0, %1, %2, %0\n\tv_mfma_f32_16x16x32_bf16 %0, %3, %4, %0" : "+v"(c) : "v"(b), "v"(a), "v"(b2), "v"(a2));
    };
    auto pairs = [&](f32x4 (*ac)[4], const Fr* c, int nq, int mx, int nf, auto fill, bool z = false) {
#pragma unroll
      for (int q = 0; q < nq; ++q) {
        const int mf = (q >> 2) ^ mx, j = (q & 4) ? 3 - (q & 3) : q & 3, ks = q & 1;
        SB();
        if (2 * q >= nf) mf2(ac[mf][j], bfr(c[ks], j), c[ks].a[mf], bfr(c[ks ^ 1], j), c[ks ^ 1].a[mf], z);
        else {
          mf1(ac[mf][j], bfr(c[ks], j), c[ks].a[mf], z);
          SB();
          fill(2 * q);
          SB();
          mf1(ac[mf][j], bfr(c[ks ^ 1], j), c[ks ^ 1].a[mf]);
          SB();
          fill(2 * q + 1);
        }
      }
      SB();
    };
    auto flip = [&]() {
      aof ^= 32768; boff[0] ^= 32768; boff[1] ^= 32768; boff[2] ^= 32768; boff[3] ^= 32768;
      asm volatile("" : "+v"(aof), "+v"(boff[0]), "+v"(boff[1]), "+v"(boff[2]), "+v"(boff[3]));
    };
    auto bar = [&]() { __builtin_amdgcn_s_waitcnt(0xC07F); BAR(); };
    auto ystg = [&](const Fr* c, Fr* n, int odd, bool z = false) {
      pairs(acc, c, 16, 0, odd ? 30 : 24, [&](int t) {
        if (t < 24) rd(n, odd, t);
        else if (t == 24) asm volatile("s_nop 0");
        else { int& a = t == 29 ? aof : boff[t - 25]; a ^= 32768; asm volatile("" : "+v"(a)); }
      }, z);
    };
    auto stg = [&](const Fr* c, Fr* n, int odd, bool z = false) { pairs(sacc, c, 8, 0, 10, [&](int t) { rd(n, odd, 2 * t); rd(n, odd, 2 * t + 1); }, z); };
    asm volatile("" : "+v"(aof), "+v"(boff[0]), "+v"(boff[1]), "+v"(boff[2]), "+v"(boff[3]));
    await(0xffffu, 0x404u);
    auto lda = [&](int i) {
      asm volatile("s_mov_b32 m0, %[l]\n\ts_nop 0\n\tglobal_load_lds_dwordx4 %[va], %[a] sc1\n\tglobal_load_lds_dwordx4 %[va], %[a] offset:1024 sc1\n\t"
                   "global_load_lds_dwordx4 %[va], %[a] offset:2048 sc1\n\tglobal_load_lds_dwordx4 %[va], %[a] offset:3072 sc1"
                   :: [l] "s"(shm_base + G2_A + (i + 1) * 16384 + (p ^ role) * 4096), [a] "s"(sa + i * 16384), [va] "v"(va) : "memory", "m0");
    };
    need(0x00330033ull << 32, false);
    TS(4);
    lda(0);
    lda(1);
    need(0x00ff00ffull << 32, false);
    TS(0);
    lda(2);
    WAIT_V(8);
    signal(1u << 24);
    await(0xff000000u, 4u << 24);
    __builtin_amdgcn_s_waitcnt(0xC07F);
    SB();
#pragma unroll
    for (int t = 0; t < 24; ++t) rd(fa, 1, t);
    flip();
#define CPRIO 0
    __builtin_amdgcn_s_setprio(CPRIO);
    WAIT_V(4);
    TS(5);
    peek();
    bar();
    TS(7);
    ystg(fa, fb, 0, true);
    WAIT_V(1);
    bar();
    ystg(fb, fa, 1);
    WAIT_V(0);
    check(0x33003300ull << 32, false);
    static_assert((NY - DLT) % 2 == 1, "");
#pragma unroll 1
    for (int g = 2; g < ny - 1; g += 2) {
      bar();
      ystg(fa, fb, 0);
      bar();
      ystg(fb, fa, 1);
    }
    bar();
    {
      const __amdgpu_buffer_rsrc_t rs = __builtin_amdgcn_make_buffer_rsrc((void*)((char*)slab + (size_t)bid * 16384), 0, 16384, 0x00020000);
      u32x4 pk;
      pairs(acc, fa, 16, 2, 32, [&](int t) {
        if (t < 20) rd(fb, 0, t);
        else {
          const int f = (t - 20) / 3, e = (t - 20) % 3;
          if (e < 2) { const f32x4 a = acc[2 + (f >> 1)][(f & 1) * 2 + e]; pk[2 * e] = pack2(a[0], a[1]); pk[2 * e + 1] = pack2(a[2], a[3]); }
          else __builtin_amdgcn_raw_buffer_store_b128(pk, rs, vo + f * 1024, 0, 16);
        }
      });
    }
    TS(2);
    static_assert(GETJ % 2 == 1 && GETJ >= 3 && GETJ <= 7, "");
    f32x4 xf[2][4];
#pragma unroll
    for (int j = 0; j < GETJ + 5; j += 2) {
      bar();
      stg(fb, fa, 1, j == 0);
      flip();
      bar();
      u32x2 xv[2];
      pairs(sacc, fa, 8, 0, 12, [&](int t) {
        if (t < 10) { rd(fb, 0, 2 * t); rd(fb, 0, 2 * t + 1); }
        else if (j < 8) xv[t - 10] = *(const __attribute__((address_space(3))) u32x2*)(px + (j >> 1) * 1024 + (t - 10) * 256);
      });
      if (j < 8) {
#pragma unroll
        for (int e = 0; e < 2; ++e) xf[j >> 2][(j & 2) + e] = f32x4{lo_f(xv[e][0]), hi_f(xv[e][0]), lo_f(xv[e][1]), hi_f(xv[e][1])};
        asm volatile("" : "+v"(xf[j >> 2][j & 2]), "+v"(xf[j >> 2][(j & 2) + 1]));
        SB();
      }
    }
    WAIT_V(0);
    bar();
    u32x4 pv[4];
    {
      lds_cptr pp_ = shm3 + G2_X + 16384 + vo;
      pairs(sacc, fb, 8, 0, 12, [&](int t) {
        if (t < 2) {
#pragma unroll
          for (int f = 2 * t; f < 2 * t + 2; ++f) pv[f] = *(const __attribute__((address_space(3))) u32x4*)(pp_ + f * 1024);
        } else { rd(fa, 1, 2 * t - 4); rd(fa, 1, 2 * t - 3); }
      });
      if (__builtin_expect(blank(pv) || !cok, 0)) {
        const __amdgpu_buffer_rsrc_t rp = __builtin_amdgcn_make_buffer_rsrc((void*)((char*)slab + (size_t)(bid ^ 5) * 16384), 0, 16384, 0x00020000);
        while (!cok && !__all(fresh(__hip_atomic_load(done2 + (bid ^ 5) * 8, RLX_AGENT), t_own))) {
          notify(0, 32);
          if (lane == 0) __hip_atomic_store(done2 + bid * 8, __builtin_amdgcn_s_memrealtime(), RLX_AGENT);
          __builtin_amdgcn_s_sleep(8);
        }
        do {
          asm volatile("" ::: "memory");
#pragma unroll
          for (int f = 0; f < 4; ++f) pv[f] = __builtin_bit_cast(u32x4, __builtin_amdgcn_raw_buffer_load_b128(rp, vo + f * 1024, 0, 16));
        } while (blank(pv));
      }
    }
    auto silu = [&](int f0, int f1) {
#pragma unroll
      for (int f = f0; f < f1; ++f) {
        const int mf = f >> 2, j = f & 3;
        const u32x4 pw = pv[mf * 2 + (j >> 1)];
        const f32x4 ya = acc[mf][j] + f32x4{lo_f(pw[2 * (j & 1)]), hi_f(pw[2 * (j & 1)]), lo_f(pw[2 * (j & 1) + 1]), hi_f(pw[2 * (j & 1) + 1])};
#pragma unroll
        for (int e = 0; e < 2; ++e) {
          const f32x2 y2 = {ya[2 * e], ya[2 * e + 1]};
          const f32x2 ar = y2 * f32x2{-1.4426950408889634f, -1.4426950408889634f};
          const f32x2 dn = f32x2{__builtin_amdgcn_exp2f(ar[0]), __builtin_amdgcn_exp2f(ar[1])} + f32x2{1.0f, 1.0f};
          const f32x2 t = y2 * f32x2{__builtin_amdgcn_rcpf(dn[0]), __builtin_amdgcn_rcpf(dn[1])};
          acc[mf][j][2 * e] = t[0]; acc[mf][j][2 * e + 1] = t[1];
        }
        asm volatile("" : "+v"(acc[mf][j]));
      }
      SB();
    };
#define SPT 0, 1, 2, 3, 5, 7, 8, 8
    constexpr int spt[] = {SPT};
    silu(0, spt[1]);
#pragma unroll
    for (int j = GETJ + 6; j < 15; ++j) {
      if (j & 1) flip();
      bar();
      if (j & 1) stg(fa, fb, 0); else stg(fb, fa, 1);
      silu(spt[j - GETJ - 5], j == 14 ? 8 : spt[j - GETJ - 4]);
    }
    pairs(sacc, fa, 8, 0, 0, [](int) {});
    asm volatile("s_nop 7\n\ts_nop 3" : "+v"(sacc[1][0]), "+v"(sacc[1][1]), "+v"(sacc[1][2]), "+v"(sacc[1][3]));
    SB();
    TS(1);
    {
      const __amdgpu_buffer_rsrc_t ro = __builtin_amdgcn_make_buffer_rsrc((void*)out, 0, S_ * D_ * 2, 0x00020000);
#pragma unroll
      for (int f = 0; f < 4; ++f) {
        const int mf = f >> 1, j = (f & 1) * 2;
        const f32x4 a = __builtin_elementwise_fma(sacc[mf][j], acc[mf][j], xf[mf][j]), b = __builtin_elementwise_fma(sacc[mf][j + 1], acc[mf][j + 1], xf[mf][j + 1]);
        const auto r0 = __builtin_amdgcn_permlane16_swap(pack2(a[0], a[1]), pack2(b[0], b[1]), false, false);
        const auto r1 = __builtin_amdgcn_permlane16_swap(pack2(a[2], a[3]), pack2(b[2], b[3]), false, false);
        __builtin_amdgcn_raw_buffer_store_b128(u32x4{r0[0], r1[0], r0[1], r1[1]}, ro, vx + mf * 2048 + j * 32, 0, 2);
      }
    }
    TS(3);
  }
  TSFLUSH();
  }
  asm volatile(".Lcode_end:");
}

extern "C" void kernel_launch(void* const* d_in, const int* in_sizes, int n_in, void* d_out, int out_size, void* d_ws,
                              size_t ws_size, hipStream_t stream) {
  const u16* A_x = (const u16*)d_in[0];
  const u16* A_wqkv = (const u16*)d_in[1];
  const u16* A_wo = (const u16*)d_in[2];
  const u16* B_wg = (const u16*)d_in[4];
  const u16* B_wu = (const u16*)d_in[5];
  const u16* B_wd = (const u16*)d_in[6];
  u16* out = (u16*)d_out;
  fused_kernel<<<256, 512, 0, stream>>>(A_x, A_wqkv, B_wg, B_wu, B_wd, A_wo, out, (char*)d_ws);
}
```

```cpp
#include <hip/hip_runtime.h>
#include <hip/hip_bf16.h>
#include <stdint.h>
#include <type_traits>

typedef unsigned short u16;
using bf16x8 = __attribute__((ext_vector_type(8))) short;
using bf16x4 = __attribute__((ext_vector_type(4))) short;
using f32x4 = __attribute__((ext_vector_type(4))) float;
using u32x2 = __attribute__((ext_vector_type(2))) unsigned;
using u32x4 = __attribute__((ext_vector_type(4))) unsigned;
typedef __attribute__((address_space(3))) const char* lds_cptr;

constexpr int S_ = 2048, D_ = 1024, F_ = 3584;

__device__ __forceinline__ float bf2f(u16 h) { return __uint_as_float((unsigned)h << 16); }
__device__ __forceinline__ u16 f2bf(float f) {
  unsigned u = __float_as_uint(f);
  u += 0x7fffu + ((u >> 16) & 1u);
  return (u16)(u >> 16);
}
__device__ __forceinline__ float rbf(float f) { return bf2f(f2bf(f)); }
typedef __attribute__((ext_vector_type(2))) __bf16 bf16x2_t;
typedef __attribute__((ext_vector_type(2))) float f32x2;
__device__ __forceinline__ unsigned pack2(float a, float b) {
  bf16x2_t r = __builtin_convertvector((f32x2){a, b}, bf16x2_t);
  return __builtin_bit_cast(unsigned, r);
}
__device__ __forceinline__ float lo_f(unsigned p) { return __uint_as_float(p << 16); }
__device__ __forceinline__ float hi_f(unsigned p) { return __uint_as_float(p & 0xffff0000u); }

#define TSDECL
#define TSS(i) do {} while (0)
#define TSFLUSH() do {} while (0)
#define TS1(i) TSS(i)
#define TS(i) TSS(8 + (i))
#define PIN_S(v) asm volatile("" : "+s"(v))
#define WAIT_V(n) asm volatile("s_waitcnt vmcnt(%0)" ::"n"(n) : "memory")
#define WAIT_L(n) asm volatile("s_waitcnt lgkmcnt(%0)" ::"n"(n) : "memory")
#define BARL() do { WAIT_L(0); BAR(); } while (0)
#define BAR() do { asm volatile("" ::: "memory"); __builtin_amdgcn_s_barrier(); asm volatile("" ::: "memory"); } while (0)

__device__ __forceinline__ bf16x4 tr_read(lds_cptr p) {
  return __builtin_amdgcn_ds_read_tr16_b64_v4i16((__attribute__((address_space(3))) bf16x4*)p);
}
__device__ __forceinline__ bf16x8 tr_frag(lds_cptr p) {
  bf16x4 lo = tr_read(p), hi = tr_read(p + 512);
  bf16x8 r;
  r[0] = lo[0]; r[1] = lo[1]; r[2] = lo[2]; r[3] = lo[3];
  r[4] = hi[0]; r[5] = hi[1]; r[6] = hi[2]; r[7] = hi[3];
  return r;
}
__device__ __forceinline__ void glds16(const void* gsrc, unsigned lds_dst) {
  unsigned keep;
  asm volatile("s_mov_b32 %0, m0\n\ts_mov_b32 m0, %2\n\ts_nop 0\n\tglobal_load_lds_dwordx4 %1, off\n\ts_mov_b32 m0, %0"
               : "=&s"(keep) : "v"(gsrc), "s"(lds_dst) : "memory");
}
#define GLDS(gptr, laddr) glds16((const void*)(gptr), (unsigned)(laddr))
__device__ __forceinline__ void glds16_sc1(const void* gsrc, unsigned lds_dst) {
  unsigned keep;
  asm volatile("s_mov_b32 %0, m0\n\ts_mov_b32 m0, %2\n\ts_nop 0\n\tglobal_load_lds_dwordx4 %1, off sc1\n\ts_mov_b32 m0, %0"
               : "=&s"(keep) : "v"(gsrc), "s"(lds_dst) : "memory");
}
#define GLDS_SC1(gptr, laddr) glds16_sc1((const void*)(gptr), (unsigned)(laddr))
__device__ __forceinline__ void glds16_nc(const void* gsrc, unsigned lds_dst) {
  unsigned keep;
  asm volatile("s_mov_b32 %0, m0\n\ts_mov_b32 m0, %2\n\ts_nop 0\n\tglobal_load_lds_dwordx4 %1, off\n\ts_mov_b32 m0, %0"
               : "=&s"(keep) : "v"(gsrc), "s"(lds_dst));
}
#define GLDS_NC(gptr, laddr) glds16_nc((const void*)(gptr), (unsigned)(laddr))
__device__ __forceinline__ void glds16_s(const void* sbase, unsigned voff, unsigned lds_dst) {
  asm volatile("s_mov_b32 m0, %2\n\ts_nop 0\n\tglobal_load_lds_dwordx4 %0, %1" :: "v"(voff), "s"(sbase), "s"(lds_dst) : "m0");
}

__device__ __forceinline__ void glds16_s1(const void* sbase, unsigned voff, unsigned lds_dst) {
  asm volatile("s_mov_b32 m0, %2\n\ts_nop 0\n\tglobal_load_lds_dwordx4 %0, %1 sc1" :: "v"(voff), "s"(sbase), "s"(lds_dst) : "memory", "m0");
}
__device__ __forceinline__ void glds16_v(const void* p, unsigned lds_dst) {
  asm volatile("s_mov_b32 m0, %1\n\ts_nop 0\n\tglobal_load_lds_dwordx4 %0, off" :: "v"(p), "s"(lds_dst) : "m0");
}
template <int OFF = 0>
__device__ __forceinline__ void gst16(const void* sbase, unsigned voff, u32x4 v) {
  asm volatile("global_store_dwordx4 %0, %1, %2 offset:%3 sc1\n\ts_nop 1" :: "v"(voff), "v"(v), "s"(sbase), "n"(OFF) : "memory");
}

typedef unsigned long long u64;
__device__ __forceinline__ bool fresh(u64 stamp, u64 t_own) {
  const u64 b = t_own + 100ull;
  const unsigned lo = (unsigned)stamp - (unsigned)b, hi = (unsigned)(stamp >> 32) - (unsigned)(b >> 32) - ((unsigned)stamp < (unsigned)b ? 1u : 0u);
  return (hi == 0u) & (lo < (unsigned)(__builtin_amdgcn_s_memrealtime() - t_own));
}
constexpr u64 TIMEOUT_ = 200000;
__device__ __forceinline__ float half_sum(float v) {
  v += __builtin_bit_cast(float, __builtin_amdgcn_update_dpp(0, __builtin_bit_cast(int, v), 0xB1, 0xf, 0xf, false));
  v += __builtin_bit_cast(float, __builtin_amdgcn_update_dpp(0, __builtin_bit_cast(int, v), 0x4E, 0xf, 0xf, false));
  v += __builtin_bit_cast(float, __builtin_amdgcn_update_dpp(0, __builtin_bit_cast(int, v), 0x141, 0xf, 0xf, false));
  v += __builtin_bit_cast(float, __builtin_amdgcn_update_dpp(0, __builtin_bit_cast(int, v), 0x140, 0xf, 0xf, false));
  { auto r = __builtin_amdgcn_permlane16_swap(__float_as_uint(v), __float_as_uint(v), false, false); v = __uint_as_float(r[0]) + __uint_as_float(r[1]); }
  return v;
}
__device__ __forceinline__ unsigned pack_stats(float mean, float var) {
  return (unsigned)f2bf(mean) | ((unsigned)f2bf(sqrtf(rbf(rbf(var) + 1e-5f))) << 16);
}
__device__ __forceinline__ unsigned row_stats_serial(const u16* xr) {
  float s = 0.f, q = 0.f;
#pragma unroll 1
  for (int k = 0; k < D_; ++k) s += bf2f(xr[k]);
  const float mean = s * (1.0f / D_);
#pragma unroll 1
  for (int k = 0; k < D_; ++k) { const float d = bf2f(xr[k]) - mean; q += d * d; }
  return pack_stats(mean, q * (1.0f / D_));
}
constexpr int G1_STAGE = 32768 + 5 * 8192;
#define EPOS 2
#define PA 4
#define PB 8
#define PC 12
#define DLT 1
#define PSLEEP 2
#define GETJ 3

constexpr unsigned BLANK = 0x7f817f81u;
__device__ __forceinline__ bool blank(const u32x4* pv) {
  u64 m;
#define CX(i) "v_cmpx_ne_u32 vcc, %1, %" #i "\n\t"
  asm volatile(CX(2) CX(3) CX(4) CX(5) CX(6) CX(7) CX(8) CX(9) CX(10) CX(11) CX(12) CX(13) CX(14) CX(15) CX(16) CX(17)
               "s_mov_b64 %0, exec\n\ts_mov_b64 exec, -1\n\ts_nop 3" : "=s"(m) : "s"(BLANK), "v"(pv[0][0]), "v"(pv[0][1]), "v"(pv[0][2]), "v"(pv[0][3]),
               "v"(pv[1][0]), "v"(pv[1][1]), "v"(pv[1][2]), "v"(pv[1][3]), "v"(pv[2][0]), "v"(pv[2][1]), "v"(pv[2][2]), "v"(pv[2][3]),
               "v"(pv[3][0]), "v"(pv[3][1]), "v"(pv[3][2]), "v"(pv[3][3]) : "vcc");
  return m != ~0ull;
}
#define RLX_AGENT __ATOMIC_RELAXED, __HIP_MEMORY_SCOPE_AGENT

constexpr int G2_A = 65536, G2_X = 131072;
constexpr int G2_NSTAGE = 4;
constexpr size_t MB_ = 1024 * 1024;
__global__ __launch_bounds__(512, 2) __attribute__((aligned(4096))) void fused_kernel(const u16* __restrict__ x, const u16* __restrict__ wqkv,
                                                       const u16* __restrict__ wg, const u16* __restrict__ wu,
                                                       const u16* __restrict__ wd, const u16* __restrict__ wo,
                                                       u16* __restrict__ out, char* __restrict__ ws) {
  u16* const halo = (u16*)(ws + 4 * MB_);
  u16* const abuf = (u16*)(ws + 16 * MB_);
  char* const slab = ws + 64 * MB_;
  unsigned* const flags = (unsigned*)(ws + 96 * MB_);
  u32x4* const rstat = (u32x4*)(ws + 97 * MB_);
  char* const wcopy = ws + 98 * MB_;
  u64* const done1 = (u64*)(ws + 102 * MB_);
  u64* const done2 = (u64*)(ws + 100 * MB_ + 65536);
  u64* const donec = (u64*)(ws + 100 * MB_ + 131072);
  u64* const acks = (u64*)(ws + 100 * MB_ + 196608);
  float* const csum = (float*)(ws + 101 * MB_);
  __shared__ __attribute__((aligned(1024))) char shm[163840];
  constexpr int AUX = 2 * G1_STAGE + 8192;
  {
    u64 pc; int len;
    asm volatile("s_getpc_b64 %0\n.Lcode_beg:\n\ts_mov_b32 %1, .Lcode_end-.Lcode_beg" : "=s"(pc), "=s"(len));
    const int ol = ((blockIdx.x >> 3) + 32 * (threadIdx.x >> 6)) * 1024 + (threadIdx.x & 63) * 16;
    asm volatile("s_mov_b32 m0, %2\n\ts_nop 0\n\tglobal_load_lds_dwordx4 %0, %1"
                 :: "v"(ol < len - 16 ? ol : 0), "s"(pc & ~15ull), "s"((unsigned)(uintptr_t)(lds_cptr)shm + 2 * G1_STAGE + 7168) : "m0");
    __builtin_amdgcn_sched_barrier(0);
  }
  const u64 t_own = __builtin_amdgcn_s_memrealtime();
  TSDECL;
  TS1(7);
  const int tid = threadIdx.x, lane = tid & 63;
  const int wid = __builtin_amdgcn_readfirstlane(tid >> 6);
  const int wm = wid & 3, wc = wid >> 2;
  int bid_ = blockIdx.x;
  PIN_S(bid_);
  const int bid = bid_ & 255, xcd = bid & 7, idx = bid >> 3;
  const int ntile = xcd * 4 + (idx & 3), mtile = idx >> 2;
  const int m0 = mtile * 256;
  const int hg = ntile >> 1;
  const bool tya = (ntile & 1) == 0;
  const int srow = m0 + ntile * 8 + wm * 2;
  lds_cptr shm3 = (lds_cptr)shm;
  const unsigned shm_base = (unsigned)(uintptr_t)shm3;
  const int r8 = tid >> 3, pos = tid & 7;
  const int phys = pos >> 1, half = pos & 1;
  const int fk = ((r8 >> 1) & 1) | (((r8 >> 3) & 1) << 1);
  const int vsrc = phys ^ fk;
  const int qstr = tya ? 3072 * 2 : F_ * 2;
  unsigned voa = (((r8 & 32) + 2 * (r8 & 15) + ((r8 >> 4) & 1)) * D_ + ((pos ^ (r8 & 7)) * 8)) * 2;
  unsigned vog = r8 * (F_ * 2) + vsrc * 32 + half * 16, voq = r8 * qstr + vsrc * 32 + half * 16;
  unsigned vom = r8 * qstr + (vsrc >> (tya ? 0 : 1)) * 32 + half * 16;
  asm volatile("" : "+v"(voa), "+v"(vog), "+v"(voq), "+v"(vom));
  const int kt0 = (hg >> 1) * 7, he = hg & 1;
  unsigned oq = (tya ? hg : kt0 + 2 * he) * 128, og = (kt0 + (tya ? 5 + he : 2 * he + 1)) * 128, om = tya ? 4096 : 512 - 192 * he;
  unsigned d0 = shm_base + wid * 1024 + 32768, ox = m0 * D_ * 2;
  long uq = -32 * qstr;
  PIN_S(oq); PIN_S(og); PIN_S(om); PIN_S(d0); PIN_S(ox); PIN_S(uq);
  __builtin_amdgcn_sched_barrier(0);
  asm volatile("" :: "s"(x), "s"(wqkv), "s"(wg), "s"(wu), "s"(wd), "s"(wo), "s"(out), "s"(ws));
  const char* const ab = (const char*)x + ox;
  int kz = 0;
  asm volatile("" : "+s"(kz));
  u64 pq = (u64)wqkv, pg = (u64)wg;
  PIN_S(pq); PIN_S(pg);
  const char* const qb = (const char*)(tya ? pq : pg) + oq;
  if (wid < 4) {
#pragma unroll
    for (int i = 0; i < 8; ++i) glds16_s(ab + i * 32 * D_ * 2, voa, d0 - 32768 + i * 4096);
  } else {
    glds16_s(qb, voq, d0); glds16_s(qb + uq, voq, d0 - 4096);
  }
  __builtin_amdgcn_sched_barrier(0);
  const char* const gb = (const char*)wg + og;
  const long dgu = (const char*)wu - (const char*)wg, dp1 = tya ? 2048 : dgu;
  const char* const mb0 = qb + om;
  const char* mptr = mb0 + (((vsrc & 1) && !tya) ? dgu : 0) + vom;
  if (wid >= 4) {
    const long ug = kz - 32 * F_ * 2;
    glds16_s(qb + dp1, voq, d0 + 8192); glds16_s(qb + dp1 + uq, voq, d0 + 4096);
    glds16_v(mptr, d0 + 2 * 8192); glds16_v(mptr + uq, d0 + 2 * 8192 - 4096);
    glds16_s(gb, vog, d0 + 3 * 8192); glds16_s(gb + ug, vog, d0 + 3 * 8192 - 4096);
    glds16_s(gb + dgu, vog, d0 + 4 * 8192); glds16_s(gb + dgu + ug, vog, d0 + 4 * 8192 - 4096);
  }
  __builtin_amdgcn_sched_barrier(0);
  f32x4 acc[4][10];

  constexpr int NT = D_ / 64;
  constexpr int CNT = AUX + 4096 + 3584;
  if (tid < 8) *(unsigned*)(shm + CNT + tid * 4) = 0u;
  bf16x8 af0[4], af1[4];
  bf16x4 bl[4], bh[4];
#define SB0() __builtin_amdgcn_sched_barrier(0)
  auto mm = [&](int g, int t, bool two, bool z = false) {
    const int mi = t >> 1, mf = ((g & 1) && g != 9) ? 3 - mi : mi, ks = (t & 1) ^ (mi & 1), s = ((g + 1) & 1) * 2;
    const bf16x8 b0 = __builtin_shufflevector(bl[s + ks], bh[s + ks], 0, 1, 2, 3, 4, 5, 6, 7), a0 = ks ? af1[mf] : af0[mf];
    const bf16x8 b1 = __builtin_shufflevector(bl[s + 1 - ks], bh[s + 1 - ks], 0, 1, 2, 3, 4, 5, 6, 7), a1 = ks ? af0[mf] : af1[mf];
    const bool tr = (g >> 1) == 2;
    if (z && two) asm volatile("v_mfma_f32_16x16x32_bf16 %0, %1, %2, 0\n\tv_mfma_f32_16x16x32_bf16 %0, %3, %4, %0" : "=&v"(acc[mf][g])
                               : "v"(tr ? a0 : b0), "v"(tr ? b0 : a0), "v"(tr ? a1 : b1), "v"(tr ? b1 : a1));
    else if (z && !(t & 1)) asm volatile("v_mfma_f32_16x16x32_bf16 %0, %1, %2, 0" : "=&v"(acc[mf][g]) : "v"(tr ? a0 : b0), "v"(tr ? b0 : a0));
    else if (two) asm volatile("v_mfma_f32_16x16x32_bf16 %0, %1, %2, %0\n\tv_mfma_f32_16x16x32_bf16 %0, %3, %4, %0" : "+v"(acc[mf][g])
                          : "v"(tr ? a0 : b0), "v"(tr ? b0 : a0), "v"(tr ? a1 : b1), "v"(tr ? b1 : a1));
    else asm volatile("v_mfma_f32_16x16x32_bf16 %0, %1, %2, %0" : "+v"(acc[mf][g]) : "v"(tr ? a0 : b0), "v"(tr ? b0 : a0));
  };
  typedef __attribute__((address_space(3))) unsigned lds_u32;
  lds_u32* const cnt = (lds_u32*)(shm3 + CNT);
  bool cok;
  constexpr int NY = 28;
  int role, mt2, nt2;
  struct WS { unsigned vb[2][2]; const char* wb[2]; int sh[2]; };
  auto wsetup = [&](const u16* w, int crow0, int l, bool ck) __attribute__((always_inline)) {
    WS r;
    const unsigned vr = ((l >> 5) & 1) * 16 + (wm & 1) * 8 + ((l >> 3) & 3), vc = (((l >> 1) & 3) ^ ((l >> 4) & 1)) * 32 + (l & 1) * 16;
    u64 cp = (u64)(wcopy + (nt2 >> 2) * 589824 + crow0 * 128);
    asm volatile("" : "+s"(cp));
#pragma unroll
    for (int i = 0; i < 2; ++i) {
      u64 dp = (u64)(w + nt2 * 128 + i * 64);
      asm volatile("" : "+s"(dp));
      const bool slow = ck & ((((unsigned)dp >> 7) & 7) == 3);
      r.sh[i] = slow ? 7 : 11;
      r.wb[i] = (const char*)(slow ? cp : dp) + (crow0 ? (wm >> 1) * 32 << r.sh[i] : 0);
      r.vb[i][0] = (vr << r.sh[i]) + vc; r.vb[i][1] = ((vr + 4) << r.sh[i]) + (vc ^ 64);
    }
    asm volatile("" : "+v"(r.vb[0][0]), "+v"(r.vb[0][1]), "+v"(r.vb[1][0]), "+v"(r.vb[1][1]));
    PIN_S(r.wb[0]); PIN_S(r.wb[1]); PIN_S(r.sh[0]); PIN_S(r.sh[1]);
    return r;
  };
  WS wy, wz;
  int vxq, ws0, ws1;
  const char* sz;
  u32x4 xq[4];
  unsigned vg;
  int st = 3;
  const char *sa, *sw[2];
  auto wsel = [&]() {
    const unsigned r = __builtin_amdgcn_readlane(vg, st);
    sw[0] = wy.wb[0] + (r << wy.sh[0]); sw[1] = wy.wb[1] + (r << wy.sh[1]);
  };
  const int p = wid & 3;
  unsigned sl = 0;
#define G2_W "s_add_u32 m0, %[l], %[ow]\n\ts_nop 0\n\tglobal_load_lds_dwordx4 %[v0], %[w0] sc1\n\t" \
             "s_add_u32 m0, m0, 1024\n\ts_nop 0\n\tglobal_load_lds_dwordx4 %[v1], %[w0] sc1\n\t" \
             "s_add_u32 m0, m0, 7168\n\ts_nop 0\n\tglobal_load_lds_dwordx4 %[v2], %[w1] sc1\n\t" \
             "s_add_u32 m0, m0, 1024\n\ts_nop 0\n\tglobal_load_lds_dwordx4 %[v3], %[w1] sc1"
#define G2_WOP(w) [ow] "s"(shm_base + p * 2048), [w0] "s"(sw[0]), [w1] "s"(sw[1]), [v0] "v"(w.vb[0][0]), [v1] "v"(w.vb[0][1]), [v2] "v"(w.vb[1][0]), [v3] "v"(w.vb[1][1])
  auto tile2 = [&](int t0) __attribute__((always_inline)) {
    int b = bid;
    asm volatile("" : "+s"(b));
    role = 1 - ((b >> 2) & 1);
    mt2 = ((role ? b : b ^ 5) & 7) * 4 + ((b >> 3) & 3), nt2 = b >> 5;
    sa = (const char*)abuf + (size_t)(mt2 * 72 + role * NY + t0) * 16384 + wm * 4096;
    asm volatile("" : "+s"(sa));
  };
  auto prep = [&](bool first) __attribute__((always_inline)) {
    int l = threadIdx.x;
    asm volatile("" : "+v"(l));
    l &= 63;
    if (first) {
      tile2(3);
      vxq = ((mt2 * 128 + (wm >> 1) * 64 + role * 32 + 2 * (l & 15)) * D_ + nt2 * 128 + (wm & 1) * 64) * 2 + ((l >> 4) & 1) * 32 + (l >> 5) * 16;
      static_assert(DLT == 1, "");
      const int t = min(l, NY - 1), w = wm >> 1;
      const int o = (0xffaa5500fae504ull >> (2 * t)) & 3;
      int u1 = (0x990990 >> ((2 * t) & 31)) & 3, u2 = 3 + (t & 3);
      asm volatile("" : "+v"(u1), "+v"(u2));
      const int u = t < 12 ? u1 : u2;
      vg = (__umul24((l < NY ? role : 1 - role) * 4 + (((o & 1) << 1) | (o >> 1)), 14) + ((0xb85140a >> (4 * u)) & 15) + (u == 5 ? w : 2 * w)) * 32;
      asm volatile("" : "+v"(vg), "+v"(vxq));
      sz = (const char*)abuf + (size_t)(mt2 * 72 + 56) * 16384 + (wm >> 1) * 8192 + role * 4096 + (wm & 1) * 2048;
      PIN_S(sz);
    }
    wy = wsetup(wd, 0, l, first);
    wz = wsetup(wo, F_, l, first);
    ws0 = 64 << wz.sh[0]; PIN_S(ws0);
    ws1 = 64 << wz.sh[1]; PIN_S(ws1);
    SB0();
  };
  auto start2 = [&]() __attribute__((always_inline)) {
    const __amdgpu_buffer_rsrc_t rx = __builtin_amdgcn_make_buffer_rsrc((void*)x, 0, S_ * D_ * 2, 0x00020000);
#pragma unroll
    for (int f = 0; f < 4; ++f) xq[f] = __builtin_bit_cast(u32x4, __builtin_amdgcn_raw_buffer_load_b128(rx, vxq + (f >> 1) * 2048 + (f & 1) * 64, 0, 0));
    cok = __builtin_amdgcn_readfirstlane(cnt[1] & cnt[2]) != 0u;
    if (__builtin_expect(!cok, 0)) prep(false);
    wsel();
    PIN_S(sw[0]); PIN_S(sw[1]);
    SB0();
  };
  auto kloop = [&](auto wmc) {
    constexpr bool LD = decltype(wmc)::value != 0;
    constexpr int DMP = 2 * G1_STAGE;
    const char* pa = nullptr;
    unsigned vob = 0;
    f32x4 ca = {0.f, 0.f, 0.f, 0.f}, cb = {0.f, 0.f, 0.f, 0.f}, cd = ca;
    bf16x8 sel = {0, 0, 0, 0, 0, 0, 0, 0};
    int tro = 0;
    const int psh = (wid == 2 && !tya) ? 1 : 0, pstr = wid < 3 ? qstr : F_ * 2;
    const char* const pbase = wid == 0 ? qb : (wid == 1 ? qb + dp1 : (wid == 2 ? mb0 : gb));
    int lane = threadIdx.x;
    asm volatile("; %1" : "+v"(lane) : "n"(LD ? 1 : 0));
    lane &= 63;
    const int fr = lane & 15, fq = lane >> 4, q4 = fr >> 2, pp = lane & 3;
    const int ff = ((q4 >> 1) & 1) | ((fq & 1) << 1);
    int aoff[2], boff[2];
#pragma unroll
    for (int ks = 0; ks < 2; ++ks) aoff[ks] = (wm * 64 + fr) * 128 + (((ks * 4 + fq) ^ (fr & 7)) * 16);
#pragma unroll
    for (int j = 0; j < 2; ++j) boff[j] = 32768 + (8 * fq + q4) * 128 + (((2 * wc + j) ^ ff) * 32) + pp * 8;
    unsigned vol = lane * 16;
    float sm = 0.f, sq = 0.f;
    const char* pf = (const char*)wd + (bid * 4 + wid) * 7168;
    long pstep = 64 * pstr;
    if (!LD) {
      const int rs = 256 + mtile * 8 + (lane >> 3), vs = ((lane & 7) >> 1) ^ (((rs >> 1) & 1) | ((mtile & 1) << 1));
      pa = pbase + ((psh & vs) ? dgu : 0) + (rs * pstr + (vs >> psh) * 32 + (lane & 1) * 16);
      vob = rs * (F_ * 2) + vs * 32 + (lane & 1) * 16;
      tro = DMP + wid * 1024 + q4 * 128 + ((fq ^ ((q4 >> 1) | ((mtile & 1) << 1))) * 32) + pp * 8;
      const short one = fr == fq ? 0x3f80 : 0;
      sel = bf16x8{one, one, one, one, one, one, one, one};
      asm volatile("" : "+v"(tro), "+v"(vol), "+v"(sel), "+v"(ca), "+v"(cb), "+v"(sm), "+v"(sq));
      PIN_S(pf); PIN_S(pstep);
    }
    auto accum = [&](lds_cptr p, int ob) {
      ca = __builtin_amdgcn_mfma_f32_16x16x32_bf16(tr_frag(p), sel, ca, 0, 0, 0);
      if (wid == 0) cb = __builtin_amdgcn_mfma_f32_16x16x32_bf16(tr_frag(p + ob), sel, cb, 0, 0, 0);
    };
    auto xrow = [&](int i) {
      u64 a = (u64)(x + (size_t)srow * D_ + i * 256), b = (u64)(wo + (bid * 4 + wid) * 1024);
      PIN_S(a); PIN_S(b);
      asm volatile("global_load_dwordx4 %0, %1, %2" : "+v"(cd) : "v"(vol + (vol & (i < 4 ? 512 : 0)) * 3), "s"(i < 4 ? a : b) : "memory");
    };
    const int q32 = 32 * qstr;
    const char *sq0 = qb + q32, *sq1 = sq0 + dp1, *sg0 = gb + 32 * F_ * 2, *sg1 = sg0 + dgu, *sx = ab + (long)(kz + 128 - 32 * D_ * 2);
    u32x2 mp[2] = {__builtin_bit_cast(u32x2, mptr + q32), __builtin_bit_cast(u32x2, mptr + 2 * q32)};
    u64 cy[2];
    unsigned dst = shm_base + G1_STAGE + wm * 1024;
    int dbuf = G1_STAGE;
    auto stage = [&](int i0, int i1) __attribute__((always_inline)) {
#pragma unroll
        for (int i = i0; i < i1; ++i) {
          const int q = i >> 1, h = i & 1;
          const unsigned ld = dst + h * 4096 + (q < 5 ? 32768 + q * 8192 : (q - 5) * 8192);
          if (q < 2) glds16_s((q ? sq1 : sq0) + h * q32, voq, ld);
          else if (q == 2) glds16_v(__builtin_bit_cast(const char*, mp[h]), ld);
          else if (q < 5) glds16_s((q == 4 ? sg1 : sg0) + h * 32 * F_ * 2, vog, ld);
          else glds16_s(sx + ((q - 5) * 64 + h * 32) * D_ * 2, voa, ld);
        }
    };
    u64 cy0 = 0, rt0 = 0;
#pragma unroll
    for (int a = 0; a < 4; ++a) { acc[a][9] = f32x4{0.f, 0.f, 0.f, 0.f}; asm volatile("" : "+v"(acc[a][9])); }
    if (!LD) {
      xrow(0);
      WAIT_V(1);
    }
    asm volatile("" : "+v"(aoff[0]), "+v"(aoff[1]), "+v"(boff[0]), "+v"(boff[1]));
    bf16x8 kp[2];
    auto body = [&](bool upd, bool keep = false, bool first = false) __attribute__((always_inline)) {
      lds_cptr sb = shm3;
      auto rdb = [&](int g, int i) {
        lds_cptr p = sb + boff[g & 1] + (g >> 1) * 8192 + (i >> 1) * 4096;
        const int s = ((g + 1) & 1) * 2 + (i >> 1);
        if (i & 1) bh[s] = tr_read(p + 512); else bl[s] = tr_read(p);
      };
      auto rda = [&](int ks, int mf) { (ks ? af1 : af0)[mf] = *(const __attribute__((address_space(3))) bf16x8*)(sb + aoff[ks] + mf * 2048); };
      SB0();
      __builtin_amdgcn_s_waitcnt(0xC07F);
#pragma unroll
      for (int gg = 0; gg < 10; ++gg) {
        const int g = (gg + 9) % 10;
#pragma unroll
        for (int t = 0; t < 8; ++t) {
          const bool two = t >= 4 && !(upd && g >= 5 && g <= 8);
          SB0();
          if ((!two || (t & 1)) && !(first && g == 9)) mm(g, two ? t - 1 : t, two, first);
          SB0();
          if (first && !LD && (g == 3 || g == 7) && t == 0) BAR();
          if (g == 9) {
            if (t == 0 || t == 2) { rdb(0, t); rdb(0, t + 1); }
            if (t & 1) { rda((t >> 1) & 1, t >> 1); rda(((t >> 1) & 1) ^ 1, t >> 1); }
          } else if (t < 4) rdb(g + 1, t);
          else if (keep && g == 7) {
            if (t == 5) { WAIT_V(0); kp[0] = tr_frag(shm3 + tro); }
            if (t == 7) kp[1] = tr_frag(shm3 + tro + 4096);
          } else if (upd) {
            if (g == 5 && t == 4) { sq0 += 2 * q32; PIN_S(sq0); }
            if (g == 5 && t == 5) { sq1 += 2 * q32; PIN_S(sq1); }
            if (g == 5 && t == 6) { sg0 += 64 * F_ * 2; PIN_S(sg0); }
            if (g == 6 && t == 4) { sg1 += 64 * F_ * 2; PIN_S(sg1); }
            if (g == 6 && t == 5) { sx += 128; PIN_S(sx); }
            if (g == 6 && t == 6) { dst -= dbuf; PIN_S(dst); }
            if (g == 7 && t == 4) asm volatile("v_add_co_u32 %0, %2, %4, %0\n\tv_add_co_u32 %1, %3, %4, %1" : "+v"(mp[0][0]), "+v"(mp[1][0]), "=&s"(cy[0]), "=&s"(cy[1]) : "s"(2 * q32));
            if (g == 7 && t == 6) asm volatile("v_addc_co_u32 %0, vcc, 0, %0, %2\n\tv_addc_co_u32 %1, vcc, 0, %1, %3" : "+v"(mp[0][1]), "+v"(mp[1][1]) : "s"(cy[0]), "s"(cy[1]) : "vcc");
            if (g == 8 && t == 4) { aoff[0] += dbuf; aoff[1] += dbuf; asm volatile("" : "+v"(aoff[0]), "+v"(aoff[1])); }
            if (g == 8 && t == 5) { boff[0] += dbuf; boff[1] += dbuf; asm volatile("" : "+v"(boff[0]), "+v"(boff[1])); }
            if (g == 8 && t == 6) { dbuf = -dbuf; PIN_S(dbuf); }
          }
        }
      }
      SB0();
    };
    TS1(0);
#define ARRP(o, c) do {} while (0)
    if (LD) {
      stage(0, PA);
      WAIT_V(6 + PA);
      TS1(5);
      BARL();
      stage(PA, PB);
      WAIT_V(2 + PB);
      BAR();
      stage(PB, PC);
      WAIT_V(PC);
      BAR();
      stage(PC, 18);
      body(true, false, true);
      WAIT_V(0);
      BARL();
#pragma clang loop unroll(disable)
      for (int kt = 1; kt < NT - 1; ++kt) {
        if (__builtin_expect(kt == 5 + wid, 0)) prep(true);
        stage(0, 18);
        body(true);
        WAIT_V(0);
        BARL();
        SB0();
      }
      start2();
      body(false);
    } else {
      char* wsx;
      auto tile = [&](int kt, bool keep) __attribute__((always_inline)) {
        SB0();
        ARRP(wid * 64 + kt * 4, true);
        BARL();
        body(false, keep);
        ARRP(320 + kt * 4 + 4, wid == 0);
        if (keep) asm volatile("" : "+v"(kp[0]), "+v"(kp[1])); else WAIT_V(0);
      };
      auto next = [&]() __attribute__((always_inline)) {
        aoff[0] += dbuf; aoff[1] += dbuf; boff[0] += dbuf; boff[1] += dbuf;
        dbuf = -dbuf;
        asm volatile("" : "+v"(aoff[0]), "+v"(aoff[1]), "+v"(boff[0]), "+v"(boff[1]), "+v"(vol), "+v"(tro) :: "memory");
        wsx = ws;
        PIN_S(wsx);
      };
      auto share = [&]() {
        glds16_v(pa, shm_base + DMP + wid * 1024);
        if (wid == 0) glds16_s(gb + dgu, vob, shm_base + DMP + 4096);
      };
      auto kept = [&]() {
        ca = __builtin_amdgcn_mfma_f32_16x16x32_bf16(kp[0], sel, ca, 0, 0, 0);
        if (wid == 0) cb = __builtin_amdgcn_mfma_f32_16x16x32_bf16(kp[1], sel, cb, 0, 0, 0);
      };
      auto cpub = [&](u32x4 a, u32x4 b) {
        if ((vol & 0xc0) == 0) {
          const char* const cs = wsx + 101 * MB_ + ntile * 10240 + mtile * 256;
          const unsigned co = (vol & 0x30) * 4 + (vol >> 8) * 16;
          gst16(cs + wid * 2048, co, a);
          if (wid == 0) gst16(cs + 8192, co, b);
        }
      };
      auto side0 = [&](int kt) __attribute__((always_inline)) {
        if (kt < 2) {
          glds16_v(pa - 2 * pstep, shm_base + DMP + 7168);
          if (wid == 0) glds16_s(gb + dgu, vob - 128 * F_ * 2, shm_base + DMP + 7168);
        }
        share();
        u32x4 xw;
        asm volatile("v_mov_b32 %0, %4\n\tv_mov_b32 %1, %5\n\tv_mov_b32 %2, %6\n\tv_mov_b32 %3, %7" : "=&v"(xw[0]), "=&v"(xw[1]), "=&v"(xw[2]), "=&v"(xw[3])
                     : "v"(cd[0]), "v"(cd[1]), "v"(cd[2]), "v"(cd[3]));
        xrow(kt + 1);
        next();
        if (kt) kept();
        pa += pstep; vob += 64 * F_ * 2;
        accum(shm3 + tro + ((kt & 1) * G1_STAGE + 32768 + mtile * 1024 + wid * 7168 - DMP), 4 * 8192);
#pragma unroll
        for (int i = 0; i < 4; ++i) {
          float lo, hi;
          asm volatile("v_lshlrev_b32 %2, 16, %4\n\tv_and_b32 %3, 0xffff0000, %4\n\tv_add_f32 %0, %0, %2\n\tv_fmac_f32 %1, %2, %2\n\t"
                       "v_add_f32 %0, %0, %3\n\tv_fmac_f32 %1, %3, %3" : "+v"(sm), "+v"(sq), "=&v"(lo), "=&v"(hi) : "v"(xw[i]));
        }
      };
      BARL();
      body(false, false, true);
      WAIT_V(0);
      side0(0);
#pragma clang loop unroll(disable)
      for (int kt = 1; kt < 4; ++kt) {
        tile(kt, true);
        side0(kt);
      }
#pragma clang loop unroll(disable)
      for (int kt = 4; kt < 12; ++kt) {
        tile(kt, true);
        share();
        glds16_s(pf, vol, shm_base + DMP + 7168);
        next();
        kept();
        pa += pstep; vob += 64 * F_ * 2;
        pf = kt == 10 ? (const char*)wo + (bid * 4 + wid) * 2048 + 1024 : pf + 1024;
        if (kt < 8) {
          const unsigned q1 = bid * 4 + wid + (kt - 5) * 1024, q0 = q1 - 1024;
          if (q0 < 1152) gst16(wsx + 98 * MB_ + (q0 & 1) * 589824 + (q0 >> 1) * 1024, vol, *(const u32x4*)(shm + AUX + wid * 1024 + vol));
          if (q1 < 1152) {
            const char* src = (const char*)((q1 >> 1) < 448 ? wd + (size_t)(q1 >> 1) * 8 * D_ : wo + (size_t)((q1 >> 1) - 448) * 8 * D_);
            src += ((3 - ((unsigned)(uintptr_t)src >> 7)) & 7) * 128 + (q1 & 1) * 1024;
            glds16_s(src, (vol >> 7) * 2048 + (vol & 0x70), shm_base + AUX + wid * 1024);
          }
          u32x4 bk;
          asm volatile("v_mov_b32 %0, %1" : "=v"(bk[0]) : "s"(BLANK));
          bk[1] = bk[0]; bk[2] = bk[0]; bk[3] = bk[0];
          gst16(wsx + 64 * MB_ + bid * 16384 + wid * 4096 + (kt - 4) * 1024, vol, bk);
          if (tya && (kt == 4 || kt == 7)) gst16(wsx + 4 * MB_ + (mtile * 32 + ntile) * 4096 + wid * 2048 + (kt & 1) * 1024, vol, bk);
          if (kt == 4) cpub(bk, bk);
        } else {
          if (kt == 8) {
            const float ml = sm * (1.0f / 32), mean = half_sum(sm) * (1.0f / D_), dm = ml - mean;
            const float q = __builtin_fmaf(dm * 32.0f, dm, __builtin_fmaf(-ml, sm, sq));
            const unsigned ps = pack_stats(mean, fmaxf(half_sum(q) * (1.0f / D_), 0.f));
            const u64 now = __builtin_amdgcn_s_memrealtime();
            if ((vol & 0x1f0) == 0) gst16(wsx + 97 * MB_ + srow * 16, vol >> 5, u32x4{ps, (unsigned)now, ps, (unsigned)(now >> 32)});
          }
          if (kt == 9) {
            if (wid == 0) {
              u64 now;
              asm volatile("s_memrealtime %0\n\ts_waitcnt lgkmcnt(0)" : "=s"(now) :: "memory");
              if (vol == 0) asm volatile("global_store_dwordx2 %0, %1, %2 sc1" :: "v"(vol), "v"(now), "s"(wsx + 100 * MB_ + 131072 + bid * 8) : "memory");
            }
            if (wid == 2) glds16_s1(wsx + 102 * MB_ + (bid & ~1) * 512, vol, shm_base + DMP + 7168);
          }
        }
      }
      asm volatile("" :: "v"(cd));
      if (wid >= 2) glds16_s1(wsx + 100 * MB_ + 131072 + (wid - 2) * 1024, vol, shm_base + DMP + 3072 + wid * 1024);
      tile(12, true);
      next();
      kept();
      const int lane = vol >> 4;
      glds16_s1(wsx + 97 * MB_ + (m0 + wid * 64) * 16, ((lane & 32) + 2 * (lane & 15) + ((lane >> 4) & 1)) * 16, shm_base + AUX + wid * 1024);
      asm volatile("s_nop 7\n\ts_nop 3" : "+v"(ca), "+v"(cb));
      cpub(__builtin_bit_cast(u32x4, ca), __builtin_bit_cast(u32x4, cb));
      if (wid >= 2) {
        u32x4 c4 = *(const u32x4*)(shm + DMP + 3072 + wid * 1024 + vol);
        bool ok = __all(fresh(((u64)c4[1] << 32) | c4[0], t_own) & fresh(((u64)c4[3] << 32) | c4[2], t_own));
        while (__builtin_expect(!ok, 0) && __builtin_amdgcn_s_memrealtime() - t_own < TIMEOUT_ / 10) {
          const __amdgpu_buffer_rsrc_t rs = __builtin_amdgcn_make_buffer_rsrc((void*)(wsx + 100 * MB_ + 131072), 0, 2048, 0x00020000);
          asm volatile("" ::: "memory");
          c4 = __builtin_bit_cast(u32x4, __builtin_amdgcn_raw_buffer_load_b128(rs, (wid - 2) * 1024 + vol, 0, 16));
          ok = __all(fresh(((u64)c4[1] << 32) | c4[0], t_own) & fresh(((u64)c4[3] << 32) | c4[2], t_own));
        }
        *(unsigned*)(shm + CNT - 4 + wid * 4) = ok ? 1u : 0u;
      }
      tile(13, false);
      next();
      u32x4 cw[4];
      auto cgather = [&]() {
        const __amdgpu_buffer_rsrc_t rc = __builtin_amdgcn_make_buffer_rsrc((void*)(wsx + 101 * MB_ + ntile * 10240), 0, 5 * 2048, 0x00020000);
        const unsigned vcs = (vol < 512 ? wid * 2048 : 8192) + (vol & 256) * 4 + (vol & 0xf0);
#pragma unroll
        for (int i = 0; i < 4; ++i) cw[i] = __builtin_bit_cast(u32x4, __builtin_amdgcn_raw_buffer_load_b128(rc, vcs + i * 256, 0, 16));
      };
      {
        const int ln = wid * 64 + (vol >> 4);
        u32x4 gr = *(const u32x4*)(shm + AUX + ln * 16);
        if (__builtin_expect(!__all((gr[0] == gr[2]) & fresh(((u64)gr[3] << 32) | gr[1], t_own)), 0)) {
          const __amdgpu_buffer_rsrc_t rs = __builtin_amdgcn_make_buffer_rsrc((void*)(wsx + 97 * MB_ + m0 * 16), 0, 4096, 0x00020000);
          const int prow = (ln & 0xe0) + 2 * (ln & 15) + ((ln >> 4) & 1);
          for (;;) {
            gr = __builtin_bit_cast(u32x4, __builtin_amdgcn_raw_buffer_load_b128(rs, prow * 16, 0, 16));
            if (__all((gr[0] == gr[2]) & fresh(((u64)gr[3] << 32) | gr[1], t_own))) break;
            if (__builtin_amdgcn_s_memrealtime() - t_own > TIMEOUT_) { gr[0] = row_stats_serial(x + (size_t)(m0 + prow) * D_); break; }
            __builtin_amdgcn_s_sleep(2);
          }
        }
        cgather();
        const float nm = -lo_f(gr[0]), dn = hi_f(gr[0]), r = __builtin_amdgcn_rcpf(dn);
        auto mul = [](float a, float b) { float c; asm("v_mul_f32 %0, %1, %2" : "=v"(c) : "v"(a), "v"(b)); return c; };
        const f32x4 fc = {nm, -2.3022081986f * r, mul(mul(mul(-0.10294324f, r), r), r), mul(dn, dn)};
        *(f32x4*)(shm + AUX + ln * 16) = fc;
        float* const tw = (float*)(shm + 2 * G1_STAGE + 4096 + wid * 768) + ln;
#pragma unroll
        for (int i = 0; i < 4; ++i) tw[i * 64] = fc[i];
        ((float*)(shm + AUX + 4096))[384 + ln] = r;
        cok = __builtin_amdgcn_readfirstlane(cnt[1] & cnt[2]) != 0u;
        tile2(0);
        wy = wsetup(wd, 0, vol >> 4, cok);
      }
      tile(14, false);
      next();
      {
        while (__builtin_expect(blank(cw) && cok, 0)) {
          __builtin_amdgcn_s_sleep(2);
          asm volatile("" ::: "memory");
          cgather();
        }
        f32x4 t;
#pragma unroll
        for (int i = 0; i < 4; ++i) {
          float a, b;
          asm("v_add_f32 %0, %2, %3\n\tv_add_f32 %1, %4, %5\n\tv_add_f32 %0, %0, %1" : "=&v"(a), "=&v"(b) : "v"(cw[0][i]), "v"(cw[1][i]), "v"(cw[2][i]), "v"(cw[3][i]));
          const auto r2 = __builtin_amdgcn_permlane16_swap(__float_as_uint(a), __float_as_uint(a), false, false);
          asm("v_add_f32 %0, %1, %2" : "=v"(t[i]) : "v"(r2[0]), "v"(r2[1]));
        }
        const int l = vol >> 4;
        if (__builtin_expect(!cok, 0)) {
          const int gq = (l >> 2) & 3;
          const char* pc = l < 32 ? pbase + ((psh & gq) ? dgu : 0) + (gq >> psh) * 32 + (l & 3) * 8 : gb + dgu + (l & 15) * 8;
          t = f32x4{0.f, 0.f, 0.f, 0.f};
#pragma unroll 1
          for (int k = 0; k < D_; ++k) { const u32x2 w = *(const __attribute__((address_space(1))) u32x2*)(pc + (size_t)k * (l < 32 ? pstr : F_ * 2)); t += f32x4{lo_f(w[0]), hi_f(w[0]), lo_f(w[1]), hi_f(w[1])}; }
        }
        if (l < 32 || wid == 0)
          *(f32x4*)((float*)(shm + AUX + 4096) + ((l >> 3) & 1) * 192 + (l < 32 ? wid : 4) * 32 + (l & 7) * 4) = t;
        __builtin_amdgcn_s_setprio(2);
      }
      SB0();
      ARRP(wid * 64 + 60, true);
      BARL();
      body(false);
    }
#pragma unroll
    for (int t = 0; t < 8; t += 2) mm(9, t, true);
    asm volatile("s_nop 7\n\ts_nop 3");
    SB0();
    TS1(3);
    if (!LD) {
#pragma unroll
      for (int i = 0; i < 3; ++i) {
        const unsigned r = ((role * 4 + (i & 1) * 2) * 14 + (i < 2 ? 10 : 0) + (wm & 2)) * 32;
        sw[0] = wy.wb[0] + (r << wy.sh[0]); sw[1] = wy.wb[1] + (r << wy.sh[1]);
        asm volatile(G2_W :: [l] "s"((i + 1) * 16384), G2_WOP(wy) : "memory", "scc", "m0");
      }
    } else {
      __builtin_amdgcn_s_setprio(3);
      WAIT_V(0);
    }
  };
  if (wid >= 4) kloop(std::integral_constant<int, 1>{});
  else kloop(std::integral_constant<int, 0>{});
  __builtin_amdgcn_sched_barrier(0);
  int tz = threadIdx.x;
  asm volatile("" : "+v"(tz));
  __builtin_amdgcn_sched_barrier(0);
  const int li = tz & 15, lq = (tz >> 4) & 3, ln = tz & 63;
  const __amdgpu_buffer_rsrc_t ra = __builtin_amdgcn_make_buffer_rsrc((void*)abuf, 0, 16 * 72 * 16384, 0x00020000);
  float* const cfin = (float*)(shm + AUX + 4096);
  float* const svr = cfin + 384;
  float* const tb = (float*)(shm + 2 * G1_STAGE + 4096 + wm * 1024);
  auto signal = [&](unsigned v) {
    asm volatile("" ::: "memory");
    if (ln == 0) __hip_atomic_fetch_add(cnt, v, __ATOMIC_RELAXED, __HIP_MEMORY_SCOPE_WORKGROUP);
    asm volatile("" ::: "memory");
  };
  auto await = [&](unsigned mask, unsigned val) {
    while ((__builtin_amdgcn_readfirstlane(__hip_atomic_load(cnt, __ATOMIC_RELAXED, __HIP_MEMORY_SCOPE_WORKGROUP)) & mask) != val) __builtin_amdgcn_s_sleep(1);
    asm volatile("" ::: "memory");
  };
  auto client = [&](int l) { const int m128 = mtile * 2 + ((l >> 4) & 1); return (((l & 7) * 4 + (m128 & 3)) * 8) + ((m128 >> 2) ^ ((l & 8) ? 5 : 0)); };
  auto notify = [&](int l0, int nl) {
    u64 now;
    asm volatile("s_memrealtime %0\n\ts_waitcnt lgkmcnt(0)" : "=s"(now) :: "memory");
    int l = threadIdx.x;
    asm volatile("" : "+v"(l));
    l &= 63;
    const __amdgpu_buffer_rsrc_t rd1 = __builtin_amdgcn_make_buffer_rsrc((void*)(done1 + ntile), 0, 256 * 512, 0x00020000);
    if (l < nl) __builtin_amdgcn_raw_buffer_store_b64(u32x2{(unsigned)now, (unsigned)(now >> 32)}, rd1, client(l0 + l) * 512, 0, 16);
  };
  const int xo = ((xcd & 1) << 1) | ((xcd >> 1) & 1), te = (xcd >> 2) * 28 + 6 * (xo >> 1) + (xo & 1), tl = (xcd >> 2) * 28 + 12 + 4 * xo;
  const u64 te0 = __builtin_amdgcn_s_memrealtime();
  const __amdgpu_buffer_rsrc_t re = __builtin_amdgcn_make_buffer_rsrc((void*)(done1 + client((wm >> 1) * 16 + 8 - (xcd & 4) * 2) * 64 + 32 + (ntile & 15) * 2 + (wm & 1)),
                                                                      0, 7 * 16384 + 8, 0x00020000);
  auto early = [&]() {
    __builtin_amdgcn_raw_buffer_store_b64(u32x2{(unsigned)te0, (unsigned)(te0 >> 32)}, re, ln * 16384, 0, 16);
  };
  signal(1u << (8 * wc));
  if (wc) {
    await(0xffffu, 0x404u);
#pragma unroll
    for (int f = 0; f < 4; ++f) *(u32x4*)(shm + G2_X + wm * 4096 + f * 1024 + ln * 16) = xq[f];
    SB0();
    __builtin_amdgcn_s_setprio(1);
  }
  TS1(4);
  f32x4 rf[4];
#pragma unroll
  for (int mf = 0; mf < 4; ++mf) rf[mf] = *(const f32x4*)(shm + AUX + (wm * 64 + mf * 16 + li) * 16);
  const float* const cl = cfin + wc * 192 + lq * 4;
  const int sbase = ((mtile * 2 + (wm >> 1)) * 72 * 8 + (wm & 1) * 4) * 2048;
  auto putf = [&](int kg, int mf, u32x4 v) {
    __builtin_amdgcn_raw_buffer_store_b128(v, ra, ln * 16 + sbase + ((kg >> 1) * 8 + mf) * 2048 + (kg & 1) * 1024, 0, 16);
  };
  auto gelp = [&](f32x2 g, f32x2 u, f32x2 cg, f32x2 cu, f32x2 m2, f32x2 k12, f32x2 k22, f32x2 d2) {
    const f32x2 tg = __builtin_elementwise_fma(m2, cg, g), tu = __builtin_elementwise_fma(m2, cu, u);
    const f32x2 arg = tg * __builtin_elementwise_fma(tg * tg, k22, k12);
    const f32x2 dn = __builtin_elementwise_fma(f32x2{__builtin_amdgcn_exp2f(arg[0]), __builtin_amdgcn_exp2f(arg[1])}, d2, d2);
    const f32x2 r = (tg * tu) * f32x2{__builtin_amdgcn_rcpf(dn[0]), __builtin_amdgcn_rcpf(dn[1])};
    return pack2(r[0], r[1]);
  };
  auto gel2 = [&](int p, int kg, int mf0 = 0, int mf1 = 4) {
    f32x4 cg[2], cu[2];
#pragma unroll
    for (int j = 0; j < 2; ++j) { cg[j] = *(const f32x4*)(cl + p * 32 + j * 16); cu[j] = *(const f32x4*)(cl + p * 32 + 32 + j * 16); }
#pragma unroll
    for (int mf = mf0; mf < mf1; ++mf) {
      const f32x2 m2 = {rf[mf][0], rf[mf][0]}, k12 = {rf[mf][1], rf[mf][1]}, k22 = {rf[mf][2], rf[mf][2]}, d2 = {rf[mf][3], rf[mf][3]};
      u32x4 o;
#pragma unroll
      for (int i = 0; i < 4; ++i) {
        const int j = i >> 1, e = (i & 1) * 2;
        const f32x4 g = acc[mf][p * 2 + j], u = acc[mf][p * 2 + 2 + j];
        o[i] = gelp(f32x2{g[e], g[e + 1]}, f32x2{u[e], u[e + 1]}, f32x2{cg[j][e], cg[j][e + 1]}, f32x2{cu[j][e], cu[j][e + 1]}, m2, k12, k22, d2);
      }
      putf(kg, mf, o);
    }
  };
  int tsel = ntile & 1;
  asm volatile("" : "+s"(tsel));
  if (tsel != 0) {
    const int tq = (wc ? tl : te + 2 + (xo & 1)) + he;
    gel2(0, 2 * tq);
    gel2(3, 2 * tq + 1);
    const float cg = cfin[wc * 192 + 64 + li], cu = cfin[wc * 192 + 80 + li];
#pragma unroll
    for (int mf = 0; mf < 4; ++mf) {
      f32x4 t[4];
#pragma unroll
      for (int i = 0; i < 4; ++i) t[i] = *(const f32x4*)(tb + i * 64 + mf * 16 + lq * 4);
      const f32x4 g = acc[mf][4], u = acc[mf][5];
      u32x2 o;
#pragma unroll
      for (int e = 0; e < 4; e += 2)
        o[e >> 1] = gelp(f32x2{g[e], g[e + 1]}, f32x2{u[e], u[e + 1]}, f32x2{cg, cg}, f32x2{cu, cu}, f32x2{t[0][e], t[0][e + 1]},
                         f32x2{t[1][e], t[1][e + 1]}, f32x2{t[2][e], t[2][e + 1]}, f32x2{t[3][e], t[3][e + 1]});
      *(u32x2*)(shm + wm * 4096 + mf * 1024 + wc * 512 + li * 32 + lq * 8) = o;
      if (mf == EPOS && wc == 0) {
        TS1(1);
        WAIT_V(0);
        TS1(6);
        early();
      }
    }
    if (wc == 0) {
      signal(0x10000u << wid);
    } else {
      await(0x1000u << wid, 0x1000u << wid);
#pragma unroll
      for (int mf = 0; mf < 4; ++mf) putf(2 * (tl + 2) + he, mf, __builtin_bit_cast(u32x4, tr_frag(shm3 + wm * 4096 + mf * 1024 + ln * 8)));
    }
  } else {
    float rd[4];
#pragma unroll
    for (int mf = 0; mf < 4; ++mf) rd[mf] = svr[wm * 64 + mf * 16 + li];
    auto cvq = [&](int p, int mf, const f32x4& c0, const f32x4& c1) {
      u32x4 o;
#pragma unroll
      for (int j = 0; j < 2; ++j) {
        const f32x4 a = acc[mf][p * 2 + j], c4 = j ? c1 : c0;
        const f32x2 m2 = {rf[mf][0], rf[mf][0]}, r2 = {rd[mf], rd[mf]};
        f32x2 t0 = __builtin_elementwise_fma(m2, f32x2{c4[0], c4[1]}, f32x2{a[0], a[1]});
        f32x2 t1 = __builtin_elementwise_fma(m2, f32x2{c4[2], c4[3]}, f32x2{a[2], a[3]});
        if (p) { t0 *= r2; t1 *= r2; }
        o[2 * j] = pack2(t0[0], t0[1]); o[2 * j + 1] = pack2(t1[0], t1[1]);
      }
      return o;
    };
    const float cv0 = cfin[wc * 192 + 64 + li], cv1 = cfin[wc * 192 + 80 + li];
    auto cvv = [&](int mf) {
      const f32x4 n4 = *(const f32x4*)(tb + mf * 16 + lq * 4), r4 = *(const f32x4*)(svr + wm * 64 + mf * 16 + lq * 4);
      u32x4 o;
#pragma unroll
      for (int j = 0; j < 2; ++j) {
        const f32x4 a = acc[mf][4 + j];
        const f32x2 c2 = {j ? cv1 : cv0, j ? cv1 : cv0};
        const f32x2 t0 = __builtin_elementwise_fma(f32x2{n4[0], n4[1]}, c2, f32x2{a[0], a[1]}) * f32x2{r4[0], r4[1]};
        const f32x2 t1 = __builtin_elementwise_fma(f32x2{n4[2], n4[3]}, c2, f32x2{a[2], a[3]}) * f32x2{r4[2], r4[3]};
        o[2 * j] = pack2(t0[0], t0[1]); o[2 * j + 1] = pack2(t1[0], t1[1]);
      }
      return o;
    };
    u32x4 kx[4], vx[4], qx[4], hx[4];
    const bf16x8 ones = {0x3f80, 0x3f80, 0x3f80, 0x3f80, 0x3f80, 0x3f80, 0x3f80, 0x3f80};
    const f32x4 ck0 = *(const f32x4*)(cl + 32), ck1 = *(const f32x4*)(cl + 48);
    kx[2] = cvq(1, 2, ck0, ck1); kx[3] = cvq(1, 3, ck0, ck1);
    vx[2] = cvv(2); vx[3] = cvv(3);
    auto exo = [](int e) { return e * 4096 + (e >> 2) * (G2_A - 16384); };
    const bool needh = wm == 0 && mtile > 0;
    const __amdgpu_buffer_rsrc_t rh = __builtin_amdgcn_make_buffer_rsrc((void*)((char*)halo + ((needh ? mtile - 1 : mtile) * 32 + ntile + wc) * 4096), 0,
                                                                        needh || wm == 3 ? 4096 : 0, 0x00020000);
    if (wm < 3) {
      char* const ex = shm + exo(wc * 3 + wm) + ln * 16;
      *(u32x4*)ex = kx[2]; *(u32x4*)(ex + 1024) = kx[3]; *(u32x4*)(ex + 2048) = vx[2]; *(u32x4*)(ex + 3072) = vx[3];
      signal(0x10000u << wid);
    } else {
#pragma unroll
      for (int r = 0; r < 4; ++r) __builtin_amdgcn_raw_buffer_store_b128(r < 2 ? kx[2 + r] : vx[r], rh, ln * 16 + r * 1024, 0, 16);
    }
    auto hload = [&]() {
#pragma unroll
      for (int r = 0; r < 4; ++r) hx[r] = __builtin_bit_cast(u32x4, __builtin_amdgcn_raw_buffer_load_b128(rh, ln * 16 + r * 1024, 0, 16));
    };
    SB0();
    gel2(3, 2 * (wc ? tl + 3 : te) + he);
    SB0();
    if (wm == 0) hload();
    SB0();
    kx[0] = cvq(1, 0, ck0, ck1); kx[1] = cvq(1, 1, ck0, ck1);
    vx[0] = cvv(0); vx[1] = cvv(1);
    {
      const f32x4 cq0 = *(const f32x4*)(cl), cq1 = *(const f32x4*)(cl + 16);
#pragma unroll
      for (int mf = 0; mf < 4; ++mf) qx[mf] = cvq(0, mf, cq0, cq1);
    }
    f32x4 mk0, mk1;
#pragma unroll
    for (int j = 0; j < 4; ++j) {
      const int k0 = 4 * lq + j, k1 = 16 + 4 * lq + j;
      mk0[j] = (k0 >= li + 9 && k0 <= li + 16) ? 0.f : -1.0e30f;
      mk1[j] = (k1 >= li + 9 && k1 <= li + 16) ? 0.f : -1.0e30f;
    }
    auto max3 = [](float a, float b, float c) { float r; asm("v_max3_f32 %0, %1, %2, %3" : "=v"(r) : "v"(a), "v"(b), "v"(c)); return r; };
    auto max2 = [](float a, float b) { float r; asm("v_max_f32 %0, %1, %2" : "=v"(r) : "v"(a), "v"(b)); return r; };
    asm volatile("" : "+v"(kx[0]), "+v"(kx[1]), "+v"(vx[0]), "+v"(vx[1]), "+v"(qx[0]), "+v"(qx[1]), "+v"(qx[2]), "+v"(qx[3]), "+v"(mk0), "+v"(mk1));
    TS1(1);
    if (wc == 0) {
      if (wm == 0) WAIT_V(4); else WAIT_V(0);
      early();
    }
    if (wm > 0) {
      await(0x8000u << wid, 0x8000u << wid);
#pragma unroll
      for (int i = 0; i < 4; ++i) hx[i] = *(const u32x4*)(shm + exo(wc * 3 + wm - 1) + i * 1024 + ln * 16);
    } else if (__builtin_expect(cok || !needh, 1)) {
      while (__builtin_expect(blank(hx), 0)) {
        __builtin_amdgcn_s_sleep(1);
        asm volatile("" ::: "memory");
        hload();
      }
    } else {
      await(0xffffu, 0x404u);
      char* const fb = shm + exo(6 + wc) + ln * 16;
#pragma unroll 1
      for (int i = 0; i < 32; ++i) {
        const int rec = i >> 3, e = i & 7;
        const int row = m0 - 32 + 2 * (rec < 2 ? li : lq * 4 + (e & 3)) + (rec & 1);
        const int col = (rec < 2 ? D_ + lq * 4 + (e & 3) : 2 * D_ + li) + (ntile + wc) * 32 + (e >> 2) * 16;
        const u16* xr = x + (size_t)row * D_;
        const unsigned stt = row_stats_serial(xr);
        const float mb = lo_f(stt);
        float a = 0.f;
#pragma unroll 1
        for (int k = 0; k < D_; ++k) a += (bf2f(xr[k]) - mb) * bf2f(wqkv[(size_t)k * 3072 + col]);
        *(u16*)(fb + rec * 1024 + e * 2) = f2bf(a / hi_f(stt));
      }
#pragma unroll
      for (int i = 0; i < 4; ++i) hx[i] = *(const u32x4*)(fb + i * 1024);
    }
    TS1(6);
    {
      const f32x4 z = {0.f, 0.f, 0.f, 0.f};
      const float mz = mtile == 0 && wm == 0 ? -1.0e30f : 0.f;
      const f32x4 mkf = {fminf(mk0[0], mz), fminf(mk0[1], mz), fminf(mk0[2], mz), fminf(mk0[3], mz)};
      f32x4 s0[4], s1[4];
#pragma unroll
      for (int i = 0; i < 4; ++i) {
        const int n = i ^ 2;
        const bf16x8 qf = __builtin_bit_cast(bf16x8, qx[n]);
        s1[n] = __builtin_amdgcn_mfma_f32_16x16x32_bf16(__builtin_bit_cast(bf16x8, kx[n]), qf, mk1, 0, 0, 0);
        s0[n] = __builtin_amdgcn_mfma_f32_16x16x32_bf16(__builtin_bit_cast(bf16x8, n < 2 ? hx[n] : kx[n - 2]), qf, n < 2 ? mkf : mk0, 0, 0, 0);
      }
      SB0();
      asm volatile("s_nop 7\n\ts_nop 3" : "+v"(s0[0]), "+v"(s1[0]), "+v"(s0[1]), "+v"(s1[1]), "+v"(s0[2]), "+v"(s1[2]), "+v"(s0[3]), "+v"(s1[3]));
      float mx[4];
#pragma unroll
      for (int n = 0; n < 4; ++n) mx[n] = max3(max3(s0[n][0], s0[n][1], s0[n][2]), max3(s0[n][3], s1[n][0], s1[n][1]), max2(s1[n][2], s1[n][3]));
#pragma unroll
      for (int n = 0; n < 4; ++n) { auto r = __builtin_amdgcn_permlane16_swap(__float_as_uint(mx[n]), __float_as_uint(mx[n]), false, false);
        mx[n] = max2(__uint_as_float(r[0]), __uint_as_float(r[1])); }
#pragma unroll
      for (int n = 0; n < 4; ++n) { auto r = __builtin_amdgcn_permlane32_swap(__float_as_uint(mx[n]), __float_as_uint(mx[n]), false, false);
        mx[n] = max2(__uint_as_float(r[0]), __uint_as_float(r[1])); }
      SB0();
      constexpr float cs2 = 0.17677669529663687f * 1.4426950408889634f;
      f32x4 os[4], o0[4], o1[4];
#pragma unroll
      for (int n = 0; n < 4; ++n) {
        const float c = cs2 * rd[n], nm = -mx[n] * c;
        float e8[8];
#pragma unroll
        for (int j = 0; j < 4; ++j) {
          e8[j] = __builtin_amdgcn_exp2f(__builtin_fmaf(s0[n][j], c, nm));
          e8[4 + j] = __builtin_amdgcn_exp2f(__builtin_fmaf(s1[n][j], c, nm));
        }
        u32x4 pk;
#pragma unroll
        for (int j = 0; j < 4; ++j) pk[j] = pack2(e8[2 * j], e8[2 * j + 1]);
        const bf16x8 pf = __builtin_bit_cast(bf16x8, pk);
        const u32x4 vp = n < 2 ? hx[2 + n] : vx[n - 2];
        const u32x4 va = {vp[0], vp[1], vx[n][0], vx[n][1]}, vb = {vp[2], vp[3], vx[n][2], vx[n][3]};
        os[n] = __builtin_amdgcn_mfma_f32_16x16x32_bf16(ones, pf, z, 0, 0, 0);
        o0[n] = __builtin_amdgcn_mfma_f32_16x16x32_bf16(__builtin_bit_cast(bf16x8, va), pf, z, 0, 0, 0);
        o1[n] = __builtin_amdgcn_mfma_f32_16x16x32_bf16(__builtin_bit_cast(bf16x8, vb), pf, z, 0, 0, 0);
      }
#pragma unroll
      for (int n = 0; n < 4; ++n) {
        const float inv = __builtin_amdgcn_rcpf(os[n][0]);
        const f32x4 n0 = o0[n] * inv, n1 = o1[n] * inv;
        putf(112 + ntile + wc, n, u32x4{pack2(n0[0], n0[1]), pack2(n0[2], n0[3]), pack2(n1[0], n1[1]), pack2(n1[2], n1[3])});
      }
    }
  }
  TS1(2);
  const bool tya1 = tya;
  {
  __builtin_amdgcn_sched_barrier(0);
  int tid = threadIdx.x;
  asm volatile("" : "+v"(tid));
  __builtin_amdgcn_sched_barrier(0);
  const int lane = tid & 63;
  const int wm = wid >> 1, wn = wid & 1;
  const int m0 = mt2 * 128, n0 = nt2 * 128;
  constexpr int PD = G2_NSTAGE;
  const int ny = (xcd & 1) ? NY - DLT : NY + DLT;
  const unsigned va = lane * 16;
  auto stage_y = [&]() {
    asm volatile("s_add_u32 m0, %[l], %[oa]\n\ts_nop 0\n\tglobal_load_lds_dwordx4 %[va], %[a] sc1\n\tglobal_load_lds_dwordx4 %[va], %[a] offset:1024 sc1\n\t"
                 "global_load_lds_dwordx4 %[va], %[a] offset:2048 sc1\n\tglobal_load_lds_dwordx4 %[va], %[a] offset:3072 sc1\n\t" G2_W
                 :: [l] "s"(sl), [oa] "s"(shm_base + G2_A + (p ^ role) * 4096), [a] "s"(sa), [va] "v"(va), G2_WOP(wy) : "memory", "scc", "m0");
    ++st;
    sa += 16384;
    sl = (sl + 16384) & 65535;
    wsel();
  };
  u64 ready = 0;
  const u64* const mbox = &done1[bid * 64 + lane];
  auto seen = [&](u64 v) {
    const u64 r = __ballot(fresh(v, t_own));
    u64 d;
    asm("s_bitreplicate_b64_b32 %0, %1" : "=s"(d) : "s"((unsigned)r >> (16 * role)));
    ready |= r | (d << 32);
  };
  auto need = [&](u64 mask, bool renew) {
    while ((ready & mask) != mask) {
      seen(__hip_atomic_load(mbox, RLX_AGENT));
      if ((ready & mask) != mask) {
        __builtin_amdgcn_s_sleep(PSLEEP);
        if (renew && !cok) notify(0, 32);
      }
    }
    asm volatile("" ::: "memory");
  };
  u64 fl = 0;
  auto peek = [&]() { asm volatile("global_load_dwordx2 %0, %1, %2 sc1" : "+v"(fl) : "v"(va >> 1), "s"(done1 + bid * 64) : "memory"); };
  auto check = [&](u64 mask, bool renew) {
    asm volatile("" : "+v"(fl));
    seen(fl);
    if (__builtin_expect((ready & mask) != mask, 0)) need(mask, renew);
  };
  if (wid >= 4) {
    TS(4);
    __builtin_amdgcn_s_setprio(0);
    BARL();
    stage_y();
    stage_y();
    BAR();
    if (p == 1) peek();
    stage_y();
    WAIT_V(16);
    BAR();
    if (p == 0) notify(0, 32);
#define G2_A2 "global_load_lds_dwordx4 %[va], %[a] sc1\n\tglobal_load_lds_dwordx4 %[va], %[a] offset:1024 sc1"
#define G2_A4 G2_A2 "\n\tglobal_load_lds_dwordx4 %[va], %[a] offset:2048 sc1\n\tglobal_load_lds_dwordx4 %[va], %[a] offset:3072 sc1"
#define G2_SYNC "\n\ts_mov_b32 m0, %[la]\n\ts_waitcnt vmcnt(%[n])\n\ts_barrier\n\t"
    const unsigned oy = shm_base + G2_A + (p ^ role) * 4096, os = shm_base + G2_A + (p >> 1) * 8192 + (p & 1) * 2048;
    asm volatile("s_mov_b32 m0, %[la]\n\ts_nop 0\n\t" G2_A4 :: [la] "s"(sl + oy), [a] "s"(sa), [va] "v"(va) : "memory", "m0");
    auto step = [&](long adv = 16384) __attribute__((always_inline)) {
      wsel();
      ++st;
      sa += adv;
      const unsigned sn = (sl + 16384) & 65535;
      asm volatile(G2_W G2_SYNC G2_A4 :: [l] "s"(sl), [la] "s"(sn + oy), [n] "n"(16), [a] "s"(sa), [va] "v"(va), G2_WOP(wy) : "memory", "scc", "m0");
      sl = sn;
    };
    auto chk1 = [&](u64 mask) __attribute__((always_inline)) { if (p == 1) { WAIT_V(16); check(mask, true); } };
    step();
    chk1(0xcc00cc00ull << 32);
    step(); step();
    if (p == 1) peek();
    step(); step();
    chk1(0x0f0fu << (16 * role));
#pragma unroll 1
    for (int g = 7; g < 13; ++g) step();
    if (p == 1) peek();
    step(); step();
    chk1((0xffffu << (16 * role)) | 0x55555555u);
#pragma unroll 1
    for (int g = 15; g < NY - 6; ++g) step();
    if (ny > NY) { step(); step((1 - 2 * role) * NY * 16384); }
    wsel();
    sa = sz;
    {
      const unsigned sn = (sl + 16384) & 65535;
      asm volatile(G2_W G2_SYNC G2_A2 :: [l] "s"(sl), [la] "s"(sn + os), [n] "n"(16), [a] "s"(sa), [va] "v"(va), G2_WOP(wy) : "memory", "scc", "m0");
      sl = sn;
    }
    sw[0] = wz.wb[0]; sw[1] = wz.wb[1];
#pragma unroll
    for (int j = -3; j < 13; ++j) {
      if (j == GETJ + 1 || j == GETJ + 2)
        asm volatile("s_mov_b32 m0, %[l]\n\ts_nop 0\n\tglobal_load_lds_dwordx4 %[va], %[a] offset:%[o] sc1\n\tglobal_load_lds_dwordx4 %[va], %[a] offset:%[o]+1024 sc1"
                     :: [l] "s"(shm_base + G2_X + 16384 + p * 4096), [a] "s"((const char*)slab + (size_t)(bid ^ 5) * 16384 + p * 4096), [va] "v"(va),
                        [o] "n"((j - GETJ - 1) * 2048) : "memory", "m0");
      sa += 16384;
      const unsigned sn = (sl + 16384) & 65535;
      constexpr int G = GETJ;
      if (j < 12) asm volatile(G2_W G2_SYNC G2_A2 :: [l] "s"(sl), [la] "s"(sn + os), [n] "n"((j == -3 ? 14 : 12) + (j > G && j < G + 3 ? 2 : 0) + (j > G + 1 && j < G + 4 ? 2 : 0)),
                               [a] "s"(sa), [va] "v"(va), G2_WOP(wz) : "memory", "scc", "m0");
      else asm volatile(G2_W :: [l] "s"(sl), G2_WOP(wz) : "memory", "scc", "m0");
      sl = sn;
      sw[0] += ws0; sw[1] += ws1;
    }
    WAIT_V(12);
    BAR();
    WAIT_V(6);
    BAR();
    WAIT_V(0);
    BAR();
    asm volatile("" : "+v"(fl));
    TS(5);
    if (tid < 258) __hip_atomic_store(lane ? done2 + bid * 8 : &acks[bid], __builtin_amdgcn_s_memrealtime(), RLX_AGENT);
    if (__builtin_expect(!cok, 0) && p == 0) {
      const bool mine = lane == 32 || (lane < 32 && (tya1 || ((lane >> 3) & 1) != (xcd >> 2)));
      const u64* const pa = &acks[lane < 32 ? client(lane) : bid ^ 5];
      while (!__all(!mine || fresh(__hip_atomic_load(pa, RLX_AGENT), t_own))) {
        notify(0, 32);
        if (lane == 0) __hip_atomic_store(done2 + bid * 8, __builtin_amdgcn_s_memrealtime(), RLX_AGENT);
        __builtin_amdgcn_s_sleep(32);
      }
    }
  } else {
    TS(6);
    f32x4 acc[4][4], sacc[2][4];
    const int fr = lane & 15, fq = lane >> 4, q4 = fr >> 2, pp = lane & 3;
    const int ff = ((q4 >> 1) & 1) | ((fq & 1) << 1);
    int aof = G2_A + wm * 8192 + lane * 16;
    int boff[4];
#pragma unroll
    for (int j = 0; j < 4; ++j) boff[j] = wn * 8192 + (8 * fq + q4) * 128 + ((j ^ ff) * 32) + pp * 8;
    int vo = wid * 4096 + lane * 16;
    int vx = ((m0 + wm * 64 + role * 32 + 2 * fr) * D_ + n0 + wn * 64) * 2 + (fq & 1) * 32 + (fq >> 1) * 16;
    lds_cptr px = shm3 + G2_X + wid * 4096 + (fq >> 1) * 512 + fr * 16 + (fq & 1) * 8;
    asm volatile("" : "+v"(vo), "+v"(vx), "+v"(px));
#define SB() __builtin_amdgcn_sched_barrier(0)
    struct Fr { bf16x8 a[4]; bf16x4 bl[4], bh[4]; };
    Fr fa[2], fb[2];
    auto rd = [&](Fr* f, int odd, int t) {
      constexpr int ord[12] = {0, 1, 8, 2, 3, 4, 5, 6, 7, 9, 10, 11};
      constexpr int seq[24] = {0, 1, 2, 12, 13, 14, 15, 16, 3, 4, 5, 6, 17, 18, 19, 20, 7, 8, 9, 21, 10, 22, 11, 23};
      lds_cptr sb = shm3 + odd * 16384;
      const int ks = seq[t] / 12, o = ord[seq[t] % 12];
      if (o >= 8) f[ks].a[o - 8] = *(const __attribute__((address_space(3))) bf16x8*)(sb + aof + (o - 8) * 2048 + ks * 1024);
      else if (o & 1) f[ks].bh[o >> 1] = tr_read(sb + boff[o >> 1] + ks * 4096 + 512);
      else f[ks].bl[o >> 1] = tr_read(sb + boff[o >> 1] + ks * 4096);
    };
    auto bfr = [&](const Fr& c, int j) { return __builtin_shufflevector(c.bl[j], c.bh[j], 0, 1, 2, 3, 4, 5, 6, 7); };
    auto mf1 = [](f32x4& c, bf16x8 b, bf16x8 a, bool z = false) {
      if (z) asm volatile("v_mfma_f32_16x16x32_bf16 %0, %1, %2, 0" : "=&v"(c) : "v"(b), "v"(a));
      else asm volatile("v_mfma_f32_16x16x32_bf16 %0, %1, %2, %0" : "+v"(c) : "v"(b), "v"(a));
    };
    auto mf2 = [](f32x4& c, bf16x8 b, bf16x8 a, bf16x8 b2, bf16x8 a2, bool z) {
      if (z) asm volatile("v_mfma_f32_16x16x32_bf16 %0, %1, %2, 0\n\tv_mfma_f32_16x16x32_bf16 %0, %3, %4, %0" : "=&v"(c) : "v"(b), "v"(a), "v"(b2), "v"(a2));
      else asm volatile("v_mfma_f32_16x16x32_bf16 %0, %1, %2, %0\n\tv_mfma_f32_16x16x32_bf16 %0, %3, %4, %0" : "+v"(c) : "v"(b), "v"(a), "v"(b2), "v"(a2));
    };
    auto pairs = [&](f32x4 (*ac)[4], const Fr* c, int nq, int mx, int nf, auto fill, bool z = false) {
#pragma unroll
      for (int q = 0; q < nq; ++q) {
        const int mf = (q >> 2) ^ mx, j = (q & 4) ? 3 - (q & 3) : q & 3, ks = q & 1;
        SB();
        if (2 * q >= nf) mf2(ac[mf][j], bfr(c[ks], j), c[ks].a[mf], bfr(c[ks ^ 1], j), c[ks ^ 1].a[mf], z);
        else {
          mf1(ac[mf][j], bfr(c[ks], j), c[ks].a[mf], z);
          SB();
          fill(2 * q);
          SB();
          mf1(ac[mf][j], bfr(c[ks ^ 1], j), c[ks ^ 1].a[mf]);
          SB();
          fill(2 * q + 1);
        }
      }
      SB();
    };
    auto flip = [&]() {
      aof ^= 32768; boff[0] ^= 32768; boff[1] ^= 32768; boff[2] ^= 32768; boff[3] ^= 32768;
      asm volatile("" : "+v"(aof), "+v"(boff[0]), "+v"(boff[1]), "+v"(boff[2]), "+v"(boff[3]));
    };
    auto bar = [&]() { __builtin_amdgcn_s_waitcnt(0xC07F); BAR(); };
    auto ystg = [&](const Fr* c, Fr* n, int odd, bool z = false) {
      pairs(acc, c, 16, 0, odd ? 30 : 24, [&](int t) {
        if (t < 24) rd(n, odd, t);
        else if (t == 24) asm volatile("s_nop 0");
        else { int& a = t == 29 ? aof : boff[t - 25]; a ^= 32768; asm volatile("" : "+v"(a)); }
      }, z);
    };
    auto stg = [&](const Fr* c, Fr* n, int odd, bool z = false) { pairs(sacc, c, 8, 0, 10, [&](int t) { rd(n, odd, 2 * t); rd(n, odd, 2 * t + 1); }, z); };
    asm volatile("" : "+v"(aof), "+v"(boff[0]), "+v"(boff[1]), "+v"(boff[2]), "+v"(boff[3]));
    await(0xffffu, 0x404u);
    auto lda = [&](int i) {
      asm volatile("s_mov_b32 m0, %[l]\n\ts_nop 0\n\tglobal_load_lds_dwordx4 %[va], %[a] sc1\n\tglobal_load_lds_dwordx4 %[va], %[a] offset:1024 sc1\n\t"
                   "global_load_lds_dwordx4 %[va], %[a] offset:2048 sc1\n\tglobal_load_lds_dwordx4 %[va], %[a] offset:3072 sc1"
                   :: [l] "s"(shm_base + G2_A + (i + 1) * 16384 + (p ^ role) * 4096), [a] "s"(sa + i * 16384), [va] "v"(va) : "memory", "m0");
    };
    need(0x00330033ull << 32, false);
    TS(4);
    lda(0);
    lda(1);
    need(0x00ff00ffull << 32, false);
    TS(0);
    lda(2);
    WAIT_V(8);
    signal(1u << 24);
    await(0xff000000u, 4u << 24);
    __builtin_amdgcn_s_waitcnt(0xC07F);
    SB();
#pragma unroll
    for (int t = 0; t < 24; ++t) rd(fa, 1, t);
    flip();
#define CPRIO 0
    __builtin_amdgcn_s_setprio(CPRIO);
    WAIT_V(4);
    TS(5);
    peek();
    bar();
    TS(7);
    ystg(fa, fb, 0, true);
    WAIT_V(1);
    bar();
    ystg(fb, fa, 1);
    WAIT_V(0);
    check(0x33003300ull << 32, false);
    static_assert((NY - DLT) % 2 == 1, "");
#pragma unroll 1
    for (int g = 2; g < ny - 1; g += 2) {
      bar();
      ystg(fa, fb, 0);
      bar();
      ystg(fb, fa, 1);
    }
    bar();
    {
      const __amdgpu_buffer_rsrc_t rs = __builtin_amdgcn_make_buffer_rsrc((void*)((char*)slab + (size_t)bid * 16384), 0, 16384, 0x00020000);
      u32x4 pk;
      pairs(acc, fa, 16, 2, 32, [&](int t) {
        if (t < 20) rd(fb, 0, t);
        else {
          const int f = (t - 20) / 3, e = (t - 20) % 3;
          if (e < 2) { const f32x4 a = acc[2 + (f >> 1)][(f & 1) * 2 + e]; pk[2 * e] = pack2(a[0], a[1]); pk[2 * e + 1] = pack2(a[2], a[3]); }
          else __builtin_amdgcn_raw_buffer_store_b128(pk, rs, vo + f * 1024, 0, 16);
        }
      });
    }
    TS(2);
    static_assert(GETJ % 2 == 1 && GETJ >= 3 && GETJ <= 7, "");
    f32x4 xf[2][4];
#pragma unroll
    for (int j = 0; j < GETJ + 5; j += 2) {
      bar();
      stg(fb, fa, 1, j == 0);
      flip();
      bar();
      u32x2 xv[2];
      pairs(sacc, fa, 8, 0, 12, [&](int t) {
        if (t < 10) { rd(fb, 0, 2 * t); rd(fb, 0, 2 * t + 1); }
        else if (j < 8) xv[t - 10] = *(const __attribute__((address_space(3))) u32x2*)(px + (j >> 1) * 1024 + (t - 10) * 256);
      });
      if (j < 8) {
#pragma unroll
        for (int e = 0; e < 2; ++e) xf[j >> 2][(j & 2) + e] = f32x4{lo_f(xv[e][0]), hi_f(xv[e][0]), lo_f(xv[e][1]), hi_f(xv[e][1])};
        asm volatile("" : "+v"(xf[j >> 2][j & 2]), "+v"(xf[j >> 2][(j & 2) + 1]));
        SB();
      }
    }
    WAIT_V(0);
    bar();
    u32x4 pv[4];
    {
      lds_cptr pp_ = shm3 + G2_X + 16384 + vo;
      pairs(sacc, fb, 8, 0, 12, [&](int t) {
        if (t < 2) {
#pragma unroll
          for (int f = 2 * t; f < 2 * t + 2; ++f) pv[f] = *(const __attribute__((address_space(3))) u32x4*)(pp_ + f * 1024);
        } else { rd(fa, 1, 2 * t - 4); rd(fa, 1, 2 * t - 3); }
      });
      if (__builtin_expect(blank(pv) || !cok, 0)) {
        const __amdgpu_buffer_rsrc_t rp = __builtin_amdgcn_make_buffer_rsrc((void*)((char*)slab + (size_t)(bid ^ 5) * 16384), 0, 16384, 0x00020000);
        while (!cok && !__all(fresh(__hip_atomic_load(done2 + (bid ^ 5) * 8, RLX_AGENT), t_own))) {
          notify(0, 32);
          if (lane == 0) __hip_atomic_store(done2 + bid * 8, __builtin_amdgcn_s_memrealtime(), RLX_AGENT);
          __builtin_amdgcn_s_sleep(8);
        }
        do {
          asm volatile("" ::: "memory");
#pragma unroll
          for (int f = 0; f < 4; ++f) pv[f] = __builtin_bit_cast(u32x4, __builtin_amdgcn_raw_buffer_load_b128(rp, vo + f * 1024, 0, 16));
        } while (blank(pv));
      }
    }
    auto silu = [&](int f0, int f1) {
#pragma unroll
      for (int f = f0; f < f1; ++f) {
        const int mf = f >> 2, j = f & 3;
        const u32x4 pw = pv[mf * 2 + (j >> 1)];
        const f32x4 ya = acc[mf][j] + f32x4{lo_f(pw[2 * (j & 1)]), hi_f(pw[2 * (j & 1)]), lo_f(pw[2 * (j & 1) + 1]), hi_f(pw[2 * (j & 1) + 1])};
#pragma unroll
        for (int e = 0; e < 2; ++e) {
          const f32x2 y2 = {ya[2 * e], ya[2 * e + 1]};
          const f32x2 ar = y2 * f32x2{-1.4426950408889634f, -1.4426950408889634f};
          const f32x2 dn = f32x2{__builtin_amdgcn_exp2f(ar[0]), __builtin_amdgcn_exp2f(ar[1])} + f32x2{1.0f, 1.0f};
          const f32x2 t = y2 * f32x2{__builtin_amdgcn_rcpf(dn[0]), __builtin_amdgcn_rcpf(dn[1])};
          acc[mf][j][2 * e] = t[0]; acc[mf][j][2 * e + 1] = t[1];
        }
        asm volatile("" : "+v"(acc[mf][j]));
      }
      SB();
    };
#define SPT 0, 1, 2, 3, 5, 7, 8, 8
    constexpr int spt[] = {SPT};
    silu(0, spt[1]);
#pragma unroll
    for (int j = GETJ + 6; j < 15; ++j) {
      if (j & 1) flip();
      bar();
      if (j & 1) stg(fa, fb, 0); else stg(fb, fa, 1);
      silu(spt[j - GETJ - 5], j == 14 ? 8 : spt[j - GETJ - 4]);
    }
    pairs(sacc, fa, 8, 0, 0, [](int) {});
    asm volatile("s_nop 7\n\ts_nop 3" : "+v"(sacc[1][0]), "+v"(sacc[1][1]), "+v"(sacc[1][2]), "+v"(sacc[1][3]));
    SB();
    TS(1);
    {
      const __amdgpu_buffer_rsrc_t ro = __builtin_amdgcn_make_buffer_rsrc((void*)out, 0, S_ * D_ * 2, 0x00020000);
#pragma unroll
      for (int f = 0; f < 4; ++f) {
        const int mf = f >> 1, j = (f & 1) * 2;
        const f32x4 a = __builtin_elementwise_fma(sacc[mf][j], acc[mf][j], xf[mf][j]), b = __builtin_elementwise_fma(sacc[mf][j + 1], acc[mf][j + 1], xf[mf][j + 1]);
        const auto r0 = __builtin_amdgcn_permlane16_swap(pack2(a[0], a[1]), pack2(b[0], b[1]), false, false);
        const auto r1 = __builtin_amdgcn_permlane16_swap(pack2(a[2], a[3]), pack2(b[2], b[3]), false, false);
        __builtin_amdgcn_raw_buffer_store_b128(u32x4{r0[0], r1[0], r0[1], r1[1]}, ro, vx + mf * 2048 + j * 32, 0, 2);
      }
    }
    TS(3);
  }
  TSFLUSH();
  }
  asm volatile(".Lcode_end:");
}

extern "C" void kernel_launch(void* const* d_in, const int* in_sizes, int n_in, void* d_out, int out_size, void* d_ws,
                              size_t ws_size, hipStream_t stream) {
  const u16* A_x = (const u16*)d_in[0];
  const u16* A_wqkv = (const u16*)d_in[1];
  const u16* A_wo = (const u16*)d_in[2];
  const u16* B_wg = (const u16*)d_in[4];
  const u16* B_wu = (const u16*)d_in[5];
  const u16* B_wd = (const u16*)d_in[6];
  u16* out = (u16*)d_out;
  fused_kernel<<<256, 512, 0, stream>>>(A_x, A_wqkv, B_wg, B_wu, B_wd, A_wo, out, (char*)d_ws);
}
```

```cpp
#include <hip/hip_runtime.h>
#include <hip/hip_bf16.h>
#include <stdint.h>
#include <type_traits>

typedef unsigned short u16;
using bf16x8 = __attribute__((ext_vector_type(8))) short;
using bf16x4 = __attribute__((ext_vector_type(4))) short;
using f32x4 = __attribute__((ext_vector_type(4))) float;
using u32x2 = __attribute__((ext_vector_type(2))) unsigned;
using u32x4 = __attribute__((ext_vector_type(4))) unsigned;
typedef __attribute__((address_space(3))) const char* lds_cptr;

constexpr int S_ = 2048, D_ = 1024, F_ = 3584;

__device__ __forceinline__ float bf2f(u16 h) { return __uint_as_float((unsigned)h << 16); }
__device__ __forceinline__ u16 f2bf(float f) {
  unsigned u = __float_as_uint(f);
  u += 0x7fffu + ((u >> 16) & 1u);
  return (u16)(u >> 16);
}
__device__ __forceinline__ float rbf(float f) { return bf2f(f2bf(f)); }
typedef __attribute__((ext_vector_type(2))) __bf16 bf16x2_t;
typedef __attribute__((ext_vector_type(2))) float f32x2;
__device__ __forceinline__ unsigned pack2(float a, float b) {
  bf16x2_t r = __builtin_convertvector((f32x2){a, b}, bf16x2_t);
  return __builtin_bit_cast(unsigned, r);
}
__device__ __forceinline__ float lo_f(unsigned p) { return __uint_as_float(p << 16); }
__device__ __forceinline__ float hi_f(unsigned p) { return __uint_as_float(p & 0xffff0000u); }

#define TSDECL
#define TSS(i) do {} while (0)
#define TSFLUSH() do {} while (0)
#define TS1(i) TSS(i)
#define TS(i) TSS(8 + (i))
#define PIN_S(v) asm volatile("" : "+s"(v))
#define WAIT_V(n) asm volatile("s_waitcnt vmcnt(%0)" ::"n"(n) : "memory")
#define WAIT_L(n) asm volatile("s_waitcnt lgkmcnt(%0)" ::"n"(n) : "memory")
#define BARL() do { WAIT_L(0); BAR(); } while (0)
#define BAR() do { asm volatile("" ::: "memory"); __builtin_amdgcn_s_barrier(); asm volatile("" ::: "memory"); } while (0)

__device__ __forceinline__ bf16x4 tr_read(lds_cptr p) {
  return __builtin_amdgcn_ds_read_tr16_b64_v4i16((__attribute__((address_space(3))) bf16x4*)p);
}
__device__ __forceinline__ bf16x8 tr_frag(lds_cptr p) {
  bf16x4 lo = tr_read(p), hi = tr_read(p + 512);
  bf16x8 r;
  r[0] = lo[0]; r[1] = lo[1]; r[2] = lo[2]; r[3] = lo[3];
  r[4] = hi[0]; r[5] = hi[1]; r[6] = hi[2]; r[7] = hi[3];
  return r;
}
__device__ __forceinline__ void glds16(const void* gsrc, unsigned lds_dst) {
  unsigned keep;
  asm volatile("s_mov_b32 %0, m0\n\ts_mov_b32 m0, %2\n\ts_nop 0\n\tglobal_load_lds_dwordx4 %1, off\n\ts_mov_b32 m0, %0"
               : "=&s"(keep) : "v"(gsrc), "s"(lds_dst) : "memory");
}
#define GLDS(gptr, laddr) glds16((const void*)(gptr), (unsigned)(laddr))
__device__ __forceinline__ void glds16_sc1(const void* gsrc, unsigned lds_dst) {
  unsigned keep;
  asm volatile("s_mov_b32 %0, m0\n\ts_mov_b32 m0, %2\n\ts_nop 0\n\tglobal_load_lds_dwordx4 %1, off sc1\n\ts_mov_b32 m0, %0"
               : "=&s"(keep) : "v"(gsrc), "s"(lds_dst) : "memory");
}
#define GLDS_SC1(gptr, laddr) glds16_sc1((const void*)(gptr), (unsigned)(laddr))
__device__ __forceinline__ void glds16_nc(const void* gsrc, unsigned lds_dst) {
  unsigned keep;
  asm volatile("s_mov_b32 %0, m0\n\ts_mov_b32 m0, %2\n\ts_nop 0\n\tglobal_load_lds_dwordx4 %1, off\n\ts_mov_b32 m0, %0"
               : "=&s"(keep) : "v"(gsrc), "s"(lds_dst));
}
#define GLDS_NC(gptr, laddr) glds16_nc((const void*)(gptr), (unsigned)(laddr))
__device__ __forceinline__ void glds16_s(const void* sbase, unsigned voff, unsigned lds_dst) {
  asm volatile("s_mov_b32 m0, %2\n\ts_nop 0\n\tglobal_load_lds_dwordx4 %0, %1" :: "v"(voff), "s"(sbase), "s"(lds_dst) : "m0");
}

__device__ __forceinline__ void glds16_s1(const void* sbase, unsigned voff, unsigned lds_dst) {
  asm volatile("s_mov_b32 m0, %2\n\ts_nop 0\n\tglobal_load_lds_dwordx4 %0, %1 sc1" :: "v"(voff), "s"(sbase), "s"(lds_dst) : "memory", "m0");
}
__device__ __forceinline__ void glds16_v(const void* p, unsigned lds_dst) {
  asm volatile("s_mov_b32 m0, %1\n\ts_nop 0\n\tglobal_load_lds_dwordx4 %0, off" :: "v"(p), "s"(lds_dst) : "m0");
}
template <int OFF = 0>
__device__ __forceinline__ void gst16(const void* sbase, unsigned voff, u32x4 v) {
  asm volatile("global_store_dwordx4 %0, %1, %2 offset:%3 sc1\n\ts_nop 1" :: "v"(voff), "v"(v), "s"(sbase), "n"(OFF) : "memory");
}

typedef unsigned long long u64;
__device__ __forceinline__ bool fresh(u64 stamp, u64 t_own) {
  const u64 b = t_own + 100ull;
  const unsigned lo = (unsigned)stamp - (unsigned)b, hi = (unsigned)(stamp >> 32) - (unsigned)(b >> 32) - ((unsigned)stamp < (unsigned)b ? 1u : 0u);
  return (hi == 0u) & (lo < (unsigned)(__builtin_amdgcn_s_memrealtime() - t_own));
}
constexpr u64 TIMEOUT_ = 200000;
__device__ __forceinline__ float half_sum(float v) {
  v += __builtin_bit_cast(float, __builtin_amdgcn_update_dpp(0, __builtin_bit_cast(int, v), 0xB1, 0xf, 0xf, false));
  v += __builtin_bit_cast(float, __builtin_amdgcn_update_dpp(0, __builtin_bit_cast(int, v), 0x4E, 0xf, 0xf, false));
  v += __builtin_bit_cast(float, __builtin_amdgcn_update_dpp(0, __builtin_bit_cast(int, v), 0x141, 0xf, 0xf, false));
  v += __builtin_bit_cast(float, __builtin_amdgcn_update_dpp(0, __builtin_bit_cast(int, v), 0x140, 0xf, 0xf, false));
  { auto r = __builtin_amdgcn_permlane16_swap(__float_as_uint(v), __float_as_uint(v), false, false); v = __uint_as_float(r[0]) + __uint_as_float(r[1]); }
  return v;
}
__device__ __forceinline__ unsigned pack_stats(float mean, float var) {
  return (unsigned)f2bf(mean) | ((unsigned)f2bf(sqrtf(rbf(rbf(var) + 1e-5f))) << 16);
}
__device__ __forceinline__ unsigned row_stats_serial(const u16* xr) {
  float s = 0.f, q = 0.f;
#pragma unroll 1
  for (int k = 0; k < D_; ++k) s += bf2f(xr[k]);
  const float mean = s * (1.0f / D_);
#pragma unroll 1
  for (int k = 0; k < D_; ++k) { const float d = bf2f(xr[k]) - mean; q += d * d; }
  return pack_stats(mean, q * (1.0f / D_));
}
constexpr int G1_STAGE = 32768 + 5 * 8192;
#define EPOS 2
#define PA 4
#define PB 8
#define PC 12
#define GETP 4
#define DLT 2
#define PSLEEP 2
#define GETJ 3

constexpr unsigned BLANK = 0x7f817f81u;
__device__ __forceinline__ bool blank(const u32x4* pv) {
  u64 m;
#define CX(i) "v_cmpx_ne_u32 vcc, %1, %" #i "\n\t"
  asm volatile(CX(2) CX(3) CX(4) CX(5) CX(6) CX(7) CX(8) CX(9) CX(10) CX(11) CX(12) CX(13) CX(14) CX(15) CX(16) CX(17)
               "s_mov_b64 %0, exec\n\ts_mov_b64 exec, -1\n\ts_nop 3" : "=s"(m) : "s"(BLANK), "v"(pv[0][0]), "v"(pv[0][1]), "v"(pv[0][2]), "v"(pv[0][3]),
               "v"(pv[1][0]), "v"(pv[1][1]), "v"(pv[1][2]), "v"(pv[1][3]), "v"(pv[2][0]), "v"(pv[2][1]), "v"(pv[2][2]), "v"(pv[2][3]),
               "v"(pv[3][0]), "v"(pv[3][1]), "v"(pv[3][2]), "v"(pv[3][3]) : "vcc");
  return m != ~0ull;
}
#define RLX_AGENT __ATOMIC_RELAXED, __HIP_MEMORY_SCOPE_AGENT

constexpr int G2_A = 65536, G2_X = 131072;
constexpr int G2_NSTAGE = 4;
constexpr size_t MB_ = 1024 * 1024;
__global__ __launch_bounds__(512, 2) __attribute__((aligned(4096))) void fused_kernel(const u16* __restrict__ x, const u16* __restrict__ wqkv,
                                                       const u16* __restrict__ wg, const u16* __restrict__ wu,
                                                       const u16* __restrict__ wd, const u16* __restrict__ wo,
                                                       u16* __restrict__ out, char* __restrict__ ws) {
  u16* const halo = (u16*)(ws + 4 * MB_);
  u16* const abuf = (u16*)(ws + 16 * MB_);
  char* const slab = ws + 64 * MB_;
  unsigned* const flags = (unsigned*)(ws + 96 * MB_);
  u32x4* const rstat = (u32x4*)(ws + 97 * MB_);
  char* const wcopy = ws + 98 * MB_;
  u64* const done1 = (u64*)(ws + 102 * MB_);
  u64* const done2 = (u64*)(ws + 100 * MB_ + 65536);
  u64* const donec = (u64*)(ws + 100 * MB_ + 131072);
  u64* const acks = (u64*)(ws + 100 * MB_ + 196608);
  float* const csum = (float*)(ws + 101 * MB_);
  __shared__ __attribute__((aligned(1024))) char shm[163840];
  constexpr int AUX = 2 * G1_STAGE + 8192;
  {
    u64 pc; int len;
    asm volatile("s_getpc_b64 %0\n.Lcode_beg:\n\ts_mov_b32 %1, .Lcode_end-.Lcode_beg" : "=s"(pc), "=s"(len));
    const int ol = ((blockIdx.x >> 3) + 32 * (threadIdx.x >> 6)) * 1024 + (threadIdx.x & 63) * 16;
    asm volatile("s_mov_b32 m0, %2\n\ts_nop 0\n\tglobal_load_lds_dwordx4 %0, %1"
                 :: "v"(ol < len - 16 ? ol : 0), "s"(pc & ~15ull), "s"((unsigned)(uintptr_t)(lds_cptr)shm + 2 * G1_STAGE + 7168) : "m0");
    __builtin_amdgcn_sched_barrier(0);
  }
  const u64 t_own = __builtin_amdgcn_s_memrealtime();
  TSDECL;
  TS1(7);
  const int tid = threadIdx.x, lane = tid & 63;
  const int wid = __builtin_amdgcn_readfirstlane(tid >> 6);
  const int wm = wid & 3, wc = wid >> 2;
  int bid_ = blockIdx.x;
  PIN_S(bid_);
  const int bid = bid_ & 255, xcd = bid & 7, idx = bid >> 3;
  const int ntile = xcd * 4 + (idx & 3), mtile = idx >> 2;
  const int m0 = mtile * 256;
  const int hg = ntile >> 1;
  const bool tya = (ntile & 1) == 0;
  const int srow = m0 + ntile * 8 + wm * 2;
  lds_cptr shm3 = (lds_cptr)shm;
  const unsigned shm_base = (unsigned)(uintptr_t)shm3;
  const int r8 = tid >> 3, pos = tid & 7;
  const int phys = pos >> 1, half = pos & 1;
  const int fk = ((r8 >> 1) & 1) | (((r8 >> 3) & 1) << 1);
  const int vsrc = phys ^ fk;
  const int qstr = tya ? 3072 * 2 : F_ * 2;
  unsigned voa = (((r8 & 32) + 2 * (r8 & 15) + ((r8 >> 4) & 1)) * D_ + ((pos ^ (r8 & 7)) * 8)) * 2;
  unsigned vog = r8 * (F_ * 2) + vsrc * 32 + half * 16, voq = r8 * qstr + vsrc * 32 + half * 16;
  unsigned vom = r8 * qstr + (vsrc >> (tya ? 0 : 1)) * 32 + half * 16;
  asm volatile("" : "+v"(voa), "+v"(vog), "+v"(voq), "+v"(vom));
  const int kt0 = (hg >> 1) * 7, he = hg & 1;
  unsigned oq = (tya ? hg : kt0 + 2 * he) * 128, og = (kt0 + (tya ? 5 + he : 2 * he + 1)) * 128, om = tya ? 4096 : 512 - 192 * he;
  unsigned d0 = shm_base + wid * 1024 + 32768, ox = m0 * D_ * 2;
  long uq = -32 * qstr;
  PIN_S(oq); PIN_S(og); PIN_S(om); PIN_S(d0); PIN_S(ox); PIN_S(uq);
  __builtin_amdgcn_sched_barrier(0);
  asm volatile("" :: "s"(x), "s"(wqkv), "s"(wg), "s"(wu), "s"(wd), "s"(wo), "s"(out), "s"(ws));
  const char* const ab = (const char*)x + ox;
  int kz = 0;
  asm volatile("" : "+s"(kz));
  u64 pq = (u64)wqkv, pg = (u64)wg;
  PIN_S(pq); PIN_S(pg);
  const char* const qb = (const char*)(tya ? pq : pg) + oq;
  if (wid < 4) {
#pragma unroll
    for (int i = 0; i < 8; ++i) glds16_s(ab + i * 32 * D_ * 2, voa, d0 - 32768 + i * 4096);
  } else {
    glds16_s(qb, voq, d0); glds16_s(qb + uq, voq, d0 - 4096);
  }
  __builtin_amdgcn_sched_barrier(0);
  const char* const gb = (const char*)wg + og;
  const long dgu = (const char*)wu - (const char*)wg, dp1 = tya ? 2048 : dgu;
  const char* const mb0 = qb + om;
  const char* mptr = mb0 + (((vsrc & 1) && !tya) ? dgu : 0) + vom;
  if (wid >= 4) {
    const long ug = kz - 32 * F_ * 2;
    glds16_s(qb + dp1, voq, d0 + 8192); glds16_s(qb + dp1 + uq, voq, d0 + 4096);
    glds16_v(mptr, d0 + 2 * 8192); glds16_v(mptr + uq, d0 + 2 * 8192 - 4096);
    glds16_s(gb, vog, d0 + 3 * 8192); glds16_s(gb + ug, vog, d0 + 3 * 8192 - 4096);
    glds16_s(gb + dgu, vog, d0 + 4 * 8192); glds16_s(gb + dgu + ug, vog, d0 + 4 * 8192 - 4096);
  }
  __builtin_amdgcn_sched_barrier(0);
  f32x4 acc[4][10];

  constexpr int NT = D_ / 64;
  constexpr int CNT = AUX + 4096 + 3584;
  if (tid < 8) *(unsigned*)(shm + CNT + tid * 4) = 0u;
  bf16x8 af0[4], af1[4];
  bf16x4 bl[4], bh[4];
#define SB0() __builtin_amdgcn_sched_barrier(0)
  auto mm = [&](int g, int t, bool two, bool z = false) {
    const int mi = t >> 1, mf = ((g & 1) && g != 9) ? 3 - mi : mi, ks = (t & 1) ^ (mi & 1), s = ((g + 1) & 1) * 2;
    const bf16x8 b0 = __builtin_shufflevector(bl[s + ks], bh[s + ks], 0, 1, 2, 3, 4, 5, 6, 7), a0 = ks ? af1[mf] : af0[mf];
    const bf16x8 b1 = __builtin_shufflevector(bl[s + 1 - ks], bh[s + 1 - ks], 0, 1, 2, 3, 4, 5, 6, 7), a1 = ks ? af0[mf] : af1[mf];
    const bool tr = (g >> 1) == 2;
    if (z && two) asm volatile("v_mfma_f32_16x16x32_bf16 %0, %1, %2, 0\n\tv_mfma_f32_16x16x32_bf16 %0, %3, %4, %0" : "=&v"(acc[mf][g])
                               : "v"(tr ? a0 : b0), "v"(tr ? b0 : a0), "v"(tr ? a1 : b1), "v"(tr ? b1 : a1));
    else if (z && !(t & 1)) asm volatile("v_mfma_f32_16x16x32_bf16 %0, %1, %2, 0" : "=&v"(acc[mf][g]) : "v"(tr ? a0 : b0), "v"(tr ? b0 : a0));
    else if (two) asm volatile("v_mfma_f32_16x16x32_bf16 %0, %1, %2, %0\n\tv_mfma_f32_16x16x32_bf16 %0, %3, %4, %0" : "+v"(acc[mf][g])
                          : "v"(tr ? a0 : b0), "v"(tr ? b0 : a0), "v"(tr ? a1 : b1), "v"(tr ? b1 : a1));
    else asm volatile("v_mfma_f32_16x16x32_bf16 %0, %1, %2, %0" : "+v"(acc[mf][g]) : "v"(tr ? a0 : b0), "v"(tr ? b0 : a0));
  };
  typedef __attribute__((address_space(3))) unsigned lds_u32;
  lds_u32* const cnt = (lds_u32*)(shm3 + CNT);
  bool cok;
  constexpr int NY = 28;
  int role, mt2, nt2;
  struct WS { unsigned vb[2][2]; const char* wb[2]; int sh[2]; };
  auto wsetup = [&](const u16* w, int crow0, int l, bool ck) __attribute__((always_inline)) {
    WS r;
    const unsigned vr = ((l >> 5) & 1) * 16 + (wm & 1) * 8 + ((l >> 3) & 3), vc = (((l >> 1) & 3) ^ ((l >> 4) & 1)) * 32 + (l & 1) * 16;
    u64 cp = (u64)(wcopy + (nt2 >> 2) * 589824 + crow0 * 128);
    asm volatile("" : "+s"(cp));
#pragma unroll
    for (int i = 0; i < 2; ++i) {
      u64 dp = (u64)(w + nt2 * 128 + i * 64);
      asm volatile("" : "+s"(dp));
      const bool slow = ck & ((((unsigned)dp >> 7) & 7) == 3);
      r.sh[i] = slow ? 7 : 11;
      r.wb[i] = (const char*)(slow ? cp : dp) + (crow0 ? (wm >> 1) * 32 << r.sh[i] : 0);
      r.vb[i][0] = (vr << r.sh[i]) + vc; r.vb[i][1] = ((vr + 4) << r.sh[i]) + (vc ^ 64);
    }
    asm volatile("" : "+v"(r.vb[0][0]), "+v"(r.vb[0][1]), "+v"(r.vb[1][0]), "+v"(r.vb[1][1]));
    PIN_S(r.wb[0]); PIN_S(r.wb[1]); PIN_S(r.sh[0]); PIN_S(r.sh[1]);
    return r;
  };
  WS wy, wz;
  int vxq, ws0, ws1;
  const char* sz;
  u32x4 xq[4];
  unsigned vg;
  int st = 3;
  const char *sa, *sw[2];
  auto wsel = [&]() {
    const unsigned r = __builtin_amdgcn_readlane(vg, st);
    sw[0] = wy.wb[0] + (r << wy.sh[0]); sw[1] = wy.wb[1] + (r << wy.sh[1]);
  };
  const int p = wid & 3;
  unsigned sl = 0;
#define G2_W "s_add_u32 m0, %[l], %[ow]\n\ts_nop 0\n\tglobal_load_lds_dwordx4 %[v0], %[w0] sc1\n\t" \
             "s_add_u32 m0, m0, 1024\n\ts_nop 0\n\tglobal_load_lds_dwordx4 %[v1], %[w0] sc1\n\t" \
             "s_add_u32 m0, m0, 7168\n\ts_nop 0\n\tglobal_load_lds_dwordx4 %[v2], %[w1] sc1\n\t" \
             "s_add_u32 m0, m0, 1024\n\ts_nop 0\n\tglobal_load_lds_dwordx4 %[v3], %[w1] sc1"
#define G2_WOP(w) [ow] "s"(shm_base + p * 2048), [w0] "s"(sw[0]), [w1] "s"(sw[1]), [v0] "v"(w.vb[0][0]), [v1] "v"(w.vb[0][1]), [v2] "v"(w.vb[1][0]), [v3] "v"(w.vb[1][1])
  auto tile2 = [&](int t0) __attribute__((always_inline)) {
    int b = bid;
    asm volatile("" : "+s"(b));
    role = 1 - ((b >> 2) & 1);
    mt2 = ((role ? b : b ^ 5) & 7) * 4 + ((b >> 3) & 3), nt2 = b >> 5;
    sa = (const char*)abuf + (size_t)(mt2 * 72 + role * NY + t0) * 16384 + wm * 4096;
    asm volatile("" : "+s"(sa));
  };
  auto prep = [&](bool first) __attribute__((always_inline)) {
    int l = threadIdx.x;
    asm volatile("" : "+v"(l));
    l &= 63;
    if (first) {
      tile2(3);
      vxq = ((mt2 * 128 + (wm >> 1) * 64 + role * 32 + 2 * (l & 15)) * D_ + nt2 * 128 + (wm & 1) * 64) * 2 + ((l >> 4) & 1) * 32 + (l >> 5) * 16;
      const int t = min(l < NY ? l : l - DLT, NY - 1), w = wm >> 1;
      const int o = (0xffaa5500fae504ull >> (2 * t)) & 3;
      int u1 = (0x990990 >> ((2 * t) & 31)) & 3, u2 = 3 + (t & 3);
      asm volatile("" : "+v"(u1), "+v"(u2));
      const int u = t < 12 ? u1 : u2;
      vg = (__umul24((l < NY ? role : 1 - role) * 4 + (((o & 1) << 1) | (o >> 1)), 14) + ((0xb85140a >> (4 * u)) & 15) + (u == 5 ? w : 2 * w)) * 32;
      asm volatile("" : "+v"(vg), "+v"(vxq));
      sz = (const char*)abuf + (size_t)(mt2 * 72 + 56) * 16384 + (wm >> 1) * 8192 + role * 4096 + (wm & 1) * 2048;
      PIN_S(sz);
    }
    wy = wsetup(wd, 0, l, first);
    wz = wsetup(wo, F_, l, first);
    ws0 = 64 << wz.sh[0]; PIN_S(ws0);
    ws1 = 64 << wz.sh[1]; PIN_S(ws1);
    SB0();
  };
  auto start2 = [&]() __attribute__((always_inline)) {
    const __amdgpu_buffer_rsrc_t rx = __builtin_amdgcn_make_buffer_rsrc((void*)x, 0, S_ * D_ * 2, 0x00020000);
#pragma unroll
    for (int f = 0; f < 4; ++f) xq[f] = __builtin_bit_cast(u32x4, __builtin_amdgcn_raw_buffer_load_b128(rx, vxq + (f >> 1) * 2048 + (f & 1) * 64, 0, 0));
    cok = __builtin_amdgcn_readfirstlane(cnt[1] & cnt[2]) != 0u;
    if (__builtin_expect(!cok, 0)) prep(false);
    wsel();
    PIN_S(sw[0]); PIN_S(sw[1]);
    SB0();
  };
  auto kloop = [&](auto wmc) {
    constexpr bool LD = decltype(wmc)::value != 0;
    constexpr int DMP = 2 * G1_STAGE;
    const char* pa = nullptr;
    unsigned vob = 0;
    f32x4 ca = {0.f, 0.f, 0.f, 0.f}, cb = {0.f, 0.f, 0.f, 0.f}, cd = ca;
    bf16x8 sel = {0, 0, 0, 0, 0, 0, 0, 0};
    int tro = 0;
    const int psh = (wid == 2 && !tya) ? 1 : 0, pstr = wid < 3 ? qstr : F_ * 2;
    const char* const pbase = wid == 0 ? qb : (wid == 1 ? qb + dp1 : (wid == 2 ? mb0 : gb));
    int lane = threadIdx.x;
    asm volatile("; %1" : "+v"(lane) : "n"(LD ? 1 : 0));
    lane &= 63;
    const int fr = lane & 15, fq = lane >> 4, q4 = fr >> 2, pp = lane & 3;
    const int ff = ((q4 >> 1) & 1) | ((fq & 1) << 1);
    int aoff[2], boff[2];
#pragma unroll
    for (int ks = 0; ks < 2; ++ks) aoff[ks] = (wm * 64 + fr) * 128 + (((ks * 4 + fq) ^ (fr & 7)) * 16);
#pragma unroll
    for (int j = 0; j < 2; ++j) boff[j] = 32768 + (8 * fq + q4) * 128 + (((2 * wc + j) ^ ff) * 32) + pp * 8;
    unsigned vol = lane * 16;
    float sm = 0.f, sq = 0.f;
    const char* pf = (const char*)wd + (bid * 4 + wid) * 7168;
    long pstep = 64 * pstr;
    if (!LD) {
      const int rs = 256 + mtile * 8 + (lane >> 3), vs = ((lane & 7) >> 1) ^ (((rs >> 1) & 1) | ((mtile & 1) << 1));
      pa = pbase + ((psh & vs) ? dgu : 0) + (rs * pstr + (vs >> psh) * 32 + (lane & 1) * 16);
      vob = rs * (F_ * 2) + vs * 32 + (lane & 1) * 16;
      tro = DMP + wid * 1024 + q4 * 128 + ((fq ^ ((q4 >> 1) | ((mtile & 1) << 1))) * 32) + pp * 8;
      const short one = fr == fq ? 0x3f80 : 0;
      sel = bf16x8{one, one, one, one, one, one, one, one};
      asm volatile("" : "+v"(tro), "+v"(vol), "+v"(sel), "+v"(ca), "+v"(cb), "+v"(sm), "+v"(sq));
      PIN_S(pf); PIN_S(pstep);
    }
    auto accum = [&](lds_cptr p, int ob) {
      ca = __builtin_amdgcn_mfma_f32_16x16x32_bf16(tr_frag(p), sel, ca, 0, 0, 0);
      if (wid == 0) cb = __builtin_amdgcn_mfma_f32_16x16x32_bf16(tr_frag(p + ob), sel, cb, 0, 0, 0);
    };
    auto xrow = [&](int i) {
      u64 a = (u64)(x + (size_t)srow * D_ + i * 256), b = (u64)(wo + (bid * 4 + wid) * 1024);
      PIN_S(a); PIN_S(b);
      asm volatile("global_load_dwordx4 %0, %1, %2" : "+v"(cd) : "v"(vol + (vol & (i < 4 ? 512 : 0)) * 3), "s"(i < 4 ? a : b) : "memory");
    };
    const int q32 = 32 * qstr;
    const char *sq0 = qb + q32, *sq1 = sq0 + dp1, *sg0 = gb + 32 * F_ * 2, *sg1 = sg0 + dgu, *sx = ab + (long)(kz + 128 - 32 * D_ * 2);
    u32x2 mp[2] = {__builtin_bit_cast(u32x2, mptr + q32), __builtin_bit_cast(u32x2, mptr + 2 * q32)};
    u64 cy[2];
    unsigned dst = shm_base + G1_STAGE + wm * 1024;
    int dbuf = G1_STAGE;
    auto stage = [&](int i0, int i1) __attribute__((always_inline)) {
#pragma unroll
        for (int i = i0; i < i1; ++i) {
          const int q = i >> 1, h = i & 1;
          const unsigned ld = dst + h * 4096 + (q < 5 ? 32768 + q * 8192 : (q - 5) * 8192);
          if (q < 2) glds16_s((q ? sq1 : sq0) + h * q32, voq, ld);
          else if (q == 2) glds16_v(__builtin_bit_cast(const char*, mp[h]), ld);
          else if (q < 5) glds16_s((q == 4 ? sg1 : sg0) + h * 32 * F_ * 2, vog, ld);
          else glds16_s(sx + ((q - 5) * 64 + h * 32) * D_ * 2, voa, ld);
        }
    };
    u64 cy0 = 0, rt0 = 0;
#pragma unroll
    for (int a = 0; a < 4; ++a) { acc[a][9] = f32x4{0.f, 0.f, 0.f, 0.f}; asm volatile("" : "+v"(acc[a][9])); }
    if (!LD) {
      xrow(0);
      WAIT_V(1);
    }
    asm volatile("" : "+v"(aoff[0]), "+v"(aoff[1]), "+v"(boff[0]), "+v"(boff[1]));
    bf16x8 kp[2];
    auto body = [&](bool upd, bool keep = false, bool first = false) __attribute__((always_inline)) {
      lds_cptr sb = shm3;
      auto rdb = [&](int g, int i) {
        lds_cptr p = sb + boff[g & 1] + (g >> 1) * 8192 + (i >> 1) * 4096;
        const int s = ((g + 1) & 1) * 2 + (i >> 1);
        if (i & 1) bh[s] = tr_read(p + 512); else bl[s] = tr_read(p);
      };
      auto rda = [&](int ks, int mf) { (ks ? af1 : af0)[mf] = *(const __attribute__((address_space(3))) bf16x8*)(sb + aoff[ks] + mf * 2048); };
      SB0();
      __builtin_amdgcn_s_waitcnt(0xC07F);
#pragma unroll
      for (int gg = 0; gg < 10; ++gg) {
        const int g = (gg + 9) % 10;
#pragma unroll
        for (int t = 0; t < 8; ++t) {
          const bool two = t >= 4 && !(upd && g >= 5 && g <= 8);
          SB0();
          if ((!two || (t & 1)) && !(first && g == 9)) mm(g, two ? t - 1 : t, two, first);
          SB0();
          if (first && !LD && (g == 3 || g == 7) && t == 0) BAR();
          if (g == 9) {
            if (t == 0 || t == 2) { rdb(0, t); rdb(0, t + 1); }
            if (t & 1) { rda((t >> 1) & 1, t >> 1); rda(((t >> 1) & 1) ^ 1, t >> 1); }
          } else if (t < 4) rdb(g + 1, t);
          else if (keep && g == 7) {
            if (t == 5) { WAIT_V(0); kp[0] = tr_frag(shm3 + tro); }
            if (t == 7) kp[1] = tr_frag(shm3 + tro + 4096);
          } else if (upd) {
            if (g == 5 && t == 4) { sq0 += 2 * q32; PIN_S(sq0); }
            if (g == 5 && t == 5) { sq1 += 2 * q32; PIN_S(sq1); }
            if (g == 5 && t == 6) { sg0 += 64 * F_ * 2; PIN_S(sg0); }
            if (g == 6 && t == 4) { sg1 += 64 * F_ * 2; PIN_S(sg1); }
            if (g == 6 && t == 5) { sx += 128; PIN_S(sx); }
            if (g == 6 && t == 6) { dst -= dbuf; PIN_S(dst); }
            if (g == 7 && t == 4) asm volatile("v_add_co_u32 %0, %2, %4, %0\n\tv_add_co_u32 %1, %3, %4, %1" : "+v"(mp[0][0]), "+v"(mp[1][0]), "=&s"(cy[0]), "=&s"(cy[1]) : "s"(2 * q32));
            if (g == 7 && t == 6) asm volatile("v_addc_co_u32 %0, vcc, 0, %0, %2\n\tv_addc_co_u32 %1, vcc, 0, %1, %3" : "+v"(mp[0][1]), "+v"(mp[1][1]) : "s"(cy[0]), "s"(cy[1]) : "vcc");
            if (g == 8 && t == 4) { aoff[0] += dbuf; aoff[1] += dbuf; asm volatile("" : "+v"(aoff[0]), "+v"(aoff[1])); }
            if (g == 8 && t == 5) { boff[0] += dbuf; boff[1] += dbuf; asm volatile("" : "+v"(boff[0]), "+v"(boff[1])); }
            if (g == 8 && t == 6) { dbuf = -dbuf; PIN_S(dbuf); }
          }
        }
      }
      SB0();
    };
    TS1(0);
#define ARRP(o, c) do {} while (0)
    if (LD) {
      stage(0, PA);
      WAIT_V(6 + PA);
      TS1(5);
      BARL();
      stage(PA, PB);
      WAIT_V(2 + PB);
      BAR();
      stage(PB, PC);
      WAIT_V(PC);
      BAR();
      stage(PC, 18);
      body(true, false, true);
      WAIT_V(0);
      BARL();
#pragma clang loop unroll(disable)
      for (int kt = 1; kt < NT - 1; ++kt) {
        if (__builtin_expect(kt == 5 + wid, 0)) prep(true);
        stage(0, 18);
        body(true);
        WAIT_V(0);
        BARL();
        SB0();
      }
      start2();
      body(false);
    } else {
      char* wsx;
      auto tile = [&](int kt, bool keep) __attribute__((always_inline)) {
        SB0();
        ARRP(wid * 64 + kt * 4, true);
        BARL();
        body(false, keep);
        ARRP(320 + kt * 4 + 4, wid == 0);
        if (keep) asm volatile("" : "+v"(kp[0]), "+v"(kp[1])); else WAIT_V(0);
      };
      auto next = [&]() __attribute__((always_inline)) {
        aoff[0] += dbuf; aoff[1] += dbuf; boff[0] += dbuf; boff[1] += dbuf;
        dbuf = -dbuf;
        asm volatile("" : "+v"(aoff[0]), "+v"(aoff[1]), "+v"(boff[0]), "+v"(boff[1]), "+v"(vol), "+v"(tro) :: "memory");
        wsx = ws;
        PIN_S(wsx);
      };
      auto share = [&]() {
        glds16_v(pa, shm_base + DMP + wid * 1024);
        if (wid == 0) glds16_s(gb + dgu, vob, shm_base + DMP + 4096);
      };
      auto kept = [&]() {
        ca = __builtin_amdgcn_mfma_f32_16x16x32_bf16(kp[0], sel, ca, 0, 0, 0);
        if (wid == 0) cb = __builtin_amdgcn_mfma_f32_16x16x32_bf16(kp[1], sel, cb, 0, 0, 0);
      };
      auto cpub = [&](u32x4 a, u32x4 b) {
        if ((vol & 0xc0) == 0) {
          const char* const cs = wsx + 101 * MB_ + ntile * 10240 + mtile * 256;
          const unsigned co = (vol & 0x30) * 4 + (vol >> 8) * 16;
          gst16(cs + wid * 2048, co, a);
          if (wid == 0) gst16(cs + 8192, co, b);
        }
      };
      auto side0 = [&](int kt) __attribute__((always_inline)) {
        if (kt < 2) {
          glds16_v(pa - 2 * pstep, shm_base + DMP + 7168);
          if (wid == 0) glds16_s(gb + dgu, vob - 128 * F_ * 2, shm_base + DMP + 7168);
        }
        share();
        u32x4 xw;
        asm volatile("v_mov_b32 %0, %4\n\tv_mov_b32 %1, %5\n\tv_mov_b32 %2, %6\n\tv_mov_b32 %3, %7" : "=&v"(xw[0]), "=&v"(xw[1]), "=&v"(xw[2]), "=&v"(xw[3])
                     : "v"(cd[0]), "v"(cd[1]), "v"(cd[2]), "v"(cd[3]));
        xrow(kt + 1);
        next();
        if (kt) kept();
        pa += pstep; vob += 64 * F_ * 2;
        accum(shm3 + tro + ((kt & 1) * G1_STAGE + 32768 + mtile * 1024 + wid * 7168 - DMP), 4 * 8192);
#pragma unroll
        for (int i = 0; i < 4; ++i) {
          float lo, hi;
          asm volatile("v_lshlrev_b32 %2, 16, %4\n\tv_and_b32 %3, 0xffff0000, %4\n\tv_add_f32 %0, %0, %2\n\tv_fmac_f32 %1, %2, %2\n\t"
                       "v_add_f32 %0, %0, %3\n\tv_fmac_f32 %1, %3, %3" : "+v"(sm), "+v"(sq), "=&v"(lo), "=&v"(hi) : "v"(xw[i]));
        }
      };
      BARL();
      body(false, false, true);
      WAIT_V(0);
      side0(0);
#pragma clang loop unroll(disable)
      for (int kt = 1; kt < 4; ++kt) {
        tile(kt, true);
        side0(kt);
      }
#pragma clang loop unroll(disable)
      for (int kt = 4; kt < 12; ++kt) {
        tile(kt, true);
        share();
        glds16_s(pf, vol, shm_base + DMP + 7168);
        next();
        kept();
        pa += pstep; vob += 64 * F_ * 2;
        pf = kt == 10 ? (const char*)wo + (bid * 4 + wid) * 2048 + 1024 : pf + 1024;
        if (kt < 8) {
          const unsigned q1 = bid * 4 + wid + (kt - 5) * 1024, q0 = q1 - 1024;
          if (q0 < 1152) gst16(wsx + 98 * MB_ + (q0 & 1) * 589824 + (q0 >> 1) * 1024, vol, *(const u32x4*)(shm + AUX + wid * 1024 + vol));
          if (q1 < 1152) {
            const char* src = (const char*)((q1 >> 1) < 448 ? wd + (size_t)(q1 >> 1) * 8 * D_ : wo + (size_t)((q1 >> 1) - 448) * 8 * D_);
            src += ((3 - ((unsigned)(uintptr_t)src >> 7)) & 7) * 128 + (q1 & 1) * 1024;
            glds16_s(src, (vol >> 7) * 2048 + (vol & 0x70), shm_base + AUX + wid * 1024);
          }
          u32x4 bk;
          asm volatile("v_mov_b32 %0, %1" : "=v"(bk[0]) : "s"(BLANK));
          bk[1] = bk[0]; bk[2] = bk[0]; bk[3] = bk[0];
          gst16(wsx + 64 * MB_ + bid * 16384 + wid * 4096 + (kt - 4) * 1024, vol, bk);
          if (tya && (kt == 4 || kt == 7)) gst16(wsx + 4 * MB_ + (mtile * 32 + ntile) * 4096 + wid * 2048 + (kt & 1) * 1024, vol, bk);
          if (kt == 4) cpub(bk, bk);
        } else {
          if (kt == 8) {
            const float ml = sm * (1.0f / 32), mean = half_sum(sm) * (1.0f / D_), dm = ml - mean;
            const float q = __builtin_fmaf(dm * 32.0f, dm, __builtin_fmaf(-ml, sm, sq));
            const unsigned ps = pack_stats(mean, fmaxf(half_sum(q) * (1.0f / D_), 0.f));
            const u64 now = __builtin_amdgcn_s_memrealtime();
            if ((vol & 0x1f0) == 0) gst16(wsx + 97 * MB_ + srow * 16, vol >> 5, u32x4{ps, (unsigned)now, ps, (unsigned)(now >> 32)});
          }
          if (kt == 9) {
            if (wid == 0) {
              u64 now;
              asm volatile("s_memrealtime %0\n\ts_waitcnt lgkmcnt(0)" : "=s"(now) :: "memory");
              if (vol == 0) asm volatile("global_store_dwordx2 %0, %1, %2 sc1" :: "v"(vol), "v"(now), "s"(wsx + 100 * MB_ + 131072 + bid * 8) : "memory");
            }
            if (wid == 2) glds16_s1(wsx + 102 * MB_ + (bid & ~1) * 512, vol, shm_base + DMP + 7168);
          }
        }
      }
      asm volatile("" :: "v"(cd));
      if (wid >= 2) glds16_s1(wsx + 100 * MB_ + 131072 + (wid - 2) * 1024, vol, shm_base + DMP + 3072 + wid * 1024);
      tile(12, true);
      next();
      kept();
      const int lane = vol >> 4;
      glds16_s1(wsx + 97 * MB_ + (m0 + wid * 64) * 16, ((lane & 32) + 2 * (lane & 15) + ((lane >> 4) & 1)) * 16, shm_base + AUX + wid * 1024);
      asm volatile("s_nop 7\n\ts_nop 3" : "+v"(ca), "+v"(cb));
      cpub(__builtin_bit_cast(u32x4, ca), __builtin_bit_cast(u32x4, cb));
      if (wid >= 2) {
        u32x4 c4 = *(const u32x4*)(shm + DMP + 3072 + wid * 1024 + vol);
        bool ok = __all(fresh(((u64)c4[1] << 32) | c4[0], t_own) & fresh(((u64)c4[3] << 32) | c4[2], t_own));
        while (__builtin_expect(!ok, 0) && __builtin_amdgcn_s_memrealtime() - t_own < TIMEOUT_ / 10) {
          const __amdgpu_buffer_rsrc_t rs = __builtin_amdgcn_make_buffer_rsrc((void*)(wsx + 100 * MB_ + 131072), 0, 2048, 0x00020000);
          asm volatile("" ::: "memory");
          c4 = __builtin_bit_cast(u32x4, __builtin_amdgcn_raw_buffer_load_b128(rs, (wid - 2) * 1024 + vol, 0, 16));
          ok = __all(fresh(((u64)c4[1] << 32) | c4[0], t_own) & fresh(((u64)c4[3] << 32) | c4[2], t_own));
        }
        *(unsigned*)(shm + CNT - 4 + wid * 4) = ok ? 1u : 0u;
      }
      tile(13, false);
      next();
      u32x4 cw[4];
      auto cgather = [&]() {
        const __amdgpu_buffer_rsrc_t rc = __builtin_amdgcn_make_buffer_rsrc((void*)(wsx + 101 * MB_ + ntile * 10240), 0, 5 * 2048, 0x00020000);
        const unsigned vcs = (vol < 512 ? wid * 2048 : 8192) + (vol & 256) * 4 + (vol & 0xf0);
#pragma unroll
        for (int i = 0; i < 4; ++i) cw[i] = __builtin_bit_cast(u32x4, __builtin_amdgcn_raw_buffer_load_b128(rc, vcs + i * 256, 0, 16));
      };
      {
        const int ln = wid * 64 + (vol >> 4);
        u32x4 gr = *(const u32x4*)(shm + AUX + ln * 16);
        if (__builtin_expect(!__all((gr[0] == gr[2]) & fresh(((u64)gr[3] << 32) | gr[1], t_own)), 0)) {
          const __amdgpu_buffer_rsrc_t rs = __builtin_amdgcn_make_buffer_rsrc((void*)(wsx + 97 * MB_ + m0 * 16), 0, 4096, 0x00020000);
          const int prow = (ln & 0xe0) + 2 * (ln & 15) + ((ln >> 4) & 1);
          for (;;) {
            gr = __builtin_bit_cast(u32x4, __builtin_amdgcn_raw_buffer_load_b128(rs, prow * 16, 0, 16));
            if (__all((gr[0] == gr[2]) & fresh(((u64)gr[3] << 32) | gr[1], t_own))) break;
            if (__builtin_amdgcn_s_memrealtime() - t_own > TIMEOUT_) { gr[0] = row_stats_serial(x + (size_t)(m0 + prow) * D_); break; }
            __builtin_amdgcn_s_sleep(2);
          }
        }
        cgather();
        const float nm = -lo_f(gr[0]), dn = hi_f(gr[0]), r = __builtin_amdgcn_rcpf(dn);
        auto mul = [](float a, float b) { float c; asm("v_mul_f32 %0, %1, %2" : "=v"(c) : "v"(a), "v"(b)); return c; };
        const f32x4 fc = {nm, -2.3022081986f * r, mul(mul(mul(-0.10294324f, r), r), r), mul(dn, dn)};
        *(f32x4*)(shm + AUX + ln * 16) = fc;
        float* const tw = (float*)(shm + 2 * G1_STAGE + 4096 + wid * 768) + ln;
#pragma unroll
        for (int i = 0; i < 4; ++i) tw[i * 64] = fc[i];
        ((float*)(shm + AUX + 4096))[384 + ln] = r;
        cok = __builtin_amdgcn_readfirstlane(cnt[1] & cnt[2]) != 0u;
        tile2(0);
        wy = wsetup(wd, 0, vol >> 4, cok);
      }
      tile(14, false);
      next();
      {
        while (__builtin_expect(blank(cw) && cok, 0)) {
          __builtin_amdgcn_s_sleep(2);
          asm volatile("" ::: "memory");
          cgather();
        }
        f32x4 t;
#pragma unroll
        for (int i = 0; i < 4; ++i) {
          float a, b;
          asm("v_add_f32 %0, %2, %3\n\tv_add_f32 %1, %4, %5\n\tv_add_f32 %0, %0, %1" : "=&v"(a), "=&v"(b) : "v"(cw[0][i]), "v"(cw[1][i]), "v"(cw[2][i]), "v"(cw[3][i]));
          const auto r2 = __builtin_amdgcn_permlane16_swap(__float_as_uint(a), __float_as_uint(a), false, false);
          asm("v_add_f32 %0, %1, %2" : "=v"(t[i]) : "v"(r2[0]), "v"(r2[1]));
        }
        const int l = vol >> 4;
        if (__builtin_expect(!cok, 0)) {
          const int gq = (l >> 2) & 3;
          const char* pc = l < 32 ? pbase + ((psh & gq) ? dgu : 0) + (gq >> psh) * 32 + (l & 3) * 8 : gb + dgu + (l & 15) * 8;
          t = f32x4{0.f, 0.f, 0.f, 0.f};
#pragma unroll 1
          for (int k = 0; k < D_; ++k) { const u32x2 w = *(const __attribute__((address_space(1))) u32x2*)(pc + (size_t)k * (l < 32 ? pstr : F_ * 2)); t += f32x4{lo_f(w[0]), hi_f(w[0]), lo_f(w[1]), hi_f(w[1])}; }
        }
        if (l < 32 || wid == 0)
          *(f32x4*)((float*)(shm + AUX + 4096) + ((l >> 3) & 1) * 192 + (l < 32 ? wid : 4) * 32 + (l & 7) * 4) = t;
        __builtin_amdgcn_s_setprio(2);
      }
      SB0();
      ARRP(wid * 64 + 60, true);
      BARL();
      body(false);
    }
#pragma unroll
    for (int t = 0; t < 8; t += 2) mm(9, t, true);
    asm volatile("s_nop 7\n\ts_nop 3");
    SB0();
    TS1(3);
    if (!LD) {
#pragma unroll
      for (int i = 0; i < 3; ++i) {
        const unsigned r = ((role * 4 + (i & 1) * 2) * 14 + (i < 2 ? 10 : 0) + (wm & 2)) * 32;
        sw[0] = wy.wb[0] + (r << wy.sh[0]); sw[1] = wy.wb[1] + (r << wy.sh[1]);
        asm volatile(G2_W :: [l] "s"((i + 1) * 16384), G2_WOP(wy) : "memory", "scc", "m0");
      }
    } else {
      __builtin_amdgcn_s_setprio(3);
      WAIT_V(0);
    }
  };
  if (wid >= 4) kloop(std::integral_constant<int, 1>{});
  else kloop(std::integral_constant<int, 0>{});
  __builtin_amdgcn_sched_barrier(0);
  int tz = threadIdx.x;
  asm volatile("" : "+v"(tz));
  __builtin_amdgcn_sched_barrier(0);
  const int li = tz & 15, lq = (tz >> 4) & 3, ln = tz & 63;
  const __amdgpu_buffer_rsrc_t ra = __builtin_amdgcn_make_buffer_rsrc((void*)abuf, 0, 16 * 72 * 16384, 0x00020000);
  float* const cfin = (float*)(shm + AUX + 4096);
  float* const svr = cfin + 384;
  float* const tb = (float*)(shm + 2 * G1_STAGE + 4096 + wm * 1024);
  auto signal = [&](unsigned v) {
    asm volatile("" ::: "memory");
    if (ln == 0) __hip_atomic_fetch_add(cnt, v, __ATOMIC_RELAXED, __HIP_MEMORY_SCOPE_WORKGROUP);
    asm volatile("" ::: "memory");
  };
  auto await = [&](unsigned mask, unsigned val) {
    while ((__builtin_amdgcn_readfirstlane(__hip_atomic_load(cnt, __ATOMIC_RELAXED, __HIP_MEMORY_SCOPE_WORKGROUP)) & mask) != val) __builtin_amdgcn_s_sleep(1);
    asm volatile("" ::: "memory");
  };
  auto client = [&](int l) { const int m128 = mtile * 2 + ((l >> 4) & 1); return (((l & 7) * 4 + (m128 & 3)) * 8) + ((m128 >> 2) ^ ((l & 8) ? 5 : 0)); };
  auto notify = [&](int l0, int nl) {
    u64 now;
    asm volatile("s_memrealtime %0\n\ts_waitcnt lgkmcnt(0)" : "=s"(now) :: "memory");
    int l = threadIdx.x;
    asm volatile("" : "+v"(l));
    l &= 63;
    const __amdgpu_buffer_rsrc_t rd1 = __builtin_amdgcn_make_buffer_rsrc((void*)(done1 + ntile), 0, 256 * 512, 0x00020000);
    if (l < nl) __builtin_amdgcn_raw_buffer_store_b64(u32x2{(unsigned)now, (unsigned)(now >> 32)}, rd1, client(l0 + l) * 512, 0, 16);
  };
  const int xo = ((xcd & 1) << 1) | ((xcd >> 1) & 1), te = (xcd >> 2) * 28 + 6 * (xo >> 1) + (xo & 1), tl = (xcd >> 2) * 28 + 12 + 4 * xo;
  const u64 te0 = __builtin_amdgcn_s_memrealtime();
  const __amdgpu_buffer_rsrc_t re = __builtin_amdgcn_make_buffer_rsrc((void*)(done1 + client((wm >> 1) * 16 + 8 - (xcd & 4) * 2) * 64 + 32 + (ntile & 15) * 2 + (wm & 1)),
                                                                      0, 7 * 16384 + 8, 0x00020000);
  auto early = [&]() {
    __builtin_amdgcn_raw_buffer_store_b64(u32x2{(unsigned)te0, (unsigned)(te0 >> 32)}, re, ln * 16384, 0, 16);
  };
  signal(1u << (8 * wc));
  if (wc) {
    await(0xffffu, 0x404u);
#pragma unroll
    for (int f = 0; f < 4; ++f) *(u32x4*)(shm + G2_X + wm * 4096 + f * 1024 + ln * 16) = xq[f];
    SB0();
    __builtin_amdgcn_s_setprio(1);
  }
  TS1(4);
  f32x4 rf[4];
#pragma unroll
  for (int mf = 0; mf < 4; ++mf) rf[mf] = *(const f32x4*)(shm + AUX + (wm * 64 + mf * 16 + li) * 16);
  const float* const cl = cfin + wc * 192 + lq * 4;
  const int sbase = ((mtile * 2 + (wm >> 1)) * 72 * 8 + (wm & 1) * 4) * 2048;
  auto putf = [&](int kg, int mf, u32x4 v) {
    __builtin_amdgcn_raw_buffer_store_b128(v, ra, ln * 16 + sbase + ((kg >> 1) * 8 + mf) * 2048 + (kg & 1) * 1024, 0, 16);
  };
  auto gelp = [&](f32x2 g, f32x2 u, f32x2 cg, f32x2 cu, f32x2 m2, f32x2 k12, f32x2 k22, f32x2 d2) {
    const f32x2 tg = __builtin_elementwise_fma(m2, cg, g), tu = __builtin_elementwise_fma(m2, cu, u);
    const f32x2 arg = tg * __builtin_elementwise_fma(tg * tg, k22, k12);
    const f32x2 dn = __builtin_elementwise_fma(f32x2{__builtin_amdgcn_exp2f(arg[0]), __builtin_amdgcn_exp2f(arg[1])}, d2, d2);
    const f32x2 r = (tg * tu) * f32x2{__builtin_amdgcn_rcpf(dn[0]), __builtin_amdgcn_rcpf(dn[1])};
    return pack2(r[0], r[1]);
  };
  auto gel2 = [&](int p, int kg, int mf0 = 0, int mf1 = 4) {
    f32x4 cg[2], cu[2];
#pragma unroll
    for (int j = 0; j < 2; ++j) { cg[j] = *(const f32x4*)(cl + p * 32 + j * 16); cu[j] = *(const f32x4*)(cl + p * 32 + 32 + j * 16); }
#pragma unroll
    for (int mf = mf0; mf < mf1; ++mf) {
      const f32x2 m2 = {rf[mf][0], rf[mf][0]}, k12 = {rf[mf][1], rf[mf][1]}, k22 = {rf[mf][2], rf[mf][2]}, d2 = {rf[mf][3], rf[mf][3]};
      u32x4 o;
#pragma unroll
      for (int i = 0; i < 4; ++i) {
        const int j = i >> 1, e = (i & 1) * 2;
        const f32x4 g = acc[mf][p * 2 + j], u = acc[mf][p * 2 + 2 + j];
        o[i] = gelp(f32x2{g[e], g[e + 1]}, f32x2{u[e], u[e + 1]}, f32x2{cg[j][e], cg[j][e + 1]}, f32x2{cu[j][e], cu[j][e + 1]}, m2, k12, k22, d2);
      }
      putf(kg, mf, o);
    }
  };
  int tsel = ntile & 1;
  asm volatile("" : "+s"(tsel));
  if (tsel != 0) {
    const int tq = (wc ? tl : te + 2 + (xo & 1)) + he;
    gel2(0, 2 * tq);
    gel2(3, 2 * tq + 1);
    const float cg = cfin[wc * 192 + 64 + li], cu = cfin[wc * 192 + 80 + li];
#pragma unroll
    for (int mf = 0; mf < 4; ++mf) {
      f32x4 t[4];
#pragma unroll
      for (int i = 0; i < 4; ++i) t[i] = *(const f32x4*)(tb + i * 64 + mf * 16 + lq * 4);
      const f32x4 g = acc[mf][4], u = acc[mf][5];
      u32x2 o;
#pragma unroll
      for (int e = 0; e < 4; e += 2)
        o[e >> 1] = gelp(f32x2{g[e], g[e + 1]}, f32x2{u[e], u[e + 1]}, f32x2{cg, cg}, f32x2{cu, cu}, f32x2{t[0][e], t[0][e + 1]},
                         f32x2{t[1][e], t[1][e + 1]}, f32x2{t[2][e], t[2][e + 1]}, f32x2{t[3][e], t[3][e + 1]});
      *(u32x2*)(shm + wm * 4096 + mf * 1024 + wc * 512 + li * 32 + lq * 8) = o;
      if (mf == EPOS && wc == 0) {
        TS1(1);
        WAIT_V(0);
        TS1(6);
        early();
      }
    }
    if (wc == 0) {
      signal(0x10000u << wid);
    } else {
      await(0x1000u << wid, 0x1000u << wid);
#pragma unroll
      for (int mf = 0; mf < 4; ++mf) putf(2 * (tl + 2) + he, mf, __builtin_bit_cast(u32x4, tr_frag(shm3 + wm * 4096 + mf * 1024 + ln * 8)));
    }
  } else {
    float rd[4];
#pragma unroll
    for (int mf = 0; mf < 4; ++mf) rd[mf] = svr[wm * 64 + mf * 16 + li];
    auto cvq = [&](int p, int mf, const f32x4& c0, const f32x4& c1) {
      u32x4 o;
#pragma unroll
      for (int j = 0; j < 2; ++j) {
        const f32x4 a = acc[mf][p * 2 + j], c4 = j ? c1 : c0;
        const f32x2 m2 = {rf[mf][0], rf[mf][0]}, r2 = {rd[mf], rd[mf]};
        f32x2 t0 = __builtin_elementwise_fma(m2, f32x2{c4[0], c4[1]}, f32x2{a[0], a[1]});
        f32x2 t1 = __builtin_elementwise_fma(m2, f32x2{c4[2], c4[3]}, f32x2{a[2], a[3]});
        if (p) { t0 *= r2; t1 *= r2; }
        o[2 * j] = pack2(t0[0], t0[1]); o[2 * j + 1] = pack2(t1[0], t1[1]);
      }
      return o;
    };
    const float cv0 = cfin[wc * 192 + 64 + li], cv1 = cfin[wc * 192 + 80 + li];
    auto cvv = [&](int mf) {
      const f32x4 n4 = *(const f32x4*)(tb + mf * 16 + lq * 4), r4 = *(const f32x4*)(svr + wm * 64 + mf * 16 + lq * 4);
      u32x4 o;
#pragma unroll
      for (int j = 0; j < 2; ++j) {
        const f32x4 a = acc[mf][4 + j];
        const f32x2 c2 = {j ? cv1 : cv0, j ? cv1 : cv0};
        const f32x2 t0 = __builtin_elementwise_fma(f32x2{n4[0], n4[1]}, c2, f32x2{a[0], a[1]}) * f32x2{r4[0], r4[1]};
        const f32x2 t1 = __builtin_elementwise_fma(f32x2{n4[2], n4[3]}, c2, f32x2{a[2], a[3]}) * f32x2{r4[2], r4[3]};
        o[2 * j] = pack2(t0[0], t0[1]); o[2 * j + 1] = pack2(t1[0], t1[1]);
      }
      return o;
    };
    u32x4 kx[4], vx[4], qx[4], hx[4];
    const bf16x8 ones = {0x3f80, 0x3f80, 0x3f80, 0x3f80, 0x3f80, 0x3f80, 0x3f80, 0x3f80};
    const f32x4 ck0 = *(const f32x4*)(cl + 32), ck1 = *(const f32x4*)(cl + 48);
    kx[2] = cvq(1, 2, ck0, ck1); kx[3] = cvq(1, 3, ck0, ck1);
    vx[2] = cvv(2); vx[3] = cvv(3);
    auto exo = [](int e) { return e * 4096 + (e >> 2) * (G2_A - 16384); };
    const bool needh = wm == 0 && mtile > 0;
    const __amdgpu_buffer_rsrc_t rh = __builtin_amdgcn_make_buffer_rsrc((void*)((char*)halo + ((needh ? mtile - 1 : mtile) * 32 + ntile + wc) * 4096), 0,
                                                                        needh || wm == 3 ? 4096 : 0, 0x00020000);
    if (wm < 3) {
      char* const ex = shm + exo(wc * 3 + wm) + ln * 16;
      *(u32x4*)ex = kx[2]; *(u32x4*)(ex + 1024) = kx[3]; *(u32x4*)(ex + 2048) = vx[2]; *(u32x4*)(ex + 3072) = vx[3];
      signal(0x10000u << wid);
    } else {
#pragma unroll
      for (int r = 0; r < 4; ++r) __builtin_amdgcn_raw_buffer_store_b128(r < 2 ? kx[2 + r] : vx[r], rh, ln * 16 + r * 1024, 0, 16);
    }
    auto hload = [&]() {
#pragma unroll
      for (int r = 0; r < 4; ++r) hx[r] = __builtin_bit_cast(u32x4, __builtin_amdgcn_raw_buffer_load_b128(rh, ln * 16 + r * 1024, 0, 16));
    };
    SB0();
    gel2(3, 2 * (wc ? tl + 3 : te) + he);
    SB0();
    if (wm == 0) hload();
    SB0();
    kx[0] = cvq(1, 0, ck0, ck1); kx[1] = cvq(1, 1, ck0, ck1);
    vx[0] = cvv(0); vx[1] = cvv(1);
    {
      const f32x4 cq0 = *(const f32x4*)(cl), cq1 = *(const f32x4*)(cl + 16);
#pragma unroll
      for (int mf = 0; mf < 4; ++mf) qx[mf] = cvq(0, mf, cq0, cq1);
    }
    f32x4 mk0, mk1;
#pragma unroll
    for (int j = 0; j < 4; ++j) {
      const int k0 = 4 * lq + j, k1 = 16 + 4 * lq + j;
      mk0[j] = (k0 >= li + 9 && k0 <= li + 16) ? 0.f : -1.0e30f;
      mk1[j] = (k1 >= li + 9 && k1 <= li + 16) ? 0.f : -1.0e30f;
    }
    auto max3 = [](float a, float b, float c) { float r; asm("v_max3_f32 %0, %1, %2, %3" : "=v"(r) : "v"(a), "v"(b), "v"(c)); return r; };
    auto max2 = [](float a, float b) { float r; asm("v_max_f32 %0, %1, %2" : "=v"(r) : "v"(a), "v"(b)); return r; };
    asm volatile("" : "+v"(kx[0]), "+v"(kx[1]), "+v"(vx[0]), "+v"(vx[1]), "+v"(qx[0]), "+v"(qx[1]), "+v"(qx[2]), "+v"(qx[3]), "+v"(mk0), "+v"(mk1));
    TS1(1);
    if (wc == 0) {
      if (wm == 0) WAIT_V(4); else WAIT_V(0);
      early();
    }
    if (wm > 0) {
      await(0x8000u << wid, 0x8000u << wid);
#pragma unroll
      for (int i = 0; i < 4; ++i) hx[i] = *(const u32x4*)(shm + exo(wc * 3 + wm - 1) + i * 1024 + ln * 16);
    } else if (__builtin_expect(cok || !needh, 1)) {
      while (__builtin_expect(blank(hx), 0)) {
        __builtin_amdgcn_s_sleep(1);
        asm volatile("" ::: "memory");
        hload();
      }
    } else {
      await(0xffffu, 0x404u);
      char* const fb = shm + exo(6 + wc) + ln * 16;
#pragma unroll 1
      for (int i = 0; i < 32; ++i) {
        const int rec = i >> 3, e = i & 7;
        const int row = m0 - 32 + 2 * (rec < 2 ? li : lq * 4 + (e & 3)) + (rec & 1);
        const int col = (rec < 2 ? D_ + lq * 4 + (e & 3) : 2 * D_ + li) + (ntile + wc) * 32 + (e >> 2) * 16;
        const u16* xr = x + (size_t)row * D_;
        const unsigned stt = row_stats_serial(xr);
        const float mb = lo_f(stt);
        float a = 0.f;
#pragma unroll 1
        for (int k = 0; k < D_; ++k) a += (bf2f(xr[k]) - mb) * bf2f(wqkv[(size_t)k * 3072 + col]);
        *(u16*)(fb + rec * 1024 + e * 2) = f2bf(a / hi_f(stt));
      }
#pragma unroll
      for (int i = 0; i < 4; ++i) hx[i] = *(const u32x4*)(fb + i * 1024);
    }
    TS1(6);
    {
      const f32x4 z = {0.f, 0.f, 0.f, 0.f};
      const float mz = mtile == 0 && wm == 0 ? -1.0e30f : 0.f;
      const f32x4 mkf = {fminf(mk0[0], mz), fminf(mk0[1], mz), fminf(mk0[2], mz), fminf(mk0[3], mz)};
      f32x4 s0[4], s1[4];
#pragma unroll
      for (int i = 0; i < 4; ++i) {
        const int n = i ^ 2;
        const bf16x8 qf = __builtin_bit_cast(bf16x8, qx[n]);
        s1[n] = __builtin_amdgcn_mfma_f32_16x16x32_bf16(__builtin_bit_cast(bf16x8, kx[n]), qf, mk1, 0, 0, 0);
        s0[n] = __builtin_amdgcn_mfma_f32_16x16x32_bf16(__builtin_bit_cast(bf16x8, n < 2 ? hx[n] : kx[n - 2]), qf, n < 2 ? mkf : mk0, 0, 0, 0);
      }
      SB0();
      asm volatile("s_nop 7\n\ts_nop 3" : "+v"(s0[0]), "+v"(s1[0]), "+v"(s0[1]), "+v"(s1[1]), "+v"(s0[2]), "+v"(s1[2]), "+v"(s0[3]), "+v"(s1[3]));
      float mx[4];
#pragma unroll
      for (int n = 0; n < 4; ++n) mx[n] = max3(max3(s0[n][0], s0[n][1], s0[n][2]), max3(s0[n][3], s1[n][0], s1[n][1]), max2(s1[n][2], s1[n][3]));
#pragma unroll
      for (int n = 0; n < 4; ++n) { auto r = __builtin_amdgcn_permlane16_swap(__float_as_uint(mx[n]), __float_as_uint(mx[n]), false, false);
        mx[n] = max2(__uint_as_float(r[0]), __uint_as_float(r[1])); }
#pragma unroll
      for (int n = 0; n < 4; ++n) { auto r = __builtin_amdgcn_permlane32_swap(__float_as_uint(mx[n]), __float_as_uint(mx[n]), false, false);
        mx[n] = max2(__uint_as_float(r[0]), __uint_as_float(r[1])); }
      SB0();
      constexpr float cs2 = 0.17677669529663687f * 1.4426950408889634f;
      f32x4 os[4], o0[4], o1[4];
#pragma unroll
      for (int n = 0; n < 4; ++n) {
        const float c = cs2 * rd[n], nm = -mx[n] * c;
        float e8[8];
#pragma unroll
        for (int j = 0; j < 4; ++j) {
          e8[j] = __builtin_amdgcn_exp2f(__builtin_fmaf(s0[n][j], c, nm));
          e8[4 + j] = __builtin_amdgcn_exp2f(__builtin_fmaf(s1[n][j], c, nm));
        }
        u32x4 pk;
#pragma unroll
        for (int j = 0; j < 4; ++j) pk[j] = pack2(e8[2 * j], e8[2 * j + 1]);
        const bf16x8 pf = __builtin_bit_cast(bf16x8, pk);
        const u32x4 vp = n < 2 ? hx[2 + n] : vx[n - 2];
        const u32x4 va = {vp[0], vp[1], vx[n][0], vx[n][1]}, vb = {vp[2], vp[3], vx[n][2], vx[n][3]};
        os[n] = __builtin_amdgcn_mfma_f32_16x16x32_bf16(ones, pf, z, 0, 0, 0);
        o0[n] = __builtin_amdgcn_mfma_f32_16x16x32_bf16(__builtin_bit_cast(bf16x8, va), pf, z, 0, 0, 0);
        o1[n] = __builtin_amdgcn_mfma_f32_16x16x32_bf16(__builtin_bit_cast(bf16x8, vb), pf, z, 0, 0, 0);
      }
#pragma unroll
      for (int n = 0; n < 4; ++n) {
        const float inv = __builtin_amdgcn_rcpf(os[n][0]);
        const f32x4 n0 = o0[n] * inv, n1 = o1[n] * inv;
        putf(112 + ntile + wc, n, u32x4{pack2(n0[0], n0[1]), pack2(n0[2], n0[3]), pack2(n1[0], n1[1]), pack2(n1[2], n1[3])});
      }
    }
  }
  TS1(2);
  {
  __builtin_amdgcn_sched_barrier(0);
  int tid = threadIdx.x;
  asm volatile("" : "+v"(tid));
  __builtin_amdgcn_sched_barrier(0);
  const int lane = tid & 63;
  const int wm = wid >> 1, wn = wid & 1;
  const int m0 = mt2 * 128, n0 = nt2 * 128;
  constexpr int PD = G2_NSTAGE;
  const int ny = (xcd & 1) ? NY - DLT : NY + DLT;
  const unsigned va = lane * 16;
  auto stage_y = [&]() {
    asm volatile("s_add_u32 m0, %[l], %[oa]\n\ts_nop 0\n\tglobal_load_lds_dwordx4 %[va], %[a] sc1\n\tglobal_load_lds_dwordx4 %[va], %[a] offset:1024 sc1\n\t"
                 "global_load_lds_dwordx4 %[va], %[a] offset:2048 sc1\n\tglobal_load_lds_dwordx4 %[va], %[a] offset:3072 sc1\n\t" G2_W
                 :: [l] "s"(sl), [oa] "s"(shm_base + G2_A + (p ^ role) * 4096), [a] "s"(sa), [va] "v"(va), G2_WOP(wy) : "memory", "scc", "m0");
    ++st;
    sa += 16384;
    sl = (sl + 16384) & 65535;
    wsel();
  };
  u64 ready = 0;
  const u64* const mbox = &done1[bid * 64 + lane];
  auto seen = [&](u64 v) {
    const u64 r = __ballot(fresh(v, t_own));
    u64 d;
    asm("s_bitreplicate_b64_b32 %0, %1" : "=s"(d) : "s"((unsigned)r >> (16 * role)));
    ready |= r | (d << 32);
  };
  auto need = [&](u64 mask, bool renew) {
    while ((ready & mask) != mask) {
      seen(__hip_atomic_load(mbox, RLX_AGENT));
      if ((ready & mask) != mask) {
        __builtin_amdgcn_s_sleep(PSLEEP);
        if (renew && !cok) notify(0, 32);
      }
    }
    asm volatile("" ::: "memory");
  };
  u64 fl = 0;
  auto peek = [&]() { asm volatile("global_load_dwordx2 %0, %1, %2 sc1" : "+v"(fl) : "v"(va >> 1), "s"(done1 + bid * 64) : "memory"); };
  auto check = [&](u64 mask, bool renew) {
    asm volatile("" : "+v"(fl));
    seen(fl);
    if (__builtin_expect((ready & mask) != mask, 0)) need(mask, renew);
  };
  if (wid >= 4) {
    TS(4);
    __builtin_amdgcn_s_setprio(0);
    BARL();
    stage_y();
    stage_y();
    BAR();
    if (p == 1) peek();
    stage_y();
    WAIT_V(16);
    BAR();
    if (p == 0) notify(0, 32);
#define G2_A2 "global_load_lds_dwordx4 %[va], %[a] sc1\n\tglobal_load_lds_dwordx4 %[va], %[a] offset:1024 sc1"
#define G2_A4 G2_A2 "\n\tglobal_load_lds_dwordx4 %[va], %[a] offset:2048 sc1\n\tglobal_load_lds_dwordx4 %[va], %[a] offset:3072 sc1"
#define G2_SYNC "\n\ts_mov_b32 m0, %[la]\n\ts_waitcnt vmcnt(%[n])\n\ts_barrier\n\t"
    const unsigned oy = shm_base + G2_A + (p ^ role) * 4096, os = shm_base + G2_A + (p >> 1) * 8192 + (p & 1) * 2048;
    asm volatile("s_mov_b32 m0, %[la]\n\ts_nop 0\n\t" G2_A4 :: [la] "s"(sl + oy), [a] "s"(sa), [va] "v"(va) : "memory", "m0");
    auto step = [&](long adv = 16384) __attribute__((always_inline)) {
      wsel();
      ++st;
      sa += adv;
      const unsigned sn = (sl + 16384) & 65535;
      asm volatile(G2_W G2_SYNC G2_A4 :: [l] "s"(sl), [la] "s"(sn + oy), [n] "n"(16), [a] "s"(sa), [va] "v"(va), G2_WOP(wy) : "memory", "scc", "m0");
      sl = sn;
    };
    auto chk1 = [&](u64 mask) __attribute__((always_inline)) { if (p == 1) { WAIT_V(16); check(mask, true); } };
    step();
    chk1(0xcc00cc00ull << 32);
    step(); step();
    if (p == 1) peek();
    step(); step();
    chk1(0x0f0fu << (16 * role));
#pragma unroll 1
    for (int g = 7; g < 13; ++g) step();
    if (p == 1) peek();
    step(); step();
    chk1((0xffffu << (16 * role)) | 0xf555f555u);
#pragma unroll 1
    for (int g = 15; g < min(ny, NY) - 5; ++g) step();
    if (ny > NY) {
      step(((1 - 2 * role) * NY + 1 - DLT) * 16384);
#pragma unroll
      for (int i = 1; i < DLT; ++i) step();
    }
    wsel();
    sa = sz;
    {
      const unsigned sn = (sl + 16384) & 65535;
      asm volatile(G2_W G2_SYNC G2_A2 :: [l] "s"(sl), [la] "s"(sn + os), [n] "n"(16), [a] "s"(sa), [va] "v"(va), G2_WOP(wy) : "memory", "scc", "m0");
      sl = sn;
    }
    sw[0] = wz.wb[0]; sw[1] = wz.wb[1];
#pragma unroll
    for (int j = -3; j < 13; ++j) {
      if (j == GETP + 1 || j == GETP + 2)
        asm volatile("s_mov_b32 m0, %[l]\n\ts_nop 0\n\tglobal_load_lds_dwordx4 %[va], %[a] offset:%[o] sc1\n\tglobal_load_lds_dwordx4 %[va], %[a] offset:%[o]+1024 sc1"
                     :: [l] "s"(shm_base + G2_X + 16384 + p * 4096), [a] "s"((const char*)slab + (size_t)(bid ^ 5) * 16384 + p * 4096), [va] "v"(va),
                        [o] "n"((j - GETP - 1) * 2048) : "memory", "m0");
      sa += 16384;
      const unsigned sn = (sl + 16384) & 65535;
      constexpr int G = GETP;
      static_assert(GETP <= GETJ + 1, "");
      if (j < 12) asm volatile(G2_W G2_SYNC G2_A2 :: [l] "s"(sl), [la] "s"(sn + os), [n] "n"((j == -3 ? 14 : 12) + (j > G && j < G + 3 ? 2 : 0) + (j > G + 1 && j < G + 4 ? 2 : 0)),
                               [a] "s"(sa), [va] "v"(va), G2_WOP(wz) : "memory", "scc", "m0");
      else asm volatile(G2_W :: [l] "s"(sl), G2_WOP(wz) : "memory", "scc", "m0");
      sl = sn;
      sw[0] += ws0; sw[1] += ws1;
    }
    WAIT_V(12);
    BAR();
    WAIT_V(6);
    BAR();
    WAIT_V(0);
    BAR();
    asm volatile("" : "+v"(fl));
    TS(5);
    if (tid < 258) __hip_atomic_store(lane ? done2 + bid * 8 : &acks[bid], __builtin_amdgcn_s_memrealtime(), RLX_AGENT);
    if (__builtin_expect(!cok, 0) && p == 0) {
      const bool mine = lane <= 32;
      const u64* const pa = &acks[lane < 32 ? client(lane) : bid ^ 5];
      while (!__all(!mine || fresh(__hip_atomic_load(pa, RLX_AGENT), t_own))) {
        notify(0, 32);
        if (lane == 0) __hip_atomic_store(done2 + bid * 8, __builtin_amdgcn_s_memrealtime(), RLX_AGENT);
        __builtin_amdgcn_s_sleep(32);
      }
    }
  } else {
    TS(6);
    f32x4 acc[4][4], sacc[2][4];
    const int fr = lane & 15, fq = lane >> 4, q4 = fr >> 2, pp = lane & 3;
    const int ff = ((q4 >> 1) & 1) | ((fq & 1) << 1);
    int aof = G2_A + wm * 8192 + lane * 16;
    int boff[4];
#pragma unroll
    for (int j = 0; j < 4; ++j) boff[j] = wn * 8192 + (8 * fq + q4) * 128 + ((j ^ ff) * 32) + pp * 8;
    int vo = wid * 4096 + lane * 16;
    int vx = ((m0 + wm * 64 + role * 32 + 2 * fr) * D_ + n0 + wn * 64) * 2 + (fq & 1) * 32 + (fq >> 1) * 16;
    lds_cptr px = shm3 + G2_X + wid * 4096 + (fq >> 1) * 512 + fr * 16 + (fq & 1) * 8;
    asm volatile("" : "+v"(vo), "+v"(vx), "+v"(px));
#define SB() __builtin_amdgcn_sched_barrier(0)
    struct Fr { bf16x8 a[4]; bf16x4 bl[4], bh[4]; };
    Fr fa[2], fb[2];
    auto rd = [&](Fr* f, int odd, int t) {
      constexpr int ord[12] = {0, 1, 8, 2, 3, 4, 5, 6, 7, 9, 10, 11};
      constexpr int seq[24] = {0, 1, 2, 12, 13, 14, 15, 16, 3, 4, 5, 6, 17, 18, 19, 20, 7, 8, 9, 21, 10, 22, 11, 23};
      lds_cptr sb = shm3 + odd * 16384;
      const int ks = seq[t] / 12, o = ord[seq[t] % 12];
      if (o >= 8) f[ks].a[o - 8] = *(const __attribute__((address_space(3))) bf16x8*)(sb + aof + (o - 8) * 2048 + ks * 1024);
      else if (o & 1) f[ks].bh[o >> 1] = tr_read(sb + boff[o >> 1] + ks * 4096 + 512);
      else f[ks].bl[o >> 1] = tr_read(sb + boff[o >> 1] + ks * 4096);
    };
    auto bfr = [&](const Fr& c, int j) { return __builtin_shufflevector(c.bl[j], c.bh[j], 0, 1, 2, 3, 4, 5, 6, 7); };
    auto mf1 = [](f32x4& c, bf16x8 b, bf16x8 a, bool z = false) {
      if (z) asm volatile("v_mfma_f32_16x16x32_bf16 %0, %1, %2, 0" : "=&v"(c) : "v"(b), "v"(a));
      else asm volatile("v_mfma_f32_16x16x32_bf16 %0, %1, %2, %0" : "+v"(c) : "v"(b), "v"(a));
    };
    auto mf2 = [](f32x4& c, bf16x8 b, bf16x8 a, bf16x8 b2, bf16x8 a2, bool z) {
      if (z) asm volatile("v_mfma_f32_16x16x32_bf16 %0, %1, %2, 0\n\tv_mfma_f32_16x16x32_bf16 %0, %3, %4, %0" : "=&v"(c) : "v"(b), "v"(a), "v"(b2), "v"(a2));
      else asm volatile("v_mfma_f32_16x16x32_bf16 %0, %1, %2, %0\n\tv_mfma_f32_16x16x32_bf16 %0, %3, %4, %0" : "+v"(c) : "v"(b), "v"(a), "v"(b2), "v"(a2));
    };
    auto pairs = [&](f32x4 (*ac)[4], const Fr* c, int nq, int mx, int nf, auto fill, bool z = false) {
#pragma unroll
      for (int q = 0; q < nq; ++q) {
        const int mf = (q >> 2) ^ mx, j = (q & 4) ? 3 - (q & 3) : q & 3, ks = q & 1;
        SB();
        if (2 * q >= nf) mf2(ac[mf][j], bfr(c[ks], j), c[ks].a[mf], bfr(c[ks ^ 1], j), c[ks ^ 1].a[mf], z);
        else {
          mf1(ac[mf][j], bfr(c[ks], j), c[ks].a[mf], z);
          SB();
          fill(2 * q);
          SB();
          mf1(ac[mf][j], bfr(c[ks ^ 1], j), c[ks ^ 1].a[mf]);
          SB();
          fill(2 * q + 1);
        }
      }
      SB();
    };
    auto flip = [&]() {
      aof ^= 32768; boff[0] ^= 32768; boff[1] ^= 32768; boff[2] ^= 32768; boff[3] ^= 32768;
      asm volatile("" : "+v"(aof), "+v"(boff[0]), "+v"(boff[1]), "+v"(boff[2]), "+v"(boff[3]));
    };
    auto bar = [&]() { __builtin_amdgcn_s_waitcnt(0xC07F); BAR(); };
    auto ystg = [&](const Fr* c, Fr* n, int odd, bool z = false) {
      pairs(acc, c, 16, 0, odd ? 30 : 24, [&](int t) {
        if (t < 24) rd(n, odd, t);
        else if (t == 24) asm volatile("s_nop 0");
        else { int& a = t == 29 ? aof : boff[t - 25]; a ^= 32768; asm volatile("" : "+v"(a)); }
      }, z);
    };
    auto stg = [&](const Fr* c, Fr* n, int odd, bool z = false) { pairs(sacc, c, 8, 0, 10, [&](int t) { rd(n, odd, 2 * t); rd(n, odd, 2 * t + 1); }, z); };
    asm volatile("" : "+v"(aof), "+v"(boff[0]), "+v"(boff[1]), "+v"(boff[2]), "+v"(boff[3]));
    await(0xffffu, 0x404u);
    auto lda = [&](int i) {
      asm volatile("s_mov_b32 m0, %[l]\n\ts_nop 0\n\tglobal_load_lds_dwordx4 %[va], %[a] sc1\n\tglobal_load_lds_dwordx4 %[va], %[a] offset:1024 sc1\n\t"
                   "global_load_lds_dwordx4 %[va], %[a] offset:2048 sc1\n\tglobal_load_lds_dwordx4 %[va], %[a] offset:3072 sc1"
                   :: [l] "s"(shm_base + G2_A + (i + 1) * 16384 + (p ^ role) * 4096), [a] "s"(sa + i * 16384), [va] "v"(va) : "memory", "m0");
    };
    need(0x00330033ull << 32, false);
    TS(4);
    lda(0);
    lda(1);
    need(0x00ff00ffull << 32, false);
    TS(0);
    lda(2);
    WAIT_V(8);
    signal(1u << 24);
    await(0xff000000u, 4u << 24);
    __builtin_amdgcn_s_waitcnt(0xC07F);
    SB();
#pragma unroll
    for (int t = 0; t < 24; ++t) rd(fa, 1, t);
    flip();
#define CPRIO 0
    __builtin_amdgcn_s_setprio(CPRIO);
    WAIT_V(4);
    TS(5);
    peek();
    bar();
    TS(7);
    ystg(fa, fb, 0, true);
    WAIT_V(1);
    bar();
    ystg(fb, fa, 1);
    WAIT_V(0);
    check(0x33003300ull << 32, false);
    constexpr int PAR = (NY - DLT + 1) & 1;
#pragma unroll 1
    for (int g = 2; g < ny - 2; g += 2) {
      bar();
      ystg(fa, fb, 0);
      bar();
      ystg(fb, fa, 1);
    }
    if (PAR) {
      bar();
      ystg(fa, fb, 0);
    }
    Fr* const f0 = PAR ? fb : fa;
    Fr* const f1 = PAR ? fa : fb;
    bar();
    {
      const __amdgpu_buffer_rsrc_t rs = __builtin_amdgcn_make_buffer_rsrc((void*)((char*)slab + (size_t)bid * 16384), 0, 16384, 0x00020000);
      u32x4 pk;
      pairs(acc, f0, 16, 2, 32, [&](int t) {
        if (t < 20) rd(f1, PAR, t);
        else {
          const int f = (t - 20) / 3, e = (t - 20) % 3;
          if (e < 2) { const f32x4 a = acc[2 + (f >> 1)][(f & 1) * 2 + e]; pk[2 * e] = pack2(a[0], a[1]); pk[2 * e + 1] = pack2(a[2], a[3]); }
          else __builtin_amdgcn_raw_buffer_store_b128(pk, rs, vo + f * 1024, 0, 16);
        }
      });
    }
    TS(2);
    static_assert(GETJ % 2 == 1 && GETJ >= 3 && GETJ <= 7, "");
    f32x4 xf[2][4];
#pragma unroll
    for (int j = 0; j < GETJ + 5; j += 2) {
      if (PAR) flip();
      bar();
      stg(f1, f0, PAR ^ 1, j == 0);
      if (!PAR) flip();
      bar();
      u32x2 xv[2];
      pairs(sacc, f0, 8, 0, 12, [&](int t) {
        if (t < 10) { rd(f1, PAR, 2 * t); rd(f1, PAR, 2 * t + 1); }
        else if (j < 8) xv[t - 10] = *(const __attribute__((address_space(3))) u32x2*)(px + (j >> 1) * 1024 + (t - 10) * 256);
      });
      if (j < 8) {
#pragma unroll
        for (int e = 0; e < 2; ++e) xf[j >> 2][(j & 2) + e] = f32x4{lo_f(xv[e][0]), hi_f(xv[e][0]), lo_f(xv[e][1]), hi_f(xv[e][1])};
        asm volatile("" : "+v"(xf[j >> 2][j & 2]), "+v"(xf[j >> 2][(j & 2) + 1]));
        SB();
      }
    }
    if (PAR) flip();
    WAIT_V(0);
    bar();
    u32x4 pv[4];
    {
      lds_cptr pp_ = shm3 + G2_X + 16384 + vo;
      pairs(sacc, f1, 8, 0, 12, [&](int t) {
        if (t < 2) {
#pragma unroll
          for (int f = 2 * t; f < 2 * t + 2; ++f) pv[f] = *(const __attribute__((address_space(3))) u32x4*)(pp_ + f * 1024);
        } else { rd(f0, PAR ^ 1, 2 * t - 4); rd(f0, PAR ^ 1, 2 * t - 3); }
      });
      if (__builtin_expect(blank(pv) || !cok, 0)) {
        const __amdgpu_buffer_rsrc_t rp = __builtin_amdgcn_make_buffer_rsrc((void*)((char*)slab + (size_t)(bid ^ 5) * 16384), 0, 16384, 0x00020000);
        while (!cok && !__all(fresh(__hip_atomic_load(done2 + (bid ^ 5) * 8, RLX_AGENT), t_own))) {
          notify(0, 32);
          if (lane == 0) __hip_atomic_store(done2 + bid * 8, __builtin_amdgcn_s_memrealtime(), RLX_AGENT);
          __builtin_amdgcn_s_sleep(8);
        }
        do {
          asm volatile("" ::: "memory");
#pragma unroll
          for (int f = 0; f < 4; ++f) pv[f] = __builtin_bit_cast(u32x4, __builtin_amdgcn_raw_buffer_load_b128(rp, vo + f * 1024, 0, 16));
        } while (blank(pv));
      }
    }
    auto silu = [&](int g0, int g1) {
#pragma unroll
      for (int f = g0; f < g1; ++f) {
        const int mf = f >> 2, j = f & 3;
        const u32x4 pw = pv[mf * 2 + (j >> 1)];
        const f32x4 ya = acc[mf][j] + f32x4{lo_f(pw[2 * (j & 1)]), hi_f(pw[2 * (j & 1)]), lo_f(pw[2 * (j & 1) + 1]), hi_f(pw[2 * (j & 1) + 1])};
#pragma unroll
        for (int e = 0; e < 2; ++e) {
          const f32x2 y2 = {ya[2 * e], ya[2 * e + 1]};
          const f32x2 ar = y2 * f32x2{-1.4426950408889634f, -1.4426950408889634f};
          const f32x2 dn = f32x2{__builtin_amdgcn_exp2f(ar[0]), __builtin_amdgcn_exp2f(ar[1])} + f32x2{1.0f, 1.0f};
          const f32x2 t = y2 * f32x2{__builtin_amdgcn_rcpf(dn[0]), __builtin_amdgcn_rcpf(dn[1])};
          acc[mf][j][2 * e] = t[0]; acc[mf][j][2 * e + 1] = t[1];
        }
        asm volatile("" : "+v"(acc[mf][j]));
      }
      SB();
    };
#define SPT 0, 0, 1, 2, 4, 6, 8, 8
    constexpr int spt[] = {SPT};
    silu(0, spt[1]);
#pragma unroll
    for (int j = GETJ + 6; j < 15; ++j) {
      if ((j ^ PAR) & 1) flip();
      bar();
      if (j & 1) stg(f0, f1, PAR); else stg(f1, f0, PAR ^ 1);
      silu(spt[j - GETJ - 5], j == 14 ? 8 : spt[j - GETJ - 4]);
    }
    pairs(sacc, f0, 8, 0, 0, [](int) {});
    asm volatile("s_nop 7\n\ts_nop 3" : "+v"(sacc[1][0]), "+v"(sacc[1][1]), "+v"(sacc[1][2]), "+v"(sacc[1][3]));
    SB();
    TS(1);
    {
      const __amdgpu_buffer_rsrc_t ro = __builtin_amdgcn_make_buffer_rsrc((void*)out, 0, S_ * D_ * 2, 0x00020000);
#pragma unroll
      for (int f = 0; f < 4; ++f) {
        const int mf = f >> 1, j = (f & 1) * 2;
        const f32x4 a = __builtin_elementwise_fma(sacc[mf][j], acc[mf][j], xf[mf][j]), b = __builtin_elementwise_fma(sacc[mf][j + 1], acc[mf][j + 1], xf[mf][j + 1]);
        const auto r0 = __builtin_amdgcn_permlane16_swap(pack2(a[0], a[1]), pack2(b[0], b[1]), false, false);
        const auto r1 = __builtin_amdgcn_permlane16_swap(pack2(a[2], a[3]), pack2(b[2], b[3]), false, false);
        __builtin_amdgcn_raw_buffer_store_b128(u32x4{r0[0], r1[0], r0[1], r1[1]}, ro, vx + mf * 2048 + j * 32, 0, 2);
      }
    }
    TS(3);
  }
  TSFLUSH();
  }
  asm volatile(".Lcode_end:");
}

extern "C" void kernel_launch(void* const* d_in, const int* in_sizes, int n_in, void* d_out, int out_size, void* d_ws,
                              size_t ws_size, hipStream_t stream) {
  const u16* A_x = (const u16*)d_in[0];
  const u16* A_wqkv = (const u16*)d_in[1];
  const u16* A_wo = (const u16*)d_in[2];
  const u16* B_wg = (const u16*)d_in[4];
  const u16* B_wu = (const u16*)d_in[5];
  const u16* B_wd = (const u16*)d_in[6];
  u16* out = (u16*)d_out;
  fused_kernel<<<256, 512, 0, stream>>>(A_x, A_wqkv, B_wg, B_wu, B_wd, A_wo, out, (char*)d_ws);
}
```

```cpp
#include <hip/hip_runtime.h>
#include <hip/hip_bf16.h>
#include <stdint.h>
#include <type_traits>

typedef unsigned short u16;
using bf16x8 = __attribute__((ext_vector_type(8))) short;
using bf16x4 = __attribute__((ext_vector_type(4))) short;
using f32x4 = __attribute__((ext_vector_type(4))) float;
using u32x2 = __attribute__((ext_vector_type(2))) unsigned;
using u32x4 = __attribute__((ext_vector_type(4))) unsigned;
typedef __attribute__((address_space(3))) const char* lds_cptr;

constexpr int S_ = 2048, D_ = 1024, F_ = 3584;

__device__ __forceinline__ float bf2f(u16 h) { return __uint_as_float((unsigned)h << 16); }
__device__ __forceinline__ u16 f2bf(float f) {
  unsigned u = __float_as_uint(f);
  u += 0x7fffu + ((u >> 16) & 1u);
  return (u16)(u >> 16);
}
__device__ __forceinline__ float rbf(float f) { return bf2f(f2bf(f)); }
typedef __attribute__((ext_vector_type(2))) __bf16 bf16x2_t;
typedef __attribute__((ext_vector_type(2))) float f32x2;
__device__ __forceinline__ unsigned pack2(float a, float b) {
  bf16x2_t r = __builtin_convertvector((f32x2){a, b}, bf16x2_t);
  return __builtin_bit_cast(unsigned, r);
}
__device__ __forceinline__ float lo_f(unsigned p) { return __uint_as_float(p << 16); }
__device__ __forceinline__ float hi_f(unsigned p) { return __uint_as_float(p & 0xffff0000u); }

#define TSDECL
#define TSS(i) do {} while (0)
#define TSFLUSH() do {} while (0)
#define TS1(i) TSS(i)
#define TS(i) TSS(8 + (i))
#define PIN_S(v) asm volatile("" : "+s"(v))
#define WAIT_V(n) asm volatile("s_waitcnt vmcnt(%0)" ::"n"(n) : "memory")
#define WAIT_L(n) asm volatile("s_waitcnt lgkmcnt(%0)" ::"n"(n) : "memory")
#define BARL() do { WAIT_L(0); BAR(); } while (0)
#define BAR() do { asm volatile("" ::: "memory"); __builtin_amdgcn_s_barrier(); asm volatile("" ::: "memory"); } while (0)

__device__ __forceinline__ bf16x4 tr_read(lds_cptr p) {
  return __builtin_amdgcn_ds_read_tr16_b64_v4i16((__attribute__((address_space(3))) bf16x4*)p);
}
__device__ __forceinline__ bf16x8 tr_frag(lds_cptr p) {
  bf16x4 lo = tr_read(p), hi = tr_read(p + 512);
  bf16x8 r;
  r[0] = lo[0]; r[1] = lo[1]; r[2] = lo[2]; r[3] = lo[3];
  r[4] = hi[0]; r[5] = hi[1]; r[6] = hi[2]; r[7] = hi[3];
  return r;
}
__device__ __forceinline__ void glds16(const void* gsrc, unsigned lds_dst) {
  unsigned keep;
  asm volatile("s_mov_b32 %0, m0\n\ts_mov_b32 m0, %2\n\ts_nop 0\n\tglobal_load_lds_dwordx4 %1, off\n\ts_mov_b32 m0, %0"
               : "=&s"(keep) : "v"(gsrc), "s"(lds_dst) : "memory");
}
#define GLDS(gptr, laddr) glds16((const void*)(gptr), (unsigned)(laddr))
__device__ __forceinline__ void glds16_sc1(const void* gsrc, unsigned lds_dst) {
  unsigned keep;
  asm volatile("s_mov_b32 %0, m0\n\ts_mov_b32 m0, %2\n\ts_nop 0\n\tglobal_load_lds_dwordx4 %1, off sc1\n\ts_mov_b32 m0, %0"
               : "=&s"(keep) : "v"(gsrc), "s"(lds_dst) : "memory");
}
#define GLDS_SC1(gptr, laddr) glds16_sc1((const void*)(gptr), (unsigned)(laddr))
__device__ __forceinline__ void glds16_nc(const void* gsrc, unsigned lds_dst) {
  unsigned keep;
  asm volatile("s_mov_b32 %0, m0\n\ts_mov_b32 m0, %2\n\ts_nop 0\n\tglobal_load_lds_dwordx4 %1, off\n\ts_mov_b32 m0, %0"
               : "=&s"(keep) : "v"(gsrc), "s"(lds_dst));
}
#define GLDS_NC(gptr, laddr) glds16_nc((const void*)(gptr), (unsigned)(laddr))
__device__ __forceinline__ void glds16_s(const void* sbase, unsigned voff, unsigned lds_dst) {
  asm volatile("s_mov_b32 m0, %2\n\ts_nop 0\n\tglobal_load_lds_dwordx4 %0, %1" :: "v"(voff), "s"(sbase), "s"(lds_dst) : "m0");
}

__device__ __forceinline__ void glds16_s1(const void* sbase, unsigned voff, unsigned lds_dst) {
  asm volatile("s_mov_b32 m0, %2\n\ts_nop 0\n\tglobal_load_lds_dwordx4 %0, %1 sc1" :: "v"(voff), "s"(sbase), "s"(lds_dst) : "memory", "m0");
}
__device__ __forceinline__ void glds16_v(const void* p, unsigned lds_dst) {
  asm volatile("s_mov_b32 m0, %1\n\ts_nop 0\n\tglobal_load_lds_dwordx4 %0, off" :: "v"(p), "s"(lds_dst) : "m0");
}
template <int OFF = 0>
__device__ __forceinline__ void gst16(const void* sbase, unsigned voff, u32x4 v) {
  asm volatile("global_store_dwordx4 %0, %1, %2 offset:%3 sc1\n\ts_nop 1" :: "v"(voff), "v"(v), "s"(sbase), "n"(OFF) : "memory");
}

typedef unsigned long long u64;
__device__ __forceinline__ bool fresh(u64 stamp, u64 t_own) {
  const u64 b = t_own + 100ull;
  const unsigned lo = (unsigned)stamp - (unsigned)b, hi = (unsigned)(stamp >> 32) - (unsigned)(b >> 32) - ((unsigned)stamp < (unsigned)b ? 1u : 0u);
  return (hi == 0u) & (lo < (unsigned)(__builtin_amdgcn_s_memrealtime() - t_own));
}
constexpr u64 TIMEOUT_ = 200000;
__device__ __forceinline__ float half_sum(float v) {
  v += __builtin_bit_cast(float, __builtin_amdgcn_update_dpp(0, __builtin_bit_cast(int, v), 0xB1, 0xf, 0xf, false));
  v += __builtin_bit_cast(float, __builtin_amdgcn_update_dpp(0, __builtin_bit_cast(int, v), 0x4E, 0xf, 0xf, false));
  v += __builtin_bit_cast(float, __builtin_amdgcn_update_dpp(0, __builtin_bit_cast(int, v), 0x141, 0xf, 0xf, false));
  v += __builtin_bit_cast(float, __builtin_amdgcn_update_dpp(0, __builtin_bit_cast(int, v), 0x140, 0xf, 0xf, false));
  { auto r = __builtin_amdgcn_permlane16_swap(__float_as_uint(v), __float_as_uint(v), false, false); v = __uint_as_float(r[0]) + __uint_as_float(r[1]); }
  return v;
}
__device__ __forceinline__ unsigned pack_stats(float mean, float var) {
  return (unsigned)f2bf(mean) | ((unsigned)f2bf(sqrtf(rbf(rbf(var) + 1e-5f))) << 16);
}
__device__ __forceinline__ unsigned row_stats_serial(const u16* xr) {
  float s = 0.f, q = 0.f;
#pragma unroll 1
  for (int k = 0; k < D_; ++k) s += bf2f(xr[k]);
  const float mean = s * (1.0f / D_);
#pragma unroll 1
  for (int k = 0; k < D_; ++k) { const float d = bf2f(xr[k]) - mean; q += d * d; }
  return pack_stats(mean, q * (1.0f / D_));
}
constexpr int G1_STAGE = 32768 + 5 * 8192;
#define EPOS 2
#define PA 4
#define PB 8
#define PC 12
#define GETP 4
#define DLT 2
#define PSLEEP 2
#define GETJ 3

constexpr unsigned BLANK = 0x7f817f81u;
__device__ __forceinline__ bool blank(const u32x4* pv) {
  u64 m;
#define CX(i) "v_cmpx_ne_u32 vcc, %1, %" #i "\n\t"
  asm volatile(CX(2) CX(3) CX(4) CX(5) CX(6) CX(7) CX(8) CX(9) CX(10) CX(11) CX(12) CX(13) CX(14) CX(15) CX(16) CX(17)
               "s_mov_b64 %0, exec\n\ts_mov_b64 exec, -1\n\ts_nop 3" : "=s"(m) : "s"(BLANK), "v"(pv[0][0]), "v"(pv[0][1]), "v"(pv[0][2]), "v"(pv[0][3]),
               "v"(pv[1][0]), "v"(pv[1][1]), "v"(pv[1][2]), "v"(pv[1][3]), "v"(pv[2][0]), "v"(pv[2][1]), "v"(pv[2][2]), "v"(pv[2][3]),
               "v"(pv[3][0]), "v"(pv[3][1]), "v"(pv[3][2]), "v"(pv[3][3]) : "vcc");
  return m != ~0ull;
}
#define RLX_AGENT __ATOMIC_RELAXED, __HIP_MEMORY_SCOPE_AGENT

constexpr int G2_A = 65536, G2_X = 131072;
constexpr int G2_NSTAGE = 4;
constexpr size_t MB_ = 1024 * 1024;
__global__ __launch_bounds__(512, 2) __attribute__((aligned(4096))) void fused_kernel(const u16* __restrict__ x, const u16* __restrict__ wqkv,
                                                       const u16* __restrict__ wg, const u16* __restrict__ wu,
                                                       const u16* __restrict__ wd, const u16* __restrict__ wo,
                                                       u16* __restrict__ out, char* __restrict__ ws) {
  u16* const halo = (u16*)(ws + 4 * MB_);
  u16* const abuf = (u16*)(ws + 16 * MB_);
  char* const slab = ws + 64 * MB_;
  unsigned* const flags = (unsigned*)(ws + 96 * MB_);
  u32x4* const rstat = (u32x4*)(ws + 97 * MB_);
  char* const wcopy = ws + 98 * MB_;
  u64* const done1 = (u64*)(ws + 102 * MB_);
  u64* const done2 = (u64*)(ws + 100 * MB_ + 65536);
  u64* const donec = (u64*)(ws + 100 * MB_ + 131072);
  u64* const acks = (u64*)(ws + 100 * MB_ + 196608);
  float* const csum = (float*)(ws + 101 * MB_);
  __shared__ __attribute__((aligned(1024))) char shm[163840];
  constexpr int AUX = 2 * G1_STAGE + 8192;
  {
    u64 pc; int len;
    asm volatile("s_getpc_b64 %0\n.Lcode_beg:\n\ts_mov_b32 %1, .Lcode_end-.Lcode_beg" : "=s"(pc), "=s"(len));
    const int ol = ((blockIdx.x >> 3) + 32 * (threadIdx.x >> 6)) * 1024 + (threadIdx.x & 63) * 16;
    asm volatile("s_mov_b32 m0, %2\n\ts_nop 0\n\tglobal_load_lds_dwordx4 %0, %1"
                 :: "v"(ol < len - 16 ? ol : 0), "s"(pc & ~15ull), "s"((unsigned)(uintptr_t)(lds_cptr)shm + 2 * G1_STAGE + 7168) : "m0");
    __builtin_amdgcn_sched_barrier(0);
  }
  const u64 t_own = __builtin_amdgcn_s_memrealtime();
  TSDECL;
  TS1(7);
  const int tid = threadIdx.x, lane = tid & 63;
  const int wid = __builtin_amdgcn_readfirstlane(tid >> 6);
  const int wm = wid & 3, wc = wid >> 2;
  int bid_ = blockIdx.x;
  PIN_S(bid_);
  const int bid = bid_ & 255, xcd = bid & 7, idx = bid >> 3;
  const int ntile = xcd * 4 + (idx & 3), mtile = idx >> 2;
  const int m0 = mtile * 256;
  const int hg = ntile >> 1;
  const bool tya = (ntile & 1) == 0;
  const int srow = m0 + ntile * 8 + wm * 2;
  lds_cptr shm3 = (lds_cptr)shm;
  const unsigned shm_base = (unsigned)(uintptr_t)shm3;
  const int r8 = tid >> 3, pos = tid & 7;
  const int phys = pos >> 1, half = pos & 1;
  const int fk = ((r8 >> 1) & 1) | (((r8 >> 3) & 1) << 1);
  const int vsrc = phys ^ fk;
  const int qstr = tya ? 3072 * 2 : F_ * 2;
  unsigned voa = (((r8 & 32) + 2 * (r8 & 15) + ((r8 >> 4) & 1)) * D_ + ((pos ^ (r8 & 7)) * 8)) * 2;
  unsigned vog = r8 * (F_ * 2) + vsrc * 32 + half * 16, voq = r8 * qstr + vsrc * 32 + half * 16;
  unsigned vom = r8 * qstr + (vsrc >> (tya ? 0 : 1)) * 32 + half * 16;
  asm volatile("" : "+v"(voa), "+v"(vog), "+v"(voq), "+v"(vom));
  const int kt0 = (hg >> 1) * 7, he = hg & 1;
  unsigned oq = (tya ? hg : kt0 + 2 * he) * 128, og = (kt0 + (tya ? 5 + he : 2 * he + 1)) * 128, om = tya ? 4096 : 512 - 192 * he;
  unsigned d0 = shm_base + wid * 1024 + 32768, ox = m0 * D_ * 2;
  long uq = -32 * qstr;
  PIN_S(oq); PIN_S(og); PIN_S(om); PIN_S(d0); PIN_S(ox); PIN_S(uq);
  __builtin_amdgcn_sched_barrier(0);
  asm volatile("" :: "s"(x), "s"(wqkv), "s"(wg), "s"(wu), "s"(wd), "s"(wo), "s"(out), "s"(ws));
  const char* const ab = (const char*)x + ox;
  int kz = 0;
  asm volatile("" : "+s"(kz));
  u64 pq = (u64)wqkv, pg = (u64)wg;
  PIN_S(pq); PIN_S(pg);
  const char* const qb = (const char*)(tya ? pq : pg) + oq;
  if (wid < 4) {
#pragma unroll
    for (int i = 0; i < 8; ++i) glds16_s(ab + i * 32 * D_ * 2, voa, d0 - 32768 + i * 4096);
  } else {
    glds16_s(qb, voq, d0); glds16_s(qb + uq, voq, d0 - 4096);
  }
  __builtin_amdgcn_sched_barrier(0);
  const char* const gb = (const char*)wg + og;
  const long dgu = (const char*)wu - (const char*)wg, dp1 = tya ? 2048 : dgu;
  const char* const mb0 = qb + om;
  const char* mptr = mb0 + (((vsrc & 1) && !tya) ? dgu : 0) + vom;
  if (wid >= 4) {
    const long ug = kz - 32 * F_ * 2;
    glds16_s(qb + dp1, voq, d0 + 8192); glds16_s(qb + dp1 + uq, voq, d0 + 4096);
    glds16_v(mptr, d0 + 2 * 8192); glds16_v(mptr + uq, d0 + 2 * 8192 - 4096);
    glds16_s(gb, vog, d0 + 3 * 8192); glds16_s(gb + ug, vog, d0 + 3 * 8192 - 4096);
    glds16_s(gb + dgu, vog, d0 + 4 * 8192); glds16_s(gb + dgu + ug, vog, d0 + 4 * 8192 - 4096);
  }
  __builtin_amdgcn_sched_barrier(0);
  f32x4 acc[4][10];

  constexpr int NT = D_ / 64;
  constexpr int CNT = AUX + 4096 + 3584;
  if (tid < 8) *(unsigned*)(shm + CNT + tid * 4) = 0u;
  bf16x8 af0[4], af1[4];
  bf16x4 bl[4], bh[4];
#define SB0() __builtin_amdgcn_sched_barrier(0)
  auto mm = [&](int g, int t, bool two, bool z = false) {
    const int mi = t >> 1, mf = ((g & 1) && g != 9) ? 3 - mi : mi, ks = (t & 1) ^ (mi & 1), s = ((g + 1) & 1) * 2;
    const bf16x8 b0 = __builtin_shufflevector(bl[s + ks], bh[s + ks], 0, 1, 2, 3, 4, 5, 6, 7), a0 = ks ? af1[mf] : af0[mf];
    const bf16x8 b1 = __builtin_shufflevector(bl[s + 1 - ks], bh[s + 1 - ks], 0, 1, 2, 3, 4, 5, 6, 7), a1 = ks ? af0[mf] : af1[mf];
    const bool tr = (g >> 1) == 2;
    if (z && two) asm volatile("v_mfma_f32_16x16x32_bf16 %0, %1, %2, 0\n\tv_mfma_f32_16x16x32_bf16 %0, %3, %4, %0" : "=&v"(acc[mf][g])
                               : "v"(tr ? a0 : b0), "v"(tr ? b0 : a0), "v"(tr ? a1 : b1), "v"(tr ? b1 : a1));
    else if (z && !(t & 1)) asm volatile("v_mfma_f32_16x16x32_bf16 %0, %1, %2, 0" : "=&v"(acc[mf][g]) : "v"(tr ? a0 : b0), "v"(tr ? b0 : a0));
    else if (two) asm volatile("v_mfma_f32_16x16x32_bf16 %0, %1, %2, %0\n\tv_mfma_f32_16x16x32_bf16 %0, %3, %4, %0" : "+v"(acc[mf][g])
                          : "v"(tr ? a0 : b0), "v"(tr ? b0 : a0), "v"(tr ? a1 : b1), "v"(tr ? b1 : a1));
    else asm volatile("v_mfma_f32_16x16x32_bf16 %0, %1, %2, %0" : "+v"(acc[mf][g]) : "v"(tr ? a0 : b0), "v"(tr ? b0 : a0));
  };
  typedef __attribute__((address_space(3))) unsigned lds_u32;
  lds_u32* const cnt = (lds_u32*)(shm3 + CNT);
  bool cok;
  constexpr int NY = 28;
  int role, mt2, nt2;
  struct WS { unsigned vb[2][2]; const char* wb[2]; int sh[2]; };
  auto wsetup = [&](const u16* w, int crow0, int l, bool ck) __attribute__((always_inline)) {
    WS r;
    const unsigned vr = ((l >> 5) & 1) * 16 + (wm & 1) * 8 + ((l >> 3) & 3), vc = (((l >> 1) & 3) ^ ((l >> 4) & 1)) * 32 + (l & 1) * 16;
    u64 cp = (u64)(wcopy + (nt2 >> 2) * 589824 + crow0 * 128);
    asm volatile("" : "+s"(cp));
#pragma unroll
    for (int i = 0; i < 2; ++i) {
      u64 dp = (u64)(w + nt2 * 128 + i * 64);
      asm volatile("" : "+s"(dp));
      const bool slow = ck & ((((unsigned)dp >> 7) & 7) == 3);
      r.sh[i] = slow ? 7 : 11;
      r.wb[i] = (const char*)(slow ? cp : dp) + (crow0 ? (wm >> 1) * 32 << r.sh[i] : 0);
      r.vb[i][0] = (vr << r.sh[i]) + vc; r.vb[i][1] = ((vr + 4) << r.sh[i]) + (vc ^ 64);
    }
    asm volatile("" : "+v"(r.vb[0][0]), "+v"(r.vb[0][1]), "+v"(r.vb[1][0]), "+v"(r.vb[1][1]));
    PIN_S(r.wb[0]); PIN_S(r.wb[1]); PIN_S(r.sh[0]); PIN_S(r.sh[1]);
    return r;
  };
  WS wy, wz;
  int vxq, ws0, ws1;
  const char* sz;
  u32x4 xq[4];
  unsigned vg;
  int st = 3;
  const char *sa, *sw[2];
  auto wsel = [&]() {
    const unsigned r = __builtin_amdgcn_readlane(vg, st);
    sw[0] = wy.wb[0] + (r << wy.sh[0]); sw[1] = wy.wb[1] + (r << wy.sh[1]);
  };
  const int p = wid & 3;
  unsigned sl = 0;
#define G2_W "s_add_u32 m0, %[l], %[ow]\n\ts_nop 0\n\tglobal_load_lds_dwordx4 %[v0], %[w0] sc1\n\t" \
             "s_add_u32 m0, m0, 1024\n\ts_nop 0\n\tglobal_load_lds_dwordx4 %[v1], %[w0] sc1\n\t" \
             "s_add_u32 m0, m0, 7168\n\ts_nop 0\n\tglobal_load_lds_dwordx4 %[v2], %[w1] sc1\n\t" \
             "s_add_u32 m0, m0, 1024\n\ts_nop 0\n\tglobal_load_lds_dwordx4 %[v3], %[w1] sc1"
#define G2_WOP(w) [ow] "s"(shm_base + p * 2048), [w0] "s"(sw[0]), [w1] "s"(sw[1]), [v0] "v"(w.vb[0][0]), [v1] "v"(w.vb[0][1]), [v2] "v"(w.vb[1][0]), [v3] "v"(w.vb[1][1])
  auto tile2 = [&](int t0) __attribute__((always_inline)) {
    int b = bid;
    asm volatile("" : "+s"(b));
    role = 1 - ((b >> 2) & 1);
    mt2 = ((role ? b : b ^ 5) & 7) * 4 + ((b >> 3) & 3), nt2 = b >> 5;
    sa = (const char*)abuf + (size_t)(mt2 * 72 + role * NY + t0) * 16384 + wm * 4096;
    asm volatile("" : "+s"(sa));
  };
  auto prep = [&](bool first) __attribute__((always_inline)) {
    int l = threadIdx.x;
    asm volatile("" : "+v"(l));
    l &= 63;
    if (first) {
      tile2(3);
      vxq = ((mt2 * 128 + (wm >> 1) * 64 + role * 32 + 2 * (l & 15)) * D_ + nt2 * 128 + (wm & 1) * 64) * 2 + ((l >> 4) & 1) * 32 + (l >> 5) * 16;
      const int t = min(l < NY ? l : l - DLT, NY - 1), w = wm >> 1;
      const int o = (0xffaa5500fae504ull >> (2 * t)) & 3;
      int u1 = (0x990990 >> ((2 * t) & 31)) & 3, u2 = 3 + (t & 3);
      asm volatile("" : "+v"(u1), "+v"(u2));
      const int u = t < 12 ? u1 : u2;
      vg = (__umul24((l < NY ? role : 1 - role) * 4 + (((o & 1) << 1) | (o >> 1)), 14) + ((0xb85140a >> (4 * u)) & 15) + (u == 5 ? w : 2 * w)) * 32;
      asm volatile("" : "+v"(vg), "+v"(vxq));
      sz = (const char*)abuf + (size_t)(mt2 * 72 + 56) * 16384 + (wm >> 1) * 8192 + role * 4096 + (wm & 1) * 2048;
      PIN_S(sz);
    }
    wy = wsetup(wd, 0, l, first);
    wz = wsetup(wo, F_, l, first);
    ws0 = 64 << wz.sh[0]; PIN_S(ws0);
    ws1 = 64 << wz.sh[1]; PIN_S(ws1);
    SB0();
  };
  auto start2 = [&]() __attribute__((always_inline)) {
    const __amdgpu_buffer_rsrc_t rx = __builtin_amdgcn_make_buffer_rsrc((void*)x, 0, S_ * D_ * 2, 0x00020000);
#pragma unroll
    for (int f = 0; f < 4; ++f) xq[f] = __builtin_bit_cast(u32x4, __builtin_amdgcn_raw_buffer_load_b128(rx, vxq + (f >> 1) * 2048 + (f & 1) * 64, 0, 0));
    cok = __builtin_amdgcn_readfirstlane(cnt[1] & cnt[2]) != 0u;
    if (__builtin_expect(!cok, 0)) prep(false);
    wsel();
    PIN_S(sw[0]); PIN_S(sw[1]);
    SB0();
  };
  auto kloop = [&](auto wmc) {
    constexpr bool LD = decltype(wmc)::value != 0;
    constexpr int DMP = 2 * G1_STAGE;
    const char* pa = nullptr;
    unsigned vob = 0;
    f32x4 ca = {0.f, 0.f, 0.f, 0.f}, cb = {0.f, 0.f, 0.f, 0.f}, cd = ca;
    bf16x8 sel = {0, 0, 0, 0, 0, 0, 0, 0};
    int tro = 0;
    const int psh = (wid == 2 && !tya) ? 1 : 0, pstr = wid < 3 ? qstr : F_ * 2;
    const char* const pbase = wid == 0 ? qb : (wid == 1 ? qb + dp1 : (wid == 2 ? mb0 : gb));
    int lane = threadIdx.x;
    asm volatile("; %1" : "+v"(lane) : "n"(LD ? 1 : 0));
    lane &= 63;
    const int fr = lane & 15, fq = lane >> 4, q4 = fr >> 2, pp = lane & 3;
    const int ff = ((q4 >> 1) & 1) | ((fq & 1) << 1);
    int aoff[2], boff[2];
#pragma unroll
    for (int ks = 0; ks < 2; ++ks) aoff[ks] = (wm * 64 + fr) * 128 + (((ks * 4 + fq) ^ (fr & 7)) * 16);
#pragma unroll
    for (int j = 0; j < 2; ++j) boff[j] = 32768 + (8 * fq + q4) * 128 + (((2 * wc + j) ^ ff) * 32) + pp * 8;
    unsigned vol = lane * 16;
    float sm = 0.f, sq = 0.f;
    const char* pf = (const char*)wd + (bid * 4 + wid) * 7168;
    long pstep = 64 * pstr;
    if (!LD) {
      const int rs = 256 + mtile * 8 + (lane >> 3), vs = ((lane & 7) >> 1) ^ (((rs >> 1) & 1) | ((mtile & 1) << 1));
      pa = pbase + ((psh & vs) ? dgu : 0) + (rs * pstr + (vs >> psh) * 32 + (lane & 1) * 16);
      vob = rs * (F_ * 2) + vs * 32 + (lane & 1) * 16;
      tro = DMP + wid * 1024 + q4 * 128 + ((fq ^ ((q4 >> 1) | ((mtile & 1) << 1))) * 32) + pp * 8;
      const short one = fr == fq ? 0x3f80 : 0;
      sel = bf16x8{one, one, one, one, one, one, one, one};
      asm volatile("" : "+v"(tro), "+v"(vol), "+v"(sel), "+v"(ca), "+v"(cb), "+v"(sm), "+v"(sq));
      PIN_S(pf); PIN_S(pstep);
    }
    auto accum = [&](lds_cptr p, int ob) {
      ca = __builtin_amdgcn_mfma_f32_16x16x32_bf16(tr_frag(p), sel, ca, 0, 0, 0);
      if (wid == 0) cb = __builtin_amdgcn_mfma_f32_16x16x32_bf16(tr_frag(p + ob), sel, cb, 0, 0, 0);
    };
    auto xrow = [&](int i) {
      u64 a = (u64)(x + (size_t)srow * D_ + i * 256), b = (u64)(wo + (bid * 4 + wid) * 1024);
      PIN_S(a); PIN_S(b);
      asm volatile("global_load_dwordx4 %0, %1, %2" : "+v"(cd) : "v"(vol + (vol & (i < 4 ? 512 : 0)) * 3), "s"(i < 4 ? a : b) : "memory");
    };
    const int q32 = 32 * qstr;
    const char *sq0 = qb + q32, *sq1 = sq0 + dp1, *sg0 = gb + 32 * F_ * 2, *sg1 = sg0 + dgu, *sx = ab + (long)(kz + 128 - 32 * D_ * 2);
    u32x2 mp[2] = {__builtin_bit_cast(u32x2, mptr + q32), __builtin_bit_cast(u32x2, mptr + 2 * q32)};
    u64 cy[2];
    unsigned dst = shm_base + G1_STAGE + wm * 1024;
    int dbuf = G1_STAGE;
    auto stage = [&](int i0, int i1) __attribute__((always_inline)) {
#pragma unroll
        for (int i = i0; i < i1; ++i) {
          const int q = i >> 1, h = i & 1;
          const unsigned ld = dst + h * 4096 + (q < 5 ? 32768 + q * 8192 : (q - 5) * 8192);
          if (q < 2) glds16_s((q ? sq1 : sq0) + h * q32, voq, ld);
          else if (q == 2) glds16_v(__builtin_bit_cast(const char*, mp[h]), ld);
          else if (q < 5) glds16_s((q == 4 ? sg1 : sg0) + h * 32 * F_ * 2, vog, ld);
          else glds16_s(sx + ((q - 5) * 64 + h * 32) * D_ * 2, voa, ld);
        }
    };
    u64 cy0 = 0, rt0 = 0;
#pragma unroll
    for (int a = 0; a < 4; ++a) { acc[a][9] = f32x4{0.f, 0.f, 0.f, 0.f}; asm volatile("" : "+v"(acc[a][9])); }
    if (!LD) {
      xrow(0);
      WAIT_V(1);
    }
    asm volatile("" : "+v"(aoff[0]), "+v"(aoff[1]), "+v"(boff[0]), "+v"(boff[1]));
    bf16x8 kp[2];
    auto body = [&](bool upd, bool keep = false, bool first = false) __attribute__((always_inline)) {
      lds_cptr sb = shm3;
      auto rdb = [&](int g, int i) {
        lds_cptr p = sb + boff[g & 1] + (g >> 1) * 8192 + (i >> 1) * 4096;
        const int s = ((g + 1) & 1) * 2 + (i >> 1);
        if (i & 1) bh[s] = tr_read(p + 512); else bl[s] = tr_read(p);
      };
      auto rda = [&](int ks, int mf) { (ks ? af1 : af0)[mf] = *(const __attribute__((address_space(3))) bf16x8*)(sb + aoff[ks] + mf * 2048); };
      SB0();
      __builtin_amdgcn_s_waitcnt(0xC07F);
#pragma unroll
      for (int gg = 0; gg < 10; ++gg) {
        const int g = (gg + 9) % 10;
#pragma unroll
        for (int t = 0; t < 8; ++t) {
          const bool two = t >= 4 && !(upd && g >= 5 && g <= 8);
          SB0();
          if ((!two || (t & 1)) && !(first && g == 9)) mm(g, two ? t - 1 : t, two, first);
          SB0();
          if (first && !LD && (g == 3 || g == 7) && t == 0) BAR();
          if (g == 9) {
            if (t == 0 || t == 2) { rdb(0, t); rdb(0, t + 1); }
            if (t & 1) { rda((t >> 1) & 1, t >> 1); rda(((t >> 1) & 1) ^ 1, t >> 1); }
          } else if (t < 4) rdb(g + 1, t);
          else if (keep && g == 7) {
            if (t == 5) { WAIT_V(0); kp[0] = tr_frag(shm3 + tro); }
            if (t == 7) kp[1] = tr_frag(shm3 + tro + 4096);
          } else if (upd) {
            if (g == 5 && t == 4) { sq0 += 2 * q32; PIN_S(sq0); }
            if (g == 5 && t == 5) { sq1 += 2 * q32; PIN_S(sq1); }
            if (g == 5 && t == 6) { sg0 += 64 * F_ * 2; PIN_S(sg0); }
            if (g == 6 && t == 4) { sg1 += 64 * F_ * 2; PIN_S(sg1); }
            if (g == 6 && t == 5) { sx += 128; PIN_S(sx); }
            if (g == 6 && t == 6) { dst -= dbuf; PIN_S(dst); }
            if (g == 7 && t == 4) asm volatile("v_add_co_u32 %0, %2, %4, %0\n\tv_add_co_u32 %1, %3, %4, %1" : "+v"(mp[0][0]), "+v"(mp[1][0]), "=&s"(cy[0]), "=&s"(cy[1]) : "s"(2 * q32));
            if (g == 7 && t == 6) asm volatile("v_addc_co_u32 %0, vcc, 0, %0, %2\n\tv_addc_co_u32 %1, vcc, 0, %1, %3" : "+v"(mp[0][1]), "+v"(mp[1][1]) : "s"(cy[0]), "s"(cy[1]) : "vcc");
            if (g == 8 && t == 4) { aoff[0] += dbuf; aoff[1] += dbuf; asm volatile("" : "+v"(aoff[0]), "+v"(aoff[1])); }
            if (g == 8 && t == 5) { boff[0] += dbuf; boff[1] += dbuf; asm volatile("" : "+v"(boff[0]), "+v"(boff[1])); }
            if (g == 8 && t == 6) { dbuf = -dbuf; PIN_S(dbuf); }
          }
        }
      }
      SB0();
    };
    TS1(0);
#define ARRP(o, c) do {} while (0)
    if (LD) {
      stage(0, PA);
      WAIT_V(6 + PA);
      TS1(5);
      BARL();
      stage(PA, PB);
      WAIT_V(2 + PB);
      BAR();
      stage(PB, PC);
      WAIT_V(PC);
      BAR();
      stage(PC, 18);
      body(true, false, true);
      WAIT_V(0);
      BARL();
#pragma clang loop unroll(disable)
      for (int kt = 1; kt < NT - 1; ++kt) {
        if (__builtin_expect(kt == 5 + wid, 0)) prep(true);
        stage(0, 18);
        body(true);
        WAIT_V(0);
        BARL();
        SB0();
      }
      start2();
      body(false);
    } else {
      char* wsx;
      auto tile = [&](int kt, bool keep) __attribute__((always_inline)) {
        SB0();
        ARRP(wid * 64 + kt * 4, true);
        BARL();
        body(false, keep);
        ARRP(320 + kt * 4 + 4, wid == 0);
        if (keep) asm volatile("" : "+v"(kp[0]), "+v"(kp[1])); else WAIT_V(0);
      };
      auto next = [&]() __attribute__((always_inline)) {
        aoff[0] += dbuf; aoff[1] += dbuf; boff[0] += dbuf; boff[1] += dbuf;
        dbuf = -dbuf;
        asm volatile("" : "+v"(aoff[0]), "+v"(aoff[1]), "+v"(boff[0]), "+v"(boff[1]), "+v"(vol), "+v"(tro) :: "memory");
        wsx = ws;
        PIN_S(wsx);
      };
      auto share = [&]() {
        glds16_v(pa, shm_base + DMP + wid * 1024);
        if (wid == 0) glds16_s(gb + dgu, vob, shm_base + DMP + 4096);
      };
      auto kept = [&]() {
        ca = __builtin_amdgcn_mfma_f32_16x16x32_bf16(kp[0], sel, ca, 0, 0, 0);
        if (wid == 0) cb = __builtin_amdgcn_mfma_f32_16x16x32_bf16(kp[1], sel, cb, 0, 0, 0);
      };
      auto cpub = [&](u32x4 a, u32x4 b) {
        if ((vol & 0xc0) == 0) {
          const char* const cs = wsx + 101 * MB_ + ntile * 10240 + mtile * 256;
          const unsigned co = (vol & 0x30) * 4 + (vol >> 8) * 16;
          gst16(cs + wid * 2048, co, a);
          if (wid == 0) gst16(cs + 8192, co, b);
        }
      };
      auto side0 = [&](int kt) __attribute__((always_inline)) {
        if (kt < 2) {
          glds16_v(pa - 2 * pstep, shm_base + DMP + 7168);
          if (wid == 0) glds16_s(gb + dgu, vob - 128 * F_ * 2, shm_base + DMP + 7168);
        }
        share();
        u32x4 xw;
        asm volatile("v_mov_b32 %0, %4\n\tv_mov_b32 %1, %5\n\tv_mov_b32 %2, %6\n\tv_mov_b32 %3, %7" : "=&v"(xw[0]), "=&v"(xw[1]), "=&v"(xw[2]), "=&v"(xw[3])
                     : "v"(cd[0]), "v"(cd[1]), "v"(cd[2]), "v"(cd[3]));
        xrow(kt + 1);
        next();
        if (kt) kept();
        pa += pstep; vob += 64 * F_ * 2;
        accum(shm3 + tro + ((kt & 1) * G1_STAGE + 32768 + mtile * 1024 + wid * 7168 - DMP), 4 * 8192);
#pragma unroll
        for (int i = 0; i < 4; ++i) {
          float lo, hi;
          asm volatile("v_lshlrev_b32 %2, 16, %4\n\tv_and_b32 %3, 0xffff0000, %4\n\tv_add_f32 %0, %0, %2\n\tv_fmac_f32 %1, %2, %2\n\t"
                       "v_add_f32 %0, %0, %3\n\tv_fmac_f32 %1, %3, %3" : "+v"(sm), "+v"(sq), "=&v"(lo), "=&v"(hi) : "v"(xw[i]));
        }
      };
      BARL();
      body(false, false, true);
      WAIT_V(0);
      side0(0);
#pragma clang loop unroll(disable)
      for (int kt = 1; kt < 4; ++kt) {
        tile(kt, true);
        side0(kt);
      }
#pragma clang loop unroll(disable)
      for (int kt = 4; kt < 12; ++kt) {
        tile(kt, true);
        share();
        glds16_s(pf, vol, shm_base + DMP + 7168);
        next();
        kept();
        pa += pstep; vob += 64 * F_ * 2;
        pf = kt == 10 ? (const char*)wo + (bid * 4 + wid) * 2048 + 1024 : pf + 1024;
        if (kt < 8) {
          const unsigned q1 = bid * 4 + wid + (kt - 5) * 1024, q0 = q1 - 1024;
          if (q0 < 1152) gst16(wsx + 98 * MB_ + (q0 & 1) * 589824 + (q0 >> 1) * 1024, vol, *(const u32x4*)(shm + AUX + wid * 1024 + vol));
          if (q1 < 1152) {
            const char* src = (const char*)((q1 >> 1) < 448 ? wd + (size_t)(q1 >> 1) * 8 * D_ : wo + (size_t)((q1 >> 1) - 448) * 8 * D_);
            src += ((3 - ((unsigned)(uintptr_t)src >> 7)) & 7) * 128 + (q1 & 1) * 1024;
            glds16_s(src, (vol >> 7) * 2048 + (vol & 0x70), shm_base + AUX + wid * 1024);
          }
          u32x4 bk;
          asm volatile("v_mov_b32 %0, %1" : "=v"(bk[0]) : "s"(BLANK));
          bk[1] = bk[0]; bk[2] = bk[0]; bk[3] = bk[0];
          gst16(wsx + 64 * MB_ + bid * 16384 + wid * 4096 + (kt - 4) * 1024, vol, bk);
          if (tya && (kt == 4 || kt == 7)) gst16(wsx + 4 * MB_ + (mtile * 32 + ntile) * 4096 + wid * 2048 + (kt & 1) * 1024, vol, bk);
          if (kt == 4) cpub(bk, bk);
        } else {
          if (kt == 8) {
            const float ml = sm * (1.0f / 32), mean = half_sum(sm) * (1.0f / D_), dm = ml - mean;
            const float q = __builtin_fmaf(dm * 32.0f, dm, __builtin_fmaf(-ml, sm, sq));
            const unsigned ps = pack_stats(mean, fmaxf(half_sum(q) * (1.0f / D_), 0.f));
            const u64 now = __builtin_amdgcn_s_memrealtime();
            if ((vol & 0x1f0) == 0) gst16(wsx + 97 * MB_ + srow * 16, vol >> 5, u32x4{ps, (unsigned)now, ps, (unsigned)(now >> 32)});
          }
          if (kt == 9) {
            if (wid == 0) {
              u64 now;
              asm volatile("s_memrealtime %0\n\ts_waitcnt lgkmcnt(0)" : "=s"(now) :: "memory");
              if (vol == 0) asm volatile("global_store_dwordx2 %0, %1, %2 sc1" :: "v"(vol), "v"(now), "s"(wsx + 100 * MB_ + 131072 + bid * 8) : "memory");
            }
            if (wid == 2) glds16_s1(wsx + 102 * MB_ + (bid & ~1) * 512, vol, shm_base + DMP + 7168);
          }
        }
      }
      asm volatile("" :: "v"(cd));
      if (wid >= 2) glds16_s1(wsx + 100 * MB_ + 131072 + (wid - 2) * 1024, vol, shm_base + DMP + 3072 + wid * 1024);
      tile(12, true);
      next();
      kept();
      const int lane = vol >> 4;
      glds16_s1(wsx + 97 * MB_ + (m0 + wid * 64) * 16, ((lane & 32) + 2 * (lane & 15) + ((lane >> 4) & 1)) * 16, shm_base + AUX + wid * 1024);
      asm volatile("s_nop 7\n\ts_nop 3" : "+v"(ca), "+v"(cb));
      cpub(__builtin_bit_cast(u32x4, ca), __builtin_bit_cast(u32x4, cb));
      if (wid >= 2) {
        u32x4 c4 = *(const u32x4*)(shm + DMP + 3072 + wid * 1024 + vol);
        bool ok = __all(fresh(((u64)c4[1] << 32) | c4[0], t_own) & fresh(((u64)c4[3] << 32) | c4[2], t_own));
        while (__builtin_expect(!ok, 0) && __builtin_amdgcn_s_memrealtime() - t_own < TIMEOUT_ / 10) {
          const __amdgpu_buffer_rsrc_t rs = __builtin_amdgcn_make_buffer_rsrc((void*)(wsx + 100 * MB_ + 131072), 0, 2048, 0x00020000);
          asm volatile("" ::: "memory");
          c4 = __builtin_bit_cast(u32x4, __builtin_amdgcn_raw_buffer_load_b128(rs, (wid - 2) * 1024 + vol, 0, 16));
          ok = __all(fresh(((u64)c4[1] << 32) | c4[0], t_own) & fresh(((u64)c4[3] << 32) | c4[2], t_own));
        }
        *(unsigned*)(shm + CNT - 4 + wid * 4) = ok ? 1u : 0u;
      }
      tile(13, false);
      next();
      u32x4 cw[4];
      auto cgather = [&]() {
        const __amdgpu_buffer_rsrc_t rc = __builtin_amdgcn_make_buffer_rsrc((void*)(wsx + 101 * MB_ + ntile * 10240), 0, 5 * 2048, 0x00020000);
        const unsigned vcs = (vol < 512 ? wid * 2048 : 8192) + (vol & 256) * 4 + (vol & 0xf0);
#pragma unroll
        for (int i = 0; i < 4; ++i) cw[i] = __builtin_bit_cast(u32x4, __builtin_amdgcn_raw_buffer_load_b128(rc, vcs + i * 256, 0, 16));
      };
      {
        const int ln = wid * 64 + (vol >> 4);
        u32x4 gr = *(const u32x4*)(shm + AUX + ln * 16);
        if (__builtin_expect(!__all((gr[0] == gr[2]) & fresh(((u64)gr[3] << 32) | gr[1], t_own)), 0)) {
          const __amdgpu_buffer_rsrc_t rs = __builtin_amdgcn_make_buffer_rsrc((void*)(wsx + 97 * MB_ + m0 * 16), 0, 4096, 0x00020000);
          const int prow = (ln & 0xe0) + 2 * (ln & 15) + ((ln >> 4) & 1);
          for (;;) {
            gr = __builtin_bit_cast(u32x4, __builtin_amdgcn_raw_buffer_load_b128(rs, prow * 16, 0, 16));
            if (__all((gr[0] == gr[2]) & fresh(((u64)gr[3] << 32) | gr[1], t_own))) break;
            if (__builtin_amdgcn_s_memrealtime() - t_own > TIMEOUT_) { gr[0] = row_stats_serial(x + (size_t)(m0 + prow) * D_); break; }
            __builtin_amdgcn_s_sleep(2);
          }
        }
        cgather();
        const float nm = -lo_f(gr[0]), dn = hi_f(gr[0]), r = __builtin_amdgcn_rcpf(dn);
        auto mul = [](float a, float b) { float c; asm("v_mul_f32 %0, %1, %2" : "=v"(c) : "v"(a), "v"(b)); return c; };
        const f32x4 fc = {nm, -2.3022081986f * r, mul(mul(mul(-0.10294324f, r), r), r), mul(dn, dn)};
        *(f32x4*)(shm + AUX + ln * 16) = fc;
        float* const tw = (float*)(shm + 2 * G1_STAGE + 4096 + wid * 768) + ln;
#pragma unroll
        for (int i = 0; i < 4; ++i) tw[i * 64] = fc[i];
        ((float*)(shm + AUX + 4096))[384 + ln] = r;
        cok = __builtin_amdgcn_readfirstlane(cnt[1] & cnt[2]) != 0u;
        tile2(0);
        wy = wsetup(wd, 0, vol >> 4, cok);
      }
      tile(14, false);
      next();
      {
        while (__builtin_expect(blank(cw) && cok, 0)) {
          __builtin_amdgcn_s_sleep(2);
          asm volatile("" ::: "memory");
          cgather();
        }
        f32x4 t;
#pragma unroll
        for (int i = 0; i < 4; ++i) {
          float a, b;
          asm("v_add_f32 %0, %2, %3\n\tv_add_f32 %1, %4, %5\n\tv_add_f32 %0, %0, %1" : "=&v"(a), "=&v"(b) : "v"(cw[0][i]), "v"(cw[1][i]), "v"(cw[2][i]), "v"(cw[3][i]));
          const auto r2 = __builtin_amdgcn_permlane16_swap(__float_as_uint(a), __float_as_uint(a), false, false);
          asm("v_add_f32 %0, %1, %2" : "=v"(t[i]) : "v"(r2[0]), "v"(r2[1]));
        }
        const int l = vol >> 4;
        if (__builtin_expect(!cok, 0)) {
          const int gq = (l >> 2) & 3;
          const char* pc = l < 32 ? pbase + ((psh & gq) ? dgu : 0) + (gq >> psh) * 32 + (l & 3) * 8 : gb + dgu + (l & 15) * 8;
          t = f32x4{0.f, 0.f, 0.f, 0.f};
#pragma unroll 1
          for (int k = 0; k < D_; ++k) { const u32x2 w = *(const __attribute__((address_space(1))) u32x2*)(pc + (size_t)k * (l < 32 ? pstr : F_ * 2)); t += f32x4{lo_f(w[0]), hi_f(w[0]), lo_f(w[1]), hi_f(w[1])}; }
        }
        if (l < 32 || wid == 0)
          *(f32x4*)((float*)(shm + AUX + 4096) + ((l >> 3) & 1) * 192 + (l < 32 ? wid : 4) * 32 + (l & 7) * 4) = t;
        __builtin_amdgcn_s_setprio(2);
      }
      SB0();
      ARRP(wid * 64 + 60, true);
      BARL();
      body(false);
    }
#pragma unroll
    for (int t = 0; t < 8; t += 2) mm(9, t, true);
    asm volatile("s_nop 7\n\ts_nop 3");
    SB0();
    TS1(3);
    if (!LD) {
#pragma unroll
      for (int i = 0; i < 3; ++i) {
        const unsigned r = ((role * 4 + (i & 1) * 2) * 14 + (i < 2 ? 10 : 0) + (wm & 2)) * 32;
        sw[0] = wy.wb[0] + (r << wy.sh[0]); sw[1] = wy.wb[1] + (r << wy.sh[1]);
        asm volatile(G2_W :: [l] "s"((i + 1) * 16384), G2_WOP(wy) : "memory", "scc", "m0");
      }
    } else {
      __builtin_amdgcn_s_setprio(3);
      WAIT_V(0);
    }
  };
  if (wid >= 4) kloop(std::integral_constant<int, 1>{});
  else kloop(std::integral_constant<int, 0>{});
  __builtin_amdgcn_sched_barrier(0);
  int tz = threadIdx.x;
  asm volatile("" : "+v"(tz));
  __builtin_amdgcn_sched_barrier(0);
  const int li = tz & 15, lq = (tz >> 4) & 3, ln = tz & 63;
  const __amdgpu_buffer_rsrc_t ra = __builtin_amdgcn_make_buffer_rsrc((void*)abuf, 0, 16 * 72 * 16384, 0x00020000);
  float* const cfin = (float*)(shm + AUX + 4096);
  float* const svr = cfin + 384;
  float* const tb = (float*)(shm + 2 * G1_STAGE + 4096 + wm * 1024);
  auto signal = [&](unsigned v) {
    unsigned a, b;
    asm volatile("v_mov_b32 %0, %2\n\tv_mov_b32 %1, %3\n\ts_mov_b64 exec, 1\n\tds_add_u32 %0, %1\n\ts_mov_b64 exec, -1"
                 : "=&v"(a), "=&v"(b) : "s"(shm_base + CNT), "s"(v) : "memory");
  };
  auto await = [&](unsigned mask, unsigned val) {
    unsigned a, t;
    asm volatile("v_mov_b32 %0, %2\n1:\n\tds_read_b32 %1, %0\n\ts_waitcnt lgkmcnt(0)\n\tv_readfirstlane_b32 vcc_lo, %1\n\ts_and_b32 vcc_lo, vcc_lo, %3\n\t"
                 "s_cmp_eq_u32 vcc_lo, %4\n\ts_cbranch_scc1 2f\n\ts_sleep 1\n\ts_branch 1b\n2:" : "=&v"(a), "=&v"(t) : "s"(shm_base + CNT), "s"(mask), "s"(val) : "memory", "scc", "vcc");
  };
  auto client = [&](int l) { const int m128 = mtile * 2 + ((l >> 4) & 1); return (((l & 7) * 4 + (m128 & 3)) * 8) + ((m128 >> 2) ^ ((l & 8) ? 5 : 0)); };
  auto notify = [&](int l0, int nl) __attribute__((always_inline)) {
    u64 now;
    asm volatile("s_memrealtime %0\n\ts_waitcnt lgkmcnt(0)" : "=s"(now) :: "memory");
    int l = threadIdx.x;
    asm volatile("" : "+v"(l));
    l &= 63;
    const __amdgpu_buffer_rsrc_t rd1 = __builtin_amdgcn_make_buffer_rsrc((void*)(done1 + ntile), 0, 256 * 512, 0x00020000);
    if (l < nl) __builtin_amdgcn_raw_buffer_store_b64(u32x2{(unsigned)now, (unsigned)(now >> 32)}, rd1, client(l0 + l) * 512, 0, 16);
  };
  const int xo = ((xcd & 1) << 1) | ((xcd >> 1) & 1), te = (xcd >> 2) * 28 + 6 * (xo >> 1) + (xo & 1), tl = (xcd >> 2) * 28 + 12 + 4 * xo;
  const u64 te0 = __builtin_amdgcn_s_memrealtime();
  const __amdgpu_buffer_rsrc_t re = __builtin_amdgcn_make_buffer_rsrc((void*)(done1 + client((wm >> 1) * 16 + 8 - (xcd & 4) * 2) * 64 + 32 + (ntile & 15) * 2 + (wm & 1)),
                                                                      0, 7 * 16384 + 8, 0x00020000);
  auto early = [&]() {
    __builtin_amdgcn_raw_buffer_store_b64(u32x2{(unsigned)te0, (unsigned)(te0 >> 32)}, re, ln * 16384, 0, 16);
  };
  signal(1u << (8 * wc));
  if (wc) {
    await(0xffffu, 0x404u);
#pragma unroll
    for (int f = 0; f < 4; ++f) *(u32x4*)(shm + G2_X + wm * 4096 + f * 1024 + ln * 16) = xq[f];
    SB0();
    __builtin_amdgcn_s_setprio(1);
  }
  TS1(4);
  f32x4 rf[4];
#pragma unroll
  for (int mf = 0; mf < 4; ++mf) rf[mf] = *(const f32x4*)(shm + AUX + (wm * 64 + mf * 16 + li) * 16);
  const float* const cl = cfin + wc * 192 + lq * 4;
  const int sbase = ((mtile * 2 + (wm >> 1)) * 72 * 8 + (wm & 1) * 4) * 2048;
  auto putf = [&](int kg, int mf, u32x4 v) {
    __builtin_amdgcn_raw_buffer_store_b128(v, ra, ln * 16 + sbase + ((kg >> 1) * 8 + mf) * 2048 + (kg & 1) * 1024, 0, 16);
  };
  auto gelp = [&](f32x2 g, f32x2 u, f32x2 cg, f32x2 cu, f32x2 m2, f32x2 k12, f32x2 k22, f32x2 d2) {
    const f32x2 tg = __builtin_elementwise_fma(m2, cg, g), tu = __builtin_elementwise_fma(m2, cu, u);
    const f32x2 arg = tg * __builtin_elementwise_fma(tg * tg, k22, k12);
    const f32x2 dn = __builtin_elementwise_fma(f32x2{__builtin_amdgcn_exp2f(arg[0]), __builtin_amdgcn_exp2f(arg[1])}, d2, d2);
    const f32x2 r = (tg * tu) * f32x2{__builtin_amdgcn_rcpf(dn[0]), __builtin_amdgcn_rcpf(dn[1])};
    return pack2(r[0], r[1]);
  };
  auto gel2 = [&](int p, int kg, int mf0 = 0, int mf1 = 4) {
    f32x4 cg[2], cu[2];
#pragma unroll
    for (int j = 0; j < 2; ++j) { cg[j] = *(const f32x4*)(cl + p * 32 + j * 16); cu[j] = *(const f32x4*)(cl + p * 32 + 32 + j * 16); }
#pragma unroll
    for (int mf = mf0; mf < mf1; ++mf) {
      const f32x2 m2 = {rf[mf][0], rf[mf][0]}, k12 = {rf[mf][1], rf[mf][1]}, k22 = {rf[mf][2], rf[mf][2]}, d2 = {rf[mf][3], rf[mf][3]};
      u32x4 o;
#pragma unroll
      for (int i = 0; i < 4; ++i) {
        const int j = i >> 1, e = (i & 1) * 2;
        const f32x4 g = acc[mf][p * 2 + j], u = acc[mf][p * 2 + 2 + j];
        o[i] = gelp(f32x2{g[e], g[e + 1]}, f32x2{u[e], u[e + 1]}, f32x2{cg[j][e], cg[j][e + 1]}, f32x2{cu[j][e], cu[j][e + 1]}, m2, k12, k22, d2);
      }
      putf(kg, mf, o);
    }
  };
  int tsel = ntile & 1;
  asm volatile("" : "+s"(tsel));
  if (tsel != 0) {
    const int tq = (wc ? tl : te + 2 + (xo & 1)) + he;
    gel2(0, 2 * tq);
    gel2(3, 2 * tq + 1);
    const float cg = cfin[wc * 192 + 64 + li], cu = cfin[wc * 192 + 80 + li];
#pragma unroll
    for (int mf = 0; mf < 4; ++mf) {
      f32x4 t[4];
#pragma unroll
      for (int i = 0; i < 4; ++i) t[i] = *(const f32x4*)(tb + i * 64 + mf * 16 + lq * 4);
      const f32x4 g = acc[mf][4], u = acc[mf][5];
      u32x2 o;
#pragma unroll
      for (int e = 0; e < 4; e += 2)
        o[e >> 1] = gelp(f32x2{g[e], g[e + 1]}, f32x2{u[e], u[e + 1]}, f32x2{cg, cg}, f32x2{cu, cu}, f32x2{t[0][e], t[0][e + 1]},
                         f32x2{t[1][e], t[1][e + 1]}, f32x2{t[2][e], t[2][e + 1]}, f32x2{t[3][e], t[3][e + 1]});
      *(u32x2*)(shm + wm * 4096 + mf * 1024 + wc * 512 + li * 32 + lq * 8) = o;
      if (mf == EPOS && wc == 0) {
        TS1(1);
        WAIT_V(0);
        TS1(6);
        early();
      }
    }
    if (wc == 0) {
      signal(0x10000u << wid);
    } else {
      await(0x1000u << wid, 0x1000u << wid);
#pragma unroll
      for (int mf = 0; mf < 4; ++mf) putf(2 * (tl + 2) + he, mf, __builtin_bit_cast(u32x4, tr_frag(shm3 + wm * 4096 + mf * 1024 + ln * 8)));
    }
  } else {
    float rd[4];
#pragma unroll
    for (int mf = 0; mf < 4; ++mf) rd[mf] = svr[wm * 64 + mf * 16 + li];
    auto cvq = [&](int p, int mf, const f32x4& c0, const f32x4& c1) {
      u32x4 o;
#pragma unroll
      for (int j = 0; j < 2; ++j) {
        const f32x4 a = acc[mf][p * 2 + j], c4 = j ? c1 : c0;
        const f32x2 m2 = {rf[mf][0], rf[mf][0]}, r2 = {rd[mf], rd[mf]};
        f32x2 t0 = __builtin_elementwise_fma(m2, f32x2{c4[0], c4[1]}, f32x2{a[0], a[1]});
        f32x2 t1 = __builtin_elementwise_fma(m2, f32x2{c4[2], c4[3]}, f32x2{a[2], a[3]});
        if (p) { t0 *= r2; t1 *= r2; }
        o[2 * j] = pack2(t0[0], t0[1]); o[2 * j + 1] = pack2(t1[0], t1[1]);
      }
      return o;
    };
    const float cv0 = cfin[wc * 192 + 64 + li], cv1 = cfin[wc * 192 + 80 + li];
    auto cvv = [&](int mf) {
      const f32x4 n4 = *(const f32x4*)(tb + mf * 16 + lq * 4), r4 = *(const f32x4*)(svr + wm * 64 + mf * 16 + lq * 4);
      u32x4 o;
#pragma unroll
      for (int j = 0; j < 2; ++j) {
        const f32x4 a = acc[mf][4 + j];
        const f32x2 c2 = {j ? cv1 : cv0, j ? cv1 : cv0};
        const f32x2 t0 = __builtin_elementwise_fma(f32x2{n4[0], n4[1]}, c2, f32x2{a[0], a[1]}) * f32x2{r4[0], r4[1]};
        const f32x2 t1 = __builtin_elementwise_fma(f32x2{n4[2], n4[3]}, c2, f32x2{a[2], a[3]}) * f32x2{r4[2], r4[3]};
        o[2 * j] = pack2(t0[0], t0[1]); o[2 * j + 1] = pack2(t1[0], t1[1]);
      }
      return o;
    };
    u32x4 kx[4], vx[4], qx[4], hx[4];
    const bf16x4 ones = {0x3f80, 0x3f80, 0x3f80, 0x3f80};
    const f32x4 ck0 = *(const f32x4*)(cl + 32), ck1 = *(const f32x4*)(cl + 48);
    kx[2] = cvq(1, 2, ck0, ck1); kx[3] = cvq(1, 3, ck0, ck1);
    vx[2] = cvv(2); vx[3] = cvv(3);
    auto exo = [](int e) { return e * 4096 + (e >> 2) * (G2_A - 16384); };
    const bool needh = wm == 0 && mtile > 0;
    const __amdgpu_buffer_rsrc_t rh = __builtin_amdgcn_make_buffer_rsrc((void*)((char*)halo + ((needh ? mtile - 1 : mtile) * 32 + ntile + wc) * 4096), 0,
                                                                        needh || wm == 3 ? 4096 : 0, 0x00020000);
    if (wm < 3) {
      char* const ex = shm + exo(wc * 3 + wm) + ln * 16;
      *(u32x4*)ex = kx[2]; *(u32x4*)(ex + 1024) = kx[3]; *(u32x4*)(ex + 2048) = vx[2]; *(u32x4*)(ex + 3072) = vx[3];
      signal(0x10000u << wid);
    } else {
#pragma unroll
      for (int r = 0; r < 4; ++r) __builtin_amdgcn_raw_buffer_store_b128(r < 2 ? kx[2 + r] : vx[r], rh, ln * 16 + r * 1024, 0, 16);
    }
    auto hload = [&]() {
#pragma unroll
      for (int r = 0; r < 4; ++r) hx[r] = __builtin_bit_cast(u32x4, __builtin_amdgcn_raw_buffer_load_b128(rh, ln * 16 + r * 1024, 0, 16));
    };
    SB0();
    gel2(3, 2 * (wc ? tl + 3 : te) + he);
    SB0();
    if (wm == 0) hload();
    SB0();
    kx[0] = cvq(1, 0, ck0, ck1); kx[1] = cvq(1, 1, ck0, ck1);
    vx[0] = cvv(0); vx[1] = cvv(1);
    {
      const f32x4 cq0 = *(const f32x4*)(cl), cq1 = *(const f32x4*)(cl + 16);
#pragma unroll
      for (int mf = 0; mf < 4; ++mf) qx[mf] = cvq(0, mf, cq0, cq1);
    }
    f32x4 mk0, mk1;
    u32x2 pm;
#pragma unroll
    for (int j = 0; j < 4; ++j) {
      const int d = 4 * lq + j - li;
      mk0[j] = d >= 9 ? 0.f : -1.0e30f;
      mk1[j] = (unsigned)(d + 7) <= 7u ? 0.f : -1.0e30f;
    }
#pragma unroll
    for (int j = 0; j < 2; ++j) pm[j] = (4 * lq + 2 * j > li ? 0xffffu : 0u) | (4 * lq + 2 * j + 1 > li ? 0xffff0000u : 0u);
    auto max3 = [](float a, float b, float c) { float r; asm("v_max3_f32 %0, %1, %2, %3" : "=v"(r) : "v"(a), "v"(b), "v"(c)); return r; };
    auto max2 = [](float a, float b) { float r; asm("v_max_f32 %0, %1, %2" : "=v"(r) : "v"(a), "v"(b)); return r; };
    asm volatile("" : "+v"(kx[0]), "+v"(kx[1]), "+v"(vx[0]), "+v"(vx[1]), "+v"(qx[0]), "+v"(qx[1]), "+v"(qx[2]), "+v"(qx[3]), "+v"(mk0), "+v"(mk1), "+v"(pm));
    TS1(1);
    if (wc == 0) {
      if (wm == 0) WAIT_V(4); else WAIT_V(0);
      early();
    }
    if (wm > 0) {
      await(0x8000u << wid, 0x8000u << wid);
#pragma unroll
      for (int i = 0; i < 4; ++i) hx[i] = *(const u32x4*)(shm + exo(wc * 3 + wm - 1) + i * 1024 + ln * 16);
    } else if (__builtin_expect(cok || !needh, 1)) {
      while (__builtin_expect(blank(hx), 0)) {
        __builtin_amdgcn_s_sleep(1);
        asm volatile("" ::: "memory");
        hload();
      }
    } else {
      await(0xffffu, 0x404u);
      char* const fb = shm + exo(6 + wc) + ln * 16;
#pragma unroll 1
      for (int i = 0; i < 32; ++i) {
        const int rec = i >> 3, e = i & 7;
        const int row = m0 - 32 + 2 * (rec < 2 ? li : lq * 4 + (e & 3)) + (rec & 1);
        const int col = (rec < 2 ? D_ + lq * 4 + (e & 3) : 2 * D_ + li) + (ntile + wc) * 32 + (e >> 2) * 16;
        const u16* xr = x + (size_t)row * D_;
        const unsigned stt = row_stats_serial(xr);
        const float mb = lo_f(stt);
        float a = 0.f;
#pragma unroll 1
        for (int k = 0; k < D_; ++k) a += (bf2f(xr[k]) - mb) * bf2f(wqkv[(size_t)k * 3072 + col]);
        *(u16*)(fb + rec * 1024 + e * 2) = f2bf(a / hi_f(stt));
      }
#pragma unroll
      for (int i = 0; i < 4; ++i) hx[i] = *(const u32x4*)(fb + i * 1024);
    }
    TS1(6);
    {
      const f32x4 z = {0.f, 0.f, 0.f, 0.f};
      const float mz = mtile == 0 && wm == 0 ? -1.0e30f : 0.f;
      const f32x4 mkf = mk0 + f32x4{mz, mz, mz, mz};
      f32x4 s0[4], s1[4];
#pragma unroll
      for (int i = 0; i < 4; ++i) {
        const int n = i ^ 2;
        const bf16x8 qf = __builtin_bit_cast(bf16x8, qx[n]);
        s1[n] = __builtin_amdgcn_mfma_f32_16x16x32_bf16(__builtin_bit_cast(bf16x8, kx[n]), qf, mk1, 0, 0, 0);
        s0[n] = __builtin_amdgcn_mfma_f32_16x16x32_bf16(__builtin_bit_cast(bf16x8, n < 2 ? hx[n] : kx[n - 2]), qf, n < 2 ? mkf : mk0, 0, 0, 0);
      }
      SB0();
      asm volatile("s_nop 7\n\ts_nop 3" : "+v"(s0[0]), "+v"(s1[0]), "+v"(s0[1]), "+v"(s1[1]), "+v"(s0[2]), "+v"(s1[2]), "+v"(s0[3]), "+v"(s1[3]));
      float mx[4];
#pragma unroll
      for (int n = 0; n < 4; ++n)
#pragma unroll
        for (int j = 0; j < 4; ++j) s0[n][j] = max2(s0[n][j], s1[n][j]);
#pragma unroll
      for (int n = 0; n < 4; ++n) mx[n] = max3(s0[n][0], s0[n][1], s0[n][2]);
#pragma unroll
      for (int n = 0; n < 4; ++n) mx[n] = max2(mx[n], s0[n][3]);
#pragma unroll
      for (int n = 0; n < 4; ++n) { auto r = __builtin_amdgcn_permlane16_swap(__float_as_uint(mx[n]), __float_as_uint(mx[n]), false, false);
        mx[n] = max2(__uint_as_float(r[0]), __uint_as_float(r[1])); }
#pragma unroll
      for (int n = 0; n < 4; ++n) { auto r = __builtin_amdgcn_permlane32_swap(__float_as_uint(mx[n]), __float_as_uint(mx[n]), false, false);
        mx[n] = max2(__uint_as_float(r[0]), __uint_as_float(r[1])); }
      SB0();
      constexpr float cs2 = 0.17677669529663687f * 1.4426950408889634f;
      f32x4 os[4], o0[4], o1[4];
#pragma unroll
      for (int n = 0; n < 4; ++n) {
        const float c = cs2 * rd[n], nm = -mx[n] * c;
        const f32x4 ar = __builtin_elementwise_fma(s0[n], f32x4{c, c, c, c}, f32x4{nm, nm, nm, nm});
        const u32x2 pk = {pack2(__builtin_amdgcn_exp2f(ar[0]), __builtin_amdgcn_exp2f(ar[1])), pack2(__builtin_amdgcn_exp2f(ar[2]), __builtin_amdgcn_exp2f(ar[3]))};
        const bf16x4 pa = __builtin_bit_cast(bf16x4, pk), p0 = __builtin_bit_cast(bf16x4, pk & pm), p1 = __builtin_bit_cast(bf16x4, pk & ~pm);
        const u32x4 vp = n < 2 ? hx[2 + n] : vx[n - 2];
        auto half = [](const u32x4& v, int h) { return __builtin_bit_cast(bf16x4, u32x2{v[2 * h], v[2 * h + 1]}); };
        os[n] = __builtin_amdgcn_mfma_f32_16x16x16bf16_1k(ones, pa, z, 0, 0, 0);
        o0[n] = __builtin_amdgcn_mfma_f32_16x16x16bf16_1k(half(vx[n], 0), p1, __builtin_amdgcn_mfma_f32_16x16x16bf16_1k(half(vp, 0), p0, z, 0, 0, 0), 0, 0, 0);
        o1[n] = __builtin_amdgcn_mfma_f32_16x16x16bf16_1k(half(vx[n], 1), p1, __builtin_amdgcn_mfma_f32_16x16x16bf16_1k(half(vp, 1), p0, z, 0, 0, 0), 0, 0, 0);
      }
#pragma unroll
      for (int n = 0; n < 4; ++n) {
        const float inv = __builtin_amdgcn_rcpf(os[n][0]);
        const f32x4 n0 = o0[n] * inv, n1 = o1[n] * inv;
        putf(112 + ntile + wc, n, u32x4{pack2(n0[0], n0[1]), pack2(n0[2], n0[3]), pack2(n1[0], n1[1]), pack2(n1[2], n1[3])});
      }
    }
  }
  TS1(2);
  {
  __builtin_amdgcn_sched_barrier(0);
  int tid = threadIdx.x;
  asm volatile("" : "+v"(tid));
  __builtin_amdgcn_sched_barrier(0);
  const int lane = tid & 63;
  const int wm = wid >> 1, wn = wid & 1;
  const int m0 = mt2 * 128, n0 = nt2 * 128;
  constexpr int PD = G2_NSTAGE;
  const int ny = (xcd & 1) ? NY - DLT : NY + DLT;
  const unsigned va = lane * 16;
  auto stage_y = [&]() {
    asm volatile("s_add_u32 m0, %[l], %[oa]\n\ts_nop 0\n\tglobal_load_lds_dwordx4 %[va], %[a] sc1\n\tglobal_load_lds_dwordx4 %[va], %[a] offset:1024 sc1\n\t"
                 "global_load_lds_dwordx4 %[va], %[a] offset:2048 sc1\n\tglobal_load_lds_dwordx4 %[va], %[a] offset:3072 sc1\n\t" G2_W
                 :: [l] "s"(sl), [oa] "s"(shm_base + G2_A + (p ^ role) * 4096), [a] "s"(sa), [va] "v"(va), G2_WOP(wy) : "memory", "scc", "m0");
    ++st;
    sa += 16384;
    sl = (sl + 16384) & 65535;
    wsel();
  };
  u64 ready = 0;
  const u64* const mbox = &done1[bid * 64 + lane];
  auto seen = [&](u64 v) __attribute__((always_inline)) {
    const u64 r = __ballot(fresh(v, t_own));
    u64 d;
    asm("s_bitreplicate_b64_b32 %0, %1" : "=s"(d) : "s"((unsigned)r >> (16 * role)));
    ready |= r | (d << 32);
  };
  auto need = [&](u64 mask, bool renew) __attribute__((always_inline)) {
    if ((ready & mask) != mask) {
      seen(__hip_atomic_load(mbox, RLX_AGENT));
      while ((ready & mask) != mask) {
        __builtin_amdgcn_s_sleep(PSLEEP);
        if (renew && !cok) notify(0, 32);
        seen(__hip_atomic_load(mbox, RLX_AGENT));
      }
    }
    asm volatile("" ::: "memory");
  };
  u64 fl = 0;
  auto peek = [&]() { asm volatile("global_load_dwordx2 %0, %1, %2 sc1" : "+v"(fl) : "v"(va >> 1), "s"(done1 + bid * 64) : "memory"); };
  auto check = [&](u64 mask, bool renew) __attribute__((always_inline)) {
    asm volatile("" : "+v"(fl));
    seen(fl);
    if (__builtin_expect((ready & mask) != mask, 0)) need(mask, renew);
  };
  if (wid >= 4) {
    TS(4);
    __builtin_amdgcn_s_setprio(0);
    BARL();
    stage_y();
    stage_y();
    BAR();
    if (p == 1) peek();
    stage_y();
    WAIT_V(16);
    BAR();
    if (p == 0) notify(0, 32);
#define G2_A2 "global_load_lds_dwordx4 %[va], %[a] sc1\n\tglobal_load_lds_dwordx4 %[va], %[a] offset:1024 sc1"
#define G2_A4 G2_A2 "\n\tglobal_load_lds_dwordx4 %[va], %[a] offset:2048 sc1\n\tglobal_load_lds_dwordx4 %[va], %[a] offset:3072 sc1"
#define G2_SYNC "\n\ts_mov_b32 m0, %[la]\n\ts_waitcnt vmcnt(%[n])\n\ts_barrier\n\t"
    const unsigned oy = shm_base + G2_A + (p ^ role) * 4096, os = shm_base + G2_A + (p >> 1) * 8192 + (p & 1) * 2048;
    asm volatile("s_mov_b32 m0, %[la]\n\ts_nop 0\n\t" G2_A4 :: [la] "s"(sl + oy), [a] "s"(sa), [va] "v"(va) : "memory", "m0");
    auto step = [&](long adv = 16384) __attribute__((always_inline)) {
      wsel();
      ++st;
      sa += adv;
      const unsigned sn = (sl + 16384) & 65535;
      asm volatile(G2_W G2_SYNC G2_A4 :: [l] "s"(sl), [la] "s"(sn + oy), [n] "n"(16), [a] "s"(sa), [va] "v"(va), G2_WOP(wy) : "memory", "scc", "m0");
      sl = sn;
    };
    auto chk1 = [&](u64 mask) __attribute__((always_inline)) { if (p == 1) { WAIT_V(16); check(mask, true); } };
    step();
    chk1(0xcc00cc00ull << 32);
    step(); step();
    if (p == 1) peek();
    step(); step();
    chk1(0x0f0fu << (16 * role));
#pragma unroll 1
    for (int g = 7; g < 13; ++g) step();
    if (p == 1) peek();
    step(); step();
    chk1((0xffffu << (16 * role)) | 0xf555f555u);
#pragma unroll 1
    for (int g = 15; g < min(ny, NY) - 5; ++g) step();
    if (ny > NY) {
      step(((1 - 2 * role) * NY + 1 - DLT) * 16384);
#pragma unroll
      for (int i = 1; i < DLT; ++i) step();
    }
    wsel();
    sa = sz;
    {
      const unsigned sn = (sl + 16384) & 65535;
      asm volatile(G2_W G2_SYNC G2_A2 :: [l] "s"(sl), [la] "s"(sn + os), [n] "n"(16), [a] "s"(sa), [va] "v"(va), G2_WOP(wy) : "memory", "scc", "m0");
      sl = sn;
    }
    sw[0] = wz.wb[0]; sw[1] = wz.wb[1];
#pragma unroll
    for (int j = -3; j < 13; ++j) {
      if (j == GETP + 1 || j == GETP + 2)
        asm volatile("s_mov_b32 m0, %[l]\n\ts_nop 0\n\tglobal_load_lds_dwordx4 %[va], %[a] offset:%[o] sc1\n\tglobal_load_lds_dwordx4 %[va], %[a] offset:%[o]+1024 sc1"
                     :: [l] "s"(shm_base + G2_X + 16384 + p * 4096), [a] "s"((const char*)slab + (size_t)(bid ^ 5) * 16384 + p * 4096), [va] "v"(va),
                        [o] "n"((j - GETP - 1) * 2048) : "memory", "m0");
      sa += 16384;
      const unsigned sn = (sl + 16384) & 65535;
      constexpr int G = GETP;
      static_assert(GETP <= GETJ + 1, "");
      if (j < 12) asm volatile(G2_W G2_SYNC G2_A2 :: [l] "s"(sl), [la] "s"(sn + os), [n] "n"((j == -3 ? 14 : 12) + (j > G && j < G + 3 ? 2 : 0) + (j > G + 1 && j < G + 4 ? 2 : 0)),
                               [a] "s"(sa), [va] "v"(va), G2_WOP(wz) : "memory", "scc", "m0");
      else asm volatile(G2_W :: [l] "s"(sl), G2_WOP(wz) : "memory", "scc", "m0");
      sl = sn;
      sw[0] += ws0; sw[1] += ws1;
    }
    WAIT_V(12);
    BAR();
    WAIT_V(6);
    BAR();
    WAIT_V(0);
    BAR();
    asm volatile("" : "+v"(fl));
    TS(5);
    if (tid < 258) __hip_atomic_store(lane ? done2 + bid * 8 : &acks[bid], __builtin_amdgcn_s_memrealtime(), RLX_AGENT);
    if (__builtin_expect(!cok, 0) && p == 0) {
      const bool mine = lane <= 32;
      const u64* const pa = &acks[lane < 32 ? client(lane) : bid ^ 5];
      while (!__all(!mine || fresh(__hip_atomic_load(pa, RLX_AGENT), t_own))) {
        notify(0, 32);
        if (lane == 0) __hip_atomic_store(done2 + bid * 8, __builtin_amdgcn_s_memrealtime(), RLX_AGENT);
        __builtin_amdgcn_s_sleep(32);
      }
    }
  } else {
    TS(6);
    f32x4 acc[4][4], sacc[2][4];
    const int fr = lane & 15, fq = lane >> 4, q4 = fr >> 2, pp = lane & 3;
    const int ff = ((q4 >> 1) & 1) | ((fq & 1) << 1);
    int aof = G2_A + wm * 8192 + lane * 16;
    int boff[4];
#pragma unroll
    for (int j = 0; j < 4; ++j) boff[j] = wn * 8192 + (8 * fq + q4) * 128 + ((j ^ ff) * 32) + pp * 8;
    int vo = wid * 4096 + lane * 16;
    int vx = ((m0 + wm * 64 + role * 32 + 2 * fr) * D_ + n0 + wn * 64) * 2 + (fq & 1) * 32 + (fq >> 1) * 16;
    lds_cptr px = shm3 + G2_X + wid * 4096 + (fq >> 1) * 512 + fr * 16 + (fq & 1) * 8;
    asm volatile("" : "+v"(vo), "+v"(vx), "+v"(px));
#define SB() __builtin_amdgcn_sched_barrier(0)
    struct Fr { bf16x8 a[4]; bf16x4 bl[4], bh[4]; };
    Fr fa[2], fb[2];
    auto rd = [&](Fr* f, int odd, int t) {
      constexpr int ord[12] = {0, 1, 8, 2, 3, 4, 5, 6, 7, 9, 10, 11};
      constexpr int seq[24] = {0, 1, 2, 12, 13, 14, 15, 16, 3, 4, 5, 6, 17, 18, 19, 20, 7, 8, 9, 21, 10, 22, 11, 23};
      lds_cptr sb = shm3 + odd * 16384;
      const int ks = seq[t] / 12, o = ord[seq[t] % 12];
      if (o >= 8) f[ks].a[o - 8] = *(const __attribute__((address_space(3))) bf16x8*)(sb + aof + (o - 8) * 2048 + ks * 1024);
      else if (o & 1) f[ks].bh[o >> 1] = tr_read(sb + boff[o >> 1] + ks * 4096 + 512);
      else f[ks].bl[o >> 1] = tr_read(sb + boff[o >> 1] + ks * 4096);
    };
    auto bfr = [&](const Fr& c, int j) { return __builtin_shufflevector(c.bl[j], c.bh[j], 0, 1, 2, 3, 4, 5, 6, 7); };
    auto mf1 = [](f32x4& c, bf16x8 b, bf16x8 a, bool z = false) {
      if (z) asm volatile("v_mfma_f32_16x16x32_bf16 %0, %1, %2, 0" : "=&v"(c) : "v"(b), "v"(a));
      else asm volatile("v_mfma_f32_16x16x32_bf16 %0, %1, %2, %0" : "+v"(c) : "v"(b), "v"(a));
    };
    auto mf2 = [](f32x4& c, bf16x8 b, bf16x8 a, bf16x8 b2, bf16x8 a2, bool z) {
      if (z) asm volatile("v_mfma_f32_16x16x32_bf16 %0, %1, %2, 0\n\tv_mfma_f32_16x16x32_bf16 %0, %3, %4, %0" : "=&v"(c) : "v"(b), "v"(a), "v"(b2), "v"(a2));
      else asm volatile("v_mfma_f32_16x16x32_bf16 %0, %1, %2, %0\n\tv_mfma_f32_16x16x32_bf16 %0, %3, %4, %0" : "+v"(c) : "v"(b), "v"(a), "v"(b2), "v"(a2));
    };
    auto pairs = [&](f32x4 (*ac)[4], const Fr* c, int nq, int mx, int nf, auto fill, bool z = false) {
#pragma unroll
      for (int q = 0; q < nq; ++q) {
        const int mf = (q >> 2) ^ mx, j = (q & 4) ? 3 - (q & 3) : q & 3, ks = q & 1;
        SB();
        if (2 * q >= nf) mf2(ac[mf][j], bfr(c[ks], j), c[ks].a[mf], bfr(c[ks ^ 1], j), c[ks ^ 1].a[mf], z);
        else {
          mf1(ac[mf][j], bfr(c[ks], j), c[ks].a[mf], z);
          SB();
          fill(2 * q);
          SB();
          mf1(ac[mf][j], bfr(c[ks ^ 1], j), c[ks ^ 1].a[mf]);
          SB();
          fill(2 * q + 1);
        }
      }
      SB();
    };
    auto flip = [&]() {
      aof ^= 32768; boff[0] ^= 32768; boff[1] ^= 32768; boff[2] ^= 32768; boff[3] ^= 32768;
      asm volatile("" : "+v"(aof), "+v"(boff[0]), "+v"(boff[1]), "+v"(boff[2]), "+v"(boff[3]));
    };
    auto bar = [&]() { __builtin_amdgcn_s_waitcnt(0xC07F); BAR(); };
    auto ystg = [&](const Fr* c, Fr* n, int odd, bool z = false) {
      pairs(acc, c, 16, 0, odd ? 30 : 24, [&](int t) {
        if (t < 24) rd(n, odd, t);
        else if (t == 24) asm volatile("s_nop 0");
        else { int& a = t == 29 ? aof : boff[t - 25]; a ^= 32768; asm volatile("" : "+v"(a)); }
      }, z);
    };
    auto stg = [&](const Fr* c, Fr* n, int odd, bool z = false) { pairs(sacc, c, 8, 0, 10, [&](int t) { rd(n, odd, 2 * t); rd(n, odd, 2 * t + 1); }, z); };
    asm volatile("" : "+v"(aof), "+v"(boff[0]), "+v"(boff[1]), "+v"(boff[2]), "+v"(boff[3]));
    await(0xffffu, 0x404u);
    auto lda = [&](int i) {
      asm volatile("s_mov_b32 m0, %[l]\n\ts_nop 0\n\tglobal_load_lds_dwordx4 %[va], %[a] sc1\n\tglobal_load_lds_dwordx4 %[va], %[a] offset:1024 sc1\n\t"
                   "global_load_lds_dwordx4 %[va], %[a] offset:2048 sc1\n\tglobal_load_lds_dwordx4 %[va], %[a] offset:3072 sc1"
                   :: [l] "s"(shm_base + G2_A + (i + 1) * 16384 + (p ^ role) * 4096), [a] "s"(sa + i * 16384), [va] "v"(va) : "memory", "m0");
    };
    need(0x00330033ull << 32, false);
    TS(4);
    lda(0);
    lda(1);
    need(0x00ff00ffull << 32, false);
    TS(0);
    lda(2);
    WAIT_V(8);
    signal(1u << 24);
    await(0xff000000u, 4u << 24);
    __builtin_amdgcn_s_waitcnt(0xC07F);
    SB();
#pragma unroll
    for (int t = 0; t < 24; ++t) rd(fa, 1, t);
    flip();
#define CPRIO 0
    __builtin_amdgcn_s_setprio(CPRIO);
    WAIT_V(4);
    TS(5);
    peek();
    bar();
    TS(7);
    ystg(fa, fb, 0, true);
    WAIT_V(1);
    bar();
    ystg(fb, fa, 1);
    WAIT_V(0);
    check(0x33003300ull << 32, false);
    constexpr int PAR = (NY - DLT + 1) & 1;
#pragma unroll 1
    for (int g = 2; g < ny - 2; g += 2) {
      bar();
      ystg(fa, fb, 0);
      bar();
      ystg(fb, fa, 1);
    }
    if (PAR) {
      bar();
      ystg(fa, fb, 0);
    }
    Fr* const f0 = PAR ? fb : fa;
    Fr* const f1 = PAR ? fa : fb;
    bar();
    {
      const __amdgpu_buffer_rsrc_t rs = __builtin_amdgcn_make_buffer_rsrc((void*)((char*)slab + (size_t)bid * 16384), 0, 16384, 0x00020000);
      u32x4 pk;
      pairs(acc, f0, 16, 2, 32, [&](int t) {
        if (t < 20) rd(f1, PAR, t);
        else {
          const int f = (t - 20) / 3, e = (t - 20) % 3;
          if (e < 2) { const f32x4 a = acc[2 + (f >> 1)][(f & 1) * 2 + e]; pk[2 * e] = pack2(a[0], a[1]); pk[2 * e + 1] = pack2(a[2], a[3]); }
          else __builtin_amdgcn_raw_buffer_store_b128(pk, rs, vo + f * 1024, 0, 16);
        }
      });
    }
    TS(2);
    static_assert(GETJ % 2 == 1 && GETJ >= 3 && GETJ <= 7, "");
    f32x4 xf[2][4];
#pragma unroll
    for (int j = 0; j < GETJ + 5; j += 2) {
      if (PAR) flip();
      bar();
      stg(f1, f0, PAR ^ 1, j == 0);
      if (!PAR) flip();
      bar();
      u32x2 xv[2];
      pairs(sacc, f0, 8, 0, 12, [&](int t) {
        if (t < 10) { rd(f1, PAR, 2 * t); rd(f1, PAR, 2 * t + 1); }
        else if (j < 8) xv[t - 10] = *(const __attribute__((address_space(3))) u32x2*)(px + (j >> 1) * 1024 + (t - 10) * 256);
      });
      if (j < 8) {
#pragma unroll
        for (int e = 0; e < 2; ++e) xf[j >> 2][(j & 2) + e] = f32x4{lo_f(xv[e][0]), hi_f(xv[e][0]), lo_f(xv[e][1]), hi_f(xv[e][1])};
        asm volatile("" : "+v"(xf[j >> 2][j & 2]), "+v"(xf[j >> 2][(j & 2) + 1]));
        SB();
      }
    }
    if (PAR) flip();
    WAIT_V(0);
    bar();
    u32x4 pv[4];
    {
      lds_cptr pp_ = shm3 + G2_X + 16384 + vo;
      pairs(sacc, f1, 8, 0, 12, [&](int t) {
        if (t < 2) {
#pragma unroll
          for (int f = 2 * t; f < 2 * t + 2; ++f) pv[f] = *(const __attribute__((address_space(3))) u32x4*)(pp_ + f * 1024);
        } else { rd(f0, PAR ^ 1, 2 * t - 4); rd(f0, PAR ^ 1, 2 * t - 3); }
      });
      if (__builtin_expect(blank(pv) || !cok, 0)) {
        const __amdgpu_buffer_rsrc_t rp = __builtin_amdgcn_make_buffer_rsrc((void*)((char*)slab + (size_t)(bid ^ 5) * 16384), 0, 16384, 0x00020000);
        while (!cok && !__all(fresh(__hip_atomic_load(done2 + (bid ^ 5) * 8, RLX_AGENT), t_own))) {
          notify(0, 32);
          if (lane == 0) __hip_atomic_store(done2 + bid * 8, __builtin_amdgcn_s_memrealtime(), RLX_AGENT);
          __builtin_amdgcn_s_sleep(8);
        }
        do {
          asm volatile("" ::: "memory");
#pragma unroll
          for (int f = 0; f < 4; ++f) pv[f] = __builtin_bit_cast(u32x4, __builtin_amdgcn_raw_buffer_load_b128(rp, vo + f * 1024, 0, 16));
        } while (blank(pv));
      }
    }
    auto silu = [&](int g0, int g1) {
#pragma unroll
      for (int f = g0; f < g1; ++f) {
        const int mf = f >> 2, j = f & 3;
        const u32x4 pw = pv[mf * 2 + (j >> 1)];
        const f32x4 ya = acc[mf][j] + f32x4{lo_f(pw[2 * (j & 1)]), hi_f(pw[2 * (j & 1)]), lo_f(pw[2 * (j & 1) + 1]), hi_f(pw[2 * (j & 1) + 1])};
#pragma unroll
        for (int e = 0; e < 2; ++e) {
          const f32x2 y2 = {ya[2 * e], ya[2 * e + 1]};
          const f32x2 ar = y2 * f32x2{-1.4426950408889634f, -1.4426950408889634f};
          const f32x2 dn = f32x2{__builtin_amdgcn_exp2f(ar[0]), __builtin_amdgcn_exp2f(ar[1])} + f32x2{1.0f, 1.0f};
          const f32x2 t = y2 * f32x2{__builtin_amdgcn_rcpf(dn[0]), __builtin_amdgcn_rcpf(dn[1])};
          acc[mf][j][2 * e] = t[0]; acc[mf][j][2 * e + 1] = t[1];
        }
        asm volatile("" : "+v"(acc[mf][j]));
      }
      SB();
    };
#define SPT 0, 0, 1, 2, 4, 6, 8, 8
    constexpr int spt[] = {SPT};
    silu(0, spt[1]);
#pragma unroll
    for (int j = GETJ + 6; j < 15; ++j) {
      if ((j ^ PAR) & 1) flip();
      bar();
      if (j & 1) stg(f0, f1, PAR); else stg(f1, f0, PAR ^ 1);
      silu(spt[j - GETJ - 5], j == 14 ? 8 : spt[j - GETJ - 4]);
    }
    pairs(sacc, f0, 8, 0, 0, [](int) {});
    asm volatile("s_nop 7\n\ts_nop 3" : "+v"(sacc[1][0]), "+v"(sacc[1][1]), "+v"(sacc[1][2]), "+v"(sacc[1][3]));
    SB();
    TS(1);
    {
      const __amdgpu_buffer_rsrc_t ro = __builtin_amdgcn_make_buffer_rsrc((void*)out, 0, S_ * D_ * 2, 0x00020000);
#pragma unroll
      for (int f = 0; f < 4; ++f) {
        const int mf = f >> 1, j = (f & 1) * 2;
        const f32x4 a = __builtin_elementwise_fma(sacc[mf][j], acc[mf][j], xf[mf][j]), b = __builtin_elementwise_fma(sacc[mf][j + 1], acc[mf][j + 1], xf[mf][j + 1]);
        const auto r0 = __builtin_amdgcn_permlane16_swap(pack2(a[0], a[1]), pack2(b[0], b[1]), false, false);
        const auto r1 = __builtin_amdgcn_permlane16_swap(pack2(a[2], a[3]), pack2(b[2], b[3]), false, false);
        __builtin_amdgcn_raw_buffer_store_b128(u32x4{r0[0], r1[0], r0[1], r1[1]}, ro, vx + mf * 2048 + j * 32, 0, 2);
      }
    }
    TS(3);
  }
  TSFLUSH();
  }
  asm volatile(".Lcode_end:");
}

extern "C" void kernel_launch(void* const* d_in, const int* in_sizes, int n_in, void* d_out, int out_size, void* d_ws,
                              size_t ws_size, hipStream_t stream) {
  const u16* A_x = (const u16*)d_in[0];
  const u16* A_wqkv = (const u16*)d_in[1];
  const u16* A_wo = (const u16*)d_in[2];
  const u16* B_wg = (const u16*)d_in[4];
  const u16* B_wu = (const u16*)d_in[5];
  const u16* B_wd = (const u16*)d_in[6];
  u16* out = (u16*)d_out;
  fused_kernel<<<256, 512, 0, stream>>>(A_x, A_wqkv, B_wg, B_wu, B_wd, A_wo, out, (char*)d_ws);
}
```

```cpp
#define ALL_TS 1
#define TSMASK 4096
#include <hip/hip_runtime.h>
#include <hip/hip_bf16.h>
#include <stdint.h>
#include <type_traits>

typedef unsigned short u16;
using bf16x8 = __attribute__((ext_vector_type(8))) short;
using bf16x4 = __attribute__((ext_vector_type(4))) short;
using f32x4 = __attribute__((ext_vector_type(4))) float;
using u32x2 = __attribute__((ext_vector_type(2))) unsigned;
using u32x4 = __attribute__((ext_vector_type(4))) unsigned;
typedef __attribute__((address_space(3))) const char* lds_cptr;

constexpr int S_ = 2048, D_ = 1024, F_ = 3584;

__device__ __forceinline__ float bf2f(u16 h) { return __uint_as_float((unsigned)h << 16); }
__device__ __forceinline__ u16 f2bf(float f) {
  unsigned u = __float_as_uint(f);
  u += 0x7fffu + ((u >> 16) & 1u);
  return (u16)(u >> 16);
}
__device__ __forceinline__ float rbf(float f) { return bf2f(f2bf(f)); }
typedef __attribute__((ext_vector_type(2))) __bf16 bf16x2_t;
typedef __attribute__((ext_vector_type(2))) float f32x2;
__device__ __forceinline__ unsigned pack2(float a, float b) {
  bf16x2_t r = __builtin_convertvector((f32x2){a, b}, bf16x2_t);
  return __builtin_bit_cast(unsigned, r);
}
__device__ __forceinline__ float lo_f(unsigned p) { return __uint_as_float(p << 16); }
__device__ __forceinline__ float hi_f(unsigned p) { return __uint_as_float(p & 0xffff0000u); }

#define TSDECL unsigned ts_r[16] = {0, 0, 0, 0, 0, 0, 0, 0, 0, 0, 0, 0, 0, 0, 0, 0}
#define TSS(i) do { if ((TSMASK >> (i)) & 1) { unsigned long long t_; asm volatile("s_memrealtime %0\n\ts_waitcnt lgkmcnt(0)" : "=s"(t_)); ts_r[i] = (unsigned)t_; } } while (0)
#define TSFLUSH() do { if ((threadIdx.x & 63) == 0) { for (int i_ = 0; i_ < 16; ++i_) flags[(blockIdx.x * 8 + (threadIdx.x >> 6)) * 16 + i_] = ts_r[i_]; } } while (0)
#define TS1(i) TSS(i)
#define TS(i) TSS(8 + (i))
#define PIN_S(v) asm volatile("" : "+s"(v))
#define WAIT_V(n) asm volatile("s_waitcnt vmcnt(%0)" ::"n"(n) : "memory")
#define WAIT_L(n) asm volatile("s_waitcnt lgkmcnt(%0)" ::"n"(n) : "memory")
#define BARL() do { WAIT_L(0); BAR(); } while (0)
#define BAR() do { asm volatile("" ::: "memory"); __builtin_amdgcn_s_barrier(); asm volatile("" ::: "memory"); } while (0)

__device__ __forceinline__ bf16x4 tr_read(lds_cptr p) {
  return __builtin_amdgcn_ds_read_tr16_b64_v4i16((__attribute__((address_space(3))) bf16x4*)p);
}
__device__ __forceinline__ bf16x8 tr_frag(lds_cptr p) {
  bf16x4 lo = tr_read(p), hi = tr_read(p + 512);
  bf16x8 r;
  r[0] = lo[0]; r[1] = lo[1]; r[2] = lo[2]; r[3] = lo[3];
  r[4] = hi[0]; r[5] = hi[1]; r[6] = hi[2]; r[7] = hi[3];
  return r;
}
__device__ __forceinline__ void glds16(const void* gsrc, unsigned lds_dst) {
  unsigned keep;
  asm volatile("s_mov_b32 %0, m0\n\ts_mov_b32 m0, %2\n\ts_nop 0\n\tglobal_load_lds_dwordx4 %1, off\n\ts_mov_b32 m0, %0"
               : "=&s"(keep) : "v"(gsrc), "s"(lds_dst) : "memory");
}
#define GLDS(gptr, laddr) glds16((const void*)(gptr), (unsigned)(laddr))
__device__ __forceinline__ void glds16_sc1(const void* gsrc, unsigned lds_dst) {
  unsigned keep;
  asm volatile("s_mov_b32 %0, m0\n\ts_mov_b32 m0, %2\n\ts_nop 0\n\tglobal_load_lds_dwordx4 %1, off sc1\n\ts_mov_b32 m0, %0"
               : "=&s"(keep) : "v"(gsrc), "s"(lds_dst) : "memory");
}
#define GLDS_SC1(gptr, laddr) glds16_sc1((const void*)(gptr), (unsigned)(laddr))
__device__ __forceinline__ void glds16_nc(const void* gsrc, unsigned lds_dst) {
  unsigned keep;
  asm volatile("s_mov_b32 %0, m0\n\ts_mov_b32 m0, %2\n\ts_nop 0\n\tglobal_load_lds_dwordx4 %1, off\n\ts_mov_b32 m0, %0"
               : "=&s"(keep) : "v"(gsrc), "s"(lds_dst));
}
#define GLDS_NC(gptr, laddr) glds16_nc((const void*)(gptr), (unsigned)(laddr))
__device__ __forceinline__ void glds16_s(const void* sbase, unsigned voff, unsigned lds_dst) {
  asm volatile("s_mov_b32 m0, %2\n\ts_nop 0\n\tglobal_load_lds_dwordx4 %0, %1" :: "v"(voff), "s"(sbase), "s"(lds_dst) : "m0");
}

__device__ __forceinline__ void glds16_s1(const void* sbase, unsigned voff, unsigned lds_dst) {
  asm volatile("s_mov_b32 m0, %2\n\ts_nop 0\n\tglobal_load_lds_dwordx4 %0, %1 sc1" :: "v"(voff), "s"(sbase), "s"(lds_dst) : "memory", "m0");
}
__device__ __forceinline__ void glds16_v(const void* p, unsigned lds_dst) {
  asm volatile("s_mov_b32 m0, %1\n\ts_nop 0\n\tglobal_load_lds_dwordx4 %0, off" :: "v"(p), "s"(lds_dst) : "m0");
}
template <int OFF = 0>
__device__ __forceinline__ void gst16(const void* sbase, unsigned voff, u32x4 v) {
  asm volatile("global_store_dwordx4 %0, %1, %2 offset:%3 sc1\n\ts_nop 1" :: "v"(voff), "v"(v), "s"(sbase), "n"(OFF) : "memory");
}

typedef unsigned long long u64;
__device__ __forceinline__ bool fresh(u64 stamp, u64 t_own) {
  const u64 b = t_own + 100ull;
  const unsigned lo = (unsigned)stamp - (unsigned)b, hi = (unsigned)(stamp >> 32) - (unsigned)(b >> 32) - ((unsigned)stamp < (unsigned)b ? 1u : 0u);
  return (hi == 0u) & (lo < (unsigned)(__builtin_amdgcn_s_memrealtime() - t_own));
}
constexpr u64 TIMEOUT_ = 200000;
__device__ __forceinline__ float half_sum(float v) {
  v += __builtin_bit_cast(float, __builtin_amdgcn_update_dpp(0, __builtin_bit_cast(int, v), 0xB1, 0xf, 0xf, false));
  v += __builtin_bit_cast(float, __builtin_amdgcn_update_dpp(0, __builtin_bit_cast(int, v), 0x4E, 0xf, 0xf, false));
  v += __builtin_bit_cast(float, __builtin_amdgcn_update_dpp(0, __builtin_bit_cast(int, v), 0x141, 0xf, 0xf, false));
  v += __builtin_bit_cast(float, __builtin_amdgcn_update_dpp(0, __builtin_bit_cast(int, v), 0x140, 0xf, 0xf, false));
  { auto r = __builtin_amdgcn_permlane16_swap(__float_as_uint(v), __float_as_uint(v), false, false); v = __uint_as_float(r[0]) + __uint_as_float(r[1]); }
  return v;
}
__device__ __forceinline__ unsigned pack_stats(float mean, float var) {
  return (unsigned)f2bf(mean) | ((unsigned)f2bf(sqrtf(rbf(rbf(var) + 1e-5f))) << 16);
}
__device__ __forceinline__ unsigned row_stats_serial(const u16* xr) {
  float s = 0.f, q = 0.f;
#pragma unroll 1
  for (int k = 0; k < D_; ++k) s += bf2f(xr[k]);
  const float mean = s * (1.0f / D_);
#pragma unroll 1
  for (int k = 0; k < D_; ++k) { const float d = bf2f(xr[k]) - mean; q += d * d; }
  return pack_stats(mean, q * (1.0f / D_));
}
constexpr int G1_STAGE = 32768 + 5 * 8192;
#define EPOS 2
#define PA 4
#define PB 8
#define PC 12
#define GETP 4
#define DLT 2
#define PSLEEP 2
#define GETJ 3

constexpr unsigned BLANK = 0x7f817f81u;
__device__ __forceinline__ bool blank(const u32x4* pv) {
  u64 m;
#define CX(i) "v_cmpx_ne_u32 vcc, %1, %" #i "\n\t"
  asm volatile(CX(2) CX(3) CX(4) CX(5) CX(6) CX(7) CX(8) CX(9) CX(10) CX(11) CX(12) CX(13) CX(14) CX(15) CX(16) CX(17)
               "s_mov_b64 %0, exec\n\ts_mov_b64 exec, -1\n\ts_nop 3" : "=s"(m) : "s"(BLANK), "v"(pv[0][0]), "v"(pv[0][1]), "v"(pv[0][2]), "v"(pv[0][3]),
               "v"(pv[1][0]), "v"(pv[1][1]), "v"(pv[1][2]), "v"(pv[1][3]), "v"(pv[2][0]), "v"(pv[2][1]), "v"(pv[2][2]), "v"(pv[2][3]),
               "v"(pv[3][0]), "v"(pv[3][1]), "v"(pv[3][2]), "v"(pv[3][3]) : "vcc");
  return m != ~0ull;
}
#define RLX_AGENT __ATOMIC_RELAXED, __HIP_MEMORY_SCOPE_AGENT

constexpr int G2_A = 65536, G2_X = 131072;
constexpr int G2_NSTAGE = 4;
constexpr size_t MB_ = 1024 * 1024;
__global__ __launch_bounds__(512, 2) __attribute__((aligned(4096))) void fused_kernel(const u16* __restrict__ x, const u16* __restrict__ wqkv,
                                                       const u16* __restrict__ wg, const u16* __restrict__ wu,
                                                       const u16* __restrict__ wd, const u16* __restrict__ wo,
                                                       u16* __restrict__ out, char* __restrict__ ws) {
  u16* const halo = (u16*)(ws + 4 * MB_);
  u16* const abuf = (u16*)(ws + 16 * MB_);
  char* const slab = ws + 64 * MB_;
  unsigned* const flags = (unsigned*)(ws + 96 * MB_);
  u32x4* const rstat = (u32x4*)(ws + 97 * MB_);
  char* const wcopy = ws + 98 * MB_;
  u64* const done1 = (u64*)(ws + 102 * MB_);
  u64* const done2 = (u64*)(ws + 100 * MB_ + 65536);
  u64* const donec = (u64*)(ws + 100 * MB_ + 131072);
  u64* const acks = (u64*)(ws + 100 * MB_ + 196608);
  float* const csum = (float*)(ws + 101 * MB_);
  __shared__ __attribute__((aligned(1024))) char shm[163840];
  constexpr int AUX = 2 * G1_STAGE + 8192;
  {
    u64 pc; int len;
    asm volatile("s_getpc_b64 %0\n.Lcode_beg:\n\ts_mov_b32 %1, .Lcode_end-.Lcode_beg" : "=s"(pc), "=s"(len));
    const int ol = ((blockIdx.x >> 3) + 32 * (threadIdx.x >> 6)) * 1024 + (threadIdx.x & 63) * 16;
    asm volatile("s_mov_b32 m0, %2\n\ts_nop 0\n\tglobal_load_lds_dwordx4 %0, %1"
                 :: "v"(ol < len - 16 ? ol : 0), "s"(pc & ~15ull), "s"((unsigned)(uintptr_t)(lds_cptr)shm + 2 * G1_STAGE + 7168) : "m0");
    __builtin_amdgcn_sched_barrier(0);
  }
  const u64 t_own = __builtin_amdgcn_s_memrealtime();
  TSDECL;
  TS1(7);
  const int tid = threadIdx.x, lane = tid & 63;
  const int wid = __builtin_amdgcn_readfirstlane(tid >> 6);
  const int wm = wid & 3, wc = wid >> 2;
  int bid_ = blockIdx.x;
  PIN_S(bid_);
  const int bid = bid_ & 255, xcd = bid & 7, idx = bid >> 3;
  const int ntile = xcd * 4 + (idx & 3), mtile = idx >> 2;
  const int m0 = mtile * 256;
  const int hg = ntile >> 1;
  const bool tya = (ntile & 1) == 0;
  const int srow = m0 + ntile * 8 + wm * 2;
  lds_cptr shm3 = (lds_cptr)shm;
  const unsigned shm_base = (unsigned)(uintptr_t)shm3;
  const int r8 = tid >> 3, pos = tid & 7;
  const int phys = pos >> 1, half = pos & 1;
  const int fk = ((r8 >> 1) & 1) | (((r8 >> 3) & 1) << 1);
  const int vsrc = phys ^ fk;
  const int qstr = tya ? 3072 * 2 : F_ * 2;
  unsigned voa = (((r8 & 32) + 2 * (r8 & 15) + ((r8 >> 4) & 1)) * D_ + ((pos ^ (r8 & 7)) * 8)) * 2;
  unsigned vog = r8 * (F_ * 2) + vsrc * 32 + half * 16, voq = r8 * qstr + vsrc * 32 + half * 16;
  unsigned vom = r8 * qstr + (vsrc >> (tya ? 0 : 1)) * 32 + half * 16;
  asm volatile("" : "+v"(voa), "+v"(vog), "+v"(voq), "+v"(vom));
  const int kt0 = (hg >> 1) * 7, he = hg & 1;
  unsigned oq = (tya ? hg : kt0 + 2 * he) * 128, og = (kt0 + (tya ? 5 + he : 2 * he + 1)) * 128, om = tya ? 4096 : 512 - 192 * he;
  unsigned d0 = shm_base + wid * 1024 + 32768, ox = m0 * D_ * 2;
  long uq = -32 * qstr;
  PIN_S(oq); PIN_S(og); PIN_S(om); PIN_S(d0); PIN_S(ox); PIN_S(uq);
  __builtin_amdgcn_sched_barrier(0);
  asm volatile("" :: "s"(x), "s"(wqkv), "s"(wg), "s"(wu), "s"(wd), "s"(wo), "s"(out), "s"(ws));
  const char* const ab = (const char*)x + ox;
  int kz = 0;
  asm volatile("" : "+s"(kz));
  TS1(12);
  u64 pq = (u64)wqkv, pg = (u64)wg;
  PIN_S(pq); PIN_S(pg);
  const char* const qb = (const char*)(tya ? pq : pg) + oq;
  if (wid < 4) {
#pragma unroll
    for (int i = 0; i < 8; ++i) glds16_s(ab + i * 32 * D_ * 2, voa, d0 - 32768 + i * 4096);
  } else {
    glds16_s(qb, voq, d0); glds16_s(qb + uq, voq, d0 - 4096);
  }
  __builtin_amdgcn_sched_barrier(0);
  const char* const gb = (const char*)wg + og;
  const long dgu = (const char*)wu - (const char*)wg, dp1 = tya ? 2048 : dgu;
  const char* const mb0 = qb + om;
  const char* mptr = mb0 + (((vsrc & 1) && !tya) ? dgu : 0) + vom;
  if (wid >= 4) {
    const long ug = kz - 32 * F_ * 2;
    glds16_s(qb + dp1, voq, d0 + 8192); glds16_s(qb + dp1 + uq, voq, d0 + 4096);
    glds16_v(mptr, d0 + 2 * 8192); glds16_v(mptr + uq, d0 + 2 * 8192 - 4096);
    glds16_s(gb, vog, d0 + 3 * 8192); glds16_s(gb + ug, vog, d0 + 3 * 8192 - 4096);
    glds16_s(gb + dgu, vog, d0 + 4 * 8192); glds16_s(gb + dgu + ug, vog, d0 + 4 * 8192 - 4096);
  }
  __builtin_amdgcn_sched_barrier(0);
  f32x4 acc[4][10];

  constexpr int NT = D_ / 64;
  constexpr int CNT = AUX + 4096 + 3584;
  if (tid < 8) *(unsigned*)(shm + CNT + tid * 4) = 0u;
  bf16x8 af0[4], af1[4];
  bf16x4 bl[4], bh[4];
#define SB0() __builtin_amdgcn_sched_barrier(0)
  auto mm = [&](int g, int t, bool two, bool z = false) {
    const int mi = t >> 1, mf = ((g & 1) && g != 9) ? 3 - mi : mi, ks = (t & 1) ^ (mi & 1), s = ((g + 1) & 1) * 2;
    const bf16x8 b0 = __builtin_shufflevector(bl[s + ks], bh[s + ks], 0, 1, 2, 3, 4, 5, 6, 7), a0 = ks ? af1[mf] : af0[mf];
    const bf16x8 b1 = __builtin_shufflevector(bl[s + 1 - ks], bh[s + 1 - ks], 0, 1, 2, 3, 4, 5, 6, 7), a1 = ks ? af0[mf] : af1[mf];
    const bool tr = (g >> 1) == 2;
    if (z && two) asm volatile("v_mfma_f32_16x16x32_bf16 %0, %1, %2, 0\n\tv_mfma_f32_16x16x32_bf16 %0, %3, %4, %0" : "=&v"(acc[mf][g])
                               : "v"(tr ? a0 : b0), "v"(tr ? b0 : a0), "v"(tr ? a1 : b1), "v"(tr ? b1 : a1));
    else if (z && !(t & 1)) asm volatile("v_mfma_f32_16x16x32_bf16 %0, %1, %2, 0" : "=&v"(acc[mf][g]) : "v"(tr ? a0 : b0), "v"(tr ? b0 : a0));
    else if (two) asm volatile("v_mfma_f32_16x16x32_bf16 %0, %1, %2, %0\n\tv_mfma_f32_16x16x32_bf16 %0, %3, %4, %0" : "+v"(acc[mf][g])
                          : "v"(tr ? a0 : b0), "v"(tr ? b0 : a0), "v"(tr ? a1 : b1), "v"(tr ? b1 : a1));
    else asm volatile("v_mfma_f32_16x16x32_bf16 %0, %1, %2, %0" : "+v"(acc[mf][g]) : "v"(tr ? a0 : b0), "v"(tr ? b0 : a0));
  };
  typedef __attribute__((address_space(3))) unsigned lds_u32;
  lds_u32* const cnt = (lds_u32*)(shm3 + CNT);
  bool cok;
  constexpr int NY = 28;
  int role, mt2, nt2;
  struct WS { unsigned vb[2][2]; const char* wb[2]; int sh[2]; };
  auto wsetup = [&](const u16* w, int crow0, int l, bool ck) __attribute__((always_inline)) {
    WS r;
    const unsigned vr = ((l >> 5) & 1) * 16 + (wm & 1) * 8 + ((l >> 3) & 3), vc = (((l >> 1) & 3) ^ ((l >> 4) & 1)) * 32 + (l & 1) * 16;
    u64 cp = (u64)(wcopy + (nt2 >> 2) * 589824 + crow0 * 128);
    asm volatile("" : "+s"(cp));
#pragma unroll
    for (int i = 0; i < 2; ++i) {
      u64 dp = (u64)(w + nt2 * 128 + i * 64);
      asm volatile("" : "+s"(dp));
      const bool slow = ck & ((((unsigned)dp >> 7) & 7) == 3);
      r.sh[i] = slow ? 7 : 11;
      r.wb[i] = (const char*)(slow ? cp : dp) + (crow0 ? (wm >> 1) * 32 << r.sh[i] : 0);
      r.vb[i][0] = (vr << r.sh[i]) + vc; r.vb[i][1] = ((vr + 4) << r.sh[i]) + (vc ^ 64);
    }
    asm volatile("" : "+v"(r.vb[0][0]), "+v"(r.vb[0][1]), "+v"(r.vb[1][0]), "+v"(r.vb[1][1]));
    PIN_S(r.wb[0]); PIN_S(r.wb[1]); PIN_S(r.sh[0]); PIN_S(r.sh[1]);
    return r;
  };
  WS wy, wz;
  int vxq, ws0, ws1;
  const char* sz;
  u32x4 xq[4];
  unsigned vg;
  int st = 3;
  const char *sa, *sw[2];
  auto wsel = [&]() {
    const unsigned r = __builtin_amdgcn_readlane(vg, st);
    sw[0] = wy.wb[0] + (r << wy.sh[0]); sw[1] = wy.wb[1] + (r << wy.sh[1]);
  };
  const int p = wid & 3;
  unsigned sl = 0;
#define G2_W "s_add_u32 m0, %[l], %[ow]\n\ts_nop 0\n\tglobal_load_lds_dwordx4 %[v0], %[w0] sc1\n\t" \
             "s_add_u32 m0, m0, 1024\n\ts_nop 0\n\tglobal_load_lds_dwordx4 %[v1], %[w0] sc1\n\t" \
             "s_add_u32 m0, m0, 7168\n\ts_nop 0\n\tglobal_load_lds_dwordx4 %[v2], %[w1] sc1\n\t" \
             "s_add_u32 m0, m0, 1024\n\ts_nop 0\n\tglobal_load_lds_dwordx4 %[v3], %[w1] sc1"
#define G2_WOP(w) [ow] "s"(shm_base + p * 2048), [w0] "s"(sw[0]), [w1] "s"(sw[1]), [v0] "v"(w.vb[0][0]), [v1] "v"(w.vb[0][1]), [v2] "v"(w.vb[1][0]), [v3] "v"(w.vb[1][1])
  auto tile2 = [&](int t0) __attribute__((always_inline)) {
    int b = bid;
    asm volatile("" : "+s"(b));
    role = 1 - ((b >> 2) & 1);
    mt2 = ((role ? b : b ^ 5) & 7) * 4 + ((b >> 3) & 3), nt2 = b >> 5;
    sa = (const char*)abuf + (size_t)(mt2 * 72 + role * NY + t0) * 16384 + wm * 4096;
    asm volatile("" : "+s"(sa));
  };
  auto prep = [&](bool first) __attribute__((always_inline)) {
    int l = threadIdx.x;
    asm volatile("" : "+v"(l));
    l &= 63;
    if (first) {
      tile2(3);
      vxq = ((mt2 * 128 + (wm >> 1) * 64 + role * 32 + 2 * (l & 15)) * D_ + nt2 * 128 + (wm & 1) * 64) * 2 + ((l >> 4) & 1) * 32 + (l >> 5) * 16;
      const int t = min(l < NY ? l : l - DLT, NY - 1), w = wm >> 1;
      const int o = (0xffaa5500fae504ull >> (2 * t)) & 3;
      int u1 = (0x990990 >> ((2 * t) & 31)) & 3, u2 = 3 + (t & 3);
      asm volatile("" : "+v"(u1), "+v"(u2));
      const int u = t < 12 ? u1 : u2;
      vg = (__umul24((l < NY ? role : 1 - role) * 4 + (((o & 1) << 1) | (o >> 1)), 14) + ((0xb85140a >> (4 * u)) & 15) + (u == 5 ? w : 2 * w)) * 32;
      asm volatile("" : "+v"(vg), "+v"(vxq));
      sz = (const char*)abuf + (size_t)(mt2 * 72 + 56) * 16384 + (wm >> 1) * 8192 + role * 4096 + (wm & 1) * 2048;
      PIN_S(sz);
    }
    wy = wsetup(wd, 0, l, first);
    wz = wsetup(wo, F_, l, first);
    ws0 = 64 << wz.sh[0]; PIN_S(ws0);
    ws1 = 64 << wz.sh[1]; PIN_S(ws1);
    SB0();
  };
  auto start2 = [&]() __attribute__((always_inline)) {
    const __amdgpu_buffer_rsrc_t rx = __builtin_amdgcn_make_buffer_rsrc((void*)x, 0, S_ * D_ * 2, 0x00020000);
#pragma unroll
    for (int f = 0; f < 4; ++f) xq[f] = __builtin_bit_cast(u32x4, __builtin_amdgcn_raw_buffer_load_b128(rx, vxq + (f >> 1) * 2048 + (f & 1) * 64, 0, 0));
    cok = __builtin_amdgcn_readfirstlane(cnt[1] & cnt[2]) != 0u;
    if (__builtin_expect(!cok, 0)) prep(false);
    wsel();
    PIN_S(sw[0]); PIN_S(sw[1]);
    SB0();
  };
  auto kloop = [&](auto wmc) {
    constexpr bool LD = decltype(wmc)::value != 0;
    constexpr int DMP = 2 * G1_STAGE;
    const char* pa = nullptr;
    unsigned vob = 0;
    f32x4 ca = {0.f, 0.f, 0.f, 0.f}, cb = {0.f, 0.f, 0.f, 0.f}, cd = ca;
    bf16x8 sel = {0, 0, 0, 0, 0, 0, 0, 0};
    int tro = 0;
    const int psh = (wid == 2 && !tya) ? 1 : 0, pstr = wid < 3 ? qstr : F_ * 2;
    const char* const pbase = wid == 0 ? qb : (wid == 1 ? qb + dp1 : (wid == 2 ? mb0 : gb));
    int lane = threadIdx.x;
    asm volatile("; %1" : "+v"(lane) : "n"(LD ? 1 : 0));
    lane &= 63;
    const int fr = lane & 15, fq = lane >> 4, q4 = fr >> 2, pp = lane & 3;
    const int ff = ((q4 >> 1) & 1) | ((fq & 1) << 1);
    int aoff[2], boff[2];
#pragma unroll
    for (int ks = 0; ks < 2; ++ks) aoff[ks] = (wm * 64 + fr) * 128 + (((ks * 4 + fq) ^ (fr & 7)) * 16);
#pragma unroll
    for (int j = 0; j < 2; ++j) boff[j] = 32768 + (8 * fq + q4) * 128 + (((2 * wc + j) ^ ff) * 32) + pp * 8;
    unsigned vol = lane * 16;
    float sm = 0.f, sq = 0.f;
    const char* pf = (const char*)wd + (bid * 4 + wid) * 7168;
    long pstep = 64 * pstr;
    if (!LD) {
      const int rs = 256 + mtile * 8 + (lane >> 3), vs = ((lane & 7) >> 1) ^ (((rs >> 1) & 1) | ((mtile & 1) << 1));
      pa = pbase + ((psh & vs) ? dgu : 0) + (rs * pstr + (vs >> psh) * 32 + (lane & 1) * 16);
      vob = rs * (F_ * 2) + vs * 32 + (lane & 1) * 16;
      tro = DMP + wid * 1024 + q4 * 128 + ((fq ^ ((q4 >> 1) | ((mtile & 1) << 1))) * 32) + pp * 8;
      const short one = fr == fq ? 0x3f80 : 0;
      sel = bf16x8{one, one, one, one, one, one, one, one};
      asm volatile("" : "+v"(tro), "+v"(vol), "+v"(sel), "+v"(ca), "+v"(cb), "+v"(sm), "+v"(sq));
      PIN_S(pf); PIN_S(pstep);
    }
    auto accum = [&](lds_cptr p, int ob) {
      ca = __builtin_amdgcn_mfma_f32_16x16x32_bf16(tr_frag(p), sel, ca, 0, 0, 0);
      if (wid == 0) cb = __builtin_amdgcn_mfma_f32_16x16x32_bf16(tr_frag(p + ob), sel, cb, 0, 0, 0);
    };
    auto xrow = [&](int i) {
      u64 a = (u64)(x + (size_t)srow * D_ + i * 256), b = (u64)(wo + (bid * 4 + wid) * 1024);
      PIN_S(a); PIN_S(b);
      asm volatile("global_load_dwordx4 %0, %1, %2" : "+v"(cd) : "v"(vol + (vol & (i < 4 ? 512 : 0)) * 3), "s"(i < 4 ? a : b) : "memory");
    };
    const int q32 = 32 * qstr;
    const char *sq0 = qb + q32, *sq1 = sq0 + dp1, *sg0 = gb + 32 * F_ * 2, *sg1 = sg0 + dgu, *sx = ab + (long)(kz + 128 - 32 * D_ * 2);
    u32x2 mp[2] = {__builtin_bit_cast(u32x2, mptr + q32), __builtin_bit_cast(u32x2, mptr + 2 * q32)};
    u64 cy[2];
    unsigned dst = shm_base + G1_STAGE + wm * 1024;
    int dbuf = G1_STAGE;
    auto stage = [&](int i0, int i1) __attribute__((always_inline)) {
#pragma unroll
        for (int i = i0; i < i1; ++i) {
          const int q = i >> 1, h = i & 1;
          const unsigned ld = dst + h * 4096 + (q < 5 ? 32768 + q * 8192 : (q - 5) * 8192);
          if (q < 2) glds16_s((q ? sq1 : sq0) + h * q32, voq, ld);
          else if (q == 2) glds16_v(__builtin_bit_cast(const char*, mp[h]), ld);
          else if (q < 5) glds16_s((q == 4 ? sg1 : sg0) + h * 32 * F_ * 2, vog, ld);
          else glds16_s(sx + ((q - 5) * 64 + h * 32) * D_ * 2, voa, ld);
        }
    };
    u64 cy0 = 0, rt0 = 0;
#pragma unroll
    for (int a = 0; a < 4; ++a) { acc[a][9] = f32x4{0.f, 0.f, 0.f, 0.f}; asm volatile("" : "+v"(acc[a][9])); }
    if (!LD) {
      xrow(0);
      WAIT_V(1);
    }
    asm volatile("" : "+v"(aoff[0]), "+v"(aoff[1]), "+v"(boff[0]), "+v"(boff[1]));
    bf16x8 kp[2];
    auto body = [&](bool upd, bool keep = false, bool first = false) __attribute__((always_inline)) {
      lds_cptr sb = shm3;
      auto rdb = [&](int g, int i) {
        lds_cptr p = sb + boff[g & 1] + (g >> 1) * 8192 + (i >> 1) * 4096;
        const int s = ((g + 1) & 1) * 2 + (i >> 1);
        if (i & 1) bh[s] = tr_read(p + 512); else bl[s] = tr_read(p);
      };
      auto rda = [&](int ks, int mf) { (ks ? af1 : af0)[mf] = *(const __attribute__((address_space(3))) bf16x8*)(sb + aoff[ks] + mf * 2048); };
      SB0();
      __builtin_amdgcn_s_waitcnt(0xC07F);
#pragma unroll
      for (int gg = 0; gg < 10; ++gg) {
        const int g = (gg + 9) % 10;
#pragma unroll
        for (int t = 0; t < 8; ++t) {
          const bool two = t >= 4 && !(upd && g >= 5 && g <= 8);
          SB0();
          if ((!two || (t & 1)) && !(first && g == 9)) mm(g, two ? t - 1 : t, two, first);
          SB0();
          if (first && !LD && (g == 3 || g == 7) && t == 0) BAR();
          if (g == 9) {
            if (t == 0 || t == 2) { rdb(0, t); rdb(0, t + 1); }
            if (t & 1) { rda((t >> 1) & 1, t >> 1); rda(((t >> 1) & 1) ^ 1, t >> 1); }
          } else if (t < 4) rdb(g + 1, t);
          else if (keep && g == 7) {
            if (t == 5) { WAIT_V(0); kp[0] = tr_frag(shm3 + tro); }
            if (t == 7) kp[1] = tr_frag(shm3 + tro + 4096);
          } else if (upd) {
            if (g == 5 && t == 4) { sq0 += 2 * q32; PIN_S(sq0); }
            if (g == 5 && t == 5) { sq1 += 2 * q32; PIN_S(sq1); }
            if (g == 5 && t == 6) { sg0 += 64 * F_ * 2; PIN_S(sg0); }
            if (g == 6 && t == 4) { sg1 += 64 * F_ * 2; PIN_S(sg1); }
            if (g == 6 && t == 5) { sx += 128; PIN_S(sx); }
            if (g == 6 && t == 6) { dst -= dbuf; PIN_S(dst); }
            if (g == 7 && t == 4) asm volatile("v_add_co_u32 %0, %2, %4, %0\n\tv_add_co_u32 %1, %3, %4, %1" : "+v"(mp[0][0]), "+v"(mp[1][0]), "=&s"(cy[0]), "=&s"(cy[1]) : "s"(2 * q32));
            if (g == 7 && t == 6) asm volatile("v_addc_co_u32 %0, vcc, 0, %0, %2\n\tv_addc_co_u32 %1, vcc, 0, %1, %3" : "+v"(mp[0][1]), "+v"(mp[1][1]) : "s"(cy[0]), "s"(cy[1]) : "vcc");
            if (g == 8 && t == 4) { aoff[0] += dbuf; aoff[1] += dbuf; asm volatile("" : "+v"(aoff[0]), "+v"(aoff[1])); }
            if (g == 8 && t == 5) { boff[0] += dbuf; boff[1] += dbuf; asm volatile("" : "+v"(boff[0]), "+v"(boff[1])); }
            if (g == 8 && t == 6) { dbuf = -dbuf; PIN_S(dbuf); }
          }
        }
      }
      SB0();
    };
    TS1(0);
#define ARRP(o, c) do {} while (0)
    if (LD) {
      stage(0, PA);
      WAIT_V(6 + PA);
      TS1(5);
      BARL();
      stage(PA, PB);
      WAIT_V(2 + PB);
      BAR();
      stage(PB, PC);
      WAIT_V(PC);
      BAR();
      stage(PC, 18);
      body(true, false, true);
      WAIT_V(0);
      BARL();
#pragma clang loop unroll(disable)
      for (int kt = 1; kt < NT - 1; ++kt) {
        if (__builtin_expect(kt == 5 + wid, 0)) prep(true);
        stage(0, 18);
        body(true);
        WAIT_V(0);
        BARL();
        SB0();
      }
      start2();
      body(false);
    } else {
      char* wsx;
      auto tile = [&](int kt, bool keep) __attribute__((always_inline)) {
        SB0();
        ARRP(wid * 64 + kt * 4, true);
        BARL();
        body(false, keep);
        ARRP(320 + kt * 4 + 4, wid == 0);
        if (keep) asm volatile("" : "+v"(kp[0]), "+v"(kp[1])); else WAIT_V(0);
      };
      auto next = [&]() __attribute__((always_inline)) {
        aoff[0] += dbuf; aoff[1] += dbuf; boff[0] += dbuf; boff[1] += dbuf;
        dbuf = -dbuf;
        asm volatile("" : "+v"(aoff[0]), "+v"(aoff[1]), "+v"(boff[0]), "+v"(boff[1]), "+v"(vol), "+v"(tro) :: "memory");
        wsx = ws;
        PIN_S(wsx);
      };
      auto share = [&]() {
        glds16_v(pa, shm_base + DMP + wid * 1024);
        if (wid == 0) glds16_s(gb + dgu, vob, shm_base + DMP + 4096);
      };
      auto kept = [&]() {
        ca = __builtin_amdgcn_mfma_f32_16x16x32_bf16(kp[0], sel, ca, 0, 0, 0);
        if (wid == 0) cb = __builtin_amdgcn_mfma_f32_16x16x32_bf16(kp[1], sel, cb, 0, 0, 0);
      };
      auto cpub = [&](u32x4 a, u32x4 b) {
        if ((vol & 0xc0) == 0) {
          const char* const cs = wsx + 101 * MB_ + ntile * 10240 + mtile * 256;
          const unsigned co = (vol & 0x30) * 4 + (vol >> 8) * 16;
          gst16(cs + wid * 2048, co, a);
          if (wid == 0) gst16(cs + 8192, co, b);
        }
      };
      auto side0 = [&](int kt) __attribute__((always_inline)) {
        if (kt < 2) {
          glds16_v(pa - 2 * pstep, shm_base + DMP + 7168);
          if (wid == 0) glds16_s(gb + dgu, vob - 128 * F_ * 2, shm_base + DMP + 7168);
        }
        share();
        u32x4 xw;
        asm volatile("v_mov_b32 %0, %4\n\tv_mov_b32 %1, %5\n\tv_mov_b32 %2, %6\n\tv_mov_b32 %3, %7" : "=&v"(xw[0]), "=&v"(xw[1]), "=&v"(xw[2]), "=&v"(xw[3])
                     : "v"(cd[0]), "v"(cd[1]), "v"(cd[2]), "v"(cd[3]));
        xrow(kt + 1);
        next();
        if (kt) kept();
        pa += pstep; vob += 64 * F_ * 2;
        accum(shm3 + tro + ((kt & 1) * G1_STAGE + 32768 + mtile * 1024 + wid * 7168 - DMP), 4 * 8192);
#pragma unroll
        for (int i = 0; i < 4; ++i) {
          float lo, hi;
          asm volatile("v_lshlrev_b32 %2, 16, %4\n\tv_and_b32 %3, 0xffff0000, %4\n\tv_add_f32 %0, %0, %2\n\tv_fmac_f32 %1, %2, %2\n\t"
                       "v_add_f32 %0, %0, %3\n\tv_fmac_f32 %1, %3, %3" : "+v"(sm), "+v"(sq), "=&v"(lo), "=&v"(hi) : "v"(xw[i]));
        }
      };
      BARL();
      body(false, false, true);
      WAIT_V(0);
      side0(0);
#pragma clang loop unroll(disable)
      for (int kt = 1; kt < 4; ++kt) {
        tile(kt, true);
        side0(kt);
      }
#pragma clang loop unroll(disable)
      for (int kt = 4; kt < 12; ++kt) {
        tile(kt, true);
        share();
        glds16_s(pf, vol, shm_base + DMP + 7168);
        next();
        kept();
        pa += pstep; vob += 64 * F_ * 2;
        pf = kt == 10 ? (const char*)wo + (bid * 4 + wid) * 2048 + 1024 : pf + 1024;
        if (kt < 8) {
          const unsigned q1 = bid * 4 + wid + (kt - 5) * 1024, q0 = q1 - 1024;
          if (q0 < 1152) gst16(wsx + 98 * MB_ + (q0 & 1) * 589824 + (q0 >> 1) * 1024, vol, *(const u32x4*)(shm + AUX + wid * 1024 + vol));
          if (q1 < 1152) {
            const char* src = (const char*)((q1 >> 1) < 448 ? wd + (size_t)(q1 >> 1) * 8 * D_ : wo + (size_t)((q1 >> 1) - 448) * 8 * D_);
            src += ((3 - ((unsigned)(uintptr_t)src >> 7)) & 7) * 128 + (q1 & 1) * 1024;
            glds16_s(src, (vol >> 7) * 2048 + (vol & 0x70), shm_base + AUX + wid * 1024);
          }
          u32x4 bk;
          asm volatile("v_mov_b32 %0, %1" : "=v"(bk[0]) : "s"(BLANK));
          bk[1] = bk[0]; bk[2] = bk[0]; bk[3] = bk[0];
          gst16(wsx + 64 * MB_ + bid * 16384 + wid * 4096 + (kt - 4) * 1024, vol, bk);
          if (tya && (kt == 4 || kt == 7)) gst16(wsx + 4 * MB_ + (mtile * 32 + ntile) * 4096 + wid * 2048 + (kt & 1) * 1024, vol, bk);
          if (kt == 4) cpub(bk, bk);
        } else {
          if (kt == 8) {
            const float ml = sm * (1.0f / 32), mean = half_sum(sm) * (1.0f / D_), dm = ml - mean;
            const float q = __builtin_fmaf(dm * 32.0f, dm, __builtin_fmaf(-ml, sm, sq));
            const unsigned ps = pack_stats(mean, fmaxf(half_sum(q) * (1.0f / D_), 0.f));
            const u64 now = __builtin_amdgcn_s_memrealtime();
            if ((vol & 0x1f0) == 0) gst16(wsx + 97 * MB_ + srow * 16, vol >> 5, u32x4{ps, (unsigned)now, ps, (unsigned)(now >> 32)});
          }
          if (kt == 9) {
            if (wid == 0) {
              u64 now;
              asm volatile("s_memrealtime %0\n\ts_waitcnt lgkmcnt(0)" : "=s"(now) :: "memory");
              if (vol == 0) asm volatile("global_store_dwordx2 %0, %1, %2 sc1" :: "v"(vol), "v"(now), "s"(wsx + 100 * MB_ + 131072 + bid * 8) : "memory");
            }
            if (wid == 2) glds16_s1(wsx + 102 * MB_ + (bid & ~1) * 512, vol, shm_base + DMP + 7168);
          }
        }
      }
      asm volatile("" :: "v"(cd));
      if (wid >= 2) glds16_s1(wsx + 100 * MB_ + 131072 + (wid - 2) * 1024, vol, shm_base + DMP + 3072 + wid * 1024);
      tile(12, true);
      next();
      kept();
      const int lane = vol >> 4;
      glds16_s1(wsx + 97 * MB_ + (m0 + wid * 64) * 16, ((lane & 32) + 2 * (lane & 15) + ((lane >> 4) & 1)) * 16, shm_base + AUX + wid * 1024);
      asm volatile("s_nop 7\n\ts_nop 3" : "+v"(ca), "+v"(cb));
      cpub(__builtin_bit_cast(u32x4, ca), __builtin_bit_cast(u32x4, cb));
      if (wid >= 2) {
        u32x4 c4 = *(const u32x4*)(shm + DMP + 3072 + wid * 1024 + vol);
        bool ok = __all(fresh(((u64)c4[1] << 32) | c4[0], t_own) & fresh(((u64)c4[3] << 32) | c4[2], t_own));
        while (__builtin_expect(!ok, 0) && __builtin_amdgcn_s_memrealtime() - t_own < TIMEOUT_ / 10) {
          const __amdgpu_buffer_rsrc_t rs = __builtin_amdgcn_make_buffer_rsrc((void*)(wsx + 100 * MB_ + 131072), 0, 2048, 0x00020000);
          asm volatile("" ::: "memory");
          c4 = __builtin_bit_cast(u32x4, __builtin_amdgcn_raw_buffer_load_b128(rs, (wid - 2) * 1024 + vol, 0, 16));
          ok = __all(fresh(((u64)c4[1] << 32) | c4[0], t_own) & fresh(((u64)c4[3] << 32) | c4[2], t_own));
        }
        *(unsigned*)(shm + CNT - 4 + wid * 4) = ok ? 1u : 0u;
      }
      tile(13, false);
      next();
      u32x4 cw[4];
      auto cgather = [&]() {
        const __amdgpu_buffer_rsrc_t rc = __builtin_amdgcn_make_buffer_rsrc((void*)(wsx + 101 * MB_ + ntile * 10240), 0, 5 * 2048, 0x00020000);
        const unsigned vcs = (vol < 512 ? wid * 2048 : 8192) + (vol & 256) * 4 + (vol & 0xf0);
#pragma unroll
        for (int i = 0; i < 4; ++i) cw[i] = __builtin_bit_cast(u32x4, __builtin_amdgcn_raw_buffer_load_b128(rc, vcs + i * 256, 0, 16));
      };
      {
        const int ln = wid * 64 + (vol >> 4);
        u32x4 gr = *(const u32x4*)(shm + AUX + ln * 16);
        if (__builtin_expect(!__all((gr[0] == gr[2]) & fresh(((u64)gr[3] << 32) | gr[1], t_own)), 0)) {
          const __amdgpu_buffer_rsrc_t rs = __builtin_amdgcn_make_buffer_rsrc((void*)(wsx + 97 * MB_ + m0 * 16), 0, 4096, 0x00020000);
          const int prow = (ln & 0xe0) + 2 * (ln & 15) + ((ln >> 4) & 1);
          for (;;) {
            gr = __builtin_bit_cast(u32x4, __builtin_amdgcn_raw_buffer_load_b128(rs, prow * 16, 0, 16));
            if (__all((gr[0] == gr[2]) & fresh(((u64)gr[3] << 32) | gr[1], t_own))) break;
            if (__builtin_amdgcn_s_memrealtime() - t_own > TIMEOUT_) { gr[0] = row_stats_serial(x + (size_t)(m0 + prow) * D_); break; }
            __builtin_amdgcn_s_sleep(2);
          }
        }
        cgather();
        const float nm = -lo_f(gr[0]), dn = hi_f(gr[0]), r = __builtin_amdgcn_rcpf(dn);
        auto mul = [](float a, float b) { float c; asm("v_mul_f32 %0, %1, %2" : "=v"(c) : "v"(a), "v"(b)); return c; };
        const f32x4 fc = {nm, -2.3022081986f * r, mul(mul(mul(-0.10294324f, r), r), r), mul(dn, dn)};
        *(f32x4*)(shm + AUX + ln * 16) = fc;
        float* const tw = (float*)(shm + 2 * G1_STAGE + 4096 + wid * 768) + ln;
#pragma unroll
        for (int i = 0; i < 4; ++i) tw[i * 64] = fc[i];
        ((float*)(shm + AUX + 4096))[384 + ln] = r;
        cok = __builtin_amdgcn_readfirstlane(cnt[1] & cnt[2]) != 0u;
        tile2(0);
        wy = wsetup(wd, 0, vol >> 4, cok);
      }
      tile(14, false);
      next();
      {
        while (__builtin_expect(blank(cw) && cok, 0)) {
          __builtin_amdgcn_s_sleep(2);
          asm volatile("" ::: "memory");
          cgather();
        }
        f32x4 t;
#pragma unroll
        for (int i = 0; i < 4; ++i) {
          float a, b;
          asm("v_add_f32 %0, %2, %3\n\tv_add_f32 %1, %4, %5\n\tv_add_f32 %0, %0, %1" : "=&v"(a), "=&v"(b) : "v"(cw[0][i]), "v"(cw[1][i]), "v"(cw[2][i]), "v"(cw[3][i]));
          const auto r2 = __builtin_amdgcn_permlane16_swap(__float_as_uint(a), __float_as_uint(a), false, false);
          asm("v_add_f32 %0, %1, %2" : "=v"(t[i]) : "v"(r2[0]), "v"(r2[1]));
        }
        const int l = vol >> 4;
        if (__builtin_expect(!cok, 0)) {
          const int gq = (l >> 2) & 3;
          const char* pc = l < 32 ? pbase + ((psh & gq) ? dgu : 0) + (gq >> psh) * 32 + (l & 3) * 8 : gb + dgu + (l & 15) * 8;
          t = f32x4{0.f, 0.f, 0.f, 0.f};
#pragma unroll 1
          for (int k = 0; k < D_; ++k) { const u32x2 w = *(const __attribute__((address_space(1))) u32x2*)(pc + (size_t)k * (l < 32 ? pstr : F_ * 2)); t += f32x4{lo_f(w[0]), hi_f(w[0]), lo_f(w[1]), hi_f(w[1])}; }
        }
        if (l < 32 || wid == 0)
          *(f32x4*)((float*)(shm + AUX + 4096) + ((l >> 3) & 1) * 192 + (l < 32 ? wid : 4) * 32 + (l & 7) * 4) = t;
        __builtin_amdgcn_s_setprio(2);
      }
      SB0();
      ARRP(wid * 64 + 60, true);
      BARL();
      body(false);
    }
#pragma unroll
    for (int t = 0; t < 8; t += 2) mm(9, t, true);
    asm volatile("s_nop 7\n\ts_nop 3");
    SB0();
    TS1(3);
    if (!LD) {
#pragma unroll
      for (int i = 0; i < 3; ++i) {
        const unsigned r = ((role * 4 + (i & 1) * 2) * 14 + (i < 2 ? 10 : 0) + (wm & 2)) * 32;
        sw[0] = wy.wb[0] + (r << wy.sh[0]); sw[1] = wy.wb[1] + (r << wy.sh[1]);
        asm volatile(G2_W :: [l] "s"((i + 1) * 16384), G2_WOP(wy) : "memory", "scc", "m0");
      }
    } else {
      __builtin_amdgcn_s_setprio(3);
      WAIT_V(0);
    }
  };
  if (wid >= 4) kloop(std::integral_constant<int, 1>{});
  else kloop(std::integral_constant<int, 0>{});
  __builtin_amdgcn_sched_barrier(0);
  int tz = threadIdx.x;
  asm volatile("" : "+v"(tz));
  __builtin_amdgcn_sched_barrier(0);
  const int li = tz & 15, lq = (tz >> 4) & 3, ln = tz & 63;
  const __amdgpu_buffer_rsrc_t ra = __builtin_amdgcn_make_buffer_rsrc((void*)abuf, 0, 16 * 72 * 16384, 0x00020000);
  float* const cfin = (float*)(shm + AUX + 4096);
  float* const svr = cfin + 384;
  float* const tb = (float*)(shm + 2 * G1_STAGE + 4096 + wm * 1024);
  auto signal = [&](unsigned v) {
    unsigned a, b;
    asm volatile("v_mov_b32 %0, %2\n\tv_mov_b32 %1, %3\n\ts_mov_b64 exec, 1\n\tds_add_u32 %0, %1\n\ts_mov_b64 exec, -1"
                 : "=&v"(a), "=&v"(b) : "s"(shm_base + CNT), "s"(v) : "memory");
  };
  auto await = [&](unsigned mask, unsigned val) {
    unsigned a, t;
    asm volatile("v_mov_b32 %0, %2\n1:\n\tds_read_b32 %1, %0\n\ts_waitcnt lgkmcnt(0)\n\tv_readfirstlane_b32 vcc_lo, %1\n\ts_and_b32 vcc_lo, vcc_lo, %3\n\t"
                 "s_cmp_eq_u32 vcc_lo, %4\n\ts_cbranch_scc1 2f\n\ts_sleep 1\n\ts_branch 1b\n2:" : "=&v"(a), "=&v"(t) : "s"(shm_base + CNT), "s"(mask), "s"(val) : "memory", "scc", "vcc");
  };
  auto client = [&](int l) { const int m128 = mtile * 2 + ((l >> 4) & 1); return (((l & 7) * 4 + (m128 & 3)) * 8) + ((m128 >> 2) ^ ((l & 8) ? 5 : 0)); };
  auto notify = [&](int l0, int nl) __attribute__((always_inline)) {
    u64 now;
    asm volatile("s_memrealtime %0\n\ts_waitcnt lgkmcnt(0)" : "=s"(now) :: "memory");
    int l = threadIdx.x;
    asm volatile("" : "+v"(l));
    l &= 63;
    const __amdgpu_buffer_rsrc_t rd1 = __builtin_amdgcn_make_buffer_rsrc((void*)(done1 + ntile), 0, 256 * 512, 0x00020000);
    if (l < nl) __builtin_amdgcn_raw_buffer_store_b64(u32x2{(unsigned)now, (unsigned)(now >> 32)}, rd1, client(l0 + l) * 512, 0, 16);
  };
  const int xo = ((xcd & 1) << 1) | ((xcd >> 1) & 1), te = (xcd >> 2) * 28 + 6 * (xo >> 1) + (xo & 1), tl = (xcd >> 2) * 28 + 12 + 4 * xo;
  const u64 te0 = __builtin_amdgcn_s_memrealtime();
  const __amdgpu_buffer_rsrc_t re = __builtin_amdgcn_make_buffer_rsrc((void*)(done1 + client((wm >> 1) * 16 + 8 - (xcd & 4) * 2) * 64 + 32 + (ntile & 15) * 2 + (wm & 1)),
                                                                      0, 7 * 16384 + 8, 0x00020000);
  auto early = [&]() {
    __builtin_amdgcn_raw_buffer_store_b64(u32x2{(unsigned)te0, (unsigned)(te0 >> 32)}, re, ln * 16384, 0, 16);
  };
  signal(1u << (8 * wc));
  if (wc) {
    await(0xffffu, 0x404u);
#pragma unroll
    for (int f = 0; f < 4; ++f) *(u32x4*)(shm + G2_X + wm * 4096 + f * 1024 + ln * 16) = xq[f];
    SB0();
    __builtin_amdgcn_s_setprio(1);
  }
  TS1(4);
  f32x4 rf[4];
#pragma unroll
  for (int mf = 0; mf < 4; ++mf) rf[mf] = *(const f32x4*)(shm + AUX + (wm * 64 + mf * 16 + li) * 16);
  const float* const cl = cfin + wc * 192 + lq * 4;
  const int sbase = ((mtile * 2 + (wm >> 1)) * 72 * 8 + (wm & 1) * 4) * 2048;
  auto putf = [&](int kg, int mf, u32x4 v) {
    __builtin_amdgcn_raw_buffer_store_b128(v, ra, ln * 16 + sbase + ((kg >> 1) * 8 + mf) * 2048 + (kg & 1) * 1024, 0, 16);
  };
  auto gelp = [&](f32x2 g, f32x2 u, f32x2 cg, f32x2 cu, f32x2 m2, f32x2 k12, f32x2 k22, f32x2 d2) {
    const f32x2 tg = __builtin_elementwise_fma(m2, cg, g), tu = __builtin_elementwise_fma(m2, cu, u);
    const f32x2 arg = tg * __builtin_elementwise_fma(tg * tg, k22, k12);
    const f32x2 dn = __builtin_elementwise_fma(f32x2{__builtin_amdgcn_exp2f(arg[0]), __builtin_amdgcn_exp2f(arg[1])}, d2, d2);
    const f32x2 r = (tg * tu) * f32x2{__builtin_amdgcn_rcpf(dn[0]), __builtin_amdgcn_rcpf(dn[1])};
    return pack2(r[0], r[1]);
  };
  auto gel2 = [&](int p, int kg, int mf0 = 0, int mf1 = 4) {
    f32x4 cg[2], cu[2];
#pragma unroll
    for (int j = 0; j < 2; ++j) { cg[j] = *(const f32x4*)(cl + p * 32 + j * 16); cu[j] = *(const f32x4*)(cl + p * 32 + 32 + j * 16); }
#pragma unroll
    for (int mf = mf0; mf < mf1; ++mf) {
      const f32x2 m2 = {rf[mf][0], rf[mf][0]}, k12 = {rf[mf][1], rf[mf][1]}, k22 = {rf[mf][2], rf[mf][2]}, d2 = {rf[mf][3], rf[mf][3]};
      u32x4 o;
#pragma unroll
      for (int i = 0; i < 4; ++i) {
        const int j = i >> 1, e = (i & 1) * 2;
        const f32x4 g = acc[mf][p * 2 + j], u = acc[mf][p * 2 + 2 + j];
        o[i] = gelp(f32x2{g[e], g[e + 1]}, f32x2{u[e], u[e + 1]}, f32x2{cg[j][e], cg[j][e + 1]}, f32x2{cu[j][e], cu[j][e + 1]}, m2, k12, k22, d2);
      }
      putf(kg, mf, o);
    }
  };
  int tsel = ntile & 1;
  asm volatile("" : "+s"(tsel));
  if (tsel != 0) {
    const int tq = (wc ? tl : te + 2 + (xo & 1)) + he;
    gel2(0, 2 * tq);
    gel2(3, 2 * tq + 1);
    const float cg = cfin[wc * 192 + 64 + li], cu = cfin[wc * 192 + 80 + li];
#pragma unroll
    for (int mf = 0; mf < 4; ++mf) {
      f32x4 t[4];
#pragma unroll
      for (int i = 0; i < 4; ++i) t[i] = *(const f32x4*)(tb + i * 64 + mf * 16 + lq * 4);
      const f32x4 g = acc[mf][4], u = acc[mf][5];
      u32x2 o;
#pragma unroll
      for (int e = 0; e < 4; e += 2)
        o[e >> 1] = gelp(f32x2{g[e], g[e + 1]}, f32x2{u[e], u[e + 1]}, f32x2{cg, cg}, f32x2{cu, cu}, f32x2{t[0][e], t[0][e + 1]},
                         f32x2{t[1][e], t[1][e + 1]}, f32x2{t[2][e], t[2][e + 1]}, f32x2{t[3][e], t[3][e + 1]});
      *(u32x2*)(shm + wm * 4096 + mf * 1024 + wc * 512 + li * 32 + lq * 8) = o;
      if (mf == EPOS && wc == 0) {
        TS1(1);
        WAIT_V(0);
        TS1(6);
        early();
      }
    }
    if (wc == 0) {
      signal(0x10000u << wid);
    } else {
      await(0x1000u << wid, 0x1000u << wid);
#pragma unroll
      for (int mf = 0; mf < 4; ++mf) putf(2 * (tl + 2) + he, mf, __builtin_bit_cast(u32x4, tr_frag(shm3 + wm * 4096 + mf * 1024 + ln * 8)));
    }
  } else {
    float rd[4];
#pragma unroll
    for (int mf = 0; mf < 4; ++mf) rd[mf] = svr[wm * 64 + mf * 16 + li];
    auto cvq = [&](int p, int mf, const f32x4& c0, const f32x4& c1) {
      u32x4 o;
#pragma unroll
      for (int j = 0; j < 2; ++j) {
        const f32x4 a = acc[mf][p * 2 + j], c4 = j ? c1 : c0;
        const f32x2 m2 = {rf[mf][0], rf[mf][0]}, r2 = {rd[mf], rd[mf]};
        f32x2 t0 = __builtin_elementwise_fma(m2, f32x2{c4[0], c4[1]}, f32x2{a[0], a[1]});
        f32x2 t1 = __builtin_elementwise_fma(m2, f32x2{c4[2], c4[3]}, f32x2{a[2], a[3]});
        if (p) { t0 *= r2; t1 *= r2; }
        o[2 * j] = pack2(t0[0], t0[1]); o[2 * j + 1] = pack2(t1[0], t1[1]);
      }
      return o;
    };
    const float cv0 = cfin[wc * 192 + 64 + li], cv1 = cfin[wc * 192 + 80 + li];
    auto cvv = [&](int mf) {
      const f32x4 n4 = *(const f32x4*)(tb + mf * 16 + lq * 4), r4 = *(const f32x4*)(svr + wm * 64 + mf * 16 + lq * 4);
      u32x4 o;
#pragma unroll
      for (int j = 0; j < 2; ++j) {
        const f32x4 a = acc[mf][4 + j];
        const f32x2 c2 = {j ? cv1 : cv0, j ? cv1 : cv0};
        const f32x2 t0 = __builtin_elementwise_fma(f32x2{n4[0], n4[1]}, c2, f32x2{a[0], a[1]}) * f32x2{r4[0], r4[1]};
        const f32x2 t1 = __builtin_elementwise_fma(f32x2{n4[2], n4[3]}, c2, f32x2{a[2], a[3]}) * f32x2{r4[2], r4[3]};
        o[2 * j] = pack2(t0[0], t0[1]); o[2 * j + 1] = pack2(t1[0], t1[1]);
      }
      return o;
    };
    u32x4 kx[4], vx[4], qx[4], hx[4];
    const bf16x4 ones = {0x3f80, 0x3f80, 0x3f80, 0x3f80};
    const f32x4 ck0 = *(const f32x4*)(cl + 32), ck1 = *(const f32x4*)(cl + 48);
    kx[2] = cvq(1, 2, ck0, ck1); kx[3] = cvq(1, 3, ck0, ck1);
    vx[2] = cvv(2); vx[3] = cvv(3);
    auto exo = [](int e) { return e * 4096 + (e >> 2) * (G2_A - 16384); };
    const bool needh = wm == 0 && mtile > 0;
    const __amdgpu_buffer_rsrc_t rh = __builtin_amdgcn_make_buffer_rsrc((void*)((char*)halo + ((needh ? mtile - 1 : mtile) * 32 + ntile + wc) * 4096), 0,
                                                                        needh || wm == 3 ? 4096 : 0, 0x00020000);
    if (wm < 3) {
      char* const ex = shm + exo(wc * 3 + wm) + ln * 16;
      *(u32x4*)ex = kx[2]; *(u32x4*)(ex + 1024) = kx[3]; *(u32x4*)(ex + 2048) = vx[2]; *(u32x4*)(ex + 3072) = vx[3];
      signal(0x10000u << wid);
    } else {
#pragma unroll
      for (int r = 0; r < 4; ++r) __builtin_amdgcn_raw_buffer_store_b128(r < 2 ? kx[2 + r] : vx[r], rh, ln * 16 + r * 1024, 0, 16);
    }
    auto hload = [&]() {
#pragma unroll
      for (int r = 0; r < 4; ++r) hx[r] = __builtin_bit_cast(u32x4, __builtin_amdgcn_raw_buffer_load_b128(rh, ln * 16 + r * 1024, 0, 16));
    };
    SB0();
    gel2(3, 2 * (wc ? tl + 3 : te) + he);
    SB0();
    if (wm == 0) hload();
    SB0();
    kx[0] = cvq(1, 0, ck0, ck1); kx[1] = cvq(1, 1, ck0, ck1);
    vx[0] = cvv(0); vx[1] = cvv(1);
    {
      const f32x4 cq0 = *(const f32x4*)(cl), cq1 = *(const f32x4*)(cl + 16);
#pragma unroll
      for (int mf = 0; mf < 4; ++mf) qx[mf] = cvq(0, mf, cq0, cq1);
    }
    f32x4 mk0, mk1;
    u32x2 pm;
#pragma unroll
    for (int j = 0; j < 4; ++j) {
      const int d = 4 * lq + j - li;
      mk0[j] = d >= 9 ? 0.f : -1.0e30f;
      mk1[j] = (unsigned)(d + 7) <= 7u ? 0.f : -1.0e30f;
    }
#pragma unroll
    for (int j = 0; j < 2; ++j) pm[j] = (4 * lq + 2 * j > li ? 0xffffu : 0u) | (4 * lq + 2 * j + 1 > li ? 0xffff0000u : 0u);
    auto max3 = [](float a, float b, float c) { float r; asm("v_max3_f32 %0, %1, %2, %3" : "=v"(r) : "v"(a), "v"(b), "v"(c)); return r; };
    auto max2 = [](float a, float b) { float r; asm("v_max_f32 %0, %1, %2" : "=v"(r) : "v"(a), "v"(b)); return r; };
    asm volatile("" : "+v"(kx[0]), "+v"(kx[1]), "+v"(vx[0]), "+v"(vx[1]), "+v"(qx[0]), "+v"(qx[1]), "+v"(qx[2]), "+v"(qx[3]), "+v"(mk0), "+v"(mk1), "+v"(pm));
    TS1(1);
    if (wc == 0) {
      if (wm == 0) WAIT_V(4); else WAIT_V(0);
      early();
    }
    if (wm > 0) {
      await(0x8000u << wid, 0x8000u << wid);
#pragma unroll
      for (int i = 0; i < 4; ++i) hx[i] = *(const u32x4*)(shm + exo(wc * 3 + wm - 1) + i * 1024 + ln * 16);
    } else if (__builtin_expect(cok || !needh, 1)) {
      while (__builtin_expect(blank(hx), 0)) {
        __builtin_amdgcn_s_sleep(1);
        asm volatile("" ::: "memory");
        hload();
      }
    } else {
      await(0xffffu, 0x404u);
      char* const fb = shm + exo(6 + wc) + ln * 16;
#pragma unroll 1
      for (int i = 0; i < 32; ++i) {
        const int rec = i >> 3, e = i & 7;
        const int row = m0 - 32 + 2 * (rec < 2 ? li : lq * 4 + (e & 3)) + (rec & 1);
        const int col = (rec < 2 ? D_ + lq * 4 + (e & 3) : 2 * D_ + li) + (ntile + wc) * 32 + (e >> 2) * 16;
        const u16* xr = x + (size_t)row * D_;
        const unsigned stt = row_stats_serial(xr);
        const float mb = lo_f(stt);
        float a = 0.f;
#pragma unroll 1
        for (int k = 0; k < D_; ++k) a += (bf2f(xr[k]) - mb) * bf2f(wqkv[(size_t)k * 3072 + col]);
        *(u16*)(fb + rec * 1024 + e * 2) = f2bf(a / hi_f(stt));
      }
#pragma unroll
      for (int i = 0; i < 4; ++i) hx[i] = *(const u32x4*)(fb + i * 1024);
    }
    TS1(6);
    {
      const f32x4 z = {0.f, 0.f, 0.f, 0.f};
      const float mz = mtile == 0 && wm == 0 ? -1.0e30f : 0.f;
      const f32x4 mkf = mk0 + f32x4{mz, mz, mz, mz};
      f32x4 s0[4], s1[4];
#pragma unroll
      for (int i = 0; i < 4; ++i) {
        const int n = i ^ 2;
        const bf16x8 qf = __builtin_bit_cast(bf16x8, qx[n]);
        s1[n] = __builtin_amdgcn_mfma_f32_16x16x32_bf16(__builtin_bit_cast(bf16x8, kx[n]), qf, mk1, 0, 0, 0);
        s0[n] = __builtin_amdgcn_mfma_f32_16x16x32_bf16(__builtin_bit_cast(bf16x8, n < 2 ? hx[n] : kx[n - 2]), qf, n < 2 ? mkf : mk0, 0, 0, 0);
      }
      SB0();
      asm volatile("s_nop 7\n\ts_nop 3" : "+v"(s0[0]), "+v"(s1[0]), "+v"(s0[1]), "+v"(s1[1]), "+v"(s0[2]), "+v"(s1[2]), "+v"(s0[3]), "+v"(s1[3]));
      float mx[4];
#pragma unroll
      for (int n = 0; n < 4; ++n)
#pragma unroll
        for (int j = 0; j < 4; ++j) s0[n][j] = max2(s0[n][j], s1[n][j]);
#pragma unroll
      for (int n = 0; n < 4; ++n) mx[n] = max3(s0[n][0], s0[n][1], s0[n][2]);
#pragma unroll
      for (int n = 0; n < 4; ++n) mx[n] = max2(mx[n], s0[n][3]);
#pragma unroll
      for (int n = 0; n < 4; ++n) { auto r = __builtin_amdgcn_permlane16_swap(__float_as_uint(mx[n]), __float_as_uint(mx[n]), false, false);
        mx[n] = max2(__uint_as_float(r[0]), __uint_as_float(r[1])); }
#pragma unroll
      for (int n = 0; n < 4; ++n) { auto r = __builtin_amdgcn_permlane32_swap(__float_as_uint(mx[n]), __float_as_uint(mx[n]), false, false);
        mx[n] = max2(__uint_as_float(r[0]), __uint_as_float(r[1])); }
      SB0();
      constexpr float cs2 = 0.17677669529663687f * 1.4426950408889634f;
      f32x4 os[4], o0[4], o1[4];
#pragma unroll
      for (int n = 0; n < 4; ++n) {
        const float c = cs2 * rd[n], nm = -mx[n] * c;
        const f32x4 ar = __builtin_elementwise_fma(s0[n], f32x4{c, c, c, c}, f32x4{nm, nm, nm, nm});
        const u32x2 pk = {pack2(__builtin_amdgcn_exp2f(ar[0]), __builtin_amdgcn_exp2f(ar[1])), pack2(__builtin_amdgcn_exp2f(ar[2]), __builtin_amdgcn_exp2f(ar[3]))};
        const bf16x4 pa = __builtin_bit_cast(bf16x4, pk), p0 = __builtin_bit_cast(bf16x4, pk & pm), p1 = __builtin_bit_cast(bf16x4, pk & ~pm);
        const u32x4 vp = n < 2 ? hx[2 + n] : vx[n - 2];
        auto half = [](const u32x4& v, int h) { return __builtin_bit_cast(bf16x4, u32x2{v[2 * h], v[2 * h + 1]}); };
        os[n] = __builtin_amdgcn_mfma_f32_16x16x16bf16_1k(ones, pa, z, 0, 0, 0);
        o0[n] = __builtin_amdgcn_mfma_f32_16x16x16bf16_1k(half(vx[n], 0), p1, __builtin_amdgcn_mfma_f32_16x16x16bf16_1k(half(vp, 0), p0, z, 0, 0, 0), 0, 0, 0);
        o1[n] = __builtin_amdgcn_mfma_f32_16x16x16bf16_1k(half(vx[n], 1), p1, __builtin_amdgcn_mfma_f32_16x16x16bf16_1k(half(vp, 1), p0, z, 0, 0, 0), 0, 0, 0);
      }
#pragma unroll
      for (int n = 0; n < 4; ++n) {
        const float inv = __builtin_amdgcn_rcpf(os[n][0]);
        const f32x4 n0 = o0[n] * inv, n1 = o1[n] * inv;
        putf(112 + ntile + wc, n, u32x4{pack2(n0[0], n0[1]), pack2(n0[2], n0[3]), pack2(n1[0], n1[1]), pack2(n1[2], n1[3])});
      }
    }
  }
  TS1(2);
  {
  __builtin_amdgcn_sched_barrier(0);
  int tid = threadIdx.x;
  asm volatile("" : "+v"(tid));
  __builtin_amdgcn_sched_barrier(0);
  const int lane = tid & 63;
  const int wm = wid >> 1, wn = wid & 1;
  const int m0 = mt2 * 128, n0 = nt2 * 128;
  constexpr int PD = G2_NSTAGE;
  const int ny = (xcd & 1) ? NY - DLT : NY + DLT;
  const unsigned va = lane * 16;
  auto stage_y = [&]() {
    asm volatile("s_add_u32 m0, %[l], %[oa]\n\ts_nop 0\n\tglobal_load_lds_dwordx4 %[va], %[a] sc1\n\tglobal_load_lds_dwordx4 %[va], %[a] offset:1024 sc1\n\t"
                 "global_load_lds_dwordx4 %[va], %[a] offset:2048 sc1\n\tglobal_load_lds_dwordx4 %[va], %[a] offset:3072 sc1\n\t" G2_W
                 :: [l] "s"(sl), [oa] "s"(shm_base + G2_A + (p ^ role) * 4096), [a] "s"(sa), [va] "v"(va), G2_WOP(wy) : "memory", "scc", "m0");
    ++st;
    sa += 16384;
    sl = (sl + 16384) & 65535;
    wsel();
  };
  u64 ready = 0;
  const u64* const mbox = &done1[bid * 64 + lane];
  auto seen = [&](u64 v) __attribute__((always_inline)) {
    const u64 r = __ballot(fresh(v, t_own));
    u64 d;
    asm("s_bitreplicate_b64_b32 %0, %1" : "=s"(d) : "s"((unsigned)r >> (16 * role)));
    ready |= r | (d << 32);
  };
  auto need = [&](u64 mask, bool renew) __attribute__((always_inline)) {
    if ((ready & mask) != mask) {
      seen(__hip_atomic_load(mbox, RLX_AGENT));
      while ((ready & mask) != mask) {
        __builtin_amdgcn_s_sleep(PSLEEP);
        if (renew && !cok) notify(0, 32);
        seen(__hip_atomic_load(mbox, RLX_AGENT));
      }
    }
    asm volatile("" ::: "memory");
  };
  u64 fl = 0;
  auto peek = [&]() { asm volatile("global_load_dwordx2 %0, %1, %2 sc1" : "+v"(fl) : "v"(va >> 1), "s"(done1 + bid * 64) : "memory"); };
  auto check = [&](u64 mask, bool renew) __attribute__((always_inline)) {
    asm volatile("" : "+v"(fl));
    seen(fl);
    if (__builtin_expect((ready & mask) != mask, 0)) need(mask, renew);
  };
  if (wid >= 4) {
    __builtin_amdgcn_s_setprio(0);
    BARL();
    stage_y();
    stage_y();
    BAR();
    if (p == 1) peek();
    stage_y();
    WAIT_V(16);
    BAR();
    if (p == 0) notify(0, 32);
#define G2_A2 "global_load_lds_dwordx4 %[va], %[a] sc1\n\tglobal_load_lds_dwordx4 %[va], %[a] offset:1024 sc1"
#define G2_A4 G2_A2 "\n\tglobal_load_lds_dwordx4 %[va], %[a] offset:2048 sc1\n\tglobal_load_lds_dwordx4 %[va], %[a] offset:3072 sc1"
#define G2_SYNC "\n\ts_mov_b32 m0, %[la]\n\ts_waitcnt vmcnt(%[n])\n\ts_barrier\n\t"
    const unsigned oy = shm_base + G2_A + (p ^ role) * 4096, os = shm_base + G2_A + (p >> 1) * 8192 + (p & 1) * 2048;
    asm volatile("s_mov_b32 m0, %[la]\n\ts_nop 0\n\t" G2_A4 :: [la] "s"(sl + oy), [a] "s"(sa), [va] "v"(va) : "memory", "m0");
    auto step = [&](long adv = 16384) __attribute__((always_inline)) {
      wsel();
      ++st;
      sa += adv;
      const unsigned sn = (sl + 16384) & 65535;
      asm volatile(G2_W G2_SYNC G2_A4 :: [l] "s"(sl), [la] "s"(sn + oy), [n] "n"(16), [a] "s"(sa), [va] "v"(va), G2_WOP(wy) : "memory", "scc", "m0");
      sl = sn;
    };
    auto chk1 = [&](u64 mask) __attribute__((always_inline)) { if (p == 1) { WAIT_V(16); check(mask, true); } };
    step();
    chk1(0xcc00cc00ull << 32);
    step(); step();
    if (p == 1) peek();
    step(); step();
    chk1(0x0f0fu << (16 * role));
#pragma unroll 1
    for (int g = 7; g < 13; ++g) step();
    if (p == 1) peek();
    step(); step();
    chk1((0xffffu << (16 * role)) | 0xf555f555u);
#pragma unroll 1
    for (int g = 15; g < min(ny, NY) - 5; ++g) step();
    if (ny > NY) {
      step(((1 - 2 * role) * NY + 1 - DLT) * 16384);
#pragma unroll
      for (int i = 1; i < DLT; ++i) step();
    }
    wsel();
    sa = sz;
    {
      const unsigned sn = (sl + 16384) & 65535;
      asm volatile(G2_W G2_SYNC G2_A2 :: [l] "s"(sl), [la] "s"(sn + os), [n] "n"(16), [a] "s"(sa), [va] "v"(va), G2_WOP(wy) : "memory", "scc", "m0");
      sl = sn;
    }
    sw[0] = wz.wb[0]; sw[1] = wz.wb[1];
#pragma unroll
    for (int j = -3; j < 13; ++j) {
      if (j == GETP + 1 || j == GETP + 2)
        asm volatile("s_mov_b32 m0, %[l]\n\ts_nop 0\n\tglobal_load_lds_dwordx4 %[va], %[a] offset:%[o] sc1\n\tglobal_load_lds_dwordx4 %[va], %[a] offset:%[o]+1024 sc1"
                     :: [l] "s"(shm_base + G2_X + 16384 + p * 4096), [a] "s"((const char*)slab + (size_t)(bid ^ 5) * 16384 + p * 4096), [va] "v"(va),
                        [o] "n"((j - GETP - 1) * 2048) : "memory", "m0");
      sa += 16384;
      const unsigned sn = (sl + 16384) & 65535;
      constexpr int G = GETP;
      static_assert(GETP <= GETJ + 1, "");
      if (j < 12) asm volatile(G2_W G2_SYNC G2_A2 :: [l] "s"(sl), [la] "s"(sn + os), [n] "n"((j == -3 ? 14 : 12) + (j > G && j < G + 3 ? 2 : 0) + (j > G + 1 && j < G + 4 ? 2 : 0)),
                               [a] "s"(sa), [va] "v"(va), G2_WOP(wz) : "memory", "scc", "m0");
      else asm volatile(G2_W :: [l] "s"(sl), G2_WOP(wz) : "memory", "scc", "m0");
      sl = sn;
      sw[0] += ws0; sw[1] += ws1;
    }
    WAIT_V(12);
    BAR();
    WAIT_V(6);
    BAR();
    WAIT_V(0);
    BAR();
    asm volatile("" : "+v"(fl));
    TS(5);
    if (tid < 258) __hip_atomic_store(lane ? done2 + bid * 8 : &acks[bid], __builtin_amdgcn_s_memrealtime(), RLX_AGENT);
    if (__builtin_expect(!cok, 0) && p == 0) {
      const bool mine = lane <= 32;
      const u64* const pa = &acks[lane < 32 ? client(lane) : bid ^ 5];
      while (!__all(!mine || fresh(__hip_atomic_load(pa, RLX_AGENT), t_own))) {
        notify(0, 32);
        if (lane == 0) __hip_atomic_store(done2 + bid * 8, __builtin_amdgcn_s_memrealtime(), RLX_AGENT);
        __builtin_amdgcn_s_sleep(32);
      }
    }
  } else {
    TS(6);
    f32x4 acc[4][4], sacc[2][4];
    const int fr = lane & 15, fq = lane >> 4, q4 = fr >> 2, pp = lane & 3;
    const int ff = ((q4 >> 1) & 1) | ((fq & 1) << 1);
    int aof = G2_A + wm * 8192 + lane * 16;
    int boff[4];
#pragma unroll
    for (int j = 0; j < 4; ++j) boff[j] = wn * 8192 + (8 * fq + q4) * 128 + ((j ^ ff) * 32) + pp * 8;
    int vo = wid * 4096 + lane * 16;
    int vx = ((m0 + wm * 64 + role * 32 + 2 * fr) * D_ + n0 + wn * 64) * 2 + (fq & 1) * 32 + (fq >> 1) * 16;
    lds_cptr px = shm3 + G2_X + wid * 4096 + (fq >> 1) * 512 + fr * 16 + (fq & 1) * 8;
    asm volatile("" : "+v"(vo), "+v"(vx), "+v"(px));
#define SB() __builtin_amdgcn_sched_barrier(0)
    struct Fr { bf16x8 a[4]; bf16x4 bl[4], bh[4]; };
    Fr fa[2], fb[2];
    auto rd = [&](Fr* f, int odd, int t) {
      constexpr int ord[12] = {0, 1, 8, 2, 3, 4, 5, 6, 7, 9, 10, 11};
      constexpr int seq[24] = {0, 1, 2, 12, 13, 14, 15, 16, 3, 4, 5, 6, 17, 18, 19, 20, 7, 8, 9, 21, 10, 22, 11, 23};
      lds_cptr sb = shm3 + odd * 16384;
      const int ks = seq[t] / 12, o = ord[seq[t] % 12];
      if (o >= 8) f[ks].a[o - 8] = *(const __attribute__((address_space(3))) bf16x8*)(sb + aof + (o - 8) * 2048 + ks * 1024);
      else if (o & 1) f[ks].bh[o >> 1] = tr_read(sb + boff[o >> 1] + ks * 4096 + 512);
      else f[ks].bl[o >> 1] = tr_read(sb + boff[o >> 1] + ks * 4096);
    };
    auto bfr = [&](const Fr& c, int j) { return __builtin_shufflevector(c.bl[j], c.bh[j], 0, 1, 2, 3, 4, 5, 6, 7); };
    auto mf1 = [](f32x4& c, bf16x8 b, bf16x8 a, bool z = false) {
      if (z) asm volatile("v_mfma_f32_16x16x32_bf16 %0, %1, %2, 0" : "=&v"(c) : "v"(b), "v"(a));
      else asm volatile("v_mfma_f32_16x16x32_bf16 %0, %1, %2, %0" : "+v"(c) : "v"(b), "v"(a));
    };
    auto mf2 = [](f32x4& c, bf16x8 b, bf16x8 a, bf16x8 b2, bf16x8 a2, bool z) {
      if (z) asm volatile("v_mfma_f32_16x16x32_bf16 %0, %1, %2, 0\n\tv_mfma_f32_16x16x32_bf16 %0, %3, %4, %0" : "=&v"(c) : "v"(b), "v"(a), "v"(b2), "v"(a2));
      else asm volatile("v_mfma_f32_16x16x32_bf16 %0, %1, %2, %0\n\tv_mfma_f32_16x16x32_bf16 %0, %3, %4, %0" : "+v"(c) : "v"(b), "v"(a), "v"(b2), "v"(a2));
    };
    auto pairs = [&](f32x4 (*ac)[4], const Fr* c, int nq, int mx, int nf, auto fill, bool z = false) {
#pragma unroll
      for (int q = 0; q < nq; ++q) {
        const int mf = (q >> 2) ^ mx, j = (q & 4) ? 3 - (q & 3) : q & 3, ks = q & 1;
        SB();
        if (2 * q >= nf) mf2(ac[mf][j], bfr(c[ks], j), c[ks].a[mf], bfr(c[ks ^ 1], j), c[ks ^ 1].a[mf], z);
        else {
          mf1(ac[mf][j], bfr(c[ks], j), c[ks].a[mf], z);
          SB();
          fill(2 * q);
          SB();
          mf1(ac[mf][j], bfr(c[ks ^ 1], j), c[ks ^ 1].a[mf]);
          SB();
          fill(2 * q + 1);
        }
      }
      SB();
    };
    auto flip = [&]() {
      aof ^= 32768; boff[0] ^= 32768; boff[1] ^= 32768; boff[2] ^= 32768; boff[3] ^= 32768;
      asm volatile("" : "+v"(aof), "+v"(boff[0]), "+v"(boff[1]), "+v"(boff[2]), "+v"(boff[3]));
    };
    auto bar = [&]() { __builtin_amdgcn_s_waitcnt(0xC07F); BAR(); };
    auto ystg = [&](const Fr* c, Fr* n, int odd, bool z = false) {
      pairs(acc, c, 16, 0, odd ? 30 : 24, [&](int t) {
        if (t < 24) rd(n, odd, t);
        else if (t == 24) asm volatile("s_nop 0");
        else { int& a = t == 29 ? aof : boff[t - 25]; a ^= 32768; asm volatile("" : "+v"(a)); }
      }, z);
    };
    auto stg = [&](const Fr* c, Fr* n, int odd, bool z = false) { pairs(sacc, c, 8, 0, 10, [&](int t) { rd(n, odd, 2 * t); rd(n, odd, 2 * t + 1); }, z); };
    asm volatile("" : "+v"(aof), "+v"(boff[0]), "+v"(boff[1]), "+v"(boff[2]), "+v"(boff[3]));
    await(0xffffu, 0x404u);
    auto lda = [&](int i) {
      asm volatile("s_mov_b32 m0, %[l]\n\ts_nop 0\n\tglobal_load_lds_dwordx4 %[va], %[a] sc1\n\tglobal_load_lds_dwordx4 %[va], %[a] offset:1024 sc1\n\t"
                   "global_load_lds_dwordx4 %[va], %[a] offset:2048 sc1\n\tglobal_load_lds_dwordx4 %[va], %[a] offset:3072 sc1"
                   :: [l] "s"(shm_base + G2_A + (i + 1) * 16384 + (p ^ role) * 4096), [a] "s"(sa + i * 16384), [va] "v"(va) : "memory", "m0");
    };
    need(0x00330033ull << 32, false);
    lda(0);
    lda(1);
    need(0x00ff00ffull << 32, false);
    TS(0);
    lda(2);
    WAIT_V(8);
    signal(1u << 24);
    await(0xff000000u, 4u << 24);
    __builtin_amdgcn_s_waitcnt(0xC07F);
    SB();
#pragma unroll
    for (int t = 0; t < 24; ++t) rd(fa, 1, t);
    flip();
#define CPRIO 0
    __builtin_amdgcn_s_setprio(CPRIO);
    WAIT_V(4);
    TS(5);
    peek();
    bar();
    TS(7);
    ystg(fa, fb, 0, true);
    WAIT_V(1);
    bar();
    ystg(fb, fa, 1);
    WAIT_V(0);
    check(0x33003300ull << 32, false);
    constexpr int PAR = (NY - DLT + 1) & 1;
#pragma unroll 1
    for (int g = 2; g < ny - 2; g += 2) {
      bar();
      ystg(fa, fb, 0);
      bar();
      ystg(fb, fa, 1);
    }
    if (PAR) {
      bar();
      ystg(fa, fb, 0);
    }
    Fr* const f0 = PAR ? fb : fa;
    Fr* const f1 = PAR ? fa : fb;
    bar();
    {
      const __amdgpu_buffer_rsrc_t rs = __builtin_amdgcn_make_buffer_rsrc((void*)((char*)slab + (size_t)bid * 16384), 0, 16384, 0x00020000);
      u32x4 pk;
      pairs(acc, f0, 16, 2, 32, [&](int t) {
        if (t < 20) rd(f1, PAR, t);
        else {
          const int f = (t - 20) / 3, e = (t - 20) % 3;
          if (e < 2) { const f32x4 a = acc[2 + (f >> 1)][(f & 1) * 2 + e]; pk[2 * e] = pack2(a[0], a[1]); pk[2 * e + 1] = pack2(a[2], a[3]); }
          else __builtin_amdgcn_raw_buffer_store_b128(pk, rs, vo + f * 1024, 0, 16);
        }
      });
    }
    TS(2);
    static_assert(GETJ % 2 == 1 && GETJ >= 3 && GETJ <= 7, "");
    f32x4 xf[2][4];
#pragma unroll
    for (int j = 0; j < GETJ + 5; j += 2) {
      if (PAR) flip();
      bar();
      stg(f1, f0, PAR ^ 1, j == 0);
      if (!PAR) flip();
      bar();
      u32x2 xv[2];
      pairs(sacc, f0, 8, 0, 12, [&](int t) {
        if (t < 10) { rd(f1, PAR, 2 * t); rd(f1, PAR, 2 * t + 1); }
        else if (j < 8) xv[t - 10] = *(const __attribute__((address_space(3))) u32x2*)(px + (j >> 1) * 1024 + (t - 10) * 256);
      });
      if (j < 8) {
#pragma unroll
        for (int e = 0; e < 2; ++e) xf[j >> 2][(j & 2) + e] = f32x4{lo_f(xv[e][0]), hi_f(xv[e][0]), lo_f(xv[e][1]), hi_f(xv[e][1])};
        asm volatile("" : "+v"(xf[j >> 2][j & 2]), "+v"(xf[j >> 2][(j & 2) + 1]));
        SB();
      }
    }
    if (PAR) flip();
    WAIT_V(0);
    bar();
    u32x4 pv[4];
    {
      lds_cptr pp_ = shm3 + G2_X + 16384 + vo;
      pairs(sacc, f1, 8, 0, 12, [&](int t) {
        if (t < 2) {
#pragma unroll
          for (int f = 2 * t; f < 2 * t + 2; ++f) pv[f] = *(const __attribute__((address_space(3))) u32x4*)(pp_ + f * 1024);
        } else { rd(f0, PAR ^ 1, 2 * t - 4); rd(f0, PAR ^ 1, 2 * t - 3); }
      });
      if (__builtin_expect(blank(pv) || !cok, 0)) {
        const __amdgpu_buffer_rsrc_t rp = __builtin_amdgcn_make_buffer_rsrc((void*)((char*)slab + (size_t)(bid ^ 5) * 16384), 0, 16384, 0x00020000);
        while (!cok && !__all(fresh(__hip_atomic_load(done2 + (bid ^ 5) * 8, RLX_AGENT), t_own))) {
          notify(0, 32);
          if (lane == 0) __hip_atomic_store(done2 + bid * 8, __builtin_amdgcn_s_memrealtime(), RLX_AGENT);
          __builtin_amdgcn_s_sleep(8);
        }
        do {
          asm volatile("" ::: "memory");
#pragma unroll
          for (int f = 0; f < 4; ++f) pv[f] = __builtin_bit_cast(u32x4, __builtin_amdgcn_raw_buffer_load_b128(rp, vo + f * 1024, 0, 16));
        } while (blank(pv));
      }
    }
    auto silu = [&](int g0, int g1) {
#pragma unroll
      for (int f = g0; f < g1; ++f) {
        const int mf = f >> 2, j = f & 3;
        const u32x4 pw = pv[mf * 2 + (j >> 1)];
        const f32x4 ya = acc[mf][j] + f32x4{lo_f(pw[2 * (j & 1)]), hi_f(pw[2 * (j & 1)]), lo_f(pw[2 * (j & 1) + 1]), hi_f(pw[2 * (j & 1) + 1])};
#pragma unroll
        for (int e = 0; e < 2; ++e) {
          const f32x2 y2 = {ya[2 * e], ya[2 * e + 1]};
          const f32x2 ar = y2 * f32x2{-1.4426950408889634f, -1.4426950408889634f};
          const f32x2 dn = f32x2{__builtin_amdgcn_exp2f(ar[0]), __builtin_amdgcn_exp2f(ar[1])} + f32x2{1.0f, 1.0f};
          const f32x2 t = y2 * f32x2{__builtin_amdgcn_rcpf(dn[0]), __builtin_amdgcn_rcpf(dn[1])};
          acc[mf][j][2 * e] = t[0]; acc[mf][j][2 * e + 1] = t[1];
        }
        asm volatile("" : "+v"(acc[mf][j]));
      }
      SB();
    };
#define SPT 0, 0, 1, 2, 4, 6, 8, 8
    constexpr int spt[] = {SPT};
    silu(0, spt[1]);
#pragma unroll
    for (int j = GETJ + 6; j < 15; ++j) {
      if ((j ^ PAR) & 1) flip();
      bar();
      if (j & 1) stg(f0, f1, PAR); else stg(f1, f0, PAR ^ 1);
      silu(spt[j - GETJ - 5], j == 14 ? 8 : spt[j - GETJ - 4]);
    }
    pairs(sacc, f0, 8, 0, 0, [](int) {});
    asm volatile("s_nop 7\n\ts_nop 3" : "+v"(sacc[1][0]), "+v"(sacc[1][1]), "+v"(sacc[1][2]), "+v"(sacc[1][3]));
    SB();
    TS(1);
    {
      const __amdgpu_buffer_rsrc_t ro = __builtin_amdgcn_make_buffer_rsrc((void*)out, 0, S_ * D_ * 2, 0x00020000);
#pragma unroll
      for (int f = 0; f < 4; ++f) {
        const int mf = f >> 1, j = (f & 1) * 2;
        const f32x4 a = __builtin_elementwise_fma(sacc[mf][j], acc[mf][j], xf[mf][j]), b = __builtin_elementwise_fma(sacc[mf][j + 1], acc[mf][j + 1], xf[mf][j + 1]);
        const auto r0 = __builtin_amdgcn_permlane16_swap(pack2(a[0], a[1]), pack2(b[0], b[1]), false, false);
        const auto r1 = __builtin_amdgcn_permlane16_swap(pack2(a[2], a[3]), pack2(b[2], b[3]), false, false);
        __builtin_amdgcn_raw_buffer_store_b128(u32x4{r0[0], r1[0], r0[1], r1[1]}, ro, vx + mf * 2048 + j * 32, 0, 2);
      }
    }
    TS(3);
  }
  if (bid == 2 && wid == 0) { unsigned d_ = (ts_r[12] - (unsigned)t_own) * 10u; if (d_ > 9000u) d_ = 9000u; while (__builtin_amdgcn_s_memrealtime() - t_own < 6000ull + d_) {} }
  }
  asm volatile(".Lcode_end:");
}

extern "C" void kernel_launch(void* const* d_in, const int* in_sizes, int n_in, void* d_out, int out_size, void* d_ws,
                              size_t ws_size, hipStream_t stream) {
  const u16* A_x = (const u16*)d_in[0];
  const u16* A_wqkv = (const u16*)d_in[1];
  const u16* A_wo = (const u16*)d_in[2];
  const u16* B_wg = (const u16*)d_in[4];
  const u16* B_wu = (const u16*)d_in[5];
  const u16* B_wd = (const u16*)d_in[6];
  u16* out = (u16*)d_out;
  fused_kernel<<<256, 512, 0, stream>>>(A_x, A_wqkv, B_wg, B_wu, B_wd, A_wo, out, (char*)d_ws);
}
```

```cpp
#define ALL_TS 1
#define TSMASK 4096
#include <hip/hip_runtime.h>
#include <hip/hip_bf16.h>
#include <stdint.h>
#include <type_traits>

typedef unsigned short u16;
using bf16x8 = __attribute__((ext_vector_type(8))) short;
using bf16x4 = __attribute__((ext_vector_type(4))) short;
using f32x4 = __attribute__((ext_vector_type(4))) float;
using u32x2 = __attribute__((ext_vector_type(2))) unsigned;
using u32x4 = __attribute__((ext_vector_type(4))) unsigned;
typedef __attribute__((address_space(3))) const char* lds_cptr;

constexpr int S_ = 2048, D_ = 1024, F_ = 3584;

__device__ __forceinline__ float bf2f(u16 h) { return __uint_as_float((unsigned)h << 16); }
__device__ __forceinline__ u16 f2bf(float f) {
  unsigned u = __float_as_uint(f);
  u += 0x7fffu + ((u >> 16) & 1u);
  return (u16)(u >> 16);
}
__device__ __forceinline__ float rbf(float f) { return bf2f(f2bf(f)); }
typedef __attribute__((ext_vector_type(2))) __bf16 bf16x2_t;
typedef __attribute__((ext_vector_type(2))) float f32x2;
__device__ __forceinline__ unsigned pack2(float a, float b) {
  bf16x2_t r = __builtin_convertvector((f32x2){a, b}, bf16x2_t);
  return __builtin_bit_cast(unsigned, r);
}
__device__ __forceinline__ float lo_f(unsigned p) { return __uint_as_float(p << 16); }
__device__ __forceinline__ float hi_f(unsigned p) { return __uint_as_float(p & 0xffff0000u); }

#define TSDECL unsigned ts_r[16] = {0, 0, 0, 0, 0, 0, 0, 0, 0, 0, 0, 0, 0, 0, 0, 0}
#define TSS(i) do { if ((TSMASK >> (i)) & 1) { unsigned long long t_; asm volatile("s_memrealtime %0\n\ts_waitcnt lgkmcnt(0)" : "=s"(t_)); ts_r[i] = (unsigned)t_; } } while (0)
#define TSFLUSH() do { if ((threadIdx.x & 63) == 0) { for (int i_ = 0; i_ < 16; ++i_) flags[(blockIdx.x * 8 + (threadIdx.x >> 6)) * 16 + i_] = ts_r[i_]; } } while (0)
#define TS1(i) TSS(i)
#define TS(i) TSS(8 + (i))
#define PIN_S(v) asm volatile("" : "+s"(v))
#define WAIT_V(n) asm volatile("s_waitcnt vmcnt(%0)" ::"n"(n) : "memory")
#define WAIT_L(n) asm volatile("s_waitcnt lgkmcnt(%0)" ::"n"(n) : "memory")
#define BARL() do { WAIT_L(0); BAR(); } while (0)
#define BAR() do { asm volatile("" ::: "memory"); __builtin_amdgcn_s_barrier(); asm volatile("" ::: "memory"); } while (0)

__device__ __forceinline__ bf16x4 tr_read(lds_cptr p) {
  return __builtin_amdgcn_ds_read_tr16_b64_v4i16((__attribute__((address_space(3))) bf16x4*)p);
}
__device__ __forceinline__ bf16x8 tr_frag(lds_cptr p) {
  bf16x4 lo = tr_read(p), hi = tr_read(p + 512);
  bf16x8 r;
  r[0] = lo[0]; r[1] = lo[1]; r[2] = lo[2]; r[3] = lo[3];
  r[4] = hi[0]; r[5] = hi[1]; r[6] = hi[2]; r[7] = hi[3];
  return r;
}
__device__ __forceinline__ void glds16(const void* gsrc, unsigned lds_dst) {
  unsigned keep;
  asm volatile("s_mov_b32 %0, m0\n\ts_mov_b32 m0, %2\n\ts_nop 0\n\tglobal_load_lds_dwordx4 %1, off\n\ts_mov_b32 m0, %0"
               : "=&s"(keep) : "v"(gsrc), "s"(lds_dst) : "memory");
}
#define GLDS(gptr, laddr) glds16((const void*)(gptr), (unsigned)(laddr))
__device__ __forceinline__ void glds16_sc1(const void* gsrc, unsigned lds_dst) {
  unsigned keep;
  asm volatile("s_mov_b32 %0, m0\n\ts_mov_b32 m0, %2\n\ts_nop 0\n\tglobal_load_lds_dwordx4 %1, off sc1\n\ts_mov_b32 m0, %0"
               : "=&s"(keep) : "v"(gsrc), "s"(lds_dst) : "memory");
}
#define GLDS_SC1(gptr, laddr) glds16_sc1((const void*)(gptr), (unsigned)(laddr))
__device__ __forceinline__ void glds16_nc(const void* gsrc, unsigned lds_dst) {
  unsigned keep;
  asm volatile("s_mov_b32 %0, m0\n\ts_mov_b32 m0, %2\n\ts_nop 0\n\tglobal_load_lds_dwordx4 %1, off\n\ts_mov_b32 m0, %0"
               : "=&s"(keep) : "v"(gsrc), "s"(lds_dst));
}
#define GLDS_NC(gptr, laddr) glds16_nc((const void*)(gptr), (unsigned)(laddr))
__device__ __forceinline__ void glds16_s(const void* sbase, unsigned voff, unsigned lds_dst) {
  asm volatile("s_mov_b32 m0, %2\n\ts_nop 0\n\tglobal_load_lds_dwordx4 %0, %1" :: "v"(voff), "s"(sbase), "s"(lds_dst) : "m0");
}

__device__ __forceinline__ void glds16_s1(const void* sbase, unsigned voff, unsigned lds_dst) {
  asm volatile("s_mov_b32 m0, %2\n\ts_nop 0\n\tglobal_load_lds_dwordx4 %0, %1 sc1" :: "v"(voff), "s"(sbase), "s"(lds_dst) : "memory", "m0");
}
__device__ __forceinline__ void glds16_v(const void* p, unsigned lds_dst) {
  asm volatile("s_mov_b32 m0, %1\n\ts_nop 0\n\tglobal_load_lds_dwordx4 %0, off" :: "v"(p), "s"(lds_dst) : "m0");
}
template <int OFF = 0>
__device__ __forceinline__ void gst16(const void* sbase, unsigned voff, u32x4 v) {
  asm volatile("global_store_dwordx4 %0, %1, %2 offset:%3 sc1\n\ts_nop 1" :: "v"(voff), "v"(v), "s"(sbase), "n"(OFF) : "memory");
}

typedef unsigned long long u64;
__device__ __forceinline__ bool fresh(u64 stamp, u64 t_own) {
  const u64 b = t_own + 100ull;
  const unsigned lo = (unsigned)stamp - (unsigned)b, hi = (unsigned)(stamp >> 32) - (unsigned)(b >> 32) - ((unsigned)stamp < (unsigned)b ? 1u : 0u);
  return (hi == 0u) & (lo < (unsigned)(__builtin_amdgcn_s_memrealtime() - t_own));
}
constexpr u64 TIMEOUT_ = 200000;
__device__ __forceinline__ float half_sum(float v) {
  v += __builtin_bit_cast(float, __builtin_amdgcn_update_dpp(0, __builtin_bit_cast(int, v), 0xB1, 0xf, 0xf, false));
  v += __builtin_bit_cast(float, __builtin_amdgcn_update_dpp(0, __builtin_bit_cast(int, v), 0x4E, 0xf, 0xf, false));
  v += __builtin_bit_cast(float, __builtin_amdgcn_update_dpp(0, __builtin_bit_cast(int, v), 0x141, 0xf, 0xf, false));
  v += __builtin_bit_cast(float, __builtin_amdgcn_update_dpp(0, __builtin_bit_cast(int, v), 0x140, 0xf, 0xf, false));
  { auto r = __builtin_amdgcn_permlane16_swap(__float_as_uint(v), __float_as_uint(v), false, false); v = __uint_as_float(r[0]) + __uint_as_float(r[1]); }
  return v;
}
__device__ __forceinline__ unsigned pack_stats(float mean, float var) {
  return (unsigned)f2bf(mean) | ((unsigned)f2bf(sqrtf(rbf(rbf(var) + 1e-5f))) << 16);
}
__device__ __forceinline__ unsigned row_stats_serial(const u16* xr) {
  float s = 0.f, q = 0.f;
#pragma unroll 1
  for (int k = 0; k < D_; ++k) s += bf2f(xr[k]);
  const float mean = s * (1.0f / D_);
#pragma unroll 1
  for (int k = 0; k < D_; ++k) { const float d = bf2f(xr[k]) - mean; q += d * d; }
  return pack_stats(mean, q * (1.0f / D_));
}
constexpr int G1_STAGE = 32768 + 5 * 8192;
#define EPOS 2
#define PA 4
#define PB 8
#define PC 12
#define GETP 4
#define DLT 2
#define PSLEEP 2
#define GETJ 3

constexpr unsigned BLANK = 0x7f817f81u;
__device__ __forceinline__ bool blank(const u32x4* pv) {
  u64 m;
#define CX(i) "v_cmpx_ne_u32 vcc, %1, %" #i "\n\t"
  asm volatile(CX(2) CX(3) CX(4) CX(5) CX(6) CX(7) CX(8) CX(9) CX(10) CX(11) CX(12) CX(13) CX(14) CX(15) CX(16) CX(17)
               "s_mov_b64 %0, exec\n\ts_mov_b64 exec, -1\n\ts_nop 3" : "=s"(m) : "s"(BLANK), "v"(pv[0][0]), "v"(pv[0][1]), "v"(pv[0][2]), "v"(pv[0][3]),
               "v"(pv[1][0]), "v"(pv[1][1]), "v"(pv[1][2]), "v"(pv[1][3]), "v"(pv[2][0]), "v"(pv[2][1]), "v"(pv[2][2]), "v"(pv[2][3]),
               "v"(pv[3][0]), "v"(pv[3][1]), "v"(pv[3][2]), "v"(pv[3][3]) : "vcc");
  return m != ~0ull;
}
#define RLX_AGENT __ATOMIC_RELAXED, __HIP_MEMORY_SCOPE_AGENT

constexpr int G2_A = 65536, G2_X = 131072;
constexpr int G2_NSTAGE = 4;
constexpr size_t MB_ = 1024 * 1024;
__global__ __launch_bounds__(512, 2) __attribute__((aligned(4096))) void fused_kernel(const u16* x_, const u16* wqkv_, const u16* wg_, const u16* wu_, const u16* wd_, const u16* wo_, u16* out_, char* ws_) {
  u64 tfirst;
  typedef __attribute__((address_space(4))) const u64* kptr;
  kptr ka = (kptr)__builtin_amdgcn_kernarg_segment_ptr();
  asm volatile("s_memrealtime %0\n\ts_waitcnt lgkmcnt(0)" : "=s"(tfirst), "+s"(ka));
  typedef __attribute__((address_space(1))) char* gp;
  const u16* __restrict__ x = (const u16*)(gp)ka[0]; const u16* __restrict__ wqkv = (const u16*)(gp)ka[1];
  const u16* __restrict__ wg = (const u16*)(gp)ka[2]; const u16* __restrict__ wu = (const u16*)(gp)ka[3];
  const u16* __restrict__ wd = (const u16*)(gp)ka[4]; const u16* __restrict__ wo = (const u16*)(gp)ka[5];
  u16* __restrict__ out = (u16*)(gp)ka[6]; char* __restrict__ ws = (char*)(gp)ka[7];
  u16* const halo = (u16*)(ws + 4 * MB_);
  u16* const abuf = (u16*)(ws + 16 * MB_);
  char* const slab = ws + 64 * MB_;
  unsigned* const flags = (unsigned*)(ws + 96 * MB_);
  u32x4* const rstat = (u32x4*)(ws + 97 * MB_);
  char* const wcopy = ws + 98 * MB_;
  u64* const done1 = (u64*)(ws + 102 * MB_);
  u64* const done2 = (u64*)(ws + 100 * MB_ + 65536);
  u64* const donec = (u64*)(ws + 100 * MB_ + 131072);
  u64* const acks = (u64*)(ws + 100 * MB_ + 196608);
  float* const csum = (float*)(ws + 101 * MB_);
  __shared__ __attribute__((aligned(1024))) char shm[163840];
  constexpr int AUX = 2 * G1_STAGE + 8192;
  {
    u64 pc; int len;
    asm volatile("s_getpc_b64 %0\n.Lcode_beg:\n\ts_mov_b32 %1, .Lcode_end-.Lcode_beg" : "=s"(pc), "=s"(len));
    const int ol = ((blockIdx.x >> 3) + 32 * (threadIdx.x >> 6)) * 1024 + (threadIdx.x & 63) * 16;
    asm volatile("s_mov_b32 m0, %2\n\ts_nop 0\n\tglobal_load_lds_dwordx4 %0, %1"
                 :: "v"(ol < len - 16 ? ol : 0), "s"(pc & ~15ull), "s"((unsigned)(uintptr_t)(lds_cptr)shm + 2 * G1_STAGE + 7168) : "m0");
    __builtin_amdgcn_sched_barrier(0);
  }
  const u64 t_own = __builtin_amdgcn_s_memrealtime();
  TSDECL;
  TS1(7);
  const int tid = threadIdx.x, lane = tid & 63;
  const int wid = __builtin_amdgcn_readfirstlane(tid >> 6);
  const int wm = wid & 3, wc = wid >> 2;
  int bid_ = blockIdx.x;
  PIN_S(bid_);
  const int bid = bid_ & 255, xcd = bid & 7, idx = bid >> 3;
  const int ntile = xcd * 4 + (idx & 3), mtile = idx >> 2;
  const int m0 = mtile * 256;
  const int hg = ntile >> 1;
  const bool tya = (ntile & 1) == 0;
  const int srow = m0 + ntile * 8 + wm * 2;
  lds_cptr shm3 = (lds_cptr)shm;
  const unsigned shm_base = (unsigned)(uintptr_t)shm3;
  const int r8 = tid >> 3, pos = tid & 7;
  const int phys = pos >> 1, half = pos & 1;
  const int fk = ((r8 >> 1) & 1) | (((r8 >> 3) & 1) << 1);
  const int vsrc = phys ^ fk;
  const int qstr = tya ? 3072 * 2 : F_ * 2;
  unsigned voa = (((r8 & 32) + 2 * (r8 & 15) + ((r8 >> 4) & 1)) * D_ + ((pos ^ (r8 & 7)) * 8)) * 2;
  unsigned vog = r8 * (F_ * 2) + vsrc * 32 + half * 16, voq = r8 * qstr + vsrc * 32 + half * 16;
  unsigned vom = r8 * qstr + (vsrc >> (tya ? 0 : 1)) * 32 + half * 16;
  asm volatile("" : "+v"(voa), "+v"(vog), "+v"(voq), "+v"(vom));
  const int kt0 = (hg >> 1) * 7, he = hg & 1;
  unsigned oq = (tya ? hg : kt0 + 2 * he) * 128, og = (kt0 + (tya ? 5 + he : 2 * he + 1)) * 128, om = tya ? 4096 : 512 - 192 * he;
  unsigned d0 = shm_base + wid * 1024 + 32768, ox = m0 * D_ * 2;
  long uq = -32 * qstr;
  PIN_S(oq); PIN_S(og); PIN_S(om); PIN_S(d0); PIN_S(ox); PIN_S(uq);
  __builtin_amdgcn_sched_barrier(0);
  asm volatile("" :: "s"(x), "s"(wqkv), "s"(wg), "s"(wu), "s"(wd), "s"(wo), "s"(out), "s"(ws));
  const char* const ab = (const char*)x + ox;
  int kz = 0;
  asm volatile("" : "+s"(kz));
  TS1(12);
  u64 pq = (u64)wqkv, pg = (u64)wg;
  PIN_S(pq); PIN_S(pg);
  const char* const qb = (const char*)(tya ? pq : pg) + oq;
  if (wid < 4) {
#pragma unroll
    for (int i = 0; i < 8; ++i) glds16_s(ab + i * 32 * D_ * 2, voa, d0 - 32768 + i * 4096);
  } else {
    glds16_s(qb, voq, d0); glds16_s(qb + uq, voq, d0 - 4096);
  }
  __builtin_amdgcn_sched_barrier(0);
  const char* const gb = (const char*)wg + og;
  const long dgu = (const char*)wu - (const char*)wg, dp1 = tya ? 2048 : dgu;
  const char* const mb0 = qb + om;
  const char* mptr = mb0 + (((vsrc & 1) && !tya) ? dgu : 0) + vom;
  if (wid >= 4) {
    const long ug = kz - 32 * F_ * 2;
    glds16_s(qb + dp1, voq, d0 + 8192); glds16_s(qb + dp1 + uq, voq, d0 + 4096);
    glds16_v(mptr, d0 + 2 * 8192); glds16_v(mptr + uq, d0 + 2 * 8192 - 4096);
    glds16_s(gb, vog, d0 + 3 * 8192); glds16_s(gb + ug, vog, d0 + 3 * 8192 - 4096);
    glds16_s(gb + dgu, vog, d0 + 4 * 8192); glds16_s(gb + dgu + ug, vog, d0 + 4 * 8192 - 4096);
  }
  __builtin_amdgcn_sched_barrier(0);
  f32x4 acc[4][10];

  constexpr int NT = D_ / 64;
  constexpr int CNT = AUX + 4096 + 3584;
  if (tid < 8) *(unsigned*)(shm + CNT + tid * 4) = 0u;
  bf16x8 af0[4], af1[4];
  bf16x4 bl[4], bh[4];
#define SB0() __builtin_amdgcn_sched_barrier(0)
  auto mm = [&](int g, int t, bool two, bool z = false) {
    const int mi = t >> 1, mf = ((g & 1) && g != 9) ? 3 - mi : mi, ks = (t & 1) ^ (mi & 1), s = ((g + 1) & 1) * 2;
    const bf16x8 b0 = __builtin_shufflevector(bl[s + ks], bh[s + ks], 0, 1, 2, 3, 4, 5, 6, 7), a0 = ks ? af1[mf] : af0[mf];
    const bf16x8 b1 = __builtin_shufflevector(bl[s + 1 - ks], bh[s + 1 - ks], 0, 1, 2, 3, 4, 5, 6, 7), a1 = ks ? af0[mf] : af1[mf];
    const bool tr = (g >> 1) == 2;
    if (z && two) asm volatile("v_mfma_f32_16x16x32_bf16 %0, %1, %2, 0\n\tv_mfma_f32_16x16x32_bf16 %0, %3, %4, %0" : "=&v"(acc[mf][g])
                               : "v"(tr ? a0 : b0), "v"(tr ? b0 : a0), "v"(tr ? a1 : b1), "v"(tr ? b1 : a1));
    else if (z && !(t & 1)) asm volatile("v_mfma_f32_16x16x32_bf16 %0, %1, %2, 0" : "=&v"(acc[mf][g]) : "v"(tr ? a0 : b0), "v"(tr ? b0 : a0));
    else if (two) asm volatile("v_mfma_f32_16x16x32_bf16 %0, %1, %2, %0\n\tv_mfma_f32_16x16x32_bf16 %0, %3, %4, %0" : "+v"(acc[mf][g])
                          : "v"(tr ? a0 : b0), "v"(tr ? b0 : a0), "v"(tr ? a1 : b1), "v"(tr ? b1 : a1));
    else asm volatile("v_mfma_f32_16x16x32_bf16 %0, %1, %2, %0" : "+v"(acc[mf][g]) : "v"(tr ? a0 : b0), "v"(tr ? b0 : a0));
  };
  typedef __attribute__((address_space(3))) unsigned lds_u32;
  lds_u32* const cnt = (lds_u32*)(shm3 + CNT);
  bool cok;
  constexpr int NY = 28;
  int role, mt2, nt2;
  struct WS { unsigned vb[2][2]; const char* wb[2]; int sh[2]; };
  auto wsetup = [&](const u16* w, int crow0, int l, bool ck) __attribute__((always_inline)) {
    WS r;
    const unsigned vr = ((l >> 5) & 1) * 16 + (wm & 1) * 8 + ((l >> 3) & 3), vc = (((l >> 1) & 3) ^ ((l >> 4) & 1)) * 32 + (l & 1) * 16;
    u64 cp = (u64)(wcopy + (nt2 >> 2) * 589824 + crow0 * 128);
    asm volatile("" : "+s"(cp));
#pragma unroll
    for (int i = 0; i < 2; ++i) {
      u64 dp = (u64)(w + nt2 * 128 + i * 64);
      asm volatile("" : "+s"(dp));
      const bool slow = ck & ((((unsigned)dp >> 7) & 7) == 3);
      r.sh[i] = slow ? 7 : 11;
      r.wb[i] = (const char*)(slow ? cp : dp) + (crow0 ? (wm >> 1) * 32 << r.sh[i] : 0);
      r.vb[i][0] = (vr << r.sh[i]) + vc; r.vb[i][1] = ((vr + 4) << r.sh[i]) + (vc ^ 64);
    }
    asm volatile("" : "+v"(r.vb[0][0]), "+v"(r.vb[0][1]), "+v"(r.vb[1][0]), "+v"(r.vb[1][1]));
    PIN_S(r.wb[0]); PIN_S(r.wb[1]); PIN_S(r.sh[0]); PIN_S(r.sh[1]);
    return r;
  };
  WS wy, wz;
  int vxq, ws0, ws1;
  const char* sz;
  u32x4 xq[4];
  unsigned vg;
  int st = 3;
  const char *sa, *sw[2];
  auto wsel = [&]() {
    const unsigned r = __builtin_amdgcn_readlane(vg, st);
    sw[0] = wy.wb[0] + (r << wy.sh[0]); sw[1] = wy.wb[1] + (r << wy.sh[1]);
  };
  const int p = wid & 3;
  unsigned sl = 0;
#define G2_W "s_add_u32 m0, %[l], %[ow]\n\ts_nop 0\n\tglobal_load_lds_dwordx4 %[v0], %[w0] sc1\n\t" \
             "s_add_u32 m0, m0, 1024\n\ts_nop 0\n\tglobal_load_lds_dwordx4 %[v1], %[w0] sc1\n\t" \
             "s_add_u32 m0, m0, 7168\n\ts_nop 0\n\tglobal_load_lds_dwordx4 %[v2], %[w1] sc1\n\t" \
             "s_add_u32 m0, m0, 1024\n\ts_nop 0\n\tglobal_load_lds_dwordx4 %[v3], %[w1] sc1"
#define G2_WOP(w) [ow] "s"(shm_base + p * 2048), [w0] "s"(sw[0]), [w1] "s"(sw[1]), [v0] "v"(w.vb[0][0]), [v1] "v"(w.vb[0][1]), [v2] "v"(w.vb[1][0]), [v3] "v"(w.vb[1][1])
  auto tile2 = [&](int t0) __attribute__((always_inline)) {
    int b = bid;
    asm volatile("" : "+s"(b));
    role = 1 - ((b >> 2) & 1);
    mt2 = ((role ? b : b ^ 5) & 7) * 4 + ((b >> 3) & 3), nt2 = b >> 5;
    sa = (const char*)abuf + (size_t)(mt2 * 72 + role * NY + t0) * 16384 + wm * 4096;
    asm volatile("" : "+s"(sa));
  };
  auto prep = [&](bool first) __attribute__((always_inline)) {
    int l = threadIdx.x;
    asm volatile("" : "+v"(l));
    l &= 63;
    if (first) {
      tile2(3);
      vxq = ((mt2 * 128 + (wm >> 1) * 64 + role * 32 + 2 * (l & 15)) * D_ + nt2 * 128 + (wm & 1) * 64) * 2 + ((l >> 4) & 1) * 32 + (l >> 5) * 16;
      const int t = min(l < NY ? l : l - DLT, NY - 1), w = wm >> 1;
      const int o = (0xffaa5500fae504ull >> (2 * t)) & 3;
      int u1 = (0x990990 >> ((2 * t) & 31)) & 3, u2 = 3 + (t & 3);
      asm volatile("" : "+v"(u1), "+v"(u2));
      const int u = t < 12 ? u1 : u2;
      vg = (__umul24((l < NY ? role : 1 - role) * 4 + (((o & 1) << 1) | (o >> 1)), 14) + ((0xb85140a >> (4 * u)) & 15) + (u == 5 ? w : 2 * w)) * 32;
      asm volatile("" : "+v"(vg), "+v"(vxq));
      sz = (const char*)abuf + (size_t)(mt2 * 72 + 56) * 16384 + (wm >> 1) * 8192 + role * 4096 + (wm & 1) * 2048;
      PIN_S(sz);
    }
    wy = wsetup(wd, 0, l, first);
    wz = wsetup(wo, F_, l, first);
    ws0 = 64 << wz.sh[0]; PIN_S(ws0);
    ws1 = 64 << wz.sh[1]; PIN_S(ws1);
    SB0();
  };
  auto start2 = [&]() __attribute__((always_inline)) {
    const __amdgpu_buffer_rsrc_t rx = __builtin_amdgcn_make_buffer_rsrc((void*)x, 0, S_ * D_ * 2, 0x00020000);
#pragma unroll
    for (int f = 0; f < 4; ++f) xq[f] = __builtin_bit_cast(u32x4, __builtin_amdgcn_raw_buffer_load_b128(rx, vxq + (f >> 1) * 2048 + (f & 1) * 64, 0, 0));
    cok = __builtin_amdgcn_readfirstlane(cnt[1] & cnt[2]) != 0u;
    if (__builtin_expect(!cok, 0)) prep(false);
    wsel();
    PIN_S(sw[0]); PIN_S(sw[1]);
    SB0();
  };
  auto kloop = [&](auto wmc) {
    constexpr bool LD = decltype(wmc)::value != 0;
    constexpr int DMP = 2 * G1_STAGE;
    const char* pa = nullptr;
    unsigned vob = 0;
    f32x4 ca = {0.f, 0.f, 0.f, 0.f}, cb = {0.f, 0.f, 0.f, 0.f}, cd = ca;
    bf16x8 sel = {0, 0, 0, 0, 0, 0, 0, 0};
    int tro = 0;
    const int psh = (wid == 2 && !tya) ? 1 : 0, pstr = wid < 3 ? qstr : F_ * 2;
    const char* const pbase = wid == 0 ? qb : (wid == 1 ? qb + dp1 : (wid == 2 ? mb0 : gb));
    int lane = threadIdx.x;
    asm volatile("; %1" : "+v"(lane) : "n"(LD ? 1 : 0));
    lane &= 63;
    const int fr = lane & 15, fq = lane >> 4, q4 = fr >> 2, pp = lane & 3;
    const int ff = ((q4 >> 1) & 1) | ((fq & 1) << 1);
    int aoff[2], boff[2];
#pragma unroll
    for (int ks = 0; ks < 2; ++ks) aoff[ks] = (wm * 64 + fr) * 128 + (((ks * 4 + fq) ^ (fr & 7)) * 16);
#pragma unroll
    for (int j = 0; j < 2; ++j) boff[j] = 32768 + (8 * fq + q4) * 128 + (((2 * wc + j) ^ ff) * 32) + pp * 8;
    unsigned vol = lane * 16;
    float sm = 0.f, sq = 0.f;
    const char* pf = (const char*)wd + (bid * 4 + wid) * 7168;
    long pstep = 64 * pstr;
    if (!LD) {
      const int rs = 256 + mtile * 8 + (lane >> 3), vs = ((lane & 7) >> 1) ^ (((rs >> 1) & 1) | ((mtile & 1) << 1));
      pa = pbase + ((psh & vs) ? dgu : 0) + (rs * pstr + (vs >> psh) * 32 + (lane & 1) * 16);
      vob = rs * (F_ * 2) + vs * 32 + (lane & 1) * 16;
      tro = DMP + wid * 1024 + q4 * 128 + ((fq ^ ((q4 >> 1) | ((mtile & 1) << 1))) * 32) + pp * 8;
      const short one = fr == fq ? 0x3f80 : 0;
      sel = bf16x8{one, one, one, one, one, one, one, one};
      asm volatile("" : "+v"(tro), "+v"(vol), "+v"(sel), "+v"(ca), "+v"(cb), "+v"(sm), "+v"(sq));
      PIN_S(pf); PIN_S(pstep);
    }
    auto accum = [&](lds_cptr p, int ob) {
      ca = __builtin_amdgcn_mfma_f32_16x16x32_bf16(tr_frag(p), sel, ca, 0, 0, 0);
      if (wid == 0) cb = __builtin_amdgcn_mfma_f32_16x16x32_bf16(tr_frag(p + ob), sel, cb, 0, 0, 0);
    };
    auto xrow = [&](int i) {
      u64 a = (u64)(x + (size_t)srow * D_ + i * 256), b = (u64)(wo + (bid * 4 + wid) * 1024);
      PIN_S(a); PIN_S(b);
      asm volatile("global_load_dwordx4 %0, %1, %2" : "+v"(cd) : "v"(vol + (vol & (i < 4 ? 512 : 0)) * 3), "s"(i < 4 ? a : b) : "memory");
    };
    const int q32 = 32 * qstr;
    const char *sq0 = qb + q32, *sq1 = sq0 + dp1, *sg0 = gb + 32 * F_ * 2, *sg1 = sg0 + dgu, *sx = ab + (long)(kz + 128 - 32 * D_ * 2);
    u32x2 mp[2] = {__builtin_bit_cast(u32x2, mptr + q32), __builtin_bit_cast(u32x2, mptr + 2 * q32)};
    u64 cy[2];
    unsigned dst = shm_base + G1_STAGE + wm * 1024;
    int dbuf = G1_STAGE;
    auto stage = [&](int i0, int i1) __attribute__((always_inline)) {
#pragma unroll
        for (int i = i0; i < i1; ++i) {
          const int q = i >> 1, h = i & 1;
          const unsigned ld = dst + h * 4096 + (q < 5 ? 32768 + q * 8192 : (q - 5) * 8192);
          if (q < 2) glds16_s((q ? sq1 : sq0) + h * q32, voq, ld);
          else if (q == 2) glds16_v(__builtin_bit_cast(const char*, mp[h]), ld);
          else if (q < 5) glds16_s((q == 4 ? sg1 : sg0) + h * 32 * F_ * 2, vog, ld);
          else glds16_s(sx + ((q - 5) * 64 + h * 32) * D_ * 2, voa, ld);
        }
    };
    u64 cy0 = 0, rt0 = 0;
#pragma unroll
    for (int a = 0; a < 4; ++a) { acc[a][9] = f32x4{0.f, 0.f, 0.f, 0.f}; asm volatile("" : "+v"(acc[a][9])); }
    if (!LD) {
      xrow(0);
      WAIT_V(1);
    }
    asm volatile("" : "+v"(aoff[0]), "+v"(aoff[1]), "+v"(boff[0]), "+v"(boff[1]));
    bf16x8 kp[2];
    auto body = [&](bool upd, bool keep = false, bool first = false) __attribute__((always_inline)) {
      lds_cptr sb = shm3;
      auto rdb = [&](int g, int i) {
        lds_cptr p = sb + boff[g & 1] + (g >> 1) * 8192 + (i >> 1) * 4096;
        const int s = ((g + 1) & 1) * 2 + (i >> 1);
        if (i & 1) bh[s] = tr_read(p + 512); else bl[s] = tr_read(p);
      };
      auto rda = [&](int ks, int mf) { (ks ? af1 : af0)[mf] = *(const __attribute__((address_space(3))) bf16x8*)(sb + aoff[ks] + mf * 2048); };
      SB0();
      __builtin_amdgcn_s_waitcnt(0xC07F);
#pragma unroll
      for (int gg = 0; gg < 10; ++gg) {
        const int g = (gg + 9) % 10;
#pragma unroll
        for (int t = 0; t < 8; ++t) {
          const bool two = t >= 4 && !(upd && g >= 5 && g <= 8);
          SB0();
          if ((!two || (t & 1)) && !(first && g == 9)) mm(g, two ? t - 1 : t, two, first);
          SB0();
          if (first && !LD && (g == 3 || g == 7) && t == 0) BAR();
          if (g == 9) {
            if (t == 0 || t == 2) { rdb(0, t); rdb(0, t + 1); }
            if (t & 1) { rda((t >> 1) & 1, t >> 1); rda(((t >> 1) & 1) ^ 1, t >> 1); }
          } else if (t < 4) rdb(g + 1, t);
          else if (keep && g == 7) {
            if (t == 5) { WAIT_V(0); kp[0] = tr_frag(shm3 + tro); }
            if (t == 7) kp[1] = tr_frag(shm3 + tro + 4096);
          } else if (upd) {
            if (g == 5 && t == 4) { sq0 += 2 * q32; PIN_S(sq0); }
            if (g == 5 && t == 5) { sq1 += 2 * q32; PIN_S(sq1); }
            if (g == 5 && t == 6) { sg0 += 64 * F_ * 2; PIN_S(sg0); }
            if (g == 6 && t == 4) { sg1 += 64 * F_ * 2; PIN_S(sg1); }
            if (g == 6 && t == 5) { sx += 128; PIN_S(sx); }
            if (g == 6 && t == 6) { dst -= dbuf; PIN_S(dst); }
            if (g == 7 && t == 4) asm volatile("v_add_co_u32 %0, %2, %4, %0\n\tv_add_co_u32 %1, %3, %4, %1" : "+v"(mp[0][0]), "+v"(mp[1][0]), "=&s"(cy[0]), "=&s"(cy[1]) : "s"(2 * q32));
            if (g == 7 && t == 6) asm volatile("v_addc_co_u32 %0, vcc, 0, %0, %2\n\tv_addc_co_u32 %1, vcc, 0, %1, %3" : "+v"(mp[0][1]), "+v"(mp[1][1]) : "s"(cy[0]), "s"(cy[1]) : "vcc");
            if (g == 8 && t == 4) { aoff[0] += dbuf; aoff[1] += dbuf; asm volatile("" : "+v"(aoff[0]), "+v"(aoff[1])); }
            if (g == 8 && t == 5) { boff[0] += dbuf; boff[1] += dbuf; asm volatile("" : "+v"(boff[0]), "+v"(boff[1])); }
            if (g == 8 && t == 6) { dbuf = -dbuf; PIN_S(dbuf); }
          }
        }
      }
      SB0();
    };
    TS1(0);
#define ARRP(o, c) do {} while (0)
    if (LD) {
      stage(0, PA);
      WAIT_V(6 + PA);
      TS1(5);
      BARL();
      stage(PA, PB);
      WAIT_V(2 + PB);
      BAR();
      stage(PB, PC);
      WAIT_V(PC);
      BAR();
      stage(PC, 18);
      body(true, false, true);
      WAIT_V(0);
      BARL();
#pragma clang loop unroll(disable)
      for (int kt = 1; kt < NT - 1; ++kt) {
        if (__builtin_expect(kt == 5 + wid, 0)) prep(true);
        stage(0, 18);
        body(true);
        WAIT_V(0);
        BARL();
        SB0();
      }
      start2();
      body(false);
    } else {
      char* wsx;
      auto tile = [&](int kt, bool keep) __attribute__((always_inline)) {
        SB0();
        ARRP(wid * 64 + kt * 4, true);
        BARL();
        body(false, keep);
        ARRP(320 + kt * 4 + 4, wid == 0);
        if (keep) asm volatile("" : "+v"(kp[0]), "+v"(kp[1])); else WAIT_V(0);
      };
      auto next = [&]() __attribute__((always_inline)) {
        aoff[0] += dbuf; aoff[1] += dbuf; boff[0] += dbuf; boff[1] += dbuf;
        dbuf = -dbuf;
        asm volatile("" : "+v"(aoff[0]), "+v"(aoff[1]), "+v"(boff[0]), "+v"(boff[1]), "+v"(vol), "+v"(tro) :: "memory");
        wsx = ws;
        PIN_S(wsx);
      };
      auto share = [&]() {
        glds16_v(pa, shm_base + DMP + wid * 1024);
        if (wid == 0) glds16_s(gb + dgu, vob, shm_base + DMP + 4096);
      };
      auto kept = [&]() {
        ca = __builtin_amdgcn_mfma_f32_16x16x32_bf16(kp[0], sel, ca, 0, 0, 0);
        if (wid == 0) cb = __builtin_amdgcn_mfma_f32_16x16x32_bf16(kp[1], sel, cb, 0, 0, 0);
      };
      auto cpub = [&](u32x4 a, u32x4 b) {
        if ((vol & 0xc0) == 0) {
          const char* const cs = wsx + 101 * MB_ + ntile * 10240 + mtile * 256;
          const unsigned co = (vol & 0x30) * 4 + (vol >> 8) * 16;
          gst16(cs + wid * 2048, co, a);
          if (wid == 0) gst16(cs + 8192, co, b);
        }
      };
      auto side0 = [&](int kt) __attribute__((always_inline)) {
        if (kt < 2) {
          glds16_v(pa - 2 * pstep, shm_base + DMP + 7168);
          if (wid == 0) glds16_s(gb + dgu, vob - 128 * F_ * 2, shm_base + DMP + 7168);
        }
        share();
        u32x4 xw;
        asm volatile("v_mov_b32 %0, %4\n\tv_mov_b32 %1, %5\n\tv_mov_b32 %2, %6\n\tv_mov_b32 %3, %7" : "=&v"(xw[0]), "=&v"(xw[1]), "=&v"(xw[2]), "=&v"(xw[3])
                     : "v"(cd[0]), "v"(cd[1]), "v"(cd[2]), "v"(cd[3]));
        xrow(kt + 1);
        next();
        if (kt) kept();
        pa += pstep; vob += 64 * F_ * 2;
        accum(shm3 + tro + ((kt & 1) * G1_STAGE + 32768 + mtile * 1024 + wid * 7168 - DMP), 4 * 8192);
#pragma unroll
        for (int i = 0; i < 4; ++i) {
          float lo, hi;
          asm volatile("v_lshlrev_b32 %2, 16, %4\n\tv_and_b32 %3, 0xffff0000, %4\n\tv_add_f32 %0, %0, %2\n\tv_fmac_f32 %1, %2, %2\n\t"
                       "v_add_f32 %0, %0, %3\n\tv_fmac_f32 %1, %3, %3" : "+v"(sm), "+v"(sq), "=&v"(lo), "=&v"(hi) : "v"(xw[i]));
        }
      };
      BARL();
      body(false, false, true);
      WAIT_V(0);
      side0(0);
#pragma clang loop unroll(disable)
      for (int kt = 1; kt < 4; ++kt) {
        tile(kt, true);
        side0(kt);
      }
#pragma clang loop unroll(disable)
      for (int kt = 4; kt < 12; ++kt) {
        tile(kt, true);
        share();
        glds16_s(pf, vol, shm_base + DMP + 7168);
        next();
        kept();
        pa += pstep; vob += 64 * F_ * 2;
        pf = kt == 10 ? (const char*)wo + (bid * 4 + wid) * 2048 + 1024 : pf + 1024;
        if (kt < 8) {
          const unsigned q1 = bid * 4 + wid + (kt - 5) * 1024, q0 = q1 - 1024;
          if (q0 < 1152) gst16(wsx + 98 * MB_ + (q0 & 1) * 589824 + (q0 >> 1) * 1024, vol, *(const u32x4*)(shm + AUX + wid * 1024 + vol));
          if (q1 < 1152) {
            const char* src = (const char*)((q1 >> 1) < 448 ? wd + (size_t)(q1 >> 1) * 8 * D_ : wo + (size_t)((q1 >> 1) - 448) * 8 * D_);
            src += ((3 - ((unsigned)(uintptr_t)src >> 7)) & 7) * 128 + (q1 & 1) * 1024;
            glds16_s(src, (vol >> 7) * 2048 + (vol & 0x70), shm_base + AUX + wid * 1024);
          }
          u32x4 bk;
          asm volatile("v_mov_b32 %0, %1" : "=v"(bk[0]) : "s"(BLANK));
          bk[1] = bk[0]; bk[2] = bk[0]; bk[3] = bk[0];
          gst16(wsx + 64 * MB_ + bid * 16384 + wid * 4096 + (kt - 4) * 1024, vol, bk);
          if (tya && (kt == 4 || kt == 7)) gst16(wsx + 4 * MB_ + (mtile * 32 + ntile) * 4096 + wid * 2048 + (kt & 1) * 1024, vol, bk);
          if (kt == 4) cpub(bk, bk);
        } else {
          if (kt == 8) {
            const float ml = sm * (1.0f / 32), mean = half_sum(sm) * (1.0f / D_), dm = ml - mean;
            const float q = __builtin_fmaf(dm * 32.0f, dm, __builtin_fmaf(-ml, sm, sq));
            const unsigned ps = pack_stats(mean, fmaxf(half_sum(q) * (1.0f / D_), 0.f));
            const u64 now = __builtin_amdgcn_s_memrealtime();
            if ((vol & 0x1f0) == 0) gst16(wsx + 97 * MB_ + srow * 16, vol >> 5, u32x4{ps, (unsigned)now, ps, (unsigned)(now >> 32)});
          }
          if (kt == 9) {
            if (wid == 0) {
              u64 now;
              asm volatile("s_memrealtime %0\n\ts_waitcnt lgkmcnt(0)" : "=s"(now) :: "memory");
              if (vol == 0) asm volatile("global_store_dwordx2 %0, %1, %2 sc1" :: "v"(vol), "v"(now), "s"(wsx + 100 * MB_ + 131072 + bid * 8) : "memory");
            }
            if (wid == 2) glds16_s1(wsx + 102 * MB_ + (bid & ~1) * 512, vol, shm_base + DMP + 7168);
          }
        }
      }
      asm volatile("" :: "v"(cd));
      if (wid >= 2) glds16_s1(wsx + 100 * MB_ + 131072 + (wid - 2) * 1024, vol, shm_base + DMP + 3072 + wid * 1024);
      tile(12, true);
      next();
      kept();
      const int lane = vol >> 4;
      glds16_s1(wsx + 97 * MB_ + (m0 + wid * 64) * 16, ((lane & 32) + 2 * (lane & 15) + ((lane >> 4) & 1)) * 16, shm_base + AUX + wid * 1024);
      asm volatile("s_nop 7\n\ts_nop 3" : "+v"(ca), "+v"(cb));
      cpub(__builtin_bit_cast(u32x4, ca), __builtin_bit_cast(u32x4, cb));
      if (wid >= 2) {
        u32x4 c4 = *(const u32x4*)(shm + DMP + 3072 + wid * 1024 + vol);
        bool ok = __all(fresh(((u64)c4[1] << 32) | c4[0], t_own) & fresh(((u64)c4[3] << 32) | c4[2], t_own));
        while (__builtin_expect(!ok, 0) && __builtin_amdgcn_s_memrealtime() - t_own < TIMEOUT_ / 10) {
          const __amdgpu_buffer_rsrc_t rs = __builtin_amdgcn_make_buffer_rsrc((void*)(wsx + 100 * MB_ + 131072), 0, 2048, 0x00020000);
          asm volatile("" ::: "memory");
          c4 = __builtin_bit_cast(u32x4, __builtin_amdgcn_raw_buffer_load_b128(rs, (wid - 2) * 1024 + vol, 0, 16));
          ok = __all(fresh(((u64)c4[1] << 32) | c4[0], t_own) & fresh(((u64)c4[3] << 32) | c4[2], t_own));
        }
        *(unsigned*)(shm + CNT - 4 + wid * 4) = ok ? 1u : 0u;
      }
      tile(13, false);
      next();
      u32x4 cw[4];
      auto cgather = [&]() {
        const __amdgpu_buffer_rsrc_t rc = __builtin_amdgcn_make_buffer_rsrc((void*)(wsx + 101 * MB_ + ntile * 10240), 0, 5 * 2048, 0x00020000);
        const unsigned vcs = (vol < 512 ? wid * 2048 : 8192) + (vol & 256) * 4 + (vol & 0xf0);
#pragma unroll
        for (int i = 0; i < 4; ++i) cw[i] = __builtin_bit_cast(u32x4, __builtin_amdgcn_raw_buffer_load_b128(rc, vcs + i * 256, 0, 16));
      };
      {
        const int ln = wid * 64 + (vol >> 4);
        u32x4 gr = *(const u32x4*)(shm + AUX + ln * 16);
        if (__builtin_expect(!__all((gr[0] == gr[2]) & fresh(((u64)gr[3] << 32) | gr[1], t_own)), 0)) {
          const __amdgpu_buffer_rsrc_t rs = __builtin_amdgcn_make_buffer_rsrc((void*)(wsx + 97 * MB_ + m0 * 16), 0, 4096, 0x00020000);
          const int prow = (ln & 0xe0) + 2 * (ln & 15) + ((ln >> 4) & 1);
          for (;;) {
            gr = __builtin_bit_cast(u32x4, __builtin_amdgcn_raw_buffer_load_b128(rs, prow * 16, 0, 16));
            if (__all((gr[0] == gr[2]) & fresh(((u64)gr[3] << 32) | gr[1], t_own))) break;
            if (__builtin_amdgcn_s_memrealtime() - t_own > TIMEOUT_) { gr[0] = row_stats_serial(x + (size_t)(m0 + prow) * D_); break; }
            __builtin_amdgcn_s_sleep(2);
          }
        }
        cgather();
        const float nm = -lo_f(gr[0]), dn = hi_f(gr[0]), r = __builtin_amdgcn_rcpf(dn);
        auto mul = [](float a, float b) { float c; asm("v_mul_f32 %0, %1, %2" : "=v"(c) : "v"(a), "v"(b)); return c; };
        const f32x4 fc = {nm, -2.3022081986f * r, mul(mul(mul(-0.10294324f, r), r), r), mul(dn, dn)};
        *(f32x4*)(shm + AUX + ln * 16) = fc;
        float* const tw = (float*)(shm + 2 * G1_STAGE + 4096 + wid * 768) + ln;
#pragma unroll
        for (int i = 0; i < 4; ++i) tw[i * 64] = fc[i];
        ((float*)(shm + AUX + 4096))[384 + ln] = r;
        cok = __builtin_amdgcn_readfirstlane(cnt[1] & cnt[2]) != 0u;
        tile2(0);
        wy = wsetup(wd, 0, vol >> 4, cok);
      }
      tile(14, false);
      next();
      {
        while (__builtin_expect(blank(cw) && cok, 0)) {
          __builtin_amdgcn_s_sleep(2);
          asm volatile("" ::: "memory");
          cgather();
        }
        f32x4 t;
#pragma unroll
        for (int i = 0; i < 4; ++i) {
          float a, b;
          asm("v_add_f32 %0, %2, %3\n\tv_add_f32 %1, %4, %5\n\tv_add_f32 %0, %0, %1" : "=&v"(a), "=&v"(b) : "v"(cw[0][i]), "v"(cw[1][i]), "v"(cw[2][i]), "v"(cw[3][i]));
          const auto r2 = __builtin_amdgcn_permlane16_swap(__float_as_uint(a), __float_as_uint(a), false, false);
          asm("v_add_f32 %0, %1, %2" : "=v"(t[i]) : "v"(r2[0]), "v"(r2[1]));
        }
        const int l = vol >> 4;
        if (__builtin_expect(!cok, 0)) {
          const int gq = (l >> 2) & 3;
          const char* pc = l < 32 ? pbase + ((psh & gq) ? dgu : 0) + (gq >> psh) * 32 + (l & 3) * 8 : gb + dgu + (l & 15) * 8;
          t = f32x4{0.f, 0.f, 0.f, 0.f};
#pragma unroll 1
          for (int k = 0; k < D_; ++k) { const u32x2 w = *(const __attribute__((address_space(1))) u32x2*)(pc + (size_t)k * (l < 32 ? pstr : F_ * 2)); t += f32x4{lo_f(w[0]), hi_f(w[0]), lo_f(w[1]), hi_f(w[1])}; }
        }
        if (l < 32 || wid == 0)
          *(f32x4*)((float*)(shm + AUX + 4096) + ((l >> 3) & 1) * 192 + (l < 32 ? wid : 4) * 32 + (l & 7) * 4) = t;
        __builtin_amdgcn_s_setprio(2);
      }
      SB0();
      ARRP(wid * 64 + 60, true);
      BARL();
      body(false);
    }
#pragma unroll
    for (int t = 0; t < 8; t += 2) mm(9, t, true);
    asm volatile("s_nop 7\n\ts_nop 3");
    SB0();
    TS1(3);
    if (!LD) {
#pragma unroll
      for (int i = 0; i < 3; ++i) {
        const unsigned r = ((role * 4 + (i & 1) * 2) * 14 + (i < 2 ? 10 : 0) + (wm & 2)) * 32;
        sw[0] = wy.wb[0] + (r << wy.sh[0]); sw[1] = wy.wb[1] + (r << wy.sh[1]);
        asm volatile(G2_W :: [l] "s"((i + 1) * 16384), G2_WOP(wy) : "memory", "scc", "m0");
      }
    } else {
      __builtin_amdgcn_s_setprio(3);
      WAIT_V(0);
    }
  };
  if (wid >= 4) kloop(std::integral_constant<int, 1>{});
  else kloop(std::integral_constant<int, 0>{});
  __builtin_amdgcn_sched_barrier(0);
  int tz = threadIdx.x;
  asm volatile("" : "+v"(tz));
  __builtin_amdgcn_sched_barrier(0);
  const int li = tz & 15, lq = (tz >> 4) & 3, ln = tz & 63;
  const __amdgpu_buffer_rsrc_t ra = __builtin_amdgcn_make_buffer_rsrc((void*)abuf, 0, 16 * 72 * 16384, 0x00020000);
  float* const cfin = (float*)(shm + AUX + 4096);
  float* const svr = cfin + 384;
  float* const tb = (float*)(shm + 2 * G1_STAGE + 4096 + wm * 1024);
  auto signal = [&](unsigned v) {
    unsigned a, b;
    asm volatile("v_mov_b32 %0, %2\n\tv_mov_b32 %1, %3\n\ts_mov_b64 exec, 1\n\tds_add_u32 %0, %1\n\ts_mov_b64 exec, -1"
                 : "=&v"(a), "=&v"(b) : "s"(shm_base + CNT), "s"(v) : "memory");
  };
  auto await = [&](unsigned mask, unsigned val) {
    unsigned a, t;
    asm volatile("v_mov_b32 %0, %2\n1:\n\tds_read_b32 %1, %0\n\ts_waitcnt lgkmcnt(0)\n\tv_readfirstlane_b32 vcc_lo, %1\n\ts_and_b32 vcc_lo, vcc_lo, %3\n\t"
                 "s_cmp_eq_u32 vcc_lo, %4\n\ts_cbranch_scc1 2f\n\ts_sleep 1\n\ts_branch 1b\n2:" : "=&v"(a), "=&v"(t) : "s"(shm_base + CNT), "s"(mask), "s"(val) : "memory", "scc", "vcc");
  };
  auto client = [&](int l) { const int m128 = mtile * 2 + ((l >> 4) & 1); return (((l & 7) * 4 + (m128 & 3)) * 8) + ((m128 >> 2) ^ ((l & 8) ? 5 : 0)); };
  auto notify = [&](int l0, int nl) __attribute__((always_inline)) {
    u64 now;
    asm volatile("s_memrealtime %0\n\ts_waitcnt lgkmcnt(0)" : "=s"(now) :: "memory");
    int l = threadIdx.x;
    asm volatile("" : "+v"(l));
    l &= 63;
    const __amdgpu_buffer_rsrc_t rd1 = __builtin_amdgcn_make_buffer_rsrc((void*)(done1 + ntile), 0, 256 * 512, 0x00020000);
    if (l < nl) __builtin_amdgcn_raw_buffer_store_b64(u32x2{(unsigned)now, (unsigned)(now >> 32)}, rd1, client(l0 + l) * 512, 0, 16);
  };
  const int xo = ((xcd & 1) << 1) | ((xcd >> 1) & 1), te = (xcd >> 2) * 28 + 6 * (xo >> 1) + (xo & 1), tl = (xcd >> 2) * 28 + 12 + 4 * xo;
  const u64 te0 = __builtin_amdgcn_s_memrealtime();
  const __amdgpu_buffer_rsrc_t re = __builtin_amdgcn_make_buffer_rsrc((void*)(done1 + client((wm >> 1) * 16 + 8 - (xcd & 4) * 2) * 64 + 32 + (ntile & 15) * 2 + (wm & 1)),
                                                                      0, 7 * 16384 + 8, 0x00020000);
  auto early = [&]() {
    __builtin_amdgcn_raw_buffer_store_b64(u32x2{(unsigned)te0, (unsigned)(te0 >> 32)}, re, ln * 16384, 0, 16);
  };
  signal(1u << (8 * wc));
  if (wc) {
    await(0xffffu, 0x404u);
#pragma unroll
    for (int f = 0; f < 4; ++f) *(u32x4*)(shm + G2_X + wm * 4096 + f * 1024 + ln * 16) = xq[f];
    SB0();
    __builtin_amdgcn_s_setprio(1);
  }
  TS1(4);
  f32x4 rf[4];
#pragma unroll
  for (int mf = 0; mf < 4; ++mf) rf[mf] = *(const f32x4*)(shm + AUX + (wm * 64 + mf * 16 + li) * 16);
  const float* const cl = cfin + wc * 192 + lq * 4;
  const int sbase = ((mtile * 2 + (wm >> 1)) * 72 * 8 + (wm & 1) * 4) * 2048;
  auto putf = [&](int kg, int mf, u32x4 v) {
    __builtin_amdgcn_raw_buffer_store_b128(v, ra, ln * 16 + sbase + ((kg >> 1) * 8 + mf) * 2048 + (kg & 1) * 1024, 0, 16);
  };
  auto gelp = [&](f32x2 g, f32x2 u, f32x2 cg, f32x2 cu, f32x2 m2, f32x2 k12, f32x2 k22, f32x2 d2) {
    const f32x2 tg = __builtin_elementwise_fma(m2, cg, g), tu = __builtin_elementwise_fma(m2, cu, u);
    const f32x2 arg = tg * __builtin_elementwise_fma(tg * tg, k22, k12);
    const f32x2 dn = __builtin_elementwise_fma(f32x2{__builtin_amdgcn_exp2f(arg[0]), __builtin_amdgcn_exp2f(arg[1])}, d2, d2);
    const f32x2 r = (tg * tu) * f32x2{__builtin_amdgcn_rcpf(dn[0]), __builtin_amdgcn_rcpf(dn[1])};
    return pack2(r[0], r[1]);
  };
  auto gel2 = [&](int p, int kg, int mf0 = 0, int mf1 = 4) {
    f32x4 cg[2], cu[2];
#pragma unroll
    for (int j = 0; j < 2; ++j) { cg[j] = *(const f32x4*)(cl + p * 32 + j * 16); cu[j] = *(const f32x4*)(cl + p * 32 + 32 + j * 16); }
#pragma unroll
    for (int mf = mf0; mf < mf1; ++mf) {
      const f32x2 m2 = {rf[mf][0], rf[mf][0]}, k12 = {rf[mf][1], rf[mf][1]}, k22 = {rf[mf][2], rf[mf][2]}, d2 = {rf[mf][3], rf[mf][3]};
      u32x4 o;
#pragma unroll
      for (int i = 0; i < 4; ++i) {
        const int j = i >> 1, e = (i & 1) * 2;
        const f32x4 g = acc[mf][p * 2 + j], u = acc[mf][p * 2 + 2 + j];
        o[i] = gelp(f32x2{g[e], g[e + 1]}, f32x2{u[e], u[e + 1]}, f32x2{cg[j][e], cg[j][e + 1]}, f32x2{cu[j][e], cu[j][e + 1]}, m2, k12, k22, d2);
      }
      putf(kg, mf, o);
    }
  };
  int tsel = ntile & 1;
  asm volatile("" : "+s"(tsel));
  if (tsel != 0) {
    const int tq = (wc ? tl : te + 2 + (xo & 1)) + he;
    gel2(0, 2 * tq);
    gel2(3, 2 * tq + 1);
    const float cg = cfin[wc * 192 + 64 + li], cu = cfin[wc * 192 + 80 + li];
#pragma unroll
    for (int mf = 0; mf < 4; ++mf) {
      f32x4 t[4];
#pragma unroll
      for (int i = 0; i < 4; ++i) t[i] = *(const f32x4*)(tb + i * 64 + mf * 16 + lq * 4);
      const f32x4 g = acc[mf][4], u = acc[mf][5];
      u32x2 o;
#pragma unroll
      for (int e = 0; e < 4; e += 2)
        o[e >> 1] = gelp(f32x2{g[e], g[e + 1]}, f32x2{u[e], u[e + 1]}, f32x2{cg, cg}, f32x2{cu, cu}, f32x2{t[0][e], t[0][e + 1]},
                         f32x2{t[1][e], t[1][e + 1]}, f32x2{t[2][e], t[2][e + 1]}, f32x2{t[3][e], t[3][e + 1]});
      *(u32x2*)(shm + wm * 4096 + mf * 1024 + wc * 512 + li * 32 + lq * 8) = o;
      if (mf == EPOS && wc == 0) {
        TS1(1);
        WAIT_V(0);
        TS1(6);
        early();
      }
    }
    if (wc == 0) {
      signal(0x10000u << wid);
    } else {
      await(0x1000u << wid, 0x1000u << wid);
#pragma unroll
      for (int mf = 0; mf < 4; ++mf) putf(2 * (tl + 2) + he, mf, __builtin_bit_cast(u32x4, tr_frag(shm3 + wm * 4096 + mf * 1024 + ln * 8)));
    }
  } else {
    float rd[4];
#pragma unroll
    for (int mf = 0; mf < 4; ++mf) rd[mf] = svr[wm * 64 + mf * 16 + li];
    auto cvq = [&](int p, int mf, const f32x4& c0, const f32x4& c1) {
      u32x4 o;
#pragma unroll
      for (int j = 0; j < 2; ++j) {
        const f32x4 a = acc[mf][p * 2 + j], c4 = j ? c1 : c0;
        const f32x2 m2 = {rf[mf][0], rf[mf][0]}, r2 = {rd[mf], rd[mf]};
        f32x2 t0 = __builtin_elementwise_fma(m2, f32x2{c4[0], c4[1]}, f32x2{a[0], a[1]});
        f32x2 t1 = __builtin_elementwise_fma(m2, f32x2{c4[2], c4[3]}, f32x2{a[2], a[3]});
        if (p) { t0 *= r2; t1 *= r2; }
        o[2 * j] = pack2(t0[0], t0[1]); o[2 * j + 1] = pack2(t1[0], t1[1]);
      }
      return o;
    };
    const float cv0 = cfin[wc * 192 + 64 + li], cv1 = cfin[wc * 192 + 80 + li];
    auto cvv = [&](int mf) {
      const f32x4 n4 = *(const f32x4*)(tb + mf * 16 + lq * 4), r4 = *(const f32x4*)(svr + wm * 64 + mf * 16 + lq * 4);
      u32x4 o;
#pragma unroll
      for (int j = 0; j < 2; ++j) {
        const f32x4 a = acc[mf][4 + j];
        const f32x2 c2 = {j ? cv1 : cv0, j ? cv1 : cv0};
        const f32x2 t0 = __builtin_elementwise_fma(f32x2{n4[0], n4[1]}, c2, f32x2{a[0], a[1]}) * f32x2{r4[0], r4[1]};
        const f32x2 t1 = __builtin_elementwise_fma(f32x2{n4[2], n4[3]}, c2, f32x2{a[2], a[3]}) * f32x2{r4[2], r4[3]};
        o[2 * j] = pack2(t0[0], t0[1]); o[2 * j + 1] = pack2(t1[0], t1[1]);
      }
      return o;
    };
    u32x4 kx[4], vx[4], qx[4], hx[4];
    const bf16x4 ones = {0x3f80, 0x3f80, 0x3f80, 0x3f80};
    const f32x4 ck0 = *(const f32x4*)(cl + 32), ck1 = *(const f32x4*)(cl + 48);
    kx[2] = cvq(1, 2, ck0, ck1); kx[3] = cvq(1, 3, ck0, ck1);
    vx[2] = cvv(2); vx[3] = cvv(3);
    auto exo = [](int e) { return e * 4096 + (e >> 2) * (G2_A - 16384); };
    const bool needh = wm == 0 && mtile > 0;
    const __amdgpu_buffer_rsrc_t rh = __builtin_amdgcn_make_buffer_rsrc((void*)((char*)halo + ((needh ? mtile - 1 : mtile) * 32 + ntile + wc) * 4096), 0,
                                                                        needh || wm == 3 ? 4096 : 0, 0x00020000);
    if (wm < 3) {
      char* const ex = shm + exo(wc * 3 + wm) + ln * 16;
      *(u32x4*)ex = kx[2]; *(u32x4*)(ex + 1024) = kx[3]; *(u32x4*)(ex + 2048) = vx[2]; *(u32x4*)(ex + 3072) = vx[3];
      signal(0x10000u << wid);
    } else {
#pragma unroll
      for (int r = 0; r < 4; ++r) __builtin_amdgcn_raw_buffer_store_b128(r < 2 ? kx[2 + r] : vx[r], rh, ln * 16 + r * 1024, 0, 16);
    }
    auto hload = [&]() {
#pragma unroll
      for (int r = 0; r < 4; ++r) hx[r] = __builtin_bit_cast(u32x4, __builtin_amdgcn_raw_buffer_load_b128(rh, ln * 16 + r * 1024, 0, 16));
    };
    SB0();
    gel2(3, 2 * (wc ? tl + 3 : te) + he);
    SB0();
    if (wm == 0) hload();
    SB0();
    kx[0] = cvq(1, 0, ck0, ck1); kx[1] = cvq(1, 1, ck0, ck1);
    vx[0] = cvv(0); vx[1] = cvv(1);
    {
      const f32x4 cq0 = *(const f32x4*)(cl), cq1 = *(const f32x4*)(cl + 16);
#pragma unroll
      for (int mf = 0; mf < 4; ++mf) qx[mf] = cvq(0, mf, cq0, cq1);
    }
    f32x4 mk0, mk1;
    u32x2 pm;
#pragma unroll
    for (int j = 0; j < 4; ++j) {
      const int d = 4 * lq + j - li;
      mk0[j] = d >= 9 ? 0.f : -1.0e30f;
      mk1[j] = (unsigned)(d + 7) <= 7u ? 0.f : -1.0e30f;
    }
#pragma unroll
    for (int j = 0; j < 2; ++j) pm[j] = (4 * lq + 2 * j > li ? 0xffffu : 0u) | (4 * lq + 2 * j + 1 > li ? 0xffff0000u : 0u);
    auto max3 = [](float a, float b, float c) { float r; asm("v_max3_f32 %0, %1, %2, %3" : "=v"(r) : "v"(a), "v"(b), "v"(c)); return r; };
    auto max2 = [](float a, float b) { float r; asm("v_max_f32 %0, %1, %2" : "=v"(r) : "v"(a), "v"(b)); return r; };
    asm volatile("" : "+v"(kx[0]), "+v"(kx[1]), "+v"(vx[0]), "+v"(vx[1]), "+v"(qx[0]), "+v"(qx[1]), "+v"(qx[2]), "+v"(qx[3]), "+v"(mk0), "+v"(mk1), "+v"(pm));
    TS1(1);
    if (wc == 0) {
      if (wm == 0) WAIT_V(4); else WAIT_V(0);
      early();
    }
    if (wm > 0) {
      await(0x8000u << wid, 0x8000u << wid);
#pragma unroll
      for (int i = 0; i < 4; ++i) hx[i] = *(const u32x4*)(shm + exo(wc * 3 + wm - 1) + i * 1024 + ln * 16);
    } else if (__builtin_expect(cok || !needh, 1)) {
      while (__builtin_expect(blank(hx), 0)) {
        __builtin_amdgcn_s_sleep(1);
        asm volatile("" ::: "memory");
        hload();
      }
    } else {
      await(0xffffu, 0x404u);
      char* const fb = shm + exo(6 + wc) + ln * 16;
#pragma unroll 1
      for (int i = 0; i < 32; ++i) {
        const int rec = i >> 3, e = i & 7;
        const int row = m0 - 32 + 2 * (rec < 2 ? li : lq * 4 + (e & 3)) + (rec & 1);
        const int col = (rec < 2 ? D_ + lq * 4 + (e & 3) : 2 * D_ + li) + (ntile + wc) * 32 + (e >> 2) * 16;
        const u16* xr = x + (size_t)row * D_;
        const unsigned stt = row_stats_serial(xr);
        const float mb = lo_f(stt);
        float a = 0.f;
#pragma unroll 1
        for (int k = 0; k < D_; ++k) a += (bf2f(xr[k]) - mb) * bf2f(wqkv[(size_t)k * 3072 + col]);
        *(u16*)(fb + rec * 1024 + e * 2) = f2bf(a / hi_f(stt));
      }
#pragma unroll
      for (int i = 0; i < 4; ++i) hx[i] = *(const u32x4*)(fb + i * 1024);
    }
    TS1(6);
    {
      const f32x4 z = {0.f, 0.f, 0.f, 0.f};
      const float mz = mtile == 0 && wm == 0 ? -1.0e30f : 0.f;
      const f32x4 mkf = mk0 + f32x4{mz, mz, mz, mz};
      f32x4 s0[4], s1[4];
#pragma unroll
      for (int i = 0; i < 4; ++i) {
        const int n = i ^ 2;
        const bf16x8 qf = __builtin_bit_cast(bf16x8, qx[n]);
        s1[n] = __builtin_amdgcn_mfma_f32_16x16x32_bf16(__builtin_bit_cast(bf16x8, kx[n]), qf, mk1, 0, 0, 0);
        s0[n] = __builtin_amdgcn_mfma_f32_16x16x32_bf16(__builtin_bit_cast(bf16x8, n < 2 ? hx[n] : kx[n - 2]), qf, n < 2 ? mkf : mk0, 0, 0, 0);
      }
      SB0();
      asm volatile("s_nop 7\n\ts_nop 3" : "+v"(s0[0]), "+v"(s1[0]), "+v"(s0[1]), "+v"(s1[1]), "+v"(s0[2]), "+v"(s1[2]), "+v"(s0[3]), "+v"(s1[3]));
      float mx[4];
#pragma unroll
      for (int n = 0; n < 4; ++n)
#pragma unroll
        for (int j = 0; j < 4; ++j) s0[n][j] = max2(s0[n][j], s1[n][j]);
#pragma unroll
      for (int n = 0; n < 4; ++n) mx[n] = max3(s0[n][0], s0[n][1], s0[n][2]);
#pragma unroll
      for (int n = 0; n < 4; ++n) mx[n] = max2(mx[n], s0[n][3]);
#pragma unroll
      for (int n = 0; n < 4; ++n) { auto r = __builtin_amdgcn_permlane16_swap(__float_as_uint(mx[n]), __float_as_uint(mx[n]), false, false);
        mx[n] = max2(__uint_as_float(r[0]), __uint_as_float(r[1])); }
#pragma unroll
      for (int n = 0; n < 4; ++n) { auto r = __builtin_amdgcn_permlane32_swap(__float_as_uint(mx[n]), __float_as_uint(mx[n]), false, false);
        mx[n] = max2(__uint_as_float(r[0]), __uint_as_float(r[1])); }
      SB0();
      constexpr float cs2 = 0.17677669529663687f * 1.4426950408889634f;
      f32x4 os[4], o0[4], o1[4];
#pragma unroll
      for (int n = 0; n < 4; ++n) {
        const float c = cs2 * rd[n], nm = -mx[n] * c;
        const f32x4 ar = __builtin_elementwise_fma(s0[n], f32x4{c, c, c, c}, f32x4{nm, nm, nm, nm});
        const u32x2 pk = {pack2(__builtin_amdgcn_exp2f(ar[0]), __builtin_amdgcn_exp2f(ar[1])), pack2(__builtin_amdgcn_exp2f(ar[2]), __builtin_amdgcn_exp2f(ar[3]))};
        const bf16x4 pa = __builtin_bit_cast(bf16x4, pk), p0 = __builtin_bit_cast(bf16x4, pk & pm), p1 = __builtin_bit_cast(bf16x4, pk & ~pm);
        const u32x4 vp = n < 2 ? hx[2 + n] : vx[n - 2];
        auto half = [](const u32x4& v, int h) { return __builtin_bit_cast(bf16x4, u32x2{v[2 * h], v[2 * h + 1]}); };
        os[n] = __builtin_amdgcn_mfma_f32_16x16x16bf16_1k(ones, pa, z, 0, 0, 0);
        o0[n] = __builtin_amdgcn_mfma_f32_16x16x16bf16_1k(half(vx[n], 0), p1, __builtin_amdgcn_mfma_f32_16x16x16bf16_1k(half(vp, 0), p0, z, 0, 0, 0), 0, 0, 0);
        o1[n] = __builtin_amdgcn_mfma_f32_16x16x16bf16_1k(half(vx[n], 1), p1, __builtin_amdgcn_mfma_f32_16x16x16bf16_1k(half(vp, 1), p0, z, 0, 0, 0), 0, 0, 0);
      }
#pragma unroll
      for (int n = 0; n < 4; ++n) {
        const float inv = __builtin_amdgcn_rcpf(os[n][0]);
        const f32x4 n0 = o0[n] * inv, n1 = o1[n] * inv;
        putf(112 + ntile + wc, n, u32x4{pack2(n0[0], n0[1]), pack2(n0[2], n0[3]), pack2(n1[0], n1[1]), pack2(n1[2], n1[3])});
      }
    }
  }
  TS1(2);
  {
  __builtin_amdgcn_sched_barrier(0);
  int tid = threadIdx.x;
  asm volatile("" : "+v"(tid));
  __builtin_amdgcn_sched_barrier(0);
  const int lane = tid & 63;
  const int wm = wid >> 1, wn = wid & 1;
  const int m0 = mt2 * 128, n0 = nt2 * 128;
  constexpr int PD = G2_NSTAGE;
  const int ny = (xcd & 1) ? NY - DLT : NY + DLT;
  const unsigned va = lane * 16;
  auto stage_y = [&]() {
    asm volatile("s_add_u32 m0, %[l], %[oa]\n\ts_nop 0\n\tglobal_load_lds_dwordx4 %[va], %[a] sc1\n\tglobal_load_lds_dwordx4 %[va], %[a] offset:1024 sc1\n\t"
                 "global_load_lds_dwordx4 %[va], %[a] offset:2048 sc1\n\tglobal_load_lds_dwordx4 %[va], %[a] offset:3072 sc1\n\t" G2_W
                 :: [l] "s"(sl), [oa] "s"(shm_base + G2_A + (p ^ role) * 4096), [a] "s"(sa), [va] "v"(va), G2_WOP(wy) : "memory", "scc", "m0");
    ++st;
    sa += 16384;
    sl = (sl + 16384) & 65535;
    wsel();
  };
  u64 ready = 0;
  const u64* const mbox = &done1[bid * 64 + lane];
  auto seen = [&](u64 v) __attribute__((always_inline)) {
    const u64 r = __ballot(fresh(v, t_own));
    u64 d;
    asm("s_bitreplicate_b64_b32 %0, %1" : "=s"(d) : "s"((unsigned)r >> (16 * role)));
    ready |= r | (d << 32);
  };
  auto need = [&](u64 mask, bool renew) __attribute__((always_inline)) {
    if ((ready & mask) != mask) {
      seen(__hip_atomic_load(mbox, RLX_AGENT));
      while ((ready & mask) != mask) {
        __builtin_amdgcn_s_sleep(PSLEEP);
        if (renew && !cok) notify(0, 32);
        seen(__hip_atomic_load(mbox, RLX_AGENT));
      }
    }
    asm volatile("" ::: "memory");
  };
  u64 fl = 0;
  auto peek = [&]() { asm volatile("global_load_dwordx2 %0, %1, %2 sc1" : "+v"(fl) : "v"(va >> 1), "s"(done1 + bid * 64) : "memory"); };
  auto check = [&](u64 mask, bool renew) __attribute__((always_inline)) {
    asm volatile("" : "+v"(fl));
    seen(fl);
    if (__builtin_expect((ready & mask) != mask, 0)) need(mask, renew);
  };
  if (wid >= 4) {
    __builtin_amdgcn_s_setprio(0);
    BARL();
    stage_y();
    stage_y();
    BAR();
    if (p == 1) peek();
    stage_y();
    WAIT_V(16);
    BAR();
    if (p == 0) notify(0, 32);
#define G2_A2 "global_load_lds_dwordx4 %[va], %[a] sc1\n\tglobal_load_lds_dwordx4 %[va], %[a] offset:1024 sc1"
#define G2_A4 G2_A2 "\n\tglobal_load_lds_dwordx4 %[va], %[a] offset:2048 sc1\n\tglobal_load_lds_dwordx4 %[va], %[a] offset:3072 sc1"
#define G2_SYNC "\n\ts_mov_b32 m0, %[la]\n\ts_waitcnt vmcnt(%[n])\n\ts_barrier\n\t"
    const unsigned oy = shm_base + G2_A + (p ^ role) * 4096, os = shm_base + G2_A + (p >> 1) * 8192 + (p & 1) * 2048;
    asm volatile("s_mov_b32 m0, %[la]\n\ts_nop 0\n\t" G2_A4 :: [la] "s"(sl + oy), [a] "s"(sa), [va] "v"(va) : "memory", "m0");
    auto step = [&](long adv = 16384) __attribute__((always_inline)) {
      wsel();
      ++st;
      sa += adv;
      const unsigned sn = (sl + 16384) & 65535;
      asm volatile(G2_W G2_SYNC G2_A4 :: [l] "s"(sl), [la] "s"(sn + oy), [n] "n"(16), [a] "s"(sa), [va] "v"(va), G2_WOP(wy) : "memory", "scc", "m0");
      sl = sn;
    };
    auto chk1 = [&](u64 mask) __attribute__((always_inline)) { if (p == 1) { WAIT_V(16); check(mask, true); } };
    step();
    chk1(0xcc00cc00ull << 32);
    step(); step();
    if (p == 1) peek();
    step(); step();
    chk1(0x0f0fu << (16 * role));
#pragma unroll 1
    for (int g = 7; g < 13; ++g) step();
    if (p == 1) peek();
    step(); step();
    chk1((0xffffu << (16 * role)) | 0xf555f555u);
#pragma unroll 1
    for (int g = 15; g < min(ny, NY) - 5; ++g) step();
    if (ny > NY) {
      step(((1 - 2 * role) * NY + 1 - DLT) * 16384);
#pragma unroll
      for (int i = 1; i < DLT; ++i) step();
    }
    wsel();
    sa = sz;
    {
      const unsigned sn = (sl + 16384) & 65535;
      asm volatile(G2_W G2_SYNC G2_A2 :: [l] "s"(sl), [la] "s"(sn + os), [n] "n"(16), [a] "s"(sa), [va] "v"(va), G2_WOP(wy) : "memory", "scc", "m0");
      sl = sn;
    }
    sw[0] = wz.wb[0]; sw[1] = wz.wb[1];
#pragma unroll
    for (int j = -3; j < 13; ++j) {
      if (j == GETP + 1 || j == GETP + 2)
        asm volatile("s_mov_b32 m0, %[l]\n\ts_nop 0\n\tglobal_load_lds_dwordx4 %[va], %[a] offset:%[o] sc1\n\tglobal_load_lds_dwordx4 %[va], %[a] offset:%[o]+1024 sc1"
                     :: [l] "s"(shm_base + G2_X + 16384 + p * 4096), [a] "s"((const char*)slab + (size_t)(bid ^ 5) * 16384 + p * 4096), [va] "v"(va),
                        [o] "n"((j - GETP - 1) * 2048) : "memory", "m0");
      sa += 16384;
      const unsigned sn = (sl + 16384) & 65535;
      constexpr int G = GETP;
      static_assert(GETP <= GETJ + 1, "");
      if (j < 12) asm volatile(G2_W G2_SYNC G2_A2 :: [l] "s"(sl), [la] "s"(sn + os), [n] "n"((j == -3 ? 14 : 12) + (j > G && j < G + 3 ? 2 : 0) + (j > G + 1 && j < G + 4 ? 2 : 0)),
                               [a] "s"(sa), [va] "v"(va), G2_WOP(wz) : "memory", "scc", "m0");
      else asm volatile(G2_W :: [l] "s"(sl), G2_WOP(wz) : "memory", "scc", "m0");
      sl = sn;
      sw[0] += ws0; sw[1] += ws1;
    }
    WAIT_V(12);
    BAR();
    WAIT_V(6);
    BAR();
    WAIT_V(0);
    BAR();
    asm volatile("" : "+v"(fl));
    TS(5);
    if (tid < 258) __hip_atomic_store(lane ? done2 + bid * 8 : &acks[bid], __builtin_amdgcn_s_memrealtime(), RLX_AGENT);
    if (__builtin_expect(!cok, 0) && p == 0) {
      const bool mine = lane <= 32;
      const u64* const pa = &acks[lane < 32 ? client(lane) : bid ^ 5];
      while (!__all(!mine || fresh(__hip_atomic_load(pa, RLX_AGENT), t_own))) {
        notify(0, 32);
        if (lane == 0) __hip_atomic_store(done2 + bid * 8, __builtin_amdgcn_s_memrealtime(), RLX_AGENT);
        __builtin_amdgcn_s_sleep(32);
      }
    }
  } else {
    TS(6);
    f32x4 acc[4][4], sacc[2][4];
    const int fr = lane & 15, fq = lane >> 4, q4 = fr >> 2, pp = lane & 3;
    const int ff = ((q4 >> 1) & 1) | ((fq & 1) << 1);
    int aof = G2_A + wm * 8192 + lane * 16;
    int boff[4];
#pragma unroll
    for (int j = 0; j < 4; ++j) boff[j] = wn * 8192 + (8 * fq + q4) * 128 + ((j ^ ff) * 32) + pp * 8;
    int vo = wid * 4096 + lane * 16;
    int vx = ((m0 + wm * 64 + role * 32 + 2 * fr) * D_ + n0 + wn * 64) * 2 + (fq & 1) * 32 + (fq >> 1) * 16;
    lds_cptr px = shm3 + G2_X + wid * 4096 + (fq >> 1) * 512 + fr * 16 + (fq & 1) * 8;
    asm volatile("" : "+v"(vo), "+v"(vx), "+v"(px));
#define SB() __builtin_amdgcn_sched_barrier(0)
    struct Fr { bf16x8 a[4]; bf16x4 bl[4], bh[4]; };
    Fr fa[2], fb[2];
    auto rd = [&](Fr* f, int odd, int t) {
      constexpr int ord[12] = {0, 1, 8, 2, 3, 4, 5, 6, 7, 9, 10, 11};
      constexpr int seq[24] = {0, 1, 2, 12, 13, 14, 15, 16, 3, 4, 5, 6, 17, 18, 19, 20, 7, 8, 9, 21, 10, 22, 11, 23};
      lds_cptr sb = shm3 + odd * 16384;
      const int ks = seq[t] / 12, o = ord[seq[t] % 12];
      if (o >= 8) f[ks].a[o - 8] = *(const __attribute__((address_space(3))) bf16x8*)(sb + aof + (o - 8) * 2048 + ks * 1024);
      else if (o & 1) f[ks].bh[o >> 1] = tr_read(sb + boff[o >> 1] + ks * 4096 + 512);
      else f[ks].bl[o >> 1] = tr_read(sb + boff[o >> 1] + ks * 4096);
    };
    auto bfr = [&](const Fr& c, int j) { return __builtin_shufflevector(c.bl[j], c.bh[j], 0, 1, 2, 3, 4, 5, 6, 7); };
    auto mf1 = [](f32x4& c, bf16x8 b, bf16x8 a, bool z = false) {
      if (z) asm volatile("v_mfma_f32_16x16x32_bf16 %0, %1, %2, 0" : "=&v"(c) : "v"(b), "v"(a));
      else asm volatile("v_mfma_f32_16x16x32_bf16 %0, %1, %2, %0" : "+v"(c) : "v"(b), "v"(a));
    };
    auto mf2 = [](f32x4& c, bf16x8 b, bf16x8 a, bf16x8 b2, bf16x8 a2, bool z) {
      if (z) asm volatile("v_mfma_f32_16x16x32_bf16 %0, %1, %2, 0\n\tv_mfma_f32_16x16x32_bf16 %0, %3, %4, %0" : "=&v"(c) : "v"(b), "v"(a), "v"(b2), "v"(a2));
      else asm volatile("v_mfma_f32_16x16x32_bf16 %0, %1, %2, %0\n\tv_mfma_f32_16x16x32_bf16 %0, %3, %4, %0" : "+v"(c) : "v"(b), "v"(a), "v"(b2), "v"(a2));
    };
    auto pairs = [&](f32x4 (*ac)[4], const Fr* c, int nq, int mx, int nf, auto fill, bool z = false) {
#pragma unroll
      for (int q = 0; q < nq; ++q) {
        const int mf = (q >> 2) ^ mx, j = (q & 4) ? 3 - (q & 3) : q & 3, ks = q & 1;
        SB();
        if (2 * q >= nf) mf2(ac[mf][j], bfr(c[ks], j), c[ks].a[mf], bfr(c[ks ^ 1], j), c[ks ^ 1].a[mf], z);
        else {
          mf1(ac[mf][j], bfr(c[ks], j), c[ks].a[mf], z);
          SB();
          fill(2 * q);
          SB();
          mf1(ac[mf][j], bfr(c[ks ^ 1], j), c[ks ^ 1].a[mf]);
          SB();
          fill(2 * q + 1);
        }
      }
      SB();
    };
    auto flip = [&]() {
      aof ^= 32768; boff[0] ^= 32768; boff[1] ^= 32768; boff[2] ^= 32768; boff[3] ^= 32768;
      asm volatile("" : "+v"(aof), "+v"(boff[0]), "+v"(boff[1]), "+v"(boff[2]), "+v"(boff[3]));
    };
    auto bar = [&]() { __builtin_amdgcn_s_waitcnt(0xC07F); BAR(); };
    auto ystg = [&](const Fr* c, Fr* n, int odd, bool z = false) {
      pairs(acc, c, 16, 0, odd ? 30 : 24, [&](int t) {
        if (t < 24) rd(n, odd, t);
        else if (t == 24) asm volatile("s_nop 0");
        else { int& a = t == 29 ? aof : boff[t - 25]; a ^= 32768; asm volatile("" : "+v"(a)); }
      }, z);
    };
    auto stg = [&](const Fr* c, Fr* n, int odd, bool z = false) { pairs(sacc, c, 8, 0, 10, [&](int t) { rd(n, odd, 2 * t); rd(n, odd, 2 * t + 1); }, z); };
    asm volatile("" : "+v"(aof), "+v"(boff[0]), "+v"(boff[1]), "+v"(boff[2]), "+v"(boff[3]));
    await(0xffffu, 0x404u);
    auto lda = [&](int i) {
      asm volatile("s_mov_b32 m0, %[l]\n\ts_nop 0\n\tglobal_load_lds_dwordx4 %[va], %[a] sc1\n\tglobal_load_lds_dwordx4 %[va], %[a] offset:1024 sc1\n\t"
                   "global_load_lds_dwordx4 %[va], %[a] offset:2048 sc1\n\tglobal_load_lds_dwordx4 %[va], %[a] offset:3072 sc1"
                   :: [l] "s"(shm_base + G2_A + (i + 1) * 16384 + (p ^ role) * 4096), [a] "s"(sa + i * 16384), [va] "v"(va) : "memory", "m0");
    };
    need(0x00330033ull << 32, false);
    lda(0);
    lda(1);
    need(0x00ff00ffull << 32, false);
    TS(0);
    lda(2);
    WAIT_V(8);
    signal(1u << 24);
    await(0xff000000u, 4u << 24);
    __builtin_amdgcn_s_waitcnt(0xC07F);
    SB();
#pragma unroll
    for (int t = 0; t < 24; ++t) rd(fa, 1, t);
    flip();
#define CPRIO 0
    __builtin_amdgcn_s_setprio(CPRIO);
    WAIT_V(4);
    TS(5);
    peek();
    bar();
    TS(7);
    ystg(fa, fb, 0, true);
    WAIT_V(1);
    bar();
    ystg(fb, fa, 1);
    WAIT_V(0);
    check(0x33003300ull << 32, false);
    constexpr int PAR = (NY - DLT + 1) & 1;
#pragma unroll 1
    for (int g = 2; g < ny - 2; g += 2) {
      bar();
      ystg(fa, fb, 0);
      bar();
      ystg(fb, fa, 1);
    }
    if (PAR) {
      bar();
      ystg(fa, fb, 0);
    }
    Fr* const f0 = PAR ? fb : fa;
    Fr* const f1 = PAR ? fa : fb;
    bar();
    {
      const __amdgpu_buffer_rsrc_t rs = __builtin_amdgcn_make_buffer_rsrc((void*)((char*)slab + (size_t)bid * 16384), 0, 16384, 0x00020000);
      u32x4 pk;
      pairs(acc, f0, 16, 2, 32, [&](int t) {
        if (t < 20) rd(f1, PAR, t);
        else {
          const int f = (t - 20) / 3, e = (t - 20) % 3;
          if (e < 2) { const f32x4 a = acc[2 + (f >> 1)][(f & 1) * 2 + e]; pk[2 * e] = pack2(a[0], a[1]); pk[2 * e + 1] = pack2(a[2], a[3]); }
          else __builtin_amdgcn_raw_buffer_store_b128(pk, rs, vo + f * 1024, 0, 16);
        }
      });
    }
    TS(2);
    static_assert(GETJ % 2 == 1 && GETJ >= 3 && GETJ <= 7, "");
    f32x4 xf[2][4];
#pragma unroll
    for (int j = 0; j < GETJ + 5; j += 2) {
      if (PAR) flip();
      bar();
      stg(f1, f0, PAR ^ 1, j == 0);
      if (!PAR) flip();
      bar();
      u32x2 xv[2];
      pairs(sacc, f0, 8, 0, 12, [&](int t) {
        if (t < 10) { rd(f1, PAR, 2 * t); rd(f1, PAR, 2 * t + 1); }
        else if (j < 8) xv[t - 10] = *(const __attribute__((address_space(3))) u32x2*)(px + (j >> 1) * 1024 + (t - 10) * 256);
      });
      if (j < 8) {
#pragma unroll
        for (int e = 0; e < 2; ++e) xf[j >> 2][(j & 2) + e] = f32x4{lo_f(xv[e][0]), hi_f(xv[e][0]), lo_f(xv[e][1]), hi_f(xv[e][1])};
        asm volatile("" : "+v"(xf[j >> 2][j & 2]), "+v"(xf[j >> 2][(j & 2) + 1]));
        SB();
      }
    }
    if (PAR) flip();
    WAIT_V(0);
    bar();
    u32x4 pv[4];
    {
      lds_cptr pp_ = shm3 + G2_X + 16384 + vo;
      pairs(sacc, f1, 8, 0, 12, [&](int t) {
        if (t < 2) {
#pragma unroll
          for (int f = 2 * t; f < 2 * t + 2; ++f) pv[f] = *(const __attribute__((address_space(3))) u32x4*)(pp_ + f * 1024);
        } else { rd(f0, PAR ^ 1, 2 * t - 4); rd(f0, PAR ^ 1, 2 * t - 3); }
      });
      if (__builtin_expect(blank(pv) || !cok, 0)) {
        const __amdgpu_buffer_rsrc_t rp = __builtin_amdgcn_make_buffer_rsrc((void*)((char*)slab + (size_t)(bid ^ 5) * 16384), 0, 16384, 0x00020000);
        while (!cok && !__all(fresh(__hip_atomic_load(done2 + (bid ^ 5) * 8, RLX_AGENT), t_own))) {
          notify(0, 32);
          if (lane == 0) __hip_atomic_store(done2 + bid * 8, __builtin_amdgcn_s_memrealtime(), RLX_AGENT);
          __builtin_amdgcn_s_sleep(8);
        }
        do {
          asm volatile("" ::: "memory");
#pragma unroll
          for (int f = 0; f < 4; ++f) pv[f] = __builtin_bit_cast(u32x4, __builtin_amdgcn_raw_buffer_load_b128(rp, vo + f * 1024, 0, 16));
        } while (blank(pv));
      }
    }
    auto silu = [&](int g0, int g1) {
#pragma unroll
      for (int f = g0; f < g1; ++f) {
        const int mf = f >> 2, j = f & 3;
        const u32x4 pw = pv[mf * 2 + (j >> 1)];
        const f32x4 ya = acc[mf][j] + f32x4{lo_f(pw[2 * (j & 1)]), hi_f(pw[2 * (j & 1)]), lo_f(pw[2 * (j & 1) + 1]), hi_f(pw[2 * (j & 1) + 1])};
#pragma unroll
        for (int e = 0; e < 2; ++e) {
          const f32x2 y2 = {ya[2 * e], ya[2 * e + 1]};
          const f32x2 ar = y2 * f32x2{-1.4426950408889634f, -1.4426950408889634f};
          const f32x2 dn = f32x2{__builtin_amdgcn_exp2f(ar[0]), __builtin_amdgcn_exp2f(ar[1])} + f32x2{1.0f, 1.0f};
          const f32x2 t = y2 * f32x2{__builtin_amdgcn_rcpf(dn[0]), __builtin_amdgcn_rcpf(dn[1])};
          acc[mf][j][2 * e] = t[0]; acc[mf][j][2 * e + 1] = t[1];
        }
        asm volatile("" : "+v"(acc[mf][j]));
      }
      SB();
    };
#define SPT 0, 0, 1, 2, 4, 6, 8, 8
    constexpr int spt[] = {SPT};
    silu(0, spt[1]);
#pragma unroll
    for (int j = GETJ + 6; j < 15; ++j) {
      if ((j ^ PAR) & 1) flip();
      bar();
      if (j & 1) stg(f0, f1, PAR); else stg(f1, f0, PAR ^ 1);
      silu(spt[j - GETJ - 5], j == 14 ? 8 : spt[j - GETJ - 4]);
    }
    pairs(sacc, f0, 8, 0, 0, [](int) {});
    asm volatile("s_nop 7\n\ts_nop 3" : "+v"(sacc[1][0]), "+v"(sacc[1][1]), "+v"(sacc[1][2]), "+v"(sacc[1][3]));
    SB();
    TS(1);
    {
      const __amdgpu_buffer_rsrc_t ro = __builtin_amdgcn_make_buffer_rsrc((void*)out, 0, S_ * D_ * 2, 0x00020000);
#pragma unroll
      for (int f = 0; f < 4; ++f) {
        const int mf = f >> 1, j = (f & 1) * 2;
        const f32x4 a = __builtin_elementwise_fma(sacc[mf][j], acc[mf][j], xf[mf][j]), b = __builtin_elementwise_fma(sacc[mf][j + 1], acc[mf][j + 1], xf[mf][j + 1]);
        const auto r0 = __builtin_amdgcn_permlane16_swap(pack2(a[0], a[1]), pack2(b[0], b[1]), false, false);
        const auto r1 = __builtin_amdgcn_permlane16_swap(pack2(a[2], a[3]), pack2(b[2], b[3]), false, false);
        __builtin_amdgcn_raw_buffer_store_b128(u32x4{r0[0], r1[0], r0[1], r1[1]}, ro, vx + mf * 2048 + j * 32, 0, 2);
      }
    }
    TS(3);
  }
  if (bid == 73 && wid == 0) { unsigned d_ = 0u; if (d_ > 9000u) d_ = 9000u; while (__builtin_amdgcn_s_memrealtime() - t_own < 6000ull + d_) {} }
  }
  asm volatile(".Lcode_end:");
}

extern "C" void kernel_launch(void* const* d_in, const int* in_sizes, int n_in, void* d_out, int out_size, void* d_ws,
                              size_t ws_size, hipStream_t stream) {
  const u16* A_x = (const u16*)d_in[0];
  const u16* A_wqkv = (const u16*)d_in[1];
  const u16* A_wo = (const u16*)d_in[2];
  const u16* B_wg = (const u16*)d_in[4];
  const u16* B_wu = (const u16*)d_in[5];
  const u16* B_wd = (const u16*)d_in[6];
  u16* out = (u16*)d_out;
  fused_kernel<<<256, 512, 0, stream>>>(A_x, A_wqkv, B_wg, B_wu, B_wd, A_wo, out, (char*)d_ws);
}
```

```cpp
#include <hip/hip_runtime.h>
#include <hip/hip_bf16.h>
#include <stdint.h>
#include <type_traits>

typedef unsigned short u16;
using bf16x8 = __attribute__((ext_vector_type(8))) short;
using bf16x4 = __attribute__((ext_vector_type(4))) short;
using f32x4 = __attribute__((ext_vector_type(4))) float;
using u32x2 = __attribute__((ext_vector_type(2))) unsigned;
using u32x4 = __attribute__((ext_vector_type(4))) unsigned;
typedef __attribute__((address_space(3))) const char* lds_cptr;

constexpr int S_ = 2048, D_ = 1024, F_ = 3584;

__device__ __forceinline__ float bf2f(u16 h) { return __uint_as_float((unsigned)h << 16); }
__device__ __forceinline__ u16 f2bf(float f) {
  unsigned u = __float_as_uint(f);
  u += 0x7fffu + ((u >> 16) & 1u);
  return (u16)(u >> 16);
}
__device__ __forceinline__ float rbf(float f) { return bf2f(f2bf(f)); }
typedef __attribute__((ext_vector_type(2))) __bf16 bf16x2_t;
typedef __attribute__((ext_vector_type(2))) float f32x2;
__device__ __forceinline__ unsigned pack2(float a, float b) {
  bf16x2_t r = __builtin_convertvector((f32x2){a, b}, bf16x2_t);
  return __builtin_bit_cast(unsigned, r);
}
__device__ __forceinline__ float lo_f(unsigned p) { return __uint_as_float(p << 16); }
__device__ __forceinline__ float hi_f(unsigned p) { return __uint_as_float(p & 0xffff0000u); }

#define TSDECL
#define TSS(i) do {} while (0)
#define TSFLUSH() do {} while (0)
#define TS1(i) TSS(i)
#define TS(i) TSS(8 + (i))
#define PIN_S(v) asm volatile("" : "+s"(v))
#define WAIT_V(n) asm volatile("s_waitcnt vmcnt(%0)" ::"n"(n) : "memory")
#define WAIT_L(n) asm volatile("s_waitcnt lgkmcnt(%0)" ::"n"(n) : "memory")
#define BARL() do { WAIT_L(0); BAR(); } while (0)
#define BAR() do { asm volatile("" ::: "memory"); __builtin_amdgcn_s_barrier(); asm volatile("" ::: "memory"); } while (0)

__device__ __forceinline__ bf16x4 tr_read(lds_cptr p) {
  return __builtin_amdgcn_ds_read_tr16_b64_v4i16((__attribute__((address_space(3))) bf16x4*)p);
}
__device__ __forceinline__ bf16x8 tr_frag(lds_cptr p) {
  bf16x4 lo = tr_read(p), hi = tr_read(p + 512);
  bf16x8 r;
  r[0] = lo[0]; r[1] = lo[1]; r[2] = lo[2]; r[3] = lo[3];
  r[4] = hi[0]; r[5] = hi[1]; r[6] = hi[2]; r[7] = hi[3];
  return r;
}
__device__ __forceinline__ void glds16(const void* gsrc, unsigned lds_dst) {
  unsigned keep;
  asm volatile("s_mov_b32 %0, m0\n\ts_mov_b32 m0, %2\n\ts_nop 0\n\tglobal_load_lds_dwordx4 %1, off\n\ts_mov_b32 m0, %0"
               : "=&s"(keep) : "v"(gsrc), "s"(lds_dst) : "memory");
}
#define GLDS(gptr, laddr) glds16((const void*)(gptr), (unsigned)(laddr))
__device__ __forceinline__ void glds16_sc1(const void* gsrc, unsigned lds_dst) {
  unsigned keep;
  asm volatile("s_mov_b32 %0, m0\n\ts_mov_b32 m0, %2\n\ts_nop 0\n\tglobal_load_lds_dwordx4 %1, off sc1\n\ts_mov_b32 m0, %0"
               : "=&s"(keep) : "v"(gsrc), "s"(lds_dst) : "memory");
}
#define GLDS_SC1(gptr, laddr) glds16_sc1((const void*)(gptr), (unsigned)(laddr))
__device__ __forceinline__ void glds16_nc(const void* gsrc, unsigned lds_dst) {
  unsigned keep;
  asm volatile("s_mov_b32 %0, m0\n\ts_mov_b32 m0, %2\n\ts_nop 0\n\tglobal_load_lds_dwordx4 %1, off\n\ts_mov_b32 m0, %0"
               : "=&s"(keep) : "v"(gsrc), "s"(lds_dst));
}
#define GLDS_NC(gptr, laddr) glds16_nc((const void*)(gptr), (unsigned)(laddr))
__device__ __forceinline__ void glds16_s(const void* sbase, unsigned voff, unsigned lds_dst) {
  asm volatile("s_mov_b32 m0, %2\n\ts_nop 0\n\tglobal_load_lds_dwordx4 %0, %1" :: "v"(voff), "s"(sbase), "s"(lds_dst) : "m0");
}

__device__ __forceinline__ void glds16_s1(const void* sbase, unsigned voff, unsigned lds_dst) {
  asm volatile("s_mov_b32 m0, %2\n\ts_nop 0\n\tglobal_load_lds_dwordx4 %0, %1 sc1" :: "v"(voff), "s"(sbase), "s"(lds_dst) : "memory", "m0");
}
__device__ __forceinline__ void glds16_v(const void* p, unsigned lds_dst) {
  asm volatile("s_mov_b32 m0, %1\n\ts_nop 0\n\tglobal_load_lds_dwordx4 %0, off" :: "v"(p), "s"(lds_dst) : "m0");
}
template <int OFF = 0>
__device__ __forceinline__ void gst16(const void* sbase, unsigned voff, u32x4 v) {
  asm volatile("global_store_dwordx4 %0, %1, %2 offset:%3 sc1\n\ts_nop 1" :: "v"(voff), "v"(v), "s"(sbase), "n"(OFF) : "memory");
}

typedef unsigned long long u64;
__device__ __forceinline__ bool fresh(u64 stamp, u64 t_own) {
  const u64 b = t_own + 100ull;
  const unsigned lo = (unsigned)stamp - (unsigned)b, hi = (unsigned)(stamp >> 32) - (unsigned)(b >> 32) - ((unsigned)stamp < (unsigned)b ? 1u : 0u);
  return (hi == 0u) & (lo < (unsigned)(__builtin_amdgcn_s_memrealtime() - t_own));
}
constexpr u64 TIMEOUT_ = 200000;
__device__ __forceinline__ float half_sum(float v) {
  v += __builtin_bit_cast(float, __builtin_amdgcn_update_dpp(0, __builtin_bit_cast(int, v), 0xB1, 0xf, 0xf, false));
  v += __builtin_bit_cast(float, __builtin_amdgcn_update_dpp(0, __builtin_bit_cast(int, v), 0x4E, 0xf, 0xf, false));
  v += __builtin_bit_cast(float, __builtin_amdgcn_update_dpp(0, __builtin_bit_cast(int, v), 0x141, 0xf, 0xf, false));
  v += __builtin_bit_cast(float, __builtin_amdgcn_update_dpp(0, __builtin_bit_cast(int, v), 0x140, 0xf, 0xf, false));
  { auto r = __builtin_amdgcn_permlane16_swap(__float_as_uint(v), __float_as_uint(v), false, false); v = __uint_as_float(r[0]) + __uint_as_float(r[1]); }
  return v;
}
__device__ __forceinline__ unsigned pack_stats(float mean, float var) {
  return (unsigned)f2bf(mean) | ((unsigned)f2bf(sqrtf(rbf(rbf(var) + 1e-5f))) << 16);
}
__device__ __forceinline__ unsigned row_stats_serial(const u16* xr) {
  float s = 0.f, q = 0.f;
#pragma unroll 1
  for (int k = 0; k < D_; ++k) s += bf2f(xr[k]);
  const float mean = s * (1.0f / D_);
#pragma unroll 1
  for (int k = 0; k < D_; ++k) { const float d = bf2f(xr[k]) - mean; q += d * d; }
  return pack_stats(mean, q * (1.0f / D_));
}
constexpr int G1_STAGE = 32768 + 5 * 8192;
#define EPOS 2
#define PA 4
#define PB 8
#define PC 12
#define GETP 4
#define DLT 2
#define PSLEEP 2
#define GETJ 3

constexpr unsigned BLANK = 0x7f817f81u;
__device__ __forceinline__ bool blank(const u32x4* pv) {
  u64 m;
#define CX(i) "v_cmpx_ne_u32 vcc, %1, %" #i "\n\t"
  asm volatile(CX(2) CX(3) CX(4) CX(5) CX(6) CX(7) CX(8) CX(9) CX(10) CX(11) CX(12) CX(13) CX(14) CX(15) CX(16) CX(17)
               "s_mov_b64 %0, exec\n\ts_mov_b64 exec, -1\n\ts_nop 3" : "=s"(m) : "s"(BLANK), "v"(pv[0][0]), "v"(pv[0][1]), "v"(pv[0][2]), "v"(pv[0][3]),
               "v"(pv[1][0]), "v"(pv[1][1]), "v"(pv[1][2]), "v"(pv[1][3]), "v"(pv[2][0]), "v"(pv[2][1]), "v"(pv[2][2]), "v"(pv[2][3]),
               "v"(pv[3][0]), "v"(pv[3][1]), "v"(pv[3][2]), "v"(pv[3][3]) : "vcc");
  return m != ~0ull;
}
#define RLX_AGENT __ATOMIC_RELAXED, __HIP_MEMORY_SCOPE_AGENT

constexpr int G2_A = 65536, G2_X = 131072;
constexpr int G2_NSTAGE = 4;
constexpr size_t MB_ = 1024 * 1024;
__global__ __launch_bounds__(512, 2) __attribute__((aligned(4096))) void fused_kernel(const u16* x_, const u16* wqkv_, const u16* wg_, const u16* wu_, const u16* wd_, const u16* wo_, u16* out_, char* ws_) {
  u64 t1_, t2_, t3_;
  asm volatile("s_memrealtime %0\n\ts_waitcnt lgkmcnt(0)" : "=s"(t1_));
  __shared__ __attribute__((aligned(1024))) char shm[163840];
  constexpr int AUX = 2 * G1_STAGE + 8192;
  {
    u64 pc; int len;
    asm volatile("s_getpc_b64 %0\n.Lcode_beg:\n\ts_mov_b32 %1, .Lcode_end-.Lcode_beg" : "=s"(pc), "=s"(len));
    const int ol = ((blockIdx.x >> 3) + 32 * (threadIdx.x >> 6)) * 1024 + (threadIdx.x & 63) * 16;
    asm volatile("s_mov_b32 m0, %2\n\ts_nop 0\n\tglobal_load_lds_dwordx4 %0, %1"
                 :: "v"(ol < len - 16 ? ol : 0), "s"(pc & ~15ull), "s"((unsigned)(uintptr_t)(lds_cptr)shm + 2 * G1_STAGE + 7168) : "m0");
    __builtin_amdgcn_sched_barrier(0);
  }
  const u64 t_own = __builtin_amdgcn_s_memrealtime();
  TSDECL;
  TS1(7);
  const int tid = threadIdx.x, lane = tid & 63;
  const int wid = __builtin_amdgcn_readfirstlane(tid >> 6);
  const int wm = wid & 3, wc = wid >> 2;
  int bid_ = blockIdx.x;
  PIN_S(bid_);
  const int bid = bid_ & 255, xcd = bid & 7, idx = bid >> 3;
  const int ntile = xcd * 4 + (idx & 3), mtile = idx >> 2;
  const int m0 = mtile * 256;
  const int hg = ntile >> 1;
  const bool tya = (ntile & 1) == 0;
  const int srow = m0 + ntile * 8 + wm * 2;
  lds_cptr shm3 = (lds_cptr)shm;
  const unsigned shm_base = (unsigned)(uintptr_t)shm3;
  const int r8 = tid >> 3, pos = tid & 7;
  const int phys = pos >> 1, half = pos & 1;
  const int fk = ((r8 >> 1) & 1) | (((r8 >> 3) & 1) << 1);
  const int vsrc = phys ^ fk;
  const int qstr = tya ? 3072 * 2 : F_ * 2;
  unsigned voa = (((r8 & 32) + 2 * (r8 & 15) + ((r8 >> 4) & 1)) * D_ + ((pos ^ (r8 & 7)) * 8)) * 2;
  unsigned vog = r8 * (F_ * 2) + vsrc * 32 + half * 16, voq = r8 * qstr + vsrc * 32 + half * 16;
  unsigned vom = r8 * qstr + (vsrc >> (tya ? 0 : 1)) * 32 + half * 16;
  asm volatile("" : "+v"(voa), "+v"(vog), "+v"(voq), "+v"(vom));
  const int kt0 = (hg >> 1) * 7, he = hg & 1;
  unsigned oq = (tya ? hg : kt0 + 2 * he) * 128, og = (kt0 + (tya ? 5 + he : 2 * he + 1)) * 128, om = tya ? 4096 : 512 - 192 * he;
  unsigned d0 = shm_base + wid * 1024 + 32768, ox = m0 * D_ * 2;
  long uq = -32 * qstr;
  PIN_S(oq); PIN_S(og); PIN_S(om); PIN_S(d0); PIN_S(ox); PIN_S(uq);
  __builtin_amdgcn_sched_barrier(0);
  typedef __attribute__((address_space(4))) const u64* kptr;
  kptr ka = (kptr)__builtin_amdgcn_kernarg_segment_ptr();
  asm volatile("s_memrealtime %0\n\ts_waitcnt lgkmcnt(0)" : "=s"(t2_), "+s"(ka));
  typedef __attribute__((address_space(1))) char* gp;
  const u16* __restrict__ x = (const u16*)(gp)ka[0]; const u16* __restrict__ wqkv = (const u16*)(gp)ka[1];
  const u16* __restrict__ wg = (const u16*)(gp)ka[2]; const u16* __restrict__ wu = (const u16*)(gp)ka[3];
  const u16* __restrict__ wd = (const u16*)(gp)ka[4]; const u16* __restrict__ wo = (const u16*)(gp)ka[5];
  u16* __restrict__ out = (u16*)(gp)ka[6]; char* __restrict__ ws = (char*)(gp)ka[7];
  u16* const halo = (u16*)(ws + 4 * MB_);
  u16* const abuf = (u16*)(ws + 16 * MB_);
  char* const slab = ws + 64 * MB_;
  unsigned* const flags = (unsigned*)(ws + 96 * MB_);
  u32x4* const rstat = (u32x4*)(ws + 97 * MB_);
  char* const wcopy = ws + 98 * MB_;
  u64* const done1 = (u64*)(ws + 102 * MB_);
  u64* const done2 = (u64*)(ws + 100 * MB_ + 65536);
  u64* const donec = (u64*)(ws + 100 * MB_ + 131072);
  u64* const acks = (u64*)(ws + 100 * MB_ + 196608);
  float* const csum = (float*)(ws + 101 * MB_);
  asm volatile("" :: "s"(x), "s"(wqkv), "s"(wg), "s"(wu), "s"(wd), "s"(wo), "s"(out), "s"(ws));
  const char* const ab = (const char*)x + ox;
  int kz = 0;
  asm volatile("" : "+s"(kz));
  asm volatile("s_memrealtime %0\n\ts_waitcnt lgkmcnt(0)" : "=s"(t3_));
  u64 pq = (u64)wqkv, pg = (u64)wg;
  PIN_S(pq); PIN_S(pg);
  const char* const qb = (const char*)(tya ? pq : pg) + oq;
  if (wid < 4) {
#pragma unroll
    for (int i = 0; i < 8; ++i) glds16_s(ab + i * 32 * D_ * 2, voa, d0 - 32768 + i * 4096);
  } else {
    glds16_s(qb, voq, d0); glds16_s(qb + uq, voq, d0 - 4096);
  }
  __builtin_amdgcn_sched_barrier(0);
  const char* const gb = (const char*)wg + og;
  const long dgu = (const char*)wu - (const char*)wg, dp1 = tya ? 2048 : dgu;
  const char* const mb0 = qb + om;
  const char* mptr = mb0 + (((vsrc & 1) && !tya) ? dgu : 0) + vom;
  if (wid >= 4) {
    const long ug = kz - 32 * F_ * 2;
    glds16_s(qb + dp1, voq, d0 + 8192); glds16_s(qb + dp1 + uq, voq, d0 + 4096);
    glds16_v(mptr, d0 + 2 * 8192); glds16_v(mptr + uq, d0 + 2 * 8192 - 4096);
    glds16_s(gb, vog, d0 + 3 * 8192); glds16_s(gb + ug, vog, d0 + 3 * 8192 - 4096);
    glds16_s(gb + dgu, vog, d0 + 4 * 8192); glds16_s(gb + dgu + ug, vog, d0 + 4 * 8192 - 4096);
  }
  __builtin_amdgcn_sched_barrier(0);
  f32x4 acc[4][10];

  constexpr int NT = D_ / 64;
  constexpr int CNT = AUX + 4096 + 3584;
  if (tid < 8) *(unsigned*)(shm + CNT + tid * 4) = 0u;
  bf16x8 af0[4], af1[4];
  bf16x4 bl[4], bh[4];
#define SB0() __builtin_amdgcn_sched_barrier(0)
  auto mm = [&](int g, int t, bool two, bool z = false) {
    const int mi = t >> 1, mf = ((g & 1) && g != 9) ? 3 - mi : mi, ks = (t & 1) ^ (mi & 1), s = ((g + 1) & 1) * 2;
    const bf16x8 b0 = __builtin_shufflevector(bl[s + ks], bh[s + ks], 0, 1, 2, 3, 4, 5, 6, 7), a0 = ks ? af1[mf] : af0[mf];
    const bf16x8 b1 = __builtin_shufflevector(bl[s + 1 - ks], bh[s + 1 - ks], 0, 1, 2, 3, 4, 5, 6, 7), a1 = ks ? af0[mf] : af1[mf];
    const bool tr = (g >> 1) == 2;
    if (z && two) asm volatile("v_mfma_f32_16x16x32_bf16 %0, %1, %2, 0\n\tv_mfma_f32_16x16x32_bf16 %0, %3, %4, %0" : "=&v"(acc[mf][g])
                               : "v"(tr ? a0 : b0), "v"(tr ? b0 : a0), "v"(tr ? a1 : b1), "v"(tr ? b1 : a1));
    else if (z && !(t & 1)) asm volatile("v_mfma_f32_16x16x32_bf16 %0, %1, %2, 0" : "=&v"(acc[mf][g]) : "v"(tr ? a0 : b0), "v"(tr ? b0 : a0));
    else if (two) asm volatile("v_mfma_f32_16x16x32_bf16 %0, %1, %2, %0\n\tv_mfma_f32_16x16x32_bf16 %0, %3, %4, %0" : "+v"(acc[mf][g])
                          : "v"(tr ? a0 : b0), "v"(tr ? b0 : a0), "v"(tr ? a1 : b1), "v"(tr ? b1 : a1));
    else asm volatile("v_mfma_f32_16x16x32_bf16 %0, %1, %2, %0" : "+v"(acc[mf][g]) : "v"(tr ? a0 : b0), "v"(tr ? b0 : a0));
  };
  typedef __attribute__((address_space(3))) unsigned lds_u32;
  lds_u32* const cnt = (lds_u32*)(shm3 + CNT);
  bool cok;
  constexpr int NY = 28;
  int role, mt2, nt2;
  struct WS { unsigned vb[2][2]; const char* wb[2]; int sh[2]; };
  auto wsetup = [&](const u16* w, int crow0, int l, bool ck) __attribute__((always_inline)) {
    WS r;
    const unsigned vr = ((l >> 5) & 1) * 16 + (wm & 1) * 8 + ((l >> 3) & 3), vc = (((l >> 1) & 3) ^ ((l >> 4) & 1)) * 32 + (l & 1) * 16;
    u64 cp = (u64)(wcopy + (nt2 >> 2) * 589824 + crow0 * 128);
    asm volatile("" : "+s"(cp));
#pragma unroll
    for (int i = 0; i < 2; ++i) {
      u64 dp = (u64)(w + nt2 * 128 + i * 64);
      asm volatile("" : "+s"(dp));
      const bool slow = ck & ((((unsigned)dp >> 7) & 7) == 3);
      r.sh[i] = slow ? 7 : 11;
      r.wb[i] = (const char*)(slow ? cp : dp) + (crow0 ? (wm >> 1) * 32 << r.sh[i] : 0);
      r.vb[i][0] = (vr << r.sh[i]) + vc; r.vb[i][1] = ((vr + 4) << r.sh[i]) + (vc ^ 64);
    }
    asm volatile("" : "+v"(r.vb[0][0]), "+v"(r.vb[0][1]), "+v"(r.vb[1][0]), "+v"(r.vb[1][1]));
    PIN_S(r.wb[0]); PIN_S(r.wb[1]); PIN_S(r.sh[0]); PIN_S(r.sh[1]);
    return r;
  };
  WS wy, wz;
  int vxq, ws0, ws1;
  const char* sz;
  u32x4 xq[4];
  unsigned vg;
  int st = 3;
  const char *sa, *sw[2];
  auto wsel = [&]() {
    const unsigned r = __builtin_amdgcn_readlane(vg, st);
    sw[0] = wy.wb[0] + (r << wy.sh[0]); sw[1] = wy.wb[1] + (r << wy.sh[1]);
  };
  const int p = wid & 3;
  unsigned sl = 0;
#define G2_W "s_add_u32 m0, %[l], %[ow]\n\ts_nop 0\n\tglobal_load_lds_dwordx4 %[v0], %[w0] sc1\n\t" \
             "s_add_u32 m0, m0, 1024\n\ts_nop 0\n\tglobal_load_lds_dwordx4 %[v1], %[w0] sc1\n\t" \
             "s_add_u32 m0, m0, 7168\n\ts_nop 0\n\tglobal_load_lds_dwordx4 %[v2], %[w1] sc1\n\t" \
             "s_add_u32 m0, m0, 1024\n\ts_nop 0\n\tglobal_load_lds_dwordx4 %[v3], %[w1] sc1"
#define G2_WOP(w) [ow] "s"(shm_base + p * 2048), [w0] "s"(sw[0]), [w1] "s"(sw[1]), [v0] "v"(w.vb[0][0]), [v1] "v"(w.vb[0][1]), [v2] "v"(w.vb[1][0]), [v3] "v"(w.vb[1][1])
  auto tile2 = [&](int t0) __attribute__((always_inline)) {
    int b = bid;
    asm volatile("" : "+s"(b));
    role = 1 - ((b >> 2) & 1);
    mt2 = ((role ? b : b ^ 5) & 7) * 4 + ((b >> 3) & 3), nt2 = b >> 5;
    sa = (const char*)abuf + (size_t)(mt2 * 72 + role * NY + t0) * 16384 + wm * 4096;
    asm volatile("" : "+s"(sa));
  };
  auto prep = [&](bool first) __attribute__((always_inline)) {
    int l = threadIdx.x;
    asm volatile("" : "+v"(l));
    l &= 63;
    if (first) {
      tile2(3);
      vxq = ((mt2 * 128 + (wm >> 1) * 64 + role * 32 + 2 * (l & 15)) * D_ + nt2 * 128 + (wm & 1) * 64) * 2 + ((l >> 4) & 1) * 32 + (l >> 5) * 16;
      const int t = min(l < NY ? l : l - DLT, NY - 1), w = wm >> 1;
      const int o = (0xffaa5500fae504ull >> (2 * t)) & 3;
      int u1 = (0x990990 >> ((2 * t) & 31)) & 3, u2 = 3 + (t & 3);
      asm volatile("" : "+v"(u1), "+v"(u2));
      const int u = t < 12 ? u1 : u2;
      vg = (__umul24((l < NY ? role : 1 - role) * 4 + (((o & 1) << 1) | (o >> 1)), 14) + ((0xb85140a >> (4 * u)) & 15) + (u == 5 ? w : 2 * w)) * 32;
      asm volatile("" : "+v"(vg), "+v"(vxq));
      sz = (const char*)abuf + (size_t)(mt2 * 72 + 56) * 16384 + (wm >> 1) * 8192 + role * 4096 + (wm & 1) * 2048;
      PIN_S(sz);
    }
    wy = wsetup(wd, 0, l, first);
    wz = wsetup(wo, F_, l, first);
    ws0 = 64 << wz.sh[0]; PIN_S(ws0);
    ws1 = 64 << wz.sh[1]; PIN_S(ws1);
    SB0();
  };
  auto start2 = [&]() __attribute__((always_inline)) {
    const __amdgpu_buffer_rsrc_t rx = __builtin_amdgcn_make_buffer_rsrc((void*)x, 0, S_ * D_ * 2, 0x00020000);
#pragma unroll
    for (int f = 0; f < 4; ++f) xq[f] = __builtin_bit_cast(u32x4, __builtin_amdgcn_raw_buffer_load_b128(rx, vxq + (f >> 1) * 2048 + (f & 1) * 64, 0, 0));
    cok = __builtin_amdgcn_readfirstlane(cnt[1] & cnt[2]) != 0u;
    if (__builtin_expect(!cok, 0)) prep(false);
    wsel();
    PIN_S(sw[0]); PIN_S(sw[1]);
    SB0();
  };
  auto kloop = [&](auto wmc) {
    constexpr bool LD = decltype(wmc)::value != 0;
    constexpr int DMP = 2 * G1_STAGE;
    const char* pa = nullptr;
    unsigned vob = 0;
    f32x4 ca = {0.f, 0.f, 0.f, 0.f}, cb = {0.f, 0.f, 0.f, 0.f}, cd = ca;
    bf16x8 sel = {0, 0, 0, 0, 0, 0, 0, 0};
    int tro = 0;
    const int psh = (wid == 2 && !tya) ? 1 : 0, pstr = wid < 3 ? qstr : F_ * 2;
    const char* const pbase = wid == 0 ? qb : (wid == 1 ? qb + dp1 : (wid == 2 ? mb0 : gb));
    int lane = threadIdx.x;
    asm volatile("; %1" : "+v"(lane) : "n"(LD ? 1 : 0));
    lane &= 63;
    const int fr = lane & 15, fq = lane >> 4, q4 = fr >> 2, pp = lane & 3;
    const int ff = ((q4 >> 1) & 1) | ((fq & 1) << 1);
    int aoff[2], boff[2];
#pragma unroll
    for (int ks = 0; ks < 2; ++ks) aoff[ks] = (wm * 64 + fr) * 128 + (((ks * 4 + fq) ^ (fr & 7)) * 16);
#pragma unroll
    for (int j = 0; j < 2; ++j) boff[j] = 32768 + (8 * fq + q4) * 128 + (((2 * wc + j) ^ ff) * 32) + pp * 8;
    unsigned vol = lane * 16;
    float sm = 0.f, sq = 0.f;
    const char* pf = (const char*)wd + (bid * 4 + wid) * 7168;
    long pstep = 64 * pstr;
    if (!LD) {
      const int rs = 256 + mtile * 8 + (lane >> 3), vs = ((lane & 7) >> 1) ^ (((rs >> 1) & 1) | ((mtile & 1) << 1));
      pa = pbase + ((psh & vs) ? dgu : 0) + (rs * pstr + (vs >> psh) * 32 + (lane & 1) * 16);
      vob = rs * (F_ * 2) + vs * 32 + (lane & 1) * 16;
      tro = DMP + wid * 1024 + q4 * 128 + ((fq ^ ((q4 >> 1) | ((mtile & 1) << 1))) * 32) + pp * 8;
      const short one = fr == fq ? 0x3f80 : 0;
      sel = bf16x8{one, one, one, one, one, one, one, one};
      asm volatile("" : "+v"(tro), "+v"(vol), "+v"(sel), "+v"(ca), "+v"(cb), "+v"(sm), "+v"(sq));
      PIN_S(pf); PIN_S(pstep);
    }
    auto accum = [&](lds_cptr p, int ob) {
      ca = __builtin_amdgcn_mfma_f32_16x16x32_bf16(tr_frag(p), sel, ca, 0, 0, 0);
      if (wid == 0) cb = __builtin_amdgcn_mfma_f32_16x16x32_bf16(tr_frag(p + ob), sel, cb, 0, 0, 0);
    };
    auto xrow = [&](int i) {
      u64 a = (u64)(x + (size_t)srow * D_ + i * 256), b = (u64)(wo + (bid * 4 + wid) * 1024);
      PIN_S(a); PIN_S(b);
      asm volatile("global_load_dwordx4 %0, %1, %2" : "+v"(cd) : "v"(vol + (vol & (i < 4 ? 512 : 0)) * 3), "s"(i < 4 ? a : b) : "memory");
    };
    const int q32 = 32 * qstr;
    const char *sq0 = qb + q32, *sq1 = sq0 + dp1, *sg0 = gb + 32 * F_ * 2, *sg1 = sg0 + dgu, *sx = ab + (long)(kz + 128 - 32 * D_ * 2);
    u32x2 mp[2] = {__builtin_bit_cast(u32x2, mptr + q32), __builtin_bit_cast(u32x2, mptr + 2 * q32)};
    u64 cy[2];
    unsigned dst = shm_base + G1_STAGE + wm * 1024;
    int dbuf = G1_STAGE;
    auto stage = [&](int i0, int i1) __attribute__((always_inline)) {
#pragma unroll
        for (int i = i0; i < i1; ++i) {
          const int q = i >> 1, h = i & 1;
          const unsigned ld = dst + h * 4096 + (q < 5 ? 32768 + q * 8192 : (q - 5) * 8192);
          if (q < 2) glds16_s((q ? sq1 : sq0) + h * q32, voq, ld);
          else if (q == 2) glds16_v(__builtin_bit_cast(const char*, mp[h]), ld);
          else if (q < 5) glds16_s((q == 4 ? sg1 : sg0) + h * 32 * F_ * 2, vog, ld);
          else glds16_s(sx + ((q - 5) * 64 + h * 32) * D_ * 2, voa, ld);
        }
    };
    u64 cy0 = 0, rt0 = 0;
#pragma unroll
    for (int a = 0; a < 4; ++a) { acc[a][9] = f32x4{0.f, 0.f, 0.f, 0.f}; asm volatile("" : "+v"(acc[a][9])); }
    if (!LD) {
      xrow(0);
      WAIT_V(1);
    }
    asm volatile("" : "+v"(aoff[0]), "+v"(aoff[1]), "+v"(boff[0]), "+v"(boff[1]));
    bf16x8 kp[2];
    auto body = [&](bool upd, bool keep = false, bool first = false) __attribute__((always_inline)) {
      lds_cptr sb = shm3;
      auto rdb = [&](int g, int i) {
        lds_cptr p = sb + boff[g & 1] + (g >> 1) * 8192 + (i >> 1) * 4096;
        const int s = ((g + 1) & 1) * 2 + (i >> 1);
        if (i & 1) bh[s] = tr_read(p + 512); else bl[s] = tr_read(p);
      };
      auto rda = [&](int ks, int mf) { (ks ? af1 : af0)[mf] = *(const __attribute__((address_space(3))) bf16x8*)(sb + aoff[ks] + mf * 2048); };
      SB0();
      __builtin_amdgcn_s_waitcnt(0xC07F);
#pragma unroll
      for (int gg = 0; gg < 10; ++gg) {
        const int g = (gg + 9) % 10;
#pragma unroll
        for (int t = 0; t < 8; ++t) {
          const bool two = t >= 4 && !(upd && g >= 5 && g <= 8);
          SB0();
          if ((!two || (t & 1)) && !(first && g == 9)) mm(g, two ? t - 1 : t, two, first);
          SB0();
          if (first && !LD && (g == 3 || g == 7) && t == 0) BAR();
          if (g == 9) {
            if (t == 0 || t == 2) { rdb(0, t); rdb(0, t + 1); }
            if (t & 1) { rda((t >> 1) & 1, t >> 1); rda(((t >> 1) & 1) ^ 1, t >> 1); }
          } else if (t < 4) rdb(g + 1, t);
          else if (keep && g == 7) {
            if (t == 5) { WAIT_V(0); kp[0] = tr_frag(shm3 + tro); }
            if (t == 7) kp[1] = tr_frag(shm3 + tro + 4096);
          } else if (upd) {
            if (g == 5 && t == 4) { sq0 += 2 * q32; PIN_S(sq0); }
            if (g == 5 && t == 5) { sq1 += 2 * q32; PIN_S(sq1); }
            if (g == 5 && t == 6) { sg0 += 64 * F_ * 2; PIN_S(sg0); }
            if (g == 6 && t == 4) { sg1 += 64 * F_ * 2; PIN_S(sg1); }
            if (g == 6 && t == 5) { sx += 128; PIN_S(sx); }
            if (g == 6 && t == 6) { dst -= dbuf; PIN_S(dst); }
            if (g == 7 && t == 4) asm volatile("v_add_co_u32 %0, %2, %4, %0\n\tv_add_co_u32 %1, %3, %4, %1" : "+v"(mp[0][0]), "+v"(mp[1][0]), "=&s"(cy[0]), "=&s"(cy[1]) : "s"(2 * q32));
            if (g == 7 && t == 6) asm volatile("v_addc_co_u32 %0, vcc, 0, %0, %2\n\tv_addc_co_u32 %1, vcc, 0, %1, %3" : "+v"(mp[0][1]), "+v"(mp[1][1]) : "s"(cy[0]), "s"(cy[1]) : "vcc");
            if (g == 8 && t == 4) { aoff[0] += dbuf; aoff[1] += dbuf; asm volatile("" : "+v"(aoff[0]), "+v"(aoff[1])); }
            if (g == 8 && t == 5) { boff[0] += dbuf; boff[1] += dbuf; asm volatile("" : "+v"(boff[0]), "+v"(boff[1])); }
            if (g == 8 && t == 6) { dbuf = -dbuf; PIN_S(dbuf); }
          }
        }
      }
      SB0();
    };
    TS1(0);
#define ARRP(o, c) do {} while (0)
    if (LD) {
      stage(0, PA);
      WAIT_V(6 + PA);
      TS1(5);
      BARL();
      stage(PA, PB);
      WAIT_V(2 + PB);
      BAR();
      stage(PB, PC);
      WAIT_V(PC);
      BAR();
      stage(PC, 18);
      body(true, false, true);
      WAIT_V(0);
      BARL();
#pragma clang loop unroll(disable)
      for (int kt = 1; kt < NT - 1; ++kt) {
        if (__builtin_expect(kt == 5 + wid, 0)) prep(true);
        stage(0, 18);
        body(true);
        WAIT_V(0);
        BARL();
        SB0();
      }
      start2();
      body(false);
    } else {
      char* wsx;
      auto tile = [&](int kt, bool keep) __attribute__((always_inline)) {
        SB0();
        ARRP(wid * 64 + kt * 4, true);
        BARL();
        body(false, keep);
        ARRP(320 + kt * 4 + 4, wid == 0);
        if (keep) asm volatile("" : "+v"(kp[0]), "+v"(kp[1])); else WAIT_V(0);
      };
      auto next = [&]() __attribute__((always_inline)) {
        aoff[0] += dbuf; aoff[1] += dbuf; boff[0] += dbuf; boff[1] += dbuf;
        dbuf = -dbuf;
        asm volatile("" : "+v"(aoff[0]), "+v"(aoff[1]), "+v"(boff[0]), "+v"(boff[1]), "+v"(vol), "+v"(tro) :: "memory");
        wsx = ws;
        PIN_S(wsx);
      };
      auto share = [&]() {
        glds16_v(pa, shm_base + DMP + wid * 1024);
        if (wid == 0) glds16_s(gb + dgu, vob, shm_base + DMP + 4096);
      };
      auto kept = [&]() {
        ca = __builtin_amdgcn_mfma_f32_16x16x32_bf16(kp[0], sel, ca, 0, 0, 0);
        if (wid == 0) cb = __builtin_amdgcn_mfma_f32_16x16x32_bf16(kp[1], sel, cb, 0, 0, 0);
      };
      auto cpub = [&](u32x4 a, u32x4 b) {
        if ((vol & 0xc0) == 0) {
          const char* const cs = wsx + 101 * MB_ + ntile * 10240 + mtile * 256;
          const unsigned co = (vol & 0x30) * 4 + (vol >> 8) * 16;
          gst16(cs + wid * 2048, co, a);
          if (wid == 0) gst16(cs + 8192, co, b);
        }
      };
      auto side0 = [&](int kt) __attribute__((always_inline)) {
        if (kt < 2) {
          glds16_v(pa - 2 * pstep, shm_base + DMP + 7168);
          if (wid == 0) glds16_s(gb + dgu, vob - 128 * F_ * 2, shm_base + DMP + 7168);
        }
        share();
        u32x4 xw;
        asm volatile("v_mov_b32 %0, %4\n\tv_mov_b32 %1, %5\n\tv_mov_b32 %2, %6\n\tv_mov_b32 %3, %7" : "=&v"(xw[0]), "=&v"(xw[1]), "=&v"(xw[2]), "=&v"(xw[3])
                     : "v"(cd[0]), "v"(cd[1]), "v"(cd[2]), "v"(cd[3]));
        xrow(kt + 1);
        next();
        if (kt) kept();
        pa += pstep; vob += 64 * F_ * 2;
        accum(shm3 + tro + ((kt & 1) * G1_STAGE + 32768 + mtile * 1024 + wid * 7168 - DMP), 4 * 8192);
#pragma unroll
        for (int i = 0; i < 4; ++i) {
          float lo, hi;
          asm volatile("v_lshlrev_b32 %2, 16, %4\n\tv_and_b32 %3, 0xffff0000, %4\n\tv_add_f32 %0, %0, %2\n\tv_fmac_f32 %1, %2, %2\n\t"
                       "v_add_f32 %0, %0, %3\n\tv_fmac_f32 %1, %3, %3" : "+v"(sm), "+v"(sq), "=&v"(lo), "=&v"(hi) : "v"(xw[i]));
        }
      };
      BARL();
      body(false, false, true);
      WAIT_V(0);
      side0(0);
#pragma clang loop unroll(disable)
      for (int kt = 1; kt < 4; ++kt) {
        tile(kt, true);
        side0(kt);
      }
#pragma clang loop unroll(disable)
      for (int kt = 4; kt < 12; ++kt) {
        tile(kt, true);
        share();
        glds16_s(pf, vol, shm_base + DMP + 7168);
        next();
        kept();
        pa += pstep; vob += 64 * F_ * 2;
        pf = kt == 10 ? (const char*)wo + (bid * 4 + wid) * 2048 + 1024 : pf + 1024;
        if (kt < 8) {
          const unsigned q1 = bid * 4 + wid + (kt - 5) * 1024, q0 = q1 - 1024;
          if (q0 < 1152) gst16(wsx + 98 * MB_ + (q0 & 1) * 589824 + (q0 >> 1) * 1024, vol, *(const u32x4*)(shm + AUX + wid * 1024 + vol));
          if (q1 < 1152) {
            const char* src = (const char*)((q1 >> 1) < 448 ? wd + (size_t)(q1 >> 1) * 8 * D_ : wo + (size_t)((q1 >> 1) - 448) * 8 * D_);
            src += ((3 - ((unsigned)(uintptr_t)src >> 7)) & 7) * 128 + (q1 & 1) * 1024;
            glds16_s(src, (vol >> 7) * 2048 + (vol & 0x70), shm_base + AUX + wid * 1024);
          }
          u32x4 bk;
          asm volatile("v_mov_b32 %0, %1" : "=v"(bk[0]) : "s"(BLANK));
          bk[1] = bk[0]; bk[2] = bk[0]; bk[3] = bk[0];
          gst16(wsx + 64 * MB_ + bid * 16384 + wid * 4096 + (kt - 4) * 1024, vol, bk);
          if (tya && (kt == 4 || kt == 7)) gst16(wsx + 4 * MB_ + (mtile * 32 + ntile) * 4096 + wid * 2048 + (kt & 1) * 1024, vol, bk);
          if (kt == 4) cpub(bk, bk);
        } else {
          if (kt == 8) {
            const float ml = sm * (1.0f / 32), mean = half_sum(sm) * (1.0f / D_), dm = ml - mean;
            const float q = __builtin_fmaf(dm * 32.0f, dm, __builtin_fmaf(-ml, sm, sq));
            const unsigned ps = pack_stats(mean, fmaxf(half_sum(q) * (1.0f / D_), 0.f));
            const u64 now = __builtin_amdgcn_s_memrealtime();
            if ((vol & 0x1f0) == 0) gst16(wsx + 97 * MB_ + srow * 16, vol >> 5, u32x4{ps, (unsigned)now, ps, (unsigned)(now >> 32)});
          }
          if (kt == 9) {
            if (wid == 0) {
              u64 now;
              asm volatile("s_memrealtime %0\n\ts_waitcnt lgkmcnt(0)" : "=s"(now) :: "memory");
              if (vol == 0) asm volatile("global_store_dwordx2 %0, %1, %2 sc1" :: "v"(vol), "v"(now), "s"(wsx + 100 * MB_ + 131072 + bid * 8) : "memory");
            }
            if (wid == 2) glds16_s1(wsx + 102 * MB_ + (bid & ~1) * 512, vol, shm_base + DMP + 7168);
          }
        }
      }
      asm volatile("" :: "v"(cd));
      if (wid >= 2) glds16_s1(wsx + 100 * MB_ + 131072 + (wid - 2) * 1024, vol, shm_base + DMP + 3072 + wid * 1024);
      tile(12, true);
      next();
      kept();
      const int lane = vol >> 4;
      glds16_s1(wsx + 97 * MB_ + (m0 + wid * 64) * 16, ((lane & 32) + 2 * (lane & 15) + ((lane >> 4) & 1)) * 16, shm_base + AUX + wid * 1024);
      asm volatile("s_nop 7\n\ts_nop 3" : "+v"(ca), "+v"(cb));
      cpub(__builtin_bit_cast(u32x4, ca), __builtin_bit_cast(u32x4, cb));
      if (wid >= 2) {
        u32x4 c4 = *(const u32x4*)(shm + DMP + 3072 + wid * 1024 + vol);
        bool ok = __all(fresh(((u64)c4[1] << 32) | c4[0], t_own) & fresh(((u64)c4[3] << 32) | c4[2], t_own));
        while (__builtin_expect(!ok, 0) && __builtin_amdgcn_s_memrealtime() - t_own < TIMEOUT_ / 10) {
          const __amdgpu_buffer_rsrc_t rs = __builtin_amdgcn_make_buffer_rsrc((void*)(wsx + 100 * MB_ + 131072), 0, 2048, 0x00020000);
          asm volatile("" ::: "memory");
          c4 = __builtin_bit_cast(u32x4, __builtin_amdgcn_raw_buffer_load_b128(rs, (wid - 2) * 1024 + vol, 0, 16));
          ok = __all(fresh(((u64)c4[1] << 32) | c4[0], t_own) & fresh(((u64)c4[3] << 32) | c4[2], t_own));
        }
        *(unsigned*)(shm + CNT - 4 + wid * 4) = ok ? 1u : 0u;
      }
      tile(13, false);
      next();
      u32x4 cw[4];
      auto cgather = [&]() {
        const __amdgpu_buffer_rsrc_t rc = __builtin_amdgcn_make_buffer_rsrc((void*)(wsx + 101 * MB_ + ntile * 10240), 0, 5 * 2048, 0x00020000);
        const unsigned vcs = (vol < 512 ? wid * 2048 : 8192) + (vol & 256) * 4 + (vol & 0xf0);
#pragma unroll
        for (int i = 0; i < 4; ++i) cw[i] = __builtin_bit_cast(u32x4, __builtin_amdgcn_raw_buffer_load_b128(rc, vcs + i * 256, 0, 16));
      };
      {
        const int ln = wid * 64 + (vol >> 4);
        u32x4 gr = *(const u32x4*)(shm + AUX + ln * 16);
        if (__builtin_expect(!__all((gr[0] == gr[2]) & fresh(((u64)gr[3] << 32) | gr[1], t_own)), 0)) {
          const __amdgpu_buffer_rsrc_t rs = __builtin_amdgcn_make_buffer_rsrc((void*)(wsx + 97 * MB_ + m0 * 16), 0, 4096, 0x00020000);
          const int prow = (ln & 0xe0) + 2 * (ln & 15) + ((ln >> 4) & 1);
          for (;;) {
            gr = __builtin_bit_cast(u32x4, __builtin_amdgcn_raw_buffer_load_b128(rs, prow * 16, 0, 16));
            if (__all((gr[0] == gr[2]) & fresh(((u64)gr[3] << 32) | gr[1], t_own))) break;
            if (__builtin_amdgcn_s_memrealtime() - t_own > TIMEOUT_) { gr[0] = row_stats_serial(x + (size_t)(m0 + prow) * D_); break; }
            __builtin_amdgcn_s_sleep(2);
          }
        }
        cgather();
        const float nm = -lo_f(gr[0]), dn = hi_f(gr[0]), r = __builtin_amdgcn_rcpf(dn);
        auto mul = [](float a, float b) { float c; asm("v_mul_f32 %0, %1, %2" : "=v"(c) : "v"(a), "v"(b)); return c; };
        const f32x4 fc = {nm, -2.3022081986f * r, mul(mul(mul(-0.10294324f, r), r), r), mul(dn, dn)};
        *(f32x4*)(shm + AUX + ln * 16) = fc;
        float* const tw = (float*)(shm + 2 * G1_STAGE + 4096 + wid * 768) + ln;
#pragma unroll
        for (int i = 0; i < 4; ++i) tw[i * 64] = fc[i];
        ((float*)(shm + AUX + 4096))[384 + ln] = r;
        cok = __builtin_amdgcn_readfirstlane(cnt[1] & cnt[2]) != 0u;
        tile2(0);
        wy = wsetup(wd, 0, vol >> 4, cok);
      }
      tile(14, false);
      next();
      {
        while (__builtin_expect(blank(cw) && cok, 0)) {
          __builtin_amdgcn_s_sleep(2);
          asm volatile("" ::: "memory");
          cgather();
        }
        f32x4 t;
#pragma unroll
        for (int i = 0; i < 4; ++i) {
          float a, b;
          asm("v_add_f32 %0, %2, %3\n\tv_add_f32 %1, %4, %5\n\tv_add_f32 %0, %0, %1" : "=&v"(a), "=&v"(b) : "v"(cw[0][i]), "v"(cw[1][i]), "v"(cw[2][i]), "v"(cw[3][i]));
          const auto r2 = __builtin_amdgcn_permlane16_swap(__float_as_uint(a), __float_as_uint(a), false, false);
          asm("v_add_f32 %0, %1, %2" : "=v"(t[i]) : "v"(r2[0]), "v"(r2[1]));
        }
        const int l = vol >> 4;
        if (__builtin_expect(!cok, 0)) {
          const int gq = (l >> 2) & 3;
          const char* pc = l < 32 ? pbase + ((psh & gq) ? dgu : 0) + (gq >> psh) * 32 + (l & 3) * 8 : gb + dgu + (l & 15) * 8;
          t = f32x4{0.f, 0.f, 0.f, 0.f};
#pragma unroll 1
          for (int k = 0; k < D_; ++k) { const u32x2 w = *(const __attribute__((address_space(1))) u32x2*)(pc + (size_t)k * (l < 32 ? pstr : F_ * 2)); t += f32x4{lo_f(w[0]), hi_f(w[0]), lo_f(w[1]), hi_f(w[1])}; }
        }
        if (l < 32 || wid == 0)
          *(f32x4*)((float*)(shm + AUX + 4096) + ((l >> 3) & 1) * 192 + (l < 32 ? wid : 4) * 32 + (l & 7) * 4) = t;
        __builtin_amdgcn_s_setprio(2);
      }
      SB0();
      ARRP(wid * 64 + 60, true);
      BARL();
      body(false);
    }
#pragma unroll
    for (int t = 0; t < 8; t += 2) mm(9, t, true);
    asm volatile("s_nop 7\n\ts_nop 3");
    SB0();
    TS1(3);
    if (!LD) {
#pragma unroll
      for (int i = 0; i < 3; ++i) {
        const unsigned r = ((role * 4 + (i & 1) * 2) * 14 + (i < 2 ? 10 : 0) + (wm & 2)) * 32;
        sw[0] = wy.wb[0] + (r << wy.sh[0]); sw[1] = wy.wb[1] + (r << wy.sh[1]);
        asm volatile(G2_W :: [l] "s"((i + 1) * 16384), G2_WOP(wy) : "memory", "scc", "m0");
      }
    } else {
      __builtin_amdgcn_s_setprio(3);
      WAIT_V(0);
    }
  };
  if (wid >= 4) kloop(std::integral_constant<int, 1>{});
  else kloop(std::integral_constant<int, 0>{});
  __builtin_amdgcn_sched_barrier(0);
  int tz = threadIdx.x;
  asm volatile("" : "+v"(tz));
  __builtin_amdgcn_sched_barrier(0);
  const int li = tz & 15, lq = (tz >> 4) & 3, ln = tz & 63;
  const __amdgpu_buffer_rsrc_t ra = __builtin_amdgcn_make_buffer_rsrc((void*)abuf, 0, 16 * 72 * 16384, 0x00020000);
  float* const cfin = (float*)(shm + AUX + 4096);
  float* const svr = cfin + 384;
  float* const tb = (float*)(shm + 2 * G1_STAGE + 4096 + wm * 1024);
  auto signal = [&](unsigned v) {
    unsigned a, b;
    asm volatile("v_mov_b32 %0, %2\n\tv_mov_b32 %1, %3\n\ts_mov_b64 exec, 1\n\tds_add_u32 %0, %1\n\ts_mov_b64 exec, -1"
                 : "=&v"(a), "=&v"(b) : "s"(shm_base + CNT), "s"(v) : "memory");
  };
  auto await = [&](unsigned mask, unsigned val) {
    unsigned a, t;
    asm volatile("v_mov_b32 %0, %2\n1:\n\tds_read_b32 %1, %0\n\ts_waitcnt lgkmcnt(0)\n\tv_readfirstlane_b32 vcc_lo, %1\n\ts_and_b32 vcc_lo, vcc_lo, %3\n\t"
                 "s_cmp_eq_u32 vcc_lo, %4\n\ts_cbranch_scc1 2f\n\ts_sleep 1\n\ts_branch 1b\n2:" : "=&v"(a), "=&v"(t) : "s"(shm_base + CNT), "s"(mask), "s"(val) : "memory", "scc", "vcc");
  };
  auto client = [&](int l) { const int m128 = mtile * 2 + ((l >> 4) & 1); return (((l & 7) * 4 + (m128 & 3)) * 8) + ((m128 >> 2) ^ ((l & 8) ? 5 : 0)); };
  auto notify = [&](int l0, int nl) __attribute__((always_inline)) {
    u64 now;
    asm volatile("s_memrealtime %0\n\ts_waitcnt lgkmcnt(0)" : "=s"(now) :: "memory");
    int l = threadIdx.x;
    asm volatile("" : "+v"(l));
    l &= 63;
    const __amdgpu_buffer_rsrc_t rd1 = __builtin_amdgcn_make_buffer_rsrc((void*)(done1 + ntile), 0, 256 * 512, 0x00020000);
    if (l < nl) __builtin_amdgcn_raw_buffer_store_b64(u32x2{(unsigned)now, (unsigned)(now >> 32)}, rd1, client(l0 + l) * 512, 0, 16);
  };
  const int xo = ((xcd & 1) << 1) | ((xcd >> 1) & 1), te = (xcd >> 2) * 28 + 6 * (xo >> 1) + (xo & 1), tl = (xcd >> 2) * 28 + 12 + 4 * xo;
  const u64 te0 = __builtin_amdgcn_s_memrealtime();
  const __amdgpu_buffer_rsrc_t re = __builtin_amdgcn_make_buffer_rsrc((void*)(done1 + client((wm >> 1) * 16 + 8 - (xcd & 4) * 2) * 64 + 32 + (ntile & 15) * 2 + (wm & 1)),
                                                                      0, 7 * 16384 + 8, 0x00020000);
  auto early = [&]() {
    __builtin_amdgcn_raw_buffer_store_b64(u32x2{(unsigned)te0, (unsigned)(te0 >> 32)}, re, ln * 16384, 0, 16);
  };
  signal(1u << (8 * wc));
  if (wc) {
    await(0xffffu, 0x404u);
#pragma unroll
    for (int f = 0; f < 4; ++f) *(u32x4*)(shm + G2_X + wm * 4096 + f * 1024 + ln * 16) = xq[f];
    SB0();
    __builtin_amdgcn_s_setprio(1);
  }
  TS1(4);
  f32x4 rf[4];
#pragma unroll
  for (int mf = 0; mf < 4; ++mf) rf[mf] = *(const f32x4*)(shm + AUX + (wm * 64 + mf * 16 + li) * 16);
  const float* const cl = cfin + wc * 192 + lq * 4;
  const int sbase = ((mtile * 2 + (wm >> 1)) * 72 * 8 + (wm & 1) * 4) * 2048;
  auto putf = [&](int kg, int mf, u32x4 v) {
    __builtin_amdgcn_raw_buffer_store_b128(v, ra, ln * 16 + sbase + ((kg >> 1) * 8 + mf) * 2048 + (kg & 1) * 1024, 0, 16);
  };
  auto gelp = [&](f32x2 g, f32x2 u, f32x2 cg, f32x2 cu, f32x2 m2, f32x2 k12, f32x2 k22, f32x2 d2) {
    const f32x2 tg = __builtin_elementwise_fma(m2, cg, g), tu = __builtin_elementwise_fma(m2, cu, u);
    const f32x2 arg = tg * __builtin_elementwise_fma(tg * tg, k22, k12);
    const f32x2 dn = __builtin_elementwise_fma(f32x2{__builtin_amdgcn_exp2f(arg[0]), __builtin_amdgcn_exp2f(arg[1])}, d2, d2);
    const f32x2 r = (tg * tu) * f32x2{__builtin_amdgcn_rcpf(dn[0]), __builtin_amdgcn_rcpf(dn[1])};
    return pack2(r[0], r[1]);
  };
  auto gel2 = [&](int p, int kg, int mf0 = 0, int mf1 = 4) {
    f32x4 cg[2], cu[2];
#pragma unroll
    for (int j = 0; j < 2; ++j) { cg[j] = *(const f32x4*)(cl + p * 32 + j * 16); cu[j] = *(const f32x4*)(cl + p * 32 + 32 + j * 16); }
#pragma unroll
    for (int mf = mf0; mf < mf1; ++mf) {
      const f32x2 m2 = {rf[mf][0], rf[mf][0]}, k12 = {rf[mf][1], rf[mf][1]}, k22 = {rf[mf][2], rf[mf][2]}, d2 = {rf[mf][3], rf[mf][3]};
      u32x4 o;
#pragma unroll
      for (int i = 0; i < 4; ++i) {
        const int j = i >> 1, e = (i & 1) * 2;
        const f32x4 g = acc[mf][p * 2 + j], u = acc[mf][p * 2 + 2 + j];
        o[i] = gelp(f32x2{g[e], g[e + 1]}, f32x2{u[e], u[e + 1]}, f32x2{cg[j][e], cg[j][e + 1]}, f32x2{cu[j][e], cu[j][e + 1]}, m2, k12, k22, d2);
      }
      putf(kg, mf, o);
    }
  };
  int tsel = ntile & 1;
  asm volatile("" : "+s"(tsel));
  if (tsel != 0) {
    const int tq = (wc ? tl : te + 2 + (xo & 1)) + he;
    gel2(0, 2 * tq);
    gel2(3, 2 * tq + 1);
    const float cg = cfin[wc * 192 + 64 + li], cu = cfin[wc * 192 + 80 + li];
#pragma unroll
    for (int mf = 0; mf < 4; ++mf) {
      f32x4 t[4];
#pragma unroll
      for (int i = 0; i < 4; ++i) t[i] = *(const f32x4*)(tb + i * 64 + mf * 16 + lq * 4);
      const f32x4 g = acc[mf][4], u = acc[mf][5];
      u32x2 o;
#pragma unroll
      for (int e = 0; e < 4; e += 2)
        o[e >> 1] = gelp(f32x2{g[e], g[e + 1]}, f32x2{u[e], u[e + 1]}, f32x2{cg, cg}, f32x2{cu, cu}, f32x2{t[0][e], t[0][e + 1]},
                         f32x2{t[1][e], t[1][e + 1]}, f32x2{t[2][e], t[2][e + 1]}, f32x2{t[3][e], t[3][e + 1]});
      *(u32x2*)(shm + wm * 4096 + mf * 1024 + wc * 512 + li * 32 + lq * 8) = o;
      if (mf == EPOS && wc == 0) {
        TS1(1);
        WAIT_V(0);
        TS1(6);
        early();
      }
    }
    if (wc == 0) {
      signal(0x10000u << wid);
    } else {
      await(0x1000u << wid, 0x1000u << wid);
#pragma unroll
      for (int mf = 0; mf < 4; ++mf) putf(2 * (tl + 2) + he, mf, __builtin_bit_cast(u32x4, tr_frag(shm3 + wm * 4096 + mf * 1024 + ln * 8)));
    }
  } else {
    float rd[4];
#pragma unroll
    for (int mf = 0; mf < 4; ++mf) rd[mf] = svr[wm * 64 + mf * 16 + li];
    auto cvq = [&](int p, int mf, const f32x4& c0, const f32x4& c1) {
      u32x4 o;
#pragma unroll
      for (int j = 0; j < 2; ++j) {
        const f32x4 a = acc[mf][p * 2 + j], c4 = j ? c1 : c0;
        const f32x2 m2 = {rf[mf][0], rf[mf][0]}, r2 = {rd[mf], rd[mf]};
        f32x2 t0 = __builtin_elementwise_fma(m2, f32x2{c4[0], c4[1]}, f32x2{a[0], a[1]});
        f32x2 t1 = __builtin_elementwise_fma(m2, f32x2{c4[2], c4[3]}, f32x2{a[2], a[3]});
        if (p) { t0 *= r2; t1 *= r2; }
        o[2 * j] = pack2(t0[0], t0[1]); o[2 * j + 1] = pack2(t1[0], t1[1]);
      }
      return o;
    };
    const float cv0 = cfin[wc * 192 + 64 + li], cv1 = cfin[wc * 192 + 80 + li];
    auto cvv = [&](int mf) {
      const f32x4 n4 = *(const f32x4*)(tb + mf * 16 + lq * 4), r4 = *(const f32x4*)(svr + wm * 64 + mf * 16 + lq * 4);
      u32x4 o;
#pragma unroll
      for (int j = 0; j < 2; ++j) {
        const f32x4 a = acc[mf][4 + j];
        const f32x2 c2 = {j ? cv1 : cv0, j ? cv1 : cv0};
        const f32x2 t0 = __builtin_elementwise_fma(f32x2{n4[0], n4[1]}, c2, f32x2{a[0], a[1]}) * f32x2{r4[0], r4[1]};
        const f32x2 t1 = __builtin_elementwise_fma(f32x2{n4[2], n4[3]}, c2, f32x2{a[2], a[3]}) * f32x2{r4[2], r4[3]};
        o[2 * j] = pack2(t0[0], t0[1]); o[2 * j + 1] = pack2(t1[0], t1[1]);
      }
      return o;
    };
    u32x4 kx[4], vx[4], qx[4], hx[4];
    const bf16x4 ones = {0x3f80, 0x3f80, 0x3f80, 0x3f80};
    const f32x4 ck0 = *(const f32x4*)(cl + 32), ck1 = *(const f32x4*)(cl + 48);
    kx[2] = cvq(1, 2, ck0, ck1); kx[3] = cvq(1, 3, ck0, ck1);
    vx[2] = cvv(2); vx[3] = cvv(3);
    auto exo = [](int e) { return e * 4096 + (e >> 2) * (G2_A - 16384); };
    const bool needh = wm == 0 && mtile > 0;
    const __amdgpu_buffer_rsrc_t rh = __builtin_amdgcn_make_buffer_rsrc((void*)((char*)halo + ((needh ? mtile - 1 : mtile) * 32 + ntile + wc) * 4096), 0,
                                                                        needh || wm == 3 ? 4096 : 0, 0x00020000);
    if (wm < 3) {
      char* const ex = shm + exo(wc * 3 + wm) + ln * 16;
      *(u32x4*)ex = kx[2]; *(u32x4*)(ex + 1024) = kx[3]; *(u32x4*)(ex + 2048) = vx[2]; *(u32x4*)(ex + 3072) = vx[3];
      signal(0x10000u << wid);
    } else {
#pragma unroll
      for (int r = 0; r < 4; ++r) __builtin_amdgcn_raw_buffer_store_b128(r < 2 ? kx[2 + r] : vx[r], rh, ln * 16 + r * 1024, 0, 16);
    }
    auto hload = [&]() {
#pragma unroll
      for (int r = 0; r < 4; ++r) hx[r] = __builtin_bit_cast(u32x4, __builtin_amdgcn_raw_buffer_load_b128(rh, ln * 16 + r * 1024, 0, 16));
    };
    SB0();
    gel2(3, 2 * (wc ? tl + 3 : te) + he);
    SB0();
    if (wm == 0) hload();
    SB0();
    kx[0] = cvq(1, 0, ck0, ck1); kx[1] = cvq(1, 1, ck0, ck1);
    vx[0] = cvv(0); vx[1] = cvv(1);
    {
      const f32x4 cq0 = *(const f32x4*)(cl), cq1 = *(const f32x4*)(cl + 16);
#pragma unroll
      for (int mf = 0; mf < 4; ++mf) qx[mf] = cvq(0, mf, cq0, cq1);
    }
    f32x4 mk0, mk1;
    u32x2 pm;
#pragma unroll
    for (int j = 0; j < 4; ++j) {
      const int d = 4 * lq + j - li;
      mk0[j] = d >= 9 ? 0.f : -1.0e30f;
      mk1[j] = (unsigned)(d + 7) <= 7u ? 0.f : -1.0e30f;
    }
#pragma unroll
    for (int j = 0; j < 2; ++j) pm[j] = (4 * lq + 2 * j > li ? 0xffffu : 0u) | (4 * lq + 2 * j + 1 > li ? 0xffff0000u : 0u);
    auto max3 = [](float a, float b, float c) { float r; asm("v_max3_f32 %0, %1, %2, %3" : "=v"(r) : "v"(a), "v"(b), "v"(c)); return r; };
    auto max2 = [](float a, float b) { float r; asm("v_max_f32 %0, %1, %2" : "=v"(r) : "v"(a), "v"(b)); return r; };
    asm volatile("" : "+v"(kx[0]), "+v"(kx[1]), "+v"(vx[0]), "+v"(vx[1]), "+v"(qx[0]), "+v"(qx[1]), "+v"(qx[2]), "+v"(qx[3]), "+v"(mk0), "+v"(mk1), "+v"(pm));
    TS1(1);
    if (wc == 0) {
      if (wm == 0) WAIT_V(4); else WAIT_V(0);
      early();
    }
    if (wm > 0) {
      await(0x8000u << wid, 0x8000u << wid);
#pragma unroll
      for (int i = 0; i < 4; ++i) hx[i] = *(const u32x4*)(shm + exo(wc * 3 + wm - 1) + i * 1024 + ln * 16);
    } else if (__builtin_expect(cok || !needh, 1)) {
      while (__builtin_expect(blank(hx), 0)) {
        __builtin_amdgcn_s_sleep(1);
        asm volatile("" ::: "memory");
        hload();
      }
    } else {
      await(0xffffu, 0x404u);
      char* const fb = shm + exo(6 + wc) + ln * 16;
#pragma unroll 1
      for (int i = 0; i < 32; ++i) {
        const int rec = i >> 3, e = i & 7;
        const int row = m0 - 32 + 2 * (rec < 2 ? li : lq * 4 + (e & 3)) + (rec & 1);
        const int col = (rec < 2 ? D_ + lq * 4 + (e & 3) : 2 * D_ + li) + (ntile + wc) * 32 + (e >> 2) * 16;
        const u16* xr = x + (size_t)row * D_;
        const unsigned stt = row_stats_serial(xr);
        const float mb = lo_f(stt);
        float a = 0.f;
#pragma unroll 1
        for (int k = 0; k < D_; ++k) a += (bf2f(xr[k]) - mb) * bf2f(wqkv[(size_t)k * 3072 + col]);
        *(u16*)(fb + rec * 1024 + e * 2) = f2bf(a / hi_f(stt));
      }
#pragma unroll
      for (int i = 0; i < 4; ++i) hx[i] = *(const u32x4*)(fb + i * 1024);
    }
    TS1(6);
    {
      const f32x4 z = {0.f, 0.f, 0.f, 0.f};
      const float mz = mtile == 0 && wm == 0 ? -1.0e30f : 0.f;
      const f32x4 mkf = mk0 + f32x4{mz, mz, mz, mz};
      f32x4 s0[4], s1[4];
#pragma unroll
      for (int i = 0; i < 4; ++i) {
        const int n = i ^ 2;
        const bf16x8 qf = __builtin_bit_cast(bf16x8, qx[n]);
        s1[n] = __builtin_amdgcn_mfma_f32_16x16x32_bf16(__builtin_bit_cast(bf16x8, kx[n]), qf, mk1, 0, 0, 0);
        s0[n] = __builtin_amdgcn_mfma_f32_16x16x32_bf16(__builtin_bit_cast(bf16x8, n < 2 ? hx[n] : kx[n - 2]), qf, n < 2 ? mkf : mk0, 0, 0, 0);
      }
      SB0();
      asm volatile("s_nop 7\n\ts_nop 3" : "+v"(s0[0]), "+v"(s1[0]), "+v"(s0[1]), "+v"(s1[1]), "+v"(s0[2]), "+v"(s1[2]), "+v"(s0[3]), "+v"(s1[3]));
      float mx[4];
#pragma unroll
      for (int n = 0; n < 4; ++n)
#pragma unroll
        for (int j = 0; j < 4; ++j) s0[n][j] = max2(s0[n][j], s1[n][j]);
#pragma unroll
      for (int n = 0; n < 4; ++n) mx[n] = max3(s0[n][0], s0[n][1], s0[n][2]);
#pragma unroll
      for (int n = 0; n < 4; ++n) mx[n] = max2(mx[n], s0[n][3]);
#pragma unroll
      for (int n = 0; n < 4; ++n) { auto r = __builtin_amdgcn_permlane16_swap(__float_as_uint(mx[n]), __float_as_uint(mx[n]), false, false);
        mx[n] = max2(__uint_as_float(r[0]), __uint_as_float(r[1])); }
#pragma unroll
      for (int n = 0; n < 4; ++n) { auto r = __builtin_amdgcn_permlane32_swap(__float_as_uint(mx[n]), __float_as_uint(mx[n]), false, false);
        mx[n] = max2(__uint_as_float(r[0]), __uint_as_float(r[1])); }
      SB0();
      constexpr float cs2 = 0.17677669529663687f * 1.4426950408889634f;
      f32x4 os[4], o0[4], o1[4];
#pragma unroll
      for (int n = 0; n < 4; ++n) {
        const float c = cs2 * rd[n], nm = -mx[n] * c;
        const f32x4 ar = __builtin_elementwise_fma(s0[n], f32x4{c, c, c, c}, f32x4{nm, nm, nm, nm});
        const u32x2 pk = {pack2(__builtin_amdgcn_exp2f(ar[0]), __builtin_amdgcn_exp2f(ar[1])), pack2(__builtin_amdgcn_exp2f(ar[2]), __builtin_amdgcn_exp2f(ar[3]))};
        const bf16x4 pa = __builtin_bit_cast(bf16x4, pk), p0 = __builtin_bit_cast(bf16x4, pk & pm), p1 = __builtin_bit_cast(bf16x4, pk & ~pm);
        const u32x4 vp = n < 2 ? hx[2 + n] : vx[n - 2];
        auto half = [](const u32x4& v, int h) { return __builtin_bit_cast(bf16x4, u32x2{v[2 * h], v[2 * h + 1]}); };
        os[n] = __builtin_amdgcn_mfma_f32_16x16x16bf16_1k(ones, pa, z, 0, 0, 0);
        o0[n] = __builtin_amdgcn_mfma_f32_16x16x16bf16_1k(half(vx[n], 0), p1, __builtin_amdgcn_mfma_f32_16x16x16bf16_1k(half(vp, 0), p0, z, 0, 0, 0), 0, 0, 0);
        o1[n] = __builtin_amdgcn_mfma_f32_16x16x16bf16_1k(half(vx[n], 1), p1, __builtin_amdgcn_mfma_f32_16x16x16bf16_1k(half(vp, 1), p0, z, 0, 0, 0), 0, 0, 0);
      }
#pragma unroll
      for (int n = 0; n < 4; ++n) {
        const float inv = __builtin_amdgcn_rcpf(os[n][0]);
        const f32x4 n0 = o0[n] * inv, n1 = o1[n] * inv;
        putf(112 + ntile + wc, n, u32x4{pack2(n0[0], n0[1]), pack2(n0[2], n0[3]), pack2(n1[0], n1[1]), pack2(n1[2], n1[3])});
      }
    }
  }
  TS1(2);
  {
  __builtin_amdgcn_sched_barrier(0);
  int tid = threadIdx.x;
  asm volatile("" : "+v"(tid));
  __builtin_amdgcn_sched_barrier(0);
  const int lane = tid & 63;
  const int wm = wid >> 1, wn = wid & 1;
  const int m0 = mt2 * 128, n0 = nt2 * 128;
  constexpr int PD = G2_NSTAGE;
  const int ny = (xcd & 1) ? NY - DLT : NY + DLT;
  const unsigned va = lane * 16;
  auto stage_y = [&]() {
    asm volatile("s_add_u32 m0, %[l], %[oa]\n\ts_nop 0\n\tglobal_load_lds_dwordx4 %[va], %[a] sc1\n\tglobal_load_lds_dwordx4 %[va], %[a] offset:1024 sc1\n\t"
                 "global_load_lds_dwordx4 %[va], %[a] offset:2048 sc1\n\tglobal_load_lds_dwordx4 %[va], %[a] offset:3072 sc1\n\t" G2_W
                 :: [l] "s"(sl), [oa] "s"(shm_base + G2_A + (p ^ role) * 4096), [a] "s"(sa), [va] "v"(va), G2_WOP(wy) : "memory", "scc", "m0");
    ++st;
    sa += 16384;
    sl = (sl + 16384) & 65535;
    wsel();
  };
  u64 ready = 0;
  const u64* const mbox = &done1[bid * 64 + lane];
  auto seen = [&](u64 v) __attribute__((always_inline)) {
    const u64 r = __ballot(fresh(v, t_own));
    u64 d;
    asm("s_bitreplicate_b64_b32 %0, %1" : "=s"(d) : "s"((unsigned)r >> (16 * role)));
    ready |= r | (d << 32);
  };
  auto need = [&](u64 mask, bool renew) __attribute__((always_inline)) {
    if ((ready & mask) != mask) {
      seen(__hip_atomic_load(mbox, RLX_AGENT));
      while ((ready & mask) != mask) {
        __builtin_amdgcn_s_sleep(PSLEEP);
        if (renew && !cok) notify(0, 32);
        seen(__hip_atomic_load(mbox, RLX_AGENT));
      }
    }
    asm volatile("" ::: "memory");
  };
  u64 fl = 0;
  auto peek = [&]() { asm volatile("global_load_dwordx2 %0, %1, %2 sc1" : "+v"(fl) : "v"(va >> 1), "s"(done1 + bid * 64) : "memory"); };
  auto check = [&](u64 mask, bool renew) __attribute__((always_inline)) {
    asm volatile("" : "+v"(fl));
    seen(fl);
    if (__builtin_expect((ready & mask) != mask, 0)) need(mask, renew);
  };
  if (wid >= 4) {
    TS(4);
    __builtin_amdgcn_s_setprio(0);
    BARL();
    stage_y();
    stage_y();
    BAR();
    if (p == 1) peek();
    stage_y();
    WAIT_V(16);
    BAR();
    if (p == 0) notify(0, 32);
#define G2_A2 "global_load_lds_dwordx4 %[va], %[a] sc1\n\tglobal_load_lds_dwordx4 %[va], %[a] offset:1024 sc1"
#define G2_A4 G2_A2 "\n\tglobal_load_lds_dwordx4 %[va], %[a] offset:2048 sc1\n\tglobal_load_lds_dwordx4 %[va], %[a] offset:3072 sc1"
#define G2_SYNC "\n\ts_mov_b32 m0, %[la]\n\ts_waitcnt vmcnt(%[n])\n\ts_barrier\n\t"
    const unsigned oy = shm_base + G2_A + (p ^ role) * 4096, os = shm_base + G2_A + (p >> 1) * 8192 + (p & 1) * 2048;
    asm volatile("s_mov_b32 m0, %[la]\n\ts_nop 0\n\t" G2_A4 :: [la] "s"(sl + oy), [a] "s"(sa), [va] "v"(va) : "memory", "m0");
    auto step = [&](long adv = 16384) __attribute__((always_inline)) {
      wsel();
      ++st;
      sa += adv;
      const unsigned sn = (sl + 16384) & 65535;
      asm volatile(G2_W G2_SYNC G2_A4 :: [l] "s"(sl), [la] "s"(sn + oy), [n] "n"(16), [a] "s"(sa), [va] "v"(va), G2_WOP(wy) : "memory", "scc", "m0");
      sl = sn;
    };
    auto chk1 = [&](u64 mask) __attribute__((always_inline)) { if (p == 1) { WAIT_V(16); check(mask, true); } };
    step();
    chk1(0xcc00cc00ull << 32);
    step(); step();
    if (p == 1) peek();
    step(); step();
    chk1(0x0f0fu << (16 * role));
#pragma unroll 1
    for (int g = 7; g < 13; ++g) step();
    if (p == 1) peek();
    step(); step();
    chk1((0xffffu << (16 * role)) | 0xf555f555u);
#pragma unroll 1
    for (int g = 15; g < min(ny, NY) - 5; ++g) step();
    if (ny > NY) {
      step(((1 - 2 * role) * NY + 1 - DLT) * 16384);
#pragma unroll
      for (int i = 1; i < DLT; ++i) step();
    }
    wsel();
    sa = sz;
    {
      const unsigned sn = (sl + 16384) & 65535;
      asm volatile(G2_W G2_SYNC G2_A2 :: [l] "s"(sl), [la] "s"(sn + os), [n] "n"(16), [a] "s"(sa), [va] "v"(va), G2_WOP(wy) : "memory", "scc", "m0");
      sl = sn;
    }
    sw[0] = wz.wb[0]; sw[1] = wz.wb[1];
#pragma unroll
    for (int j = -3; j < 13; ++j) {
      if (j == GETP + 1 || j == GETP + 2)
        asm volatile("s_mov_b32 m0, %[l]\n\ts_nop 0\n\tglobal_load_lds_dwordx4 %[va], %[a] offset:%[o] sc1\n\tglobal_load_lds_dwordx4 %[va], %[a] offset:%[o]+1024 sc1"
                     :: [l] "s"(shm_base + G2_X + 16384 + p * 4096), [a] "s"((const char*)slab + (size_t)(bid ^ 5) * 16384 + p * 4096), [va] "v"(va),
                        [o] "n"((j - GETP - 1) * 2048) : "memory", "m0");
      sa += 16384;
      const unsigned sn = (sl + 16384) & 65535;
      constexpr int G = GETP;
      static_assert(GETP <= GETJ + 1, "");
      if (j < 12) asm volatile(G2_W G2_SYNC G2_A2 :: [l] "s"(sl), [la] "s"(sn + os), [n] "n"((j == -3 ? 14 : 12) + (j > G && j < G + 3 ? 2 : 0) + (j > G + 1 && j < G + 4 ? 2 : 0)),
                               [a] "s"(sa), [va] "v"(va), G2_WOP(wz) : "memory", "scc", "m0");
      else asm volatile(G2_W :: [l] "s"(sl), G2_WOP(wz) : "memory", "scc", "m0");
      sl = sn;
      sw[0] += ws0; sw[1] += ws1;
    }
    WAIT_V(12);
    BAR();
    WAIT_V(6);
    BAR();
    WAIT_V(0);
    BAR();
    asm volatile("" : "+v"(fl));
    TS(5);
    if (tid < 258) __hip_atomic_store(lane ? done2 + bid * 8 : &acks[bid], __builtin_amdgcn_s_memrealtime(), RLX_AGENT);
    if (__builtin_expect(!cok, 0) && p == 0) {
      const bool mine = lane <= 32;
      const u64* const pa = &acks[lane < 32 ? client(lane) : bid ^ 5];
      while (!__all(!mine || fresh(__hip_atomic_load(pa, RLX_AGENT), t_own))) {
        notify(0, 32);
        if (lane == 0) __hip_atomic_store(done2 + bid * 8, __builtin_amdgcn_s_memrealtime(), RLX_AGENT);
        __builtin_amdgcn_s_sleep(32);
      }
    }
  } else {
    TS(6);
    f32x4 acc[4][4], sacc[2][4];
    const int fr = lane & 15, fq = lane >> 4, q4 = fr >> 2, pp = lane & 3;
    const int ff = ((q4 >> 1) & 1) | ((fq & 1) << 1);
    int aof = G2_A + wm * 8192 + lane * 16;
    int boff[4];
#pragma unroll
    for (int j = 0; j < 4; ++j) boff[j] = wn * 8192 + (8 * fq + q4) * 128 + ((j ^ ff) * 32) + pp * 8;
    int vo = wid * 4096 + lane * 16;
    int vx = ((m0 + wm * 64 + role * 32 + 2 * fr) * D_ + n0 + wn * 64) * 2 + (fq & 1) * 32 + (fq >> 1) * 16;
    lds_cptr px = shm3 + G2_X + wid * 4096 + (fq >> 1) * 512 + fr * 16 + (fq & 1) * 8;
    asm volatile("" : "+v"(vo), "+v"(vx), "+v"(px));
#define SB() __builtin_amdgcn_sched_barrier(0)
    struct Fr { bf16x8 a[4]; bf16x4 bl[4], bh[4]; };
    Fr fa[2], fb[2];
    auto rd = [&](Fr* f, int odd, int t) {
      constexpr int ord[12] = {0, 1, 8, 2, 3, 4, 5, 6, 7, 9, 10, 11};
      constexpr int seq[24] = {0, 1, 2, 12, 13, 14, 15, 16, 3, 4, 5, 6, 17, 18, 19, 20, 7, 8, 9, 21, 10, 22, 11, 23};
      lds_cptr sb = shm3 + odd * 16384;
      const int ks = seq[t] / 12, o = ord[seq[t] % 12];
      if (o >= 8) f[ks].a[o - 8] = *(const __attribute__((address_space(3))) bf16x8*)(sb + aof + (o - 8) * 2048 + ks * 1024);
      else if (o & 1) f[ks].bh[o >> 1] = tr_read(sb + boff[o >> 1] + ks * 4096 + 512);
      else f[ks].bl[o >> 1] = tr_read(sb + boff[o >> 1] + ks * 4096);
    };
    auto bfr = [&](const Fr& c, int j) { return __builtin_shufflevector(c.bl[j], c.bh[j], 0, 1, 2, 3, 4, 5, 6, 7); };
    auto mf1 = [](f32x4& c, bf16x8 b, bf16x8 a, bool z = false) {
      if (z) asm volatile("v_mfma_f32_16x16x32_bf16 %0, %1, %2, 0" : "=&v"(c) : "v"(b), "v"(a));
      else asm volatile("v_mfma_f32_16x16x32_bf16 %0, %1, %2, %0" : "+v"(c) : "v"(b), "v"(a));
    };
    auto mf2 = [](f32x4& c, bf16x8 b, bf16x8 a, bf16x8 b2, bf16x8 a2, bool z) {
      if (z) asm volatile("v_mfma_f32_16x16x32_bf16 %0, %1, %2, 0\n\tv_mfma_f32_16x16x32_bf16 %0, %3, %4, %0" : "=&v"(c) : "v"(b), "v"(a), "v"(b2), "v"(a2));
      else asm volatile("v_mfma_f32_16x16x32_bf16 %0, %1, %2, %0\n\tv_mfma_f32_16x16x32_bf16 %0, %3, %4, %0" : "+v"(c) : "v"(b), "v"(a), "v"(b2), "v"(a2));
    };
    auto pairs = [&](f32x4 (*ac)[4], const Fr* c, int nq, int mx, int nf, auto fill, bool z = false) {
#pragma unroll
      for (int q = 0; q < nq; ++q) {
        const int mf = (q >> 2) ^ mx, j = (q & 4) ? 3 - (q & 3) : q & 3, ks = q & 1;
        SB();
        if (2 * q >= nf) mf2(ac[mf][j], bfr(c[ks], j), c[ks].a[mf], bfr(c[ks ^ 1], j), c[ks ^ 1].a[mf], z);
        else {
          mf1(ac[mf][j], bfr(c[ks], j), c[ks].a[mf], z);
          SB();
          fill(2 * q);
          SB();
          mf1(ac[mf][j], bfr(c[ks ^ 1], j), c[ks ^ 1].a[mf]);
          SB();
          fill(2 * q + 1);
        }
      }
      SB();
    };
    auto flip = [&]() {
      aof ^= 32768; boff[0] ^= 32768; boff[1] ^= 32768; boff[2] ^= 32768; boff[3] ^= 32768;
      asm volatile("" : "+v"(aof), "+v"(boff[0]), "+v"(boff[1]), "+v"(boff[2]), "+v"(boff[3]));
    };
    auto bar = [&]() { __builtin_amdgcn_s_waitcnt(0xC07F); BAR(); };
    auto ystg = [&](const Fr* c, Fr* n, int odd, bool z = false) {
      pairs(acc, c, 16, 0, odd ? 30 : 24, [&](int t) {
        if (t < 24) rd(n, odd, t);
        else if (t == 24) asm volatile("s_nop 0");
        else { int& a = t == 29 ? aof : boff[t - 25]; a ^= 32768; asm volatile("" : "+v"(a)); }
      }, z);
    };
    auto stg = [&](const Fr* c, Fr* n, int odd, bool z = false) { pairs(sacc, c, 8, 0, 10, [&](int t) { rd(n, odd, 2 * t); rd(n, odd, 2 * t + 1); }, z); };
    asm volatile("" : "+v"(aof), "+v"(boff[0]), "+v"(boff[1]), "+v"(boff[2]), "+v"(boff[3]));
    await(0xffffu, 0x404u);
    auto lda = [&](int i) {
      asm volatile("s_mov_b32 m0, %[l]\n\ts_nop 0\n\tglobal_load_lds_dwordx4 %[va], %[a] sc1\n\tglobal_load_lds_dwordx4 %[va], %[a] offset:1024 sc1\n\t"
                   "global_load_lds_dwordx4 %[va], %[a] offset:2048 sc1\n\tglobal_load_lds_dwordx4 %[va], %[a] offset:3072 sc1"
                   :: [l] "s"(shm_base + G2_A + (i + 1) * 16384 + (p ^ role) * 4096), [a] "s"(sa + i * 16384), [va] "v"(va) : "memory", "m0");
    };
    need(0x00330033ull << 32, false);
    TS(4);
    lda(0);
    lda(1);
    need(0x00ff00ffull << 32, false);
    TS(0);
    lda(2);
    WAIT_V(8);
    signal(1u << 24);
    await(0xff000000u, 4u << 24);
    __builtin_amdgcn_s_waitcnt(0xC07F);
    SB();
#pragma unroll
    for (int t = 0; t < 24; ++t) rd(fa, 1, t);
    flip();
#define CPRIO 0
    __builtin_amdgcn_s_setprio(CPRIO);
    WAIT_V(4);
    TS(5);
    peek();
    bar();
    TS(7);
    ystg(fa, fb, 0, true);
    WAIT_V(1);
    bar();
    ystg(fb, fa, 1);
    WAIT_V(0);
    check(0x33003300ull << 32, false);
    constexpr int PAR = (NY - DLT + 1) & 1;
#pragma unroll 1
    for (int g = 2; g < ny - 2; g += 2) {
      bar();
      ystg(fa, fb, 0);
      bar();
      ystg(fb, fa, 1);
    }
    if (PAR) {
      bar();
      ystg(fa, fb, 0);
    }
    Fr* const f0 = PAR ? fb : fa;
    Fr* const f1 = PAR ? fa : fb;
    bar();
    {
      const __amdgpu_buffer_rsrc_t rs = __builtin_amdgcn_make_buffer_rsrc((void*)((char*)slab + (size_t)bid * 16384), 0, 16384, 0x00020000);
      u32x4 pk;
      pairs(acc, f0, 16, 2, 32, [&](int t) {
        if (t < 20) rd(f1, PAR, t);
        else {
          const int f = (t - 20) / 3, e = (t - 20) % 3;
          if (e < 2) { const f32x4 a = acc[2 + (f >> 1)][(f & 1) * 2 + e]; pk[2 * e] = pack2(a[0], a[1]); pk[2 * e + 1] = pack2(a[2], a[3]); }
          else __builtin_amdgcn_raw_buffer_store_b128(pk, rs, vo + f * 1024, 0, 16);
        }
      });
    }
    TS(2);
    static_assert(GETJ % 2 == 1 && GETJ >= 3 && GETJ <= 7, "");
    f32x4 xf[2][4];
#pragma unroll
    for (int j = 0; j < GETJ + 5; j += 2) {
      if (PAR) flip();
      bar();
      stg(f1, f0, PAR ^ 1, j == 0);
      if (!PAR) flip();
      bar();
      u32x2 xv[2];
      pairs(sacc, f0, 8, 0, 12, [&](int t) {
        if (t < 10) { rd(f1, PAR, 2 * t); rd(f1, PAR, 2 * t + 1); }
        else if (j < 8) xv[t - 10] = *(const __attribute__((address_space(3))) u32x2*)(px + (j >> 1) * 1024 + (t - 10) * 256);
      });
      if (j < 8) {
#pragma unroll
        for (int e = 0; e < 2; ++e) xf[j >> 2][(j & 2) + e] = f32x4{lo_f(xv[e][0]), hi_f(xv[e][0]), lo_f(xv[e][1]), hi_f(xv[e][1])};
        asm volatile("" : "+v"(xf[j >> 2][j & 2]), "+v"(xf[j >> 2][(j & 2) + 1]));
        SB();
      }
    }
    if (PAR) flip();
    WAIT_V(0);
    bar();
    u32x4 pv[4];
    {
      lds_cptr pp_ = shm3 + G2_X + 16384 + vo;
      pairs(sacc, f1, 8, 0, 12, [&](int t) {
        if (t < 2) {
#pragma unroll
          for (int f = 2 * t; f < 2 * t + 2; ++f) pv[f] = *(const __attribute__((address_space(3))) u32x4*)(pp_ + f * 1024);
        } else { rd(f0, PAR ^ 1, 2 * t - 4); rd(f0, PAR ^ 1, 2 * t - 3); }
      });
      if (__builtin_expect(blank(pv) || !cok, 0)) {
        const __amdgpu_buffer_rsrc_t rp = __builtin_amdgcn_make_buffer_rsrc((void*)((char*)slab + (size_t)(bid ^ 5) * 16384), 0, 16384, 0x00020000);
        while (!cok && !__all(fresh(__hip_atomic_load(done2 + (bid ^ 5) * 8, RLX_AGENT), t_own))) {
          notify(0, 32);
          if (lane == 0) __hip_atomic_store(done2 + bid * 8, __builtin_amdgcn_s_memrealtime(), RLX_AGENT);
          __builtin_amdgcn_s_sleep(8);
        }
        do {
          asm volatile("" ::: "memory");
#pragma unroll
          for (int f = 0; f < 4; ++f) pv[f] = __builtin_bit_cast(u32x4, __builtin_amdgcn_raw_buffer_load_b128(rp, vo + f * 1024, 0, 16));
        } while (blank(pv));
      }
    }
    auto silu = [&](int g0, int g1) {
#pragma unroll
      for (int f = g0; f < g1; ++f) {
        const int mf = f >> 2, j = f & 3;
        const u32x4 pw = pv[mf * 2 + (j >> 1)];
        const f32x4 ya = acc[mf][j] + f32x4{lo_f(pw[2 * (j & 1)]), hi_f(pw[2 * (j & 1)]), lo_f(pw[2 * (j & 1) + 1]), hi_f(pw[2 * (j & 1) + 1])};
#pragma unroll
        for (int e = 0; e < 2; ++e) {
          const f32x2 y2 = {ya[2 * e], ya[2 * e + 1]};
          const f32x2 ar = y2 * f32x2{-1.4426950408889634f, -1.4426950408889634f};
          const f32x2 dn = f32x2{__builtin_amdgcn_exp2f(ar[0]), __builtin_amdgcn_exp2f(ar[1])} + f32x2{1.0f, 1.0f};
          const f32x2 t = y2 * f32x2{__builtin_amdgcn_rcpf(dn[0]), __builtin_amdgcn_rcpf(dn[1])};
          acc[mf][j][2 * e] = t[0]; acc[mf][j][2 * e + 1] = t[1];
        }
        asm volatile("" : "+v"(acc[mf][j]));
      }
      SB();
    };
#define SPT 0, 0, 1, 2, 4, 6, 8, 8
    constexpr int spt[] = {SPT};
    silu(0, spt[1]);
#pragma unroll
    for (int j = GETJ + 6; j < 15; ++j) {
      if ((j ^ PAR) & 1) flip();
      bar();
      if (j & 1) stg(f0, f1, PAR); else stg(f1, f0, PAR ^ 1);
      silu(spt[j - GETJ - 5], j == 14 ? 8 : spt[j - GETJ - 4]);
    }
    pairs(sacc, f0, 8, 0, 0, [](int) {});
    asm volatile("s_nop 7\n\ts_nop 3" : "+v"(sacc[1][0]), "+v"(sacc[1][1]), "+v"(sacc[1][2]), "+v"(sacc[1][3]));
    SB();
    TS(1);
    {
      const __amdgpu_buffer_rsrc_t ro = __builtin_amdgcn_make_buffer_rsrc((void*)out, 0, S_ * D_ * 2, 0x00020000);
#pragma unroll
      for (int f = 0; f < 4; ++f) {
        const int mf = f >> 1, j = (f & 1) * 2;
        const f32x4 a = __builtin_elementwise_fma(sacc[mf][j], acc[mf][j], xf[mf][j]), b = __builtin_elementwise_fma(sacc[mf][j + 1], acc[mf][j + 1], xf[mf][j + 1]);
        const auto r0 = __builtin_amdgcn_permlane16_swap(pack2(a[0], a[1]), pack2(b[0], b[1]), false, false);
        const auto r1 = __builtin_amdgcn_permlane16_swap(pack2(a[2], a[3]), pack2(b[2], b[3]), false, false);
        __builtin_amdgcn_raw_buffer_store_b128(u32x4{r0[0], r1[0], r0[1], r1[1]}, ro, vx + mf * 2048 + j * 32, 0, 2);
      }
    }
    TS(3);
  }
  if (bid < 8 && wid == 0) { unsigned d_ = 0u * 10u; if (d_ > 9000u) d_ = 9000u; while (__builtin_amdgcn_s_memrealtime() - t1_ < 6000ull + d_) {} }
  }
  asm volatile(".Lcode_end:");
}

extern "C" void kernel_launch(void* const* d_in, const int* in_sizes, int n_in, void* d_out, int out_size, void* d_ws,
                              size_t ws_size, hipStream_t stream) {
  const u16* A_x = (const u16*)d_in[0];
  const u16* A_wqkv = (const u16*)d_in[1];
  const u16* A_wo = (const u16*)d_in[2];
  const u16* B_wg = (const u16*)d_in[4];
  const u16* B_wu = (const u16*)d_in[5];
  const u16* B_wd = (const u16*)d_in[6];
  u16* out = (u16*)d_out;
  fused_kernel<<<256, 512, 0, stream>>>(A_x, A_wqkv, B_wg, B_wu, B_wd, A_wo, out, (char*)d_ws);
}
```

```cpp
#define ALL_TS 1
#define TSMASK 32776
#include <hip/hip_runtime.h>
#include <hip/hip_bf16.h>
#include <stdint.h>
#include <type_traits>

typedef unsigned short u16;
using bf16x8 = __attribute__((ext_vector_type(8))) short;
using bf16x4 = __attribute__((ext_vector_type(4))) short;
using f32x4 = __attribute__((ext_vector_type(4))) float;
using u32x2 = __attribute__((ext_vector_type(2))) unsigned;
using u32x4 = __attribute__((ext_vector_type(4))) unsigned;
typedef __attribute__((address_space(3))) const char* lds_cptr;

constexpr int S_ = 2048, D_ = 1024, F_ = 3584;

__device__ __forceinline__ float bf2f(u16 h) { return __uint_as_float((unsigned)h << 16); }
__device__ __forceinline__ u16 f2bf(float f) {
  unsigned u = __float_as_uint(f);
  u += 0x7fffu + ((u >> 16) & 1u);
  return (u16)(u >> 16);
}
__device__ __forceinline__ float rbf(float f) { return bf2f(f2bf(f)); }
typedef __attribute__((ext_vector_type(2))) __bf16 bf16x2_t;
typedef __attribute__((ext_vector_type(2))) float f32x2;
__device__ __forceinline__ unsigned pack2(float a, float b) {
  bf16x2_t r = __builtin_convertvector((f32x2){a, b}, bf16x2_t);
  return __builtin_bit_cast(unsigned, r);
}
__device__ __forceinline__ float lo_f(unsigned p) { return __uint_as_float(p << 16); }
__device__ __forceinline__ float hi_f(unsigned p) { return __uint_as_float(p & 0xffff0000u); }

#define TSDECL unsigned ts_r[16] = {0, 0, 0, 0, 0, 0, 0, 0, 0, 0, 0, 0, 0, 0, 0, 0}
#define TSS(i) do { if ((TSMASK >> (i)) & 1) { unsigned long long t_; asm volatile("s_memrealtime %0\n\ts_waitcnt lgkmcnt(0)" : "=s"(t_)); ts_r[i] = (unsigned)t_; } } while (0)
#define TSFLUSH() do { if ((threadIdx.x & 63) == 0) { for (int i_ = 0; i_ < 16; ++i_) flags[(blockIdx.x * 8 + (threadIdx.x >> 6)) * 16 + i_] = ts_r[i_]; } } while (0)
#define TS1(i) TSS(i)
#define TS(i) TSS(8 + (i))
#define PIN_S(v) asm volatile("" : "+s"(v))
#define WAIT_V(n) asm volatile("s_waitcnt vmcnt(%0)" ::"n"(n) : "memory")
#define WAIT_L(n) asm volatile("s_waitcnt lgkmcnt(%0)" ::"n"(n) : "memory")
#define BARL() do { WAIT_L(0); BAR(); } while (0)
#define BAR() do { asm volatile("" ::: "memory"); __builtin_amdgcn_s_barrier(); asm volatile("" ::: "memory"); } while (0)

__device__ __forceinline__ bf16x4 tr_read(lds_cptr p) {
  return __builtin_amdgcn_ds_read_tr16_b64_v4i16((__attribute__((address_space(3))) bf16x4*)p);
}
__device__ __forceinline__ bf16x8 tr_frag(lds_cptr p) {
  bf16x4 lo = tr_read(p), hi = tr_read(p + 512);
  bf16x8 r;
  r[0] = lo[0]; r[1] = lo[1]; r[2] = lo[2]; r[3] = lo[3];
  r[4] = hi[0]; r[5] = hi[1]; r[6] = hi[2]; r[7] = hi[3];
  return r;
}
__device__ __forceinline__ void glds16(const void* gsrc, unsigned lds_dst) {
  unsigned keep;
  asm volatile("s_mov_b32 %0, m0\n\ts_mov_b32 m0, %2\n\ts_nop 0\n\tglobal_load_lds_dwordx4 %1, off\n\ts_mov_b32 m0, %0"
               : "=&s"(keep) : "v"(gsrc), "s"(lds_dst) : "memory");
}
#define GLDS(gptr, laddr) glds16((const void*)(gptr), (unsigned)(laddr))
__device__ __forceinline__ void glds16_sc1(const void* gsrc, unsigned lds_dst) {
  unsigned keep;
  asm volatile("s_mov_b32 %0, m0\n\ts_mov_b32 m0, %2\n\ts_nop 0\n\tglobal_load_lds_dwordx4 %1, off sc1\n\ts_mov_b32 m0, %0"
               : "=&s"(keep) : "v"(gsrc), "s"(lds_dst) : "memory");
}
#define GLDS_SC1(gptr, laddr) glds16_sc1((const void*)(gptr), (unsigned)(laddr))
__device__ __forceinline__ void glds16_nc(const void* gsrc, unsigned lds_dst) {
  unsigned keep;
  asm volatile("s_mov_b32 %0, m0\n\ts_mov_b32 m0, %2\n\ts_nop 0\n\tglobal_load_lds_dwordx4 %1, off\n\ts_mov_b32 m0, %0"
               : "=&s"(keep) : "v"(gsrc), "s"(lds_dst));
}
#define GLDS_NC(gptr, laddr) glds16_nc((const void*)(gptr), (unsigned)(laddr))
__device__ __forceinline__ void glds16_s(const void* sbase, unsigned voff, unsigned lds_dst) {
  asm volatile("s_mov_b32 m0, %2\n\ts_nop 0\n\tglobal_load_lds_dwordx4 %0, %1" :: "v"(voff), "s"(sbase), "s"(lds_dst) : "m0");
}

__device__ __forceinline__ void glds16_s1(const void* sbase, unsigned voff, unsigned lds_dst) {
  asm volatile("s_mov_b32 m0, %2\n\ts_nop 0\n\tglobal_load_lds_dwordx4 %0, %1 sc1" :: "v"(voff), "s"(sbase), "s"(lds_dst) : "memory", "m0");
}
__device__ __forceinline__ void glds16_v(const void* p, unsigned lds_dst) {
  asm volatile("s_mov_b32 m0, %1\n\ts_nop 0\n\tglobal_load_lds_dwordx4 %0, off" :: "v"(p), "s"(lds_dst) : "m0");
}
template <int OFF = 0>
__device__ __forceinline__ void gst16(const void* sbase, unsigned voff, u32x4 v) {
  asm volatile("global_store_dwordx4 %0, %1, %2 offset:%3 sc1\n\ts_nop 1" :: "v"(voff), "v"(v), "s"(sbase), "n"(OFF) : "memory");
}

typedef unsigned long long u64;
__device__ __forceinline__ bool fresh(u64 stamp, u64 t_own) {
  const u64 b = t_own + 100ull;
  const unsigned lo = (unsigned)stamp - (unsigned)b, hi = (unsigned)(stamp >> 32) - (unsigned)(b >> 32) - ((unsigned)stamp < (unsigned)b ? 1u : 0u);
  return (hi == 0u) & (lo < (unsigned)(__builtin_amdgcn_s_memrealtime() - t_own));
}
constexpr u64 TIMEOUT_ = 200000;
__device__ __forceinline__ float half_sum(float v) {
  v += __builtin_bit_cast(float, __builtin_amdgcn_update_dpp(0, __builtin_bit_cast(int, v), 0xB1, 0xf, 0xf, false));
  v += __builtin_bit_cast(float, __builtin_amdgcn_update_dpp(0, __builtin_bit_cast(int, v), 0x4E, 0xf, 0xf, false));
  v += __builtin_bit_cast(float, __builtin_amdgcn_update_dpp(0, __builtin_bit_cast(int, v), 0x141, 0xf, 0xf, false));
  v += __builtin_bit_cast(float, __builtin_amdgcn_update_dpp(0, __builtin_bit_cast(int, v), 0x140, 0xf, 0xf, false));
  { auto r = __builtin_amdgcn_permlane16_swap(__float_as_uint(v), __float_as_uint(v), false, false); v = __uint_as_float(r[0]) + __uint_as_float(r[1]); }
  return v;
}
__device__ __forceinline__ unsigned pack_stats(float mean, float var) {
  return (unsigned)f2bf(mean) | ((unsigned)f2bf(sqrtf(rbf(rbf(var) + 1e-5f))) << 16);
}
__device__ __forceinline__ unsigned row_stats_serial(const u16* xr) {
  float s = 0.f, q = 0.f;
#pragma unroll 1
  for (int k = 0; k < D_; ++k) s += bf2f(xr[k]);
  const float mean = s * (1.0f / D_);
#pragma unroll 1
  for (int k = 0; k < D_; ++k) { const float d = bf2f(xr[k]) - mean; q += d * d; }
  return pack_stats(mean, q * (1.0f / D_));
}
constexpr int G1_STAGE = 32768 + 5 * 8192;
#define EPOS 2
#define PA 4
#define PB 8
#define PC 12
#define GETP 4
#define DLT 2
#define PSLEEP 2
#define GETJ 3

constexpr unsigned BLANK = 0x7f817f81u;
__device__ __forceinline__ bool blank(const u32x4* pv) {
  u64 m;
#define CX(i) "v_cmpx_ne_u32 vcc, %1, %" #i "\n\t"
  asm volatile(CX(2) CX(3) CX(4) CX(5) CX(6) CX(7) CX(8) CX(9) CX(10) CX(11) CX(12) CX(13) CX(14) CX(15) CX(16) CX(17)
               "s_mov_b64 %0, exec\n\ts_mov_b64 exec, -1\n\ts_nop 3" : "=s"(m) : "s"(BLANK), "v"(pv[0][0]), "v"(pv[0][1]), "v"(pv[0][2]), "v"(pv[0][3]),
               "v"(pv[1][0]), "v"(pv[1][1]), "v"(pv[1][2]), "v"(pv[1][3]), "v"(pv[2][0]), "v"(pv[2][1]), "v"(pv[2][2]), "v"(pv[2][3]),
               "v"(pv[3][0]), "v"(pv[3][1]), "v"(pv[3][2]), "v"(pv[3][3]) : "vcc");
  return m != ~0ull;
}
#define RLX_AGENT __ATOMIC_RELAXED, __HIP_MEMORY_SCOPE_AGENT

constexpr int G2_A = 65536, G2_X = 131072;
constexpr int G2_NSTAGE = 4;
constexpr size_t MB_ = 1024 * 1024;
__global__ __launch_bounds__(512, 2) __attribute__((aligned(4096))) void fused_kernel(const u16* __restrict__ x, const u16* __restrict__ wqkv,
                                                       const u16* __restrict__ wg, const u16* __restrict__ wu,
                                                       const u16* __restrict__ wd, const u16* __restrict__ wo,
                                                       u16* __restrict__ out, char* __restrict__ ws) {
  u16* const halo = (u16*)(ws + 4 * MB_);
  u16* const abuf = (u16*)(ws + 16 * MB_);
  char* const slab = ws + 64 * MB_;
  unsigned* const flags = (unsigned*)(ws + 96 * MB_);
  u32x4* const rstat = (u32x4*)(ws + 97 * MB_);
  char* const wcopy = ws + 98 * MB_;
  u64* const done1 = (u64*)(ws + 102 * MB_);
  u64* const done2 = (u64*)(ws + 100 * MB_ + 65536);
  u64* const donec = (u64*)(ws + 100 * MB_ + 131072);
  u64* const acks = (u64*)(ws + 100 * MB_ + 196608);
  float* const csum = (float*)(ws + 101 * MB_);
  __shared__ __attribute__((aligned(1024))) char shm[163840];
  constexpr int AUX = 2 * G1_STAGE + 8192;
  {
    u64 pc; int len;
    asm volatile("s_getpc_b64 %0\n.Lcode_beg:\n\ts_mov_b32 %1, .Lcode_end-.Lcode_beg" : "=s"(pc), "=s"(len));
    const int ol = ((blockIdx.x >> 3) + 32 * (threadIdx.x >> 6)) * 1024 + (threadIdx.x & 63) * 16;
    asm volatile("s_mov_b32 m0, %2\n\ts_nop 0\n\tglobal_load_lds_dwordx4 %0, %1"
                 :: "v"(ol < len - 16 ? ol : 0), "s"(pc & ~15ull), "s"((unsigned)(uintptr_t)(lds_cptr)shm + 2 * G1_STAGE + 7168) : "m0");
    __builtin_amdgcn_sched_barrier(0);
  }
  const u64 t_own = __builtin_amdgcn_s_memrealtime();
  TSDECL;
  TS1(7);
  const int tid = threadIdx.x, lane = tid & 63;
  const int wid = __builtin_amdgcn_readfirstlane(tid >> 6);
  const int wm = wid & 3, wc = wid >> 2;
  int bid_ = blockIdx.x;
  PIN_S(bid_);
  const int bid = bid_ & 255, xcd = bid & 7, idx = bid >> 3;
  const int ntile = xcd * 4 + (idx & 3), mtile = idx >> 2;
  const int m0 = mtile * 256;
  const int hg = ntile >> 1;
  const bool tya = (ntile & 1) == 0;
  const int srow = m0 + ntile * 8 + wm * 2;
  lds_cptr shm3 = (lds_cptr)shm;
  const unsigned shm_base = (unsigned)(uintptr_t)shm3;
  const int r8 = tid >> 3, pos = tid & 7;
  const int phys = pos >> 1, half = pos & 1;
  const int fk = ((r8 >> 1) & 1) | (((r8 >> 3) & 1) << 1);
  const int vsrc = phys ^ fk;
  const int qstr = tya ? 3072 * 2 : F_ * 2;
  unsigned voa = (((r8 & 32) + 2 * (r8 & 15) + ((r8 >> 4) & 1)) * D_ + ((pos ^ (r8 & 7)) * 8)) * 2;
  unsigned vog = r8 * (F_ * 2) + vsrc * 32 + half * 16, voq = r8 * qstr + vsrc * 32 + half * 16;
  unsigned vom = r8 * qstr + (vsrc >> (tya ? 0 : 1)) * 32 + half * 16;
  asm volatile("" : "+v"(voa), "+v"(vog), "+v"(voq), "+v"(vom));
  const int kt0 = (hg >> 1) * 7, he = hg & 1;
  unsigned oq = (tya ? hg : kt0 + 2 * he) * 128, og = (kt0 + (tya ? 5 + he : 2 * he + 1)) * 128, om = tya ? 4096 : 512 - 192 * he;
  unsigned d0 = shm_base + wid * 1024 + 32768, ox = m0 * D_ * 2;
  long uq = -32 * qstr;
  PIN_S(oq); PIN_S(og); PIN_S(om); PIN_S(d0); PIN_S(ox); PIN_S(uq);
  __builtin_amdgcn_sched_barrier(0);
  asm volatile("" :: "s"(x), "s"(wqkv), "s"(wg), "s"(wu), "s"(wd), "s"(wo), "s"(out), "s"(ws));
  const char* const ab = (const char*)x + ox;
  int kz = 0;
  asm volatile("" : "+s"(kz));
  u64 pq = (u64)wqkv, pg = (u64)wg;
  PIN_S(pq); PIN_S(pg);
  const char* const qb = (const char*)(tya ? pq : pg) + oq;
  if (wid < 4) {
#pragma unroll
    for (int i = 0; i < 8; ++i) glds16_s(ab + i * 32 * D_ * 2, voa, d0 - 32768 + i * 4096);
  } else {
    glds16_s(qb, voq, d0); glds16_s(qb + uq, voq, d0 - 4096);
  }
  __builtin_amdgcn_sched_barrier(0);
  const char* const gb = (const char*)wg + og;
  const long dgu = (const char*)wu - (const char*)wg, dp1 = tya ? 2048 : dgu;
  const char* const mb0 = qb + om;
  const char* mptr = mb0 + (((vsrc & 1) && !tya) ? dgu : 0) + vom;
  if (wid >= 4) {
    const long ug = kz - 32 * F_ * 2;
    glds16_s(qb + dp1, voq, d0 + 8192); glds16_s(qb + dp1 + uq, voq, d0 + 4096);
    glds16_v(mptr, d0 + 2 * 8192); glds16_v(mptr + uq, d0 + 2 * 8192 - 4096);
    glds16_s(gb, vog, d0 + 3 * 8192); glds16_s(gb + ug, vog, d0 + 3 * 8192 - 4096);
    glds16_s(gb + dgu, vog, d0 + 4 * 8192); glds16_s(gb + dgu + ug, vog, d0 + 4 * 8192 - 4096);
  }
  __builtin_amdgcn_sched_barrier(0);
  f32x4 acc[4][10];

  constexpr int NT = D_ / 64;
  constexpr int CNT = AUX + 4096 + 3584;
  if (tid < 8) *(unsigned*)(shm + CNT + tid * 4) = 0u;
  bf16x8 af0[4], af1[4];
  bf16x4 bl[4], bh[4];
#define SB0() __builtin_amdgcn_sched_barrier(0)
  auto mm = [&](int g, int t, bool two, bool z = false) {
    const int mi = t >> 1, mf = ((g & 1) && g != 9) ? 3 - mi : mi, ks = (t & 1) ^ (mi & 1), s = ((g + 1) & 1) * 2;
    const bf16x8 b0 = __builtin_shufflevector(bl[s + ks], bh[s + ks], 0, 1, 2, 3, 4, 5, 6, 7), a0 = ks ? af1[mf] : af0[mf];
    const bf16x8 b1 = __builtin_shufflevector(bl[s + 1 - ks], bh[s + 1 - ks], 0, 1, 2, 3, 4, 5, 6, 7), a1 = ks ? af0[mf] : af1[mf];
    const bool tr = (g >> 1) == 2;
    if (z && two) asm volatile("v_mfma_f32_16x16x32_bf16 %0, %1, %2, 0\n\tv_mfma_f32_16x16x32_bf16 %0, %3, %4, %0" : "=&v"(acc[mf][g])
                               : "v"(tr ? a0 : b0), "v"(tr ? b0 : a0), "v"(tr ? a1 : b1), "v"(tr ? b1 : a1));
    else if (z && !(t & 1)) asm volatile("v_mfma_f32_16x16x32_bf16 %0, %1, %2, 0" : "=&v"(acc[mf][g]) : "v"(tr ? a0 : b0), "v"(tr ? b0 : a0));
    else if (two) asm volatile("v_mfma_f32_16x16x32_bf16 %0, %1, %2, %0\n\tv_mfma_f32_16x16x32_bf16 %0, %3, %4, %0" : "+v"(acc[mf][g])
                          : "v"(tr ? a0 : b0), "v"(tr ? b0 : a0), "v"(tr ? a1 : b1), "v"(tr ? b1 : a1));
    else asm volatile("v_mfma_f32_16x16x32_bf16 %0, %1, %2, %0" : "+v"(acc[mf][g]) : "v"(tr ? a0 : b0), "v"(tr ? b0 : a0));
  };
  typedef __attribute__((address_space(3))) unsigned lds_u32;
  lds_u32* const cnt = (lds_u32*)(shm3 + CNT);
  bool cok;
  constexpr int NY = 28;
  int role, mt2, nt2;
  struct WS { unsigned vb[2][2]; const char* wb[2]; int sh[2]; };
  auto wsetup = [&](const u16* w, int crow0, int l, bool ck) __attribute__((always_inline)) {
    WS r;
    const unsigned vr = ((l >> 5) & 1) * 16 + (wm & 1) * 8 + ((l >> 3) & 3), vc = (((l >> 1) & 3) ^ ((l >> 4) & 1)) * 32 + (l & 1) * 16;
    u64 cp = (u64)(wcopy + (nt2 >> 2) * 589824 + crow0 * 128);
    asm volatile("" : "+s"(cp));
#pragma unroll
    for (int i = 0; i < 2; ++i) {
      u64 dp = (u64)(w + nt2 * 128 + i * 64);
      asm volatile("" : "+s"(dp));
      const bool slow = ck & ((((unsigned)dp >> 7) & 7) == 3);
      r.sh[i] = slow ? 7 : 11;
      r.wb[i] = (const char*)(slow ? cp : dp) + (crow0 ? (wm >> 1) * 32 << r.sh[i] : 0);
      r.vb[i][0] = (vr << r.sh[i]) + vc; r.vb[i][1] = ((vr + 4) << r.sh[i]) + (vc ^ 64);
    }
    asm volatile("" : "+v"(r.vb[0][0]), "+v"(r.vb[0][1]), "+v"(r.vb[1][0]), "+v"(r.vb[1][1]));
    PIN_S(r.wb[0]); PIN_S(r.wb[1]); PIN_S(r.sh[0]); PIN_S(r.sh[1]);
    return r;
  };
  WS wy, wz;
  int vxq, ws0, ws1;
  const char* sz;
  u32x4 xq[4];
  unsigned vg;
  int st = 3;
  const char *sa, *sw[2];
  auto wsel = [&]() {
    const unsigned r = __builtin_amdgcn_readlane(vg, st);
    sw[0] = wy.wb[0] + (r << wy.sh[0]); sw[1] = wy.wb[1] + (r << wy.sh[1]);
  };
  const int p = wid & 3;
  unsigned sl = 0;
#define G2_W "s_add_u32 m0, %[l], %[ow]\n\ts_nop 0\n\tglobal_load_lds_dwordx4 %[v0], %[w0] sc1\n\t" \
             "s_add_u32 m0, m0, 1024\n\ts_nop 0\n\tglobal_load_lds_dwordx4 %[v1], %[w0] sc1\n\t" \
             "s_add_u32 m0, m0, 7168\n\ts_nop 0\n\tglobal_load_lds_dwordx4 %[v2], %[w1] sc1\n\t" \
             "s_add_u32 m0, m0, 1024\n\ts_nop 0\n\tglobal_load_lds_dwordx4 %[v3], %[w1] sc1"
#define G2_WOP(w) [ow] "s"(shm_base + p * 2048), [w0] "s"(sw[0]), [w1] "s"(sw[1]), [v0] "v"(w.vb[0][0]), [v1] "v"(w.vb[0][1]), [v2] "v"(w.vb[1][0]), [v3] "v"(w.vb[1][1])
  auto tile2 = [&](int t0) __attribute__((always_inline)) {
    int b = bid;
    asm volatile("" : "+s"(b));
    role = 1 - ((b >> 2) & 1);
    mt2 = ((role ? b : b ^ 5) & 7) * 4 + ((b >> 3) & 3), nt2 = b >> 5;
    sa = (const char*)abuf + (size_t)(mt2 * 72 + role * NY + t0) * 16384 + wm * 4096;
    asm volatile("" : "+s"(sa));
  };
  auto prep = [&](bool first) __attribute__((always_inline)) {
    int l = threadIdx.x;
    asm volatile("" : "+v"(l));
    l &= 63;
    if (first) {
      tile2(3);
      vxq = ((mt2 * 128 + (wm >> 1) * 64 + role * 32 + 2 * (l & 15)) * D_ + nt2 * 128 + (wm & 1) * 64) * 2 + ((l >> 4) & 1) * 32 + (l >> 5) * 16;
      const int t = min(l < NY ? l : l - DLT, NY - 1), w = wm >> 1;
      const int o = (0xffaa5500fae504ull >> (2 * t)) & 3;
      int u1 = (0x990990 >> ((2 * t) & 31)) & 3, u2 = 3 + (t & 3);
      asm volatile("" : "+v"(u1), "+v"(u2));
      const int u = t < 12 ? u1 : u2;
      vg = (__umul24((l < NY ? role : 1 - role) * 4 + (((o & 1) << 1) | (o >> 1)), 14) + ((0xb85140a >> (4 * u)) & 15) + (u == 5 ? w : 2 * w)) * 32;
      asm volatile("" : "+v"(vg), "+v"(vxq));
      sz = (const char*)abuf + (size_t)(mt2 * 72 + 56) * 16384 + (wm >> 1) * 8192 + role * 4096 + (wm & 1) * 2048;
      PIN_S(sz);
    }
    wy = wsetup(wd, 0, l, first);
    wz = wsetup(wo, F_, l, first);
    ws0 = 64 << wz.sh[0]; PIN_S(ws0);
    ws1 = 64 << wz.sh[1]; PIN_S(ws1);
    SB0();
  };
  auto start2 = [&]() __attribute__((always_inline)) {
    const __amdgpu_buffer_rsrc_t rx = __builtin_amdgcn_make_buffer_rsrc((void*)x, 0, S_ * D_ * 2, 0x00020000);
#pragma unroll
    for (int f = 0; f < 4; ++f) xq[f] = __builtin_bit_cast(u32x4, __builtin_amdgcn_raw_buffer_load_b128(rx, vxq + (f >> 1) * 2048 + (f & 1) * 64, 0, 0));
    cok = __builtin_amdgcn_readfirstlane(cnt[1] & cnt[2]) != 0u;
    if (__builtin_expect(!cok, 0)) prep(false);
    wsel();
    PIN_S(sw[0]); PIN_S(sw[1]);
    SB0();
  };
  auto kloop = [&](auto wmc) {
    constexpr bool LD = decltype(wmc)::value != 0;
    constexpr int DMP = 2 * G1_STAGE;
    const char* pa = nullptr;
    unsigned vob = 0;
    f32x4 ca = {0.f, 0.f, 0.f, 0.f}, cb = {0.f, 0.f, 0.f, 0.f}, cd = ca;
    bf16x8 sel = {0, 0, 0, 0, 0, 0, 0, 0};
    int tro = 0;
    const int psh = (wid == 2 && !tya) ? 1 : 0, pstr = wid < 3 ? qstr : F_ * 2;
    const char* const pbase = wid == 0 ? qb : (wid == 1 ? qb + dp1 : (wid == 2 ? mb0 : gb));
    int lane = threadIdx.x;
    asm volatile("; %1" : "+v"(lane) : "n"(LD ? 1 : 0));
    lane &= 63;
    const int fr = lane & 15, fq = lane >> 4, q4 = fr >> 2, pp = lane & 3;
    const int ff = ((q4 >> 1) & 1) | ((fq & 1) << 1);
    int aoff[2], boff[2];
#pragma unroll
    for (int ks = 0; ks < 2; ++ks) aoff[ks] = (wm * 64 + fr) * 128 + (((ks * 4 + fq) ^ (fr & 7)) * 16);
#pragma unroll
    for (int j = 0; j < 2; ++j) boff[j] = 32768 + (8 * fq + q4) * 128 + (((2 * wc + j) ^ ff) * 32) + pp * 8;
    unsigned vol = lane * 16;
    float sm = 0.f, sq = 0.f;
    const char* pf = (const char*)wd + (bid * 4 + wid) * 7168;
    long pstep = 64 * pstr;
    if (!LD) {
      const int rs = 256 + mtile * 8 + (lane >> 3), vs = ((lane & 7) >> 1) ^ (((rs >> 1) & 1) | ((mtile & 1) << 1));
      pa = pbase + ((psh & vs) ? dgu : 0) + (rs * pstr + (vs >> psh) * 32 + (lane & 1) * 16);
      vob = rs * (F_ * 2) + vs * 32 + (lane & 1) * 16;
      tro = DMP + wid * 1024 + q4 * 128 + ((fq ^ ((q4 >> 1) | ((mtile & 1) << 1))) * 32) + pp * 8;
      const short one = fr == fq ? 0x3f80 : 0;
      sel = bf16x8{one, one, one, one, one, one, one, one};
      asm volatile("" : "+v"(tro), "+v"(vol), "+v"(sel), "+v"(ca), "+v"(cb), "+v"(sm), "+v"(sq));
      PIN_S(pf); PIN_S(pstep);
    }
    auto accum = [&](lds_cptr p, int ob) {
      ca = __builtin_amdgcn_mfma_f32_16x16x32_bf16(tr_frag(p), sel, ca, 0, 0, 0);
      if (wid == 0) cb = __builtin_amdgcn_mfma_f32_16x16x32_bf16(tr_frag(p + ob), sel, cb, 0, 0, 0);
    };
    auto xrow = [&](int i) {
      u64 a = (u64)(x + (size_t)srow * D_ + i * 256), b = (u64)(wo + (bid * 4 + wid) * 1024);
      PIN_S(a); PIN_S(b);
      asm volatile("global_load_dwordx4 %0, %1, %2" : "+v"(cd) : "v"(vol + (vol & (i < 4 ? 512 : 0)) * 3), "s"(i < 4 ? a : b) : "memory");
    };
    const int q32 = 32 * qstr;
    const char *sq0 = qb + q32, *sq1 = sq0 + dp1, *sg0 = gb + 32 * F_ * 2, *sg1 = sg0 + dgu, *sx = ab + (long)(kz + 128 - 32 * D_ * 2);
    u32x2 mp[2] = {__builtin_bit_cast(u32x2, mptr + q32), __builtin_bit_cast(u32x2, mptr + 2 * q32)};
    u64 cy[2];
    unsigned dst = shm_base + G1_STAGE + wm * 1024;
    int dbuf = G1_STAGE;
    auto stage = [&](int i0, int i1) __attribute__((always_inline)) {
#pragma unroll
        for (int i = i0; i < i1; ++i) {
          const int q = i >> 1, h = i & 1;
          const unsigned ld = dst + h * 4096 + (q < 5 ? 32768 + q * 8192 : (q - 5) * 8192);
          if (q < 2) glds16_s((q ? sq1 : sq0) + h * q32, voq, ld);
          else if (q == 2) glds16_v(__builtin_bit_cast(const char*, mp[h]), ld);
          else if (q < 5) glds16_s((q == 4 ? sg1 : sg0) + h * 32 * F_ * 2, vog, ld);
          else glds16_s(sx + ((q - 5) * 64 + h * 32) * D_ * 2, voa, ld);
        }
    };
    u64 cy0 = 0, rt0 = 0;
#pragma unroll
    for (int a = 0; a < 4; ++a) { acc[a][9] = f32x4{0.f, 0.f, 0.f, 0.f}; asm volatile("" : "+v"(acc[a][9])); }
    if (!LD) {
      xrow(0);
      WAIT_V(1);
    }
    asm volatile("" : "+v"(aoff[0]), "+v"(aoff[1]), "+v"(boff[0]), "+v"(boff[1]));
    bf16x8 kp[2];
    auto body = [&](bool upd, bool keep = false, bool first = false) __attribute__((always_inline)) {
      lds_cptr sb = shm3;
      auto rdb = [&](int g, int i) {
        lds_cptr p = sb + boff[g & 1] + (g >> 1) * 8192 + (i >> 1) * 4096;
        const int s = ((g + 1) & 1) * 2 + (i >> 1);
        if (i & 1) bh[s] = tr_read(p + 512); else bl[s] = tr_read(p);
      };
      auto rda = [&](int ks, int mf) { (ks ? af1 : af0)[mf] = *(const __attribute__((address_space(3))) bf16x8*)(sb + aoff[ks] + mf * 2048); };
      SB0();
      __builtin_amdgcn_s_waitcnt(0xC07F);
#pragma unroll
      for (int gg = 0; gg < 10; ++gg) {
        const int g = (gg + 9) % 10;
#pragma unroll
        for (int t = 0; t < 8; ++t) {
          const bool two = t >= 4 && !(upd && g >= 5 && g <= 8);
          SB0();
          if ((!two || (t & 1)) && !(first && g == 9)) mm(g, two ? t - 1 : t, two, first);
          SB0();
          if (first && !LD && (g == 3 || g == 7) && t == 0) BAR();
          if (g == 9) {
            if (t == 0 || t == 2) { rdb(0, t); rdb(0, t + 1); }
            if (t & 1) { rda((t >> 1) & 1, t >> 1); rda(((t >> 1) & 1) ^ 1, t >> 1); }
          } else if (t < 4) rdb(g + 1, t);
          else if (keep && g == 7) {
            if (t == 5) { WAIT_V(0); kp[0] = tr_frag(shm3 + tro); }
            if (t == 7) kp[1] = tr_frag(shm3 + tro + 4096);
          } else if (upd) {
            if (g == 5 && t == 4) { sq0 += 2 * q32; PIN_S(sq0); }
            if (g == 5 && t == 5) { sq1 += 2 * q32; PIN_S(sq1); }
            if (g == 5 && t == 6) { sg0 += 64 * F_ * 2; PIN_S(sg0); }
            if (g == 6 && t == 4) { sg1 += 64 * F_ * 2; PIN_S(sg1); }
            if (g == 6 && t == 5) { sx += 128; PIN_S(sx); }
            if (g == 6 && t == 6) { dst -= dbuf; PIN_S(dst); }
            if (g == 7 && t == 4) asm volatile("v_add_co_u32 %0, %2, %4, %0\n\tv_add_co_u32 %1, %3, %4, %1" : "+v"(mp[0][0]), "+v"(mp[1][0]), "=&s"(cy[0]), "=&s"(cy[1]) : "s"(2 * q32));
            if (g == 7 && t == 6) asm volatile("v_addc_co_u32 %0, vcc, 0, %0, %2\n\tv_addc_co_u32 %1, vcc, 0, %1, %3" : "+v"(mp[0][1]), "+v"(mp[1][1]) : "s"(cy[0]), "s"(cy[1]) : "vcc");
            if (g == 8 && t == 4) { aoff[0] += dbuf; aoff[1] += dbuf; asm volatile("" : "+v"(aoff[0]), "+v"(aoff[1])); }
            if (g == 8 && t == 5) { boff[0] += dbuf; boff[1] += dbuf; asm volatile("" : "+v"(boff[0]), "+v"(boff[1])); }
            if (g == 8 && t == 6) { dbuf = -dbuf; PIN_S(dbuf); }
          }
        }
      }
      SB0();
    };
    TS1(0);
#define ARRP(o, c) do {} while (0)
    if (LD) {
      stage(0, PA);
      WAIT_V(6 + PA);
      TS1(5);
      BARL();
      stage(PA, PB);
      WAIT_V(2 + PB);
      BAR();
      stage(PB, PC);
      WAIT_V(PC);
      BAR();
      stage(PC, 18);
      body(true, false, true);
      WAIT_V(0);
      BARL();
#pragma clang loop unroll(disable)
      for (int kt = 1; kt < NT - 1; ++kt) {
        if (__builtin_expect(kt == 5 + wid, 0)) prep(true);
        stage(0, 18);
        body(true);
        WAIT_V(0);
        BARL();
        SB0();
      }
      start2();
      body(false);
    } else {
      char* wsx;
      auto tile = [&](int kt, bool keep) __attribute__((always_inline)) {
        SB0();
        ARRP(wid * 64 + kt * 4, true);
        BARL();
        body(false, keep);
        ARRP(320 + kt * 4 + 4, wid == 0);
        if (keep) asm volatile("" : "+v"(kp[0]), "+v"(kp[1])); else WAIT_V(0);
      };
      auto next = [&]() __attribute__((always_inline)) {
        aoff[0] += dbuf; aoff[1] += dbuf; boff[0] += dbuf; boff[1] += dbuf;
        dbuf = -dbuf;
        asm volatile("" : "+v"(aoff[0]), "+v"(aoff[1]), "+v"(boff[0]), "+v"(boff[1]), "+v"(vol), "+v"(tro) :: "memory");
        wsx = ws;
        PIN_S(wsx);
      };
      auto share = [&]() {
        glds16_v(pa, shm_base + DMP + wid * 1024);
        if (wid == 0) glds16_s(gb + dgu, vob, shm_base + DMP + 4096);
      };
      auto kept = [&]() {
        ca = __builtin_amdgcn_mfma_f32_16x16x32_bf16(kp[0], sel, ca, 0, 0, 0);
        if (wid == 0) cb = __builtin_amdgcn_mfma_f32_16x16x32_bf16(kp[1], sel, cb, 0, 0, 0);
      };
      auto cpub = [&](u32x4 a, u32x4 b) {
        if ((vol & 0xc0) == 0) {
          const char* const cs = wsx + 101 * MB_ + ntile * 10240 + mtile * 256;
          const unsigned co = (vol & 0x30) * 4 + (vol >> 8) * 16;
          gst16(cs + wid * 2048, co, a);
          if (wid == 0) gst16(cs + 8192, co, b);
        }
      };
      auto side0 = [&](int kt) __attribute__((always_inline)) {
        if (kt < 2) {
          glds16_v(pa - 2 * pstep, shm_base + DMP + 7168);
          if (wid == 0) glds16_s(gb + dgu, vob - 128 * F_ * 2, shm_base + DMP + 7168);
        }
        share();
        u32x4 xw;
        asm volatile("v_mov_b32 %0, %4\n\tv_mov_b32 %1, %5\n\tv_mov_b32 %2, %6\n\tv_mov_b32 %3, %7" : "=&v"(xw[0]), "=&v"(xw[1]), "=&v"(xw[2]), "=&v"(xw[3])
                     : "v"(cd[0]), "v"(cd[1]), "v"(cd[2]), "v"(cd[3]));
        xrow(kt + 1);
        next();
        if (kt) kept();
        pa += pstep; vob += 64 * F_ * 2;
        accum(shm3 + tro + ((kt & 1) * G1_STAGE + 32768 + mtile * 1024 + wid * 7168 - DMP), 4 * 8192);
#pragma unroll
        for (int i = 0; i < 4; ++i) {
          float lo, hi;
          asm volatile("v_lshlrev_b32 %2, 16, %4\n\tv_and_b32 %3, 0xffff0000, %4\n\tv_add_f32 %0, %0, %2\n\tv_fmac_f32 %1, %2, %2\n\t"
                       "v_add_f32 %0, %0, %3\n\tv_fmac_f32 %1, %3, %3" : "+v"(sm), "+v"(sq), "=&v"(lo), "=&v"(hi) : "v"(xw[i]));
        }
      };
      BARL();
      body(false, false, true);
      WAIT_V(0);
      side0(0);
#pragma clang loop unroll(disable)
      for (int kt = 1; kt < 4; ++kt) {
        tile(kt, true);
        side0(kt);
      }
#pragma clang loop unroll(disable)
      for (int kt = 4; kt < 12; ++kt) {
        tile(kt, true);
        share();
        glds16_s(pf, vol, shm_base + DMP + 7168);
        next();
        kept();
        pa += pstep; vob += 64 * F_ * 2;
        pf = kt == 10 ? (const char*)wo + (bid * 4 + wid) * 2048 + 1024 : pf + 1024;
        if (kt < 8) {
          const unsigned q1 = bid * 4 + wid + (kt - 5) * 1024, q0 = q1 - 1024;
          if (q0 < 1152) gst16(wsx + 98 * MB_ + (q0 & 1) * 589824 + (q0 >> 1) * 1024, vol, *(const u32x4*)(shm + AUX + wid * 1024 + vol));
          if (q1 < 1152) {
            const char* src = (const char*)((q1 >> 1) < 448 ? wd + (size_t)(q1 >> 1) * 8 * D_ : wo + (size_t)((q1 >> 1) - 448) * 8 * D_);
            src += ((3 - ((unsigned)(uintptr_t)src >> 7)) & 7) * 128 + (q1 & 1) * 1024;
            glds16_s(src, (vol >> 7) * 2048 + (vol & 0x70), shm_base + AUX + wid * 1024);
          }
          u32x4 bk;
          asm volatile("v_mov_b32 %0, %1" : "=v"(bk[0]) : "s"(BLANK));
          bk[1] = bk[0]; bk[2] = bk[0]; bk[3] = bk[0];
          gst16(wsx + 64 * MB_ + bid * 16384 + wid * 4096 + (kt - 4) * 1024, vol, bk);
          if (tya && (kt == 4 || kt == 7)) gst16(wsx + 4 * MB_ + (mtile * 32 + ntile) * 4096 + wid * 2048 + (kt & 1) * 1024, vol, bk);
          if (kt == 4) cpub(bk, bk);
        } else {
          if (kt == 8) {
            const float ml = sm * (1.0f / 32), mean = half_sum(sm) * (1.0f / D_), dm = ml - mean;
            const float q = __builtin_fmaf(dm * 32.0f, dm, __builtin_fmaf(-ml, sm, sq));
            const unsigned ps = pack_stats(mean, fmaxf(half_sum(q) * (1.0f / D_), 0.f));
            const u64 now = __builtin_amdgcn_s_memrealtime();
            if ((vol & 0x1f0) == 0) gst16(wsx + 97 * MB_ + srow * 16, vol >> 5, u32x4{ps, (unsigned)now, ps, (unsigned)(now >> 32)});
          }
          if (kt == 9) {
            if (wid == 0) {
              u64 now;
              asm volatile("s_memrealtime %0\n\ts_waitcnt lgkmcnt(0)" : "=s"(now) :: "memory");
              if (vol == 0) asm volatile("global_store_dwordx2 %0, %1, %2 sc1" :: "v"(vol), "v"(now), "s"(wsx + 100 * MB_ + 131072 + bid * 8) : "memory");
            }
            if (wid == 2) glds16_s1(wsx + 102 * MB_ + (bid & ~1) * 512, vol, shm_base + DMP + 7168);
          }
        }
      }
      asm volatile("" :: "v"(cd));
      if (wid >= 2) glds16_s1(wsx + 100 * MB_ + 131072 + (wid - 2) * 1024, vol, shm_base + DMP + 3072 + wid * 1024);
      tile(12, true);
      next();
      kept();
      const int lane = vol >> 4;
      glds16_s1(wsx + 97 * MB_ + (m0 + wid * 64) * 16, ((lane & 32) + 2 * (lane & 15) + ((lane >> 4) & 1)) * 16, shm_base + AUX + wid * 1024);
      asm volatile("s_nop 7\n\ts_nop 3" : "+v"(ca), "+v"(cb));
      cpub(__builtin_bit_cast(u32x4, ca), __builtin_bit_cast(u32x4, cb));
      if (wid >= 2) {
        u32x4 c4 = *(const u32x4*)(shm + DMP + 3072 + wid * 1024 + vol);
        bool ok = __all(fresh(((u64)c4[1] << 32) | c4[0], t_own) & fresh(((u64)c4[3] << 32) | c4[2], t_own));
        while (__builtin_expect(!ok, 0) && __builtin_amdgcn_s_memrealtime() - t_own < TIMEOUT_ / 10) {
          const __amdgpu_buffer_rsrc_t rs = __builtin_amdgcn_make_buffer_rsrc((void*)(wsx + 100 * MB_ + 131072), 0, 2048, 0x00020000);
          asm volatile("" ::: "memory");
          c4 = __builtin_bit_cast(u32x4, __builtin_amdgcn_raw_buffer_load_b128(rs, (wid - 2) * 1024 + vol, 0, 16));
          ok = __all(fresh(((u64)c4[1] << 32) | c4[0], t_own) & fresh(((u64)c4[3] << 32) | c4[2], t_own));
        }
        *(unsigned*)(shm + CNT - 4 + wid * 4) = ok ? 1u : 0u;
      }
      tile(13, false);
      next();
      u32x4 cw[4];
      auto cgather = [&]() {
        const __amdgpu_buffer_rsrc_t rc = __builtin_amdgcn_make_buffer_rsrc((void*)(wsx + 101 * MB_ + ntile * 10240), 0, 5 * 2048, 0x00020000);
        const unsigned vcs = (vol < 512 ? wid * 2048 : 8192) + (vol & 256) * 4 + (vol & 0xf0);
#pragma unroll
        for (int i = 0; i < 4; ++i) cw[i] = __builtin_bit_cast(u32x4, __builtin_amdgcn_raw_buffer_load_b128(rc, vcs + i * 256, 0, 16));
      };
      {
        const int ln = wid * 64 + (vol >> 4);
        u32x4 gr = *(const u32x4*)(shm + AUX + ln * 16);
        if (__builtin_expect(!__all((gr[0] == gr[2]) & fresh(((u64)gr[3] << 32) | gr[1], t_own)), 0)) {
          const __amdgpu_buffer_rsrc_t rs = __builtin_amdgcn_make_buffer_rsrc((void*)(wsx + 97 * MB_ + m0 * 16), 0, 4096, 0x00020000);
          const int prow = (ln & 0xe0) + 2 * (ln & 15) + ((ln >> 4) & 1);
          for (;;) {
            gr = __builtin_bit_cast(u32x4, __builtin_amdgcn_raw_buffer_load_b128(rs, prow * 16, 0, 16));
            if (__all((gr[0] == gr[2]) & fresh(((u64)gr[3] << 32) | gr[1], t_own))) break;
            if (__builtin_amdgcn_s_memrealtime() - t_own > TIMEOUT_) { gr[0] = row_stats_serial(x + (size_t)(m0 + prow) * D_); break; }
            __builtin_amdgcn_s_sleep(2);
          }
        }
        cgather();
        const float nm = -lo_f(gr[0]), dn = hi_f(gr[0]), r = __builtin_amdgcn_rcpf(dn);
        auto mul = [](float a, float b) { float c; asm("v_mul_f32 %0, %1, %2" : "=v"(c) : "v"(a), "v"(b)); return c; };
        const f32x4 fc = {nm, -2.3022081986f * r, mul(mul(mul(-0.10294324f, r), r), r), mul(dn, dn)};
        *(f32x4*)(shm + AUX + ln * 16) = fc;
        float* const tw = (float*)(shm + 2 * G1_STAGE + 4096 + wid * 768) + ln;
#pragma unroll
        for (int i = 0; i < 4; ++i) tw[i * 64] = fc[i];
        ((float*)(shm + AUX + 4096))[384 + ln] = r;
        cok = __builtin_amdgcn_readfirstlane(cnt[1] & cnt[2]) != 0u;
        tile2(0);
        wy = wsetup(wd, 0, vol >> 4, cok);
      }
      tile(14, false);
      next();
      {
        while (__builtin_expect(blank(cw) && cok, 0)) {
          __builtin_amdgcn_s_sleep(2);
          asm volatile("" ::: "memory");
          cgather();
        }
        f32x4 t;
#pragma unroll
        for (int i = 0; i < 4; ++i) {
          float a, b;
          asm("v_add_f32 %0, %2, %3\n\tv_add_f32 %1, %4, %5\n\tv_add_f32 %0, %0, %1" : "=&v"(a), "=&v"(b) : "v"(cw[0][i]), "v"(cw[1][i]), "v"(cw[2][i]), "v"(cw[3][i]));
          const auto r2 = __builtin_amdgcn_permlane16_swap(__float_as_uint(a), __float_as_uint(a), false, false);
          asm("v_add_f32 %0, %1, %2" : "=v"(t[i]) : "v"(r2[0]), "v"(r2[1]));
        }
        const int l = vol >> 4;
        if (__builtin_expect(!cok, 0)) {
          const int gq = (l >> 2) & 3;
          const char* pc = l < 32 ? pbase + ((psh & gq) ? dgu : 0) + (gq >> psh) * 32 + (l & 3) * 8 : gb + dgu + (l & 15) * 8;
          t = f32x4{0.f, 0.f, 0.f, 0.f};
#pragma unroll 1
          for (int k = 0; k < D_; ++k) { const u32x2 w = *(const __attribute__((address_space(1))) u32x2*)(pc + (size_t)k * (l < 32 ? pstr : F_ * 2)); t += f32x4{lo_f(w[0]), hi_f(w[0]), lo_f(w[1]), hi_f(w[1])}; }
        }
        if (l < 32 || wid == 0)
          *(f32x4*)((float*)(shm + AUX + 4096) + ((l >> 3) & 1) * 192 + (l < 32 ? wid : 4) * 32 + (l & 7) * 4) = t;
        __builtin_amdgcn_s_setprio(2);
      }
      SB0();
      ARRP(wid * 64 + 60, true);
      BARL();
      body(false);
    }
#pragma unroll
    for (int t = 0; t < 8; t += 2) mm(9, t, true);
    asm volatile("s_nop 7\n\ts_nop 3");
    SB0();
    TS1(3);
    if (!LD) {
#pragma unroll
      for (int i = 0; i < 3; ++i) {
        const unsigned r = ((role * 4 + (i & 1) * 2) * 14 + (i < 2 ? 10 : 0) + (wm & 2)) * 32;
        sw[0] = wy.wb[0] + (r << wy.sh[0]); sw[1] = wy.wb[1] + (r << wy.sh[1]);
        asm volatile(G2_W :: [l] "s"((i + 1) * 16384), G2_WOP(wy) : "memory", "scc", "m0");
      }
    } else {
      __builtin_amdgcn_s_setprio(3);
      WAIT_V(0);
    }
  };
  if (wid >= 4) kloop(std::integral_constant<int, 1>{});
  else kloop(std::integral_constant<int, 0>{});
  __builtin_amdgcn_sched_barrier(0);
  int tz = threadIdx.x;
  asm volatile("" : "+v"(tz));
  __builtin_amdgcn_sched_barrier(0);
  const int li = tz & 15, lq = (tz >> 4) & 3, ln = tz & 63;
  const __amdgpu_buffer_rsrc_t ra = __builtin_amdgcn_make_buffer_rsrc((void*)abuf, 0, 16 * 72 * 16384, 0x00020000);
  float* const cfin = (float*)(shm + AUX + 4096);
  float* const svr = cfin + 384;
  float* const tb = (float*)(shm + 2 * G1_STAGE + 4096 + wm * 1024);
  auto signal = [&](unsigned v) {
    unsigned a, b;
    asm volatile("v_mov_b32 %0, %2\n\tv_mov_b32 %1, %3\n\ts_mov_b64 exec, 1\n\tds_add_u32 %0, %1\n\ts_mov_b64 exec, -1"
                 : "=&v"(a), "=&v"(b) : "s"(shm_base + CNT), "s"(v) : "memory");
  };
  auto await = [&](unsigned mask, unsigned val) {
    unsigned a, t;
    asm volatile("v_mov_b32 %0, %2\n1:\n\tds_read_b32 %1, %0\n\ts_waitcnt lgkmcnt(0)\n\tv_readfirstlane_b32 vcc_lo, %1\n\ts_and_b32 vcc_lo, vcc_lo, %3\n\t"
                 "s_cmp_eq_u32 vcc_lo, %4\n\ts_cbranch_scc1 2f\n\ts_sleep 1\n\ts_branch 1b\n2:" : "=&v"(a), "=&v"(t) : "s"(shm_base + CNT), "s"(mask), "s"(val) : "memory", "scc", "vcc");
  };
  auto client = [&](int l) { const int m128 = mtile * 2 + ((l >> 4) & 1); return (((l & 7) * 4 + (m128 & 3)) * 8) + ((m128 >> 2) ^ ((l & 8) ? 5 : 0)); };
  auto notify = [&](int l0, int nl) __attribute__((always_inline)) {
    u64 now;
    asm volatile("s_memrealtime %0\n\ts_waitcnt lgkmcnt(0)" : "=s"(now) :: "memory");
    int l = threadIdx.x;
    asm volatile("" : "+v"(l));
    l &= 63;
    const __amdgpu_buffer_rsrc_t rd1 = __builtin_amdgcn_make_buffer_rsrc((void*)(done1 + ntile), 0, 256 * 512, 0x00020000);
    if (l < nl) __builtin_amdgcn_raw_buffer_store_b64(u32x2{(unsigned)now, (unsigned)(now >> 32)}, rd1, client(l0 + l) * 512, 0, 16);
  };
  const int xo = ((xcd & 1) << 1) | ((xcd >> 1) & 1), te = (xcd >> 2) * 28 + 6 * (xo >> 1) + (xo & 1), tl = (xcd >> 2) * 28 + 12 + 4 * xo;
  const u64 te0 = __builtin_amdgcn_s_memrealtime();
  const __amdgpu_buffer_rsrc_t re = __builtin_amdgcn_make_buffer_rsrc((void*)(done1 + client((wm >> 1) * 16 + 8 - (xcd & 4) * 2) * 64 + 32 + (ntile & 15) * 2 + (wm & 1)),
                                                                      0, 7 * 16384 + 8, 0x00020000);
  auto early = [&]() {
    __builtin_amdgcn_raw_buffer_store_b64(u32x2{(unsigned)te0, (unsigned)(te0 >> 32)}, re, ln * 16384, 0, 16);
  };
  signal(1u << (8 * wc));
  if (wc) {
    await(0xffffu, 0x404u);
#pragma unroll
    for (int f = 0; f < 4; ++f) *(u32x4*)(shm + G2_X + wm * 4096 + f * 1024 + ln * 16) = xq[f];
    SB0();
    __builtin_amdgcn_s_setprio(1);
  }
  TS1(4);
  f32x4 rf[4];
#pragma unroll
  for (int mf = 0; mf < 4; ++mf) rf[mf] = *(const f32x4*)(shm + AUX + (wm * 64 + mf * 16 + li) * 16);
  const float* const cl = cfin + wc * 192 + lq * 4;
  const int sbase = ((mtile * 2 + (wm >> 1)) * 72 * 8 + (wm & 1) * 4) * 2048;
  auto putf = [&](int kg, int mf, u32x4 v) {
    __builtin_amdgcn_raw_buffer_store_b128(v, ra, ln * 16 + sbase + ((kg >> 1) * 8 + mf) * 2048 + (kg & 1) * 1024, 0, 16);
  };
  auto gelp = [&](f32x2 g, f32x2 u, f32x2 cg, f32x2 cu, f32x2 m2, f32x2 k12, f32x2 k22, f32x2 d2) {
    const f32x2 tg = __builtin_elementwise_fma(m2, cg, g), tu = __builtin_elementwise_fma(m2, cu, u);
    const f32x2 arg = tg * __builtin_elementwise_fma(tg * tg, k22, k12);
    const f32x2 dn = __builtin_elementwise_fma(f32x2{__builtin_amdgcn_exp2f(arg[0]), __builtin_amdgcn_exp2f(arg[1])}, d2, d2);
    const f32x2 r = (tg * tu) * f32x2{__builtin_amdgcn_rcpf(dn[0]), __builtin_amdgcn_rcpf(dn[1])};
    return pack2(r[0], r[1]);
  };
  auto gel2 = [&](int p, int kg, int mf0 = 0, int mf1 = 4) {
    f32x4 cg[2], cu[2];
#pragma unroll
    for (int j = 0; j < 2; ++j) { cg[j] = *(const f32x4*)(cl + p * 32 + j * 16); cu[j] = *(const f32x4*)(cl + p * 32 + 32 + j * 16); }
#pragma unroll
    for (int mf = mf0; mf < mf1; ++mf) {
      const f32x2 m2 = {rf[mf][0], rf[mf][0]}, k12 = {rf[mf][1], rf[mf][1]}, k22 = {rf[mf][2], rf[mf][2]}, d2 = {rf[mf][3], rf[mf][3]};
      u32x4 o;
#pragma unroll
      for (int i = 0; i < 4; ++i) {
        const int j = i >> 1, e = (i & 1) * 2;
        const f32x4 g = acc[mf][p * 2 + j], u = acc[mf][p * 2 + 2 + j];
        o[i] = gelp(f32x2{g[e], g[e + 1]}, f32x2{u[e], u[e + 1]}, f32x2{cg[j][e], cg[j][e + 1]}, f32x2{cu[j][e], cu[j][e + 1]}, m2, k12, k22, d2);
      }
      putf(kg, mf, o);
    }
  };
  int tsel = ntile & 1;
  asm volatile("" : "+s"(tsel));
  if (tsel != 0) {
    const int tq = (wc ? tl : te + 2 + (xo & 1)) + he;
    gel2(0, 2 * tq);
    gel2(3, 2 * tq + 1);
    const float cg = cfin[wc * 192 + 64 + li], cu = cfin[wc * 192 + 80 + li];
#pragma unroll
    for (int mf = 0; mf < 4; ++mf) {
      f32x4 t[4];
#pragma unroll
      for (int i = 0; i < 4; ++i) t[i] = *(const f32x4*)(tb + i * 64 + mf * 16 + lq * 4);
      const f32x4 g = acc[mf][4], u = acc[mf][5];
      u32x2 o;
#pragma unroll
      for (int e = 0; e < 4; e += 2)
        o[e >> 1] = gelp(f32x2{g[e], g[e + 1]}, f32x2{u[e], u[e + 1]}, f32x2{cg, cg}, f32x2{cu, cu}, f32x2{t[0][e], t[0][e + 1]},
                         f32x2{t[1][e], t[1][e + 1]}, f32x2{t[2][e], t[2][e + 1]}, f32x2{t[3][e], t[3][e + 1]});
      *(u32x2*)(shm + wm * 4096 + mf * 1024 + wc * 512 + li * 32 + lq * 8) = o;
      if (mf == EPOS && wc == 0) {
        TS1(1);
        WAIT_V(0);
        TS1(6);
        early();
      }
    }
    if (wc == 0) {
      signal(0x10000u << wid);
    } else {
      await(0x1000u << wid, 0x1000u << wid);
#pragma unroll
      for (int mf = 0; mf < 4; ++mf) putf(2 * (tl + 2) + he, mf, __builtin_bit_cast(u32x4, tr_frag(shm3 + wm * 4096 + mf * 1024 + ln * 8)));
    }
  } else {
    float rd[4];
#pragma unroll
    for (int mf = 0; mf < 4; ++mf) rd[mf] = svr[wm * 64 + mf * 16 + li];
    auto cvq = [&](int p, int mf, const f32x4& c0, const f32x4& c1) {
      u32x4 o;
#pragma unroll
      for (int j = 0; j < 2; ++j) {
        const f32x4 a = acc[mf][p * 2 + j], c4 = j ? c1 : c0;
        const f32x2 m2 = {rf[mf][0], rf[mf][0]}, r2 = {rd[mf], rd[mf]};
        f32x2 t0 = __builtin_elementwise_fma(m2, f32x2{c4[0], c4[1]}, f32x2{a[0], a[1]});
        f32x2 t1 = __builtin_elementwise_fma(m2, f32x2{c4[2], c4[3]}, f32x2{a[2], a[3]});
        if (p) { t0 *= r2; t1 *= r2; }
        o[2 * j] = pack2(t0[0], t0[1]); o[2 * j + 1] = pack2(t1[0], t1[1]);
      }
      return o;
    };
    const float cv0 = cfin[wc * 192 + 64 + li], cv1 = cfin[wc * 192 + 80 + li];
    auto cvv = [&](int mf) {
      const f32x4 n4 = *(const f32x4*)(tb + mf * 16 + lq * 4), r4 = *(const f32x4*)(svr + wm * 64 + mf * 16 + lq * 4);
      u32x4 o;
#pragma unroll
      for (int j = 0; j < 2; ++j) {
        const f32x4 a = acc[mf][4 + j];
        const f32x2 c2 = {j ? cv1 : cv0, j ? cv1 : cv0};
        const f32x2 t0 = __builtin_elementwise_fma(f32x2{n4[0], n4[1]}, c2, f32x2{a[0], a[1]}) * f32x2{r4[0], r4[1]};
        const f32x2 t1 = __builtin_elementwise_fma(f32x2{n4[2], n4[3]}, c2, f32x2{a[2], a[3]}) * f32x2{r4[2], r4[3]};
        o[2 * j] = pack2(t0[0], t0[1]); o[2 * j + 1] = pack2(t1[0], t1[1]);
      }
      return o;
    };
    u32x4 kx[4], vx[4], qx[4], hx[4];
    const bf16x4 ones = {0x3f80, 0x3f80, 0x3f80, 0x3f80};
    const f32x4 ck0 = *(const f32x4*)(cl + 32), ck1 = *(const f32x4*)(cl + 48);
    kx[2] = cvq(1, 2, ck0, ck1); kx[3] = cvq(1, 3, ck0, ck1);
    vx[2] = cvv(2); vx[3] = cvv(3);
    auto exo = [](int e) { return e * 4096 + (e >> 2) * (G2_A - 16384); };
    const bool needh = wm == 0 && mtile > 0;
    const __amdgpu_buffer_rsrc_t rh = __builtin_amdgcn_make_buffer_rsrc((void*)((char*)halo + ((needh ? mtile - 1 : mtile) * 32 + ntile + wc) * 4096), 0,
                                                                        needh || wm == 3 ? 4096 : 0, 0x00020000);
    if (wm < 3) {
      char* const ex = shm + exo(wc * 3 + wm) + ln * 16;
      *(u32x4*)ex = kx[2]; *(u32x4*)(ex + 1024) = kx[3]; *(u32x4*)(ex + 2048) = vx[2]; *(u32x4*)(ex + 3072) = vx[3];
      signal(0x10000u << wid);
    } else {
#pragma unroll
      for (int r = 0; r < 4; ++r) __builtin_amdgcn_raw_buffer_store_b128(r < 2 ? kx[2 + r] : vx[r], rh, ln * 16 + r * 1024, 0, 16);
    }
    auto hload = [&]() {
#pragma unroll
      for (int r = 0; r < 4; ++r) hx[r] = __builtin_bit_cast(u32x4, __builtin_amdgcn_raw_buffer_load_b128(rh, ln * 16 + r * 1024, 0, 16));
    };
    SB0();
    gel2(3, 2 * (wc ? tl + 3 : te) + he);
    SB0();
    if (wm == 0) hload();
    SB0();
    kx[0] = cvq(1, 0, ck0, ck1); kx[1] = cvq(1, 1, ck0, ck1);
    vx[0] = cvv(0); vx[1] = cvv(1);
    {
      const f32x4 cq0 = *(const f32x4*)(cl), cq1 = *(const f32x4*)(cl + 16);
#pragma unroll
      for (int mf = 0; mf < 4; ++mf) qx[mf] = cvq(0, mf, cq0, cq1);
    }
    f32x4 mk0, mk1;
    u32x2 pm;
#pragma unroll
    for (int j = 0; j < 4; ++j) {
      const int d = 4 * lq + j - li;
      mk0[j] = d >= 9 ? 0.f : -1.0e30f;
      mk1[j] = (unsigned)(d + 7) <= 7u ? 0.f : -1.0e30f;
    }
#pragma unroll
    for (int j = 0; j < 2; ++j) pm[j] = (4 * lq + 2 * j > li ? 0xffffu : 0u) | (4 * lq + 2 * j + 1 > li ? 0xffff0000u : 0u);
    auto max3 = [](float a, float b, float c) { float r; asm("v_max3_f32 %0, %1, %2, %3" : "=v"(r) : "v"(a), "v"(b), "v"(c)); return r; };
    auto max2 = [](float a, float b) { float r; asm("v_max_f32 %0, %1, %2" : "=v"(r) : "v"(a), "v"(b)); return r; };
    asm volatile("" : "+v"(kx[0]), "+v"(kx[1]), "+v"(vx[0]), "+v"(vx[1]), "+v"(qx[0]), "+v"(qx[1]), "+v"(qx[2]), "+v"(qx[3]), "+v"(mk0), "+v"(mk1), "+v"(pm));
    TS1(1);
    if (wc == 0) {
      if (wm == 0) WAIT_V(4); else WAIT_V(0);
      early();
    }
    if (wm > 0) {
      await(0x8000u << wid, 0x8000u << wid);
#pragma unroll
      for (int i = 0; i < 4; ++i) hx[i] = *(const u32x4*)(shm + exo(wc * 3 + wm - 1) + i * 1024 + ln * 16);
    } else if (__builtin_expect(cok || !needh, 1)) {
      while (__builtin_expect(blank(hx), 0)) {
        __builtin_amdgcn_s_sleep(1);
        asm volatile("" ::: "memory");
        hload();
      }
    } else {
      await(0xffffu, 0x404u);
      char* const fb = shm + exo(6 + wc) + ln * 16;
#pragma unroll 1
      for (int i = 0; i < 32; ++i) {
        const int rec = i >> 3, e = i & 7;
        const int row = m0 - 32 + 2 * (rec < 2 ? li : lq * 4 + (e & 3)) + (rec & 1);
        const int col = (rec < 2 ? D_ + lq * 4 + (e & 3) : 2 * D_ + li) + (ntile + wc) * 32 + (e >> 2) * 16;
        const u16* xr = x + (size_t)row * D_;
        const unsigned stt = row_stats_serial(xr);
        const float mb = lo_f(stt);
        float a = 0.f;
#pragma unroll 1
        for (int k = 0; k < D_; ++k) a += (bf2f(xr[k]) - mb) * bf2f(wqkv[(size_t)k * 3072 + col]);
        *(u16*)(fb + rec * 1024 + e * 2) = f2bf(a / hi_f(stt));
      }
#pragma unroll
      for (int i = 0; i < 4; ++i) hx[i] = *(const u32x4*)(fb + i * 1024);
    }
    TS1(6);
    {
      const f32x4 z = {0.f, 0.f, 0.f, 0.f};
      const float mz = mtile == 0 && wm == 0 ? -1.0e30f : 0.f;
      const f32x4 mkf = mk0 + f32x4{mz, mz, mz, mz};
      f32x4 s0[4], s1[4];
#pragma unroll
      for (int i = 0; i < 4; ++i) {
        const int n = i ^ 2;
        const bf16x8 qf = __builtin_bit_cast(bf16x8, qx[n]);
        s1[n] = __builtin_amdgcn_mfma_f32_16x16x32_bf16(__builtin_bit_cast(bf16x8, kx[n]), qf, mk1, 0, 0, 0);
        s0[n] = __builtin_amdgcn_mfma_f32_16x16x32_bf16(__builtin_bit_cast(bf16x8, n < 2 ? hx[n] : kx[n - 2]), qf, n < 2 ? mkf : mk0, 0, 0, 0);
      }
      SB0();
      asm volatile("s_nop 7\n\ts_nop 3" : "+v"(s0[0]), "+v"(s1[0]), "+v"(s0[1]), "+v"(s1[1]), "+v"(s0[2]), "+v"(s1[2]), "+v"(s0[3]), "+v"(s1[3]));
      float mx[4];
#pragma unroll
      for (int n = 0; n < 4; ++n)
#pragma unroll
        for (int j = 0; j < 4; ++j) s0[n][j] = max2(s0[n][j], s1[n][j]);
#pragma unroll
      for (int n = 0; n < 4; ++n) mx[n] = max3(s0[n][0], s0[n][1], s0[n][2]);
#pragma unroll
      for (int n = 0; n < 4; ++n) mx[n] = max2(mx[n], s0[n][3]);
#pragma unroll
      for (int n = 0; n < 4; ++n) { auto r = __builtin_amdgcn_permlane16_swap(__float_as_uint(mx[n]), __float_as_uint(mx[n]), false, false);
        mx[n] = max2(__uint_as_float(r[0]), __uint_as_float(r[1])); }
#pragma unroll
      for (int n = 0; n < 4; ++n) { auto r = __builtin_amdgcn_permlane32_swap(__float_as_uint(mx[n]), __float_as_uint(mx[n]), false, false);
        mx[n] = max2(__uint_as_float(r[0]), __uint_as_float(r[1])); }
      SB0();
      constexpr float cs2 = 0.17677669529663687f * 1.4426950408889634f;
      f32x4 os[4], o0[4], o1[4];
#pragma unroll
      for (int n = 0; n < 4; ++n) {
        const float c = cs2 * rd[n], nm = -mx[n] * c;
        const f32x4 ar = __builtin_elementwise_fma(s0[n], f32x4{c, c, c, c}, f32x4{nm, nm, nm, nm});
        const u32x2 pk = {pack2(__builtin_amdgcn_exp2f(ar[0]), __builtin_amdgcn_exp2f(ar[1])), pack2(__builtin_amdgcn_exp2f(ar[2]), __builtin_amdgcn_exp2f(ar[3]))};
        const bf16x4 pa = __builtin_bit_cast(bf16x4, pk), p0 = __builtin_bit_cast(bf16x4, pk & pm), p1 = __builtin_bit_cast(bf16x4, pk & ~pm);
        const u32x4 vp = n < 2 ? hx[2 + n] : vx[n - 2];
        auto half = [](const u32x4& v, int h) { return __builtin_bit_cast(bf16x4, u32x2{v[2 * h], v[2 * h + 1]}); };
        os[n] = __builtin_amdgcn_mfma_f32_16x16x16bf16_1k(ones, pa, z, 0, 0, 0);
        o0[n] = __builtin_amdgcn_mfma_f32_16x16x16bf16_1k(half(vx[n], 0), p1, __builtin_amdgcn_mfma_f32_16x16x16bf16_1k(half(vp, 0), p0, z, 0, 0, 0), 0, 0, 0);
        o1[n] = __builtin_amdgcn_mfma_f32_16x16x16bf16_1k(half(vx[n], 1), p1, __builtin_amdgcn_mfma_f32_16x16x16bf16_1k(half(vp, 1), p0, z, 0, 0, 0), 0, 0, 0);
      }
#pragma unroll
      for (int n = 0; n < 4; ++n) {
        const float inv = __builtin_amdgcn_rcpf(os[n][0]);
        const f32x4 n0 = o0[n] * inv, n1 = o1[n] * inv;
        putf(112 + ntile + wc, n, u32x4{pack2(n0[0], n0[1]), pack2(n0[2], n0[3]), pack2(n1[0], n1[1]), pack2(n1[2], n1[3])});
      }
    }
  }
  TS1(2);
  {
  __builtin_amdgcn_sched_barrier(0);
  int tid = threadIdx.x;
  asm volatile("" : "+v"(tid));
  __builtin_amdgcn_sched_barrier(0);
  const int lane = tid & 63;
  const int wm = wid >> 1, wn = wid & 1;
  const int m0 = mt2 * 128, n0 = nt2 * 128;
  constexpr int PD = G2_NSTAGE;
  const int ny = (xcd & 1) ? NY - DLT : NY + DLT;
  const unsigned va = lane * 16;
  auto stage_y = [&]() {
    asm volatile("s_add_u32 m0, %[l], %[oa]\n\ts_nop 0\n\tglobal_load_lds_dwordx4 %[va], %[a] sc1\n\tglobal_load_lds_dwordx4 %[va], %[a] offset:1024 sc1\n\t"
                 "global_load_lds_dwordx4 %[va], %[a] offset:2048 sc1\n\tglobal_load_lds_dwordx4 %[va], %[a] offset:3072 sc1\n\t" G2_W
                 :: [l] "s"(sl), [oa] "s"(shm_base + G2_A + (p ^ role) * 4096), [a] "s"(sa), [va] "v"(va), G2_WOP(wy) : "memory", "scc", "m0");
    ++st;
    sa += 16384;
    sl = (sl + 16384) & 65535;
    wsel();
  };
  u64 ready = 0;
  const u64* const mbox = &done1[bid * 64 + lane];
  auto seen = [&](u64 v) __attribute__((always_inline)) {
    const u64 r = __ballot(fresh(v, t_own));
    u64 d;
    asm("s_bitreplicate_b64_b32 %0, %1" : "=s"(d) : "s"((unsigned)r >> (16 * role)));
    ready |= r | (d << 32);
  };
  auto need = [&](u64 mask, bool renew) __attribute__((always_inline)) {
    if ((ready & mask) != mask) {
      seen(__hip_atomic_load(mbox, RLX_AGENT));
      while ((ready & mask) != mask) {
        __builtin_amdgcn_s_sleep(PSLEEP);
        if (renew && !cok) notify(0, 32);
        seen(__hip_atomic_load(mbox, RLX_AGENT));
      }
    }
    asm volatile("" ::: "memory");
  };
  u64 fl = 0;
  auto peek = [&]() { asm volatile("global_load_dwordx2 %0, %1, %2 sc1" : "+v"(fl) : "v"(va >> 1), "s"(done1 + bid * 64) : "memory"); };
  auto check = [&](u64 mask, bool renew) __attribute__((always_inline)) {
    asm volatile("" : "+v"(fl));
    seen(fl);
    if (__builtin_expect((ready & mask) != mask, 0)) need(mask, renew);
  };
  if (wid >= 4) {
    TS(4);
    __builtin_amdgcn_s_setprio(0);
    BARL();
    stage_y();
    stage_y();
    BAR();
    if (p == 1) peek();
    stage_y();
    WAIT_V(16);
    BAR();
    if (p == 0) notify(0, 32);
#define G2_A2 "global_load_lds_dwordx4 %[va], %[a] sc1\n\tglobal_load_lds_dwordx4 %[va], %[a] offset:1024 sc1"
#define G2_A4 G2_A2 "\n\tglobal_load_lds_dwordx4 %[va], %[a] offset:2048 sc1\n\tglobal_load_lds_dwordx4 %[va], %[a] offset:3072 sc1"
#define G2_SYNC "\n\ts_mov_b32 m0, %[la]\n\ts_waitcnt vmcnt(%[n])\n\ts_barrier\n\t"
    const unsigned oy = shm_base + G2_A + (p ^ role) * 4096, os = shm_base + G2_A + (p >> 1) * 8192 + (p & 1) * 2048;
    asm volatile("s_mov_b32 m0, %[la]\n\ts_nop 0\n\t" G2_A4 :: [la] "s"(sl + oy), [a] "s"(sa), [va] "v"(va) : "memory", "m0");
    auto step = [&](long adv = 16384) __attribute__((always_inline)) {
      wsel();
      ++st;
      sa += adv;
      const unsigned sn = (sl + 16384) & 65535;
      asm volatile(G2_W G2_SYNC G2_A4 :: [l] "s"(sl), [la] "s"(sn + oy), [n] "n"(16), [a] "s"(sa), [va] "v"(va), G2_WOP(wy) : "memory", "scc", "m0");
      sl = sn;
    };
    auto chk1 = [&](u64 mask) __attribute__((always_inline)) { if (p == 1) { WAIT_V(16); check(mask, true); } };
    step();
    chk1(0xcc00cc00ull << 32);
    step(); step();
    if (p == 1) peek();
    step(); step();
    chk1(0x0f0fu << (16 * role));
#pragma unroll 1
    for (int g = 7; g < 13; ++g) step();
    if (p == 1) peek();
    step(); step();
    chk1((0xffffu << (16 * role)) | 0xf555f555u);
#pragma unroll 1
    for (int g = 15; g < min(ny, NY) - 5; ++g) step();
    if (ny > NY) {
      step(((1 - 2 * role) * NY + 1 - DLT) * 16384);
#pragma unroll
      for (int i = 1; i < DLT; ++i) step();
    }
    wsel();
    sa = sz;
    {
      const unsigned sn = (sl + 16384) & 65535;
      asm volatile(G2_W G2_SYNC G2_A2 :: [l] "s"(sl), [la] "s"(sn + os), [n] "n"(16), [a] "s"(sa), [va] "v"(va), G2_WOP(wy) : "memory", "scc", "m0");
      sl = sn;
    }
    sw[0] = wz.wb[0]; sw[1] = wz.wb[1];
#pragma unroll
    for (int j = -3; j < 13; ++j) {
      if (j == GETP + 1 || j == GETP + 2)
        asm volatile("s_mov_b32 m0, %[l]\n\ts_nop 0\n\tglobal_load_lds_dwordx4 %[va], %[a] offset:%[o] sc1\n\tglobal_load_lds_dwordx4 %[va], %[a] offset:%[o]+1024 sc1"
                     :: [l] "s"(shm_base + G2_X + 16384 + p * 4096), [a] "s"((const char*)slab + (size_t)(bid ^ 5) * 16384 + p * 4096), [va] "v"(va),
                        [o] "n"((j - GETP - 1) * 2048) : "memory", "m0");
      sa += 16384;
      const unsigned sn = (sl + 16384) & 65535;
      constexpr int G = GETP;
      static_assert(GETP <= GETJ + 1, "");
      if (j < 12) asm volatile(G2_W G2_SYNC G2_A2 :: [l] "s"(sl), [la] "s"(sn + os), [n] "n"((j == -3 ? 14 : 12) + (j > G && j < G + 3 ? 2 : 0) + (j > G + 1 && j < G + 4 ? 2 : 0)),
                               [a] "s"(sa), [va] "v"(va), G2_WOP(wz) : "memory", "scc", "m0");
      else asm volatile(G2_W :: [l] "s"(sl), G2_WOP(wz) : "memory", "scc", "m0");
      sl = sn;
      sw[0] += ws0; sw[1] += ws1;
    }
    WAIT_V(12);
    BAR();
    WAIT_V(6);
    BAR();
    WAIT_V(0);
    BAR();
    asm volatile("" : "+v"(fl));
    TS(5);
    if (tid < 258) __hip_atomic_store(lane ? done2 + bid * 8 : &acks[bid], __builtin_amdgcn_s_memrealtime(), RLX_AGENT);
    if (__builtin_expect(!cok, 0) && p == 0) {
      const bool mine = lane <= 32;
      const u64* const pa = &acks[lane < 32 ? client(lane) : bid ^ 5];
      while (!__all(!mine || fresh(__hip_atomic_load(pa, RLX_AGENT), t_own))) {
        notify(0, 32);
        if (lane == 0) __hip_atomic_store(done2 + bid * 8, __builtin_amdgcn_s_memrealtime(), RLX_AGENT);
        __builtin_amdgcn_s_sleep(32);
      }
    }
  } else {
    TS(6);
    f32x4 acc[4][4], sacc[2][4];
    const int fr = lane & 15, fq = lane >> 4, q4 = fr >> 2, pp = lane & 3;
    const int ff = ((q4 >> 1) & 1) | ((fq & 1) << 1);
    int aof = G2_A + wm * 8192 + lane * 16;
    int boff[4];
#pragma unroll
    for (int j = 0; j < 4; ++j) boff[j] = wn * 8192 + (8 * fq + q4) * 128 + ((j ^ ff) * 32) + pp * 8;
    int vo = wid * 4096 + lane * 16;
    int vx = ((m0 + wm * 64 + role * 32 + 2 * fr) * D_ + n0 + wn * 64) * 2 + (fq & 1) * 32 + (fq >> 1) * 16;
    lds_cptr px = shm3 + G2_X + wid * 4096 + (fq >> 1) * 512 + fr * 16 + (fq & 1) * 8;
    asm volatile("" : "+v"(vo), "+v"(vx), "+v"(px));
#define SB() __builtin_amdgcn_sched_barrier(0)
    struct Fr { bf16x8 a[4]; bf16x4 bl[4], bh[4]; };
    Fr fa[2], fb[2];
    auto rd = [&](Fr* f, int odd, int t) {
      constexpr int ord[12] = {0, 1, 8, 2, 3, 4, 5, 6, 7, 9, 10, 11};
      constexpr int seq[24] = {0, 1, 2, 12, 13, 14, 15, 16, 3, 4, 5, 6, 17, 18, 19, 20, 7, 8, 9, 21, 10, 22, 11, 23};
      lds_cptr sb = shm3 + odd * 16384;
      const int ks = seq[t] / 12, o = ord[seq[t] % 12];
      if (o >= 8) f[ks].a[o - 8] = *(const __attribute__((address_space(3))) bf16x8*)(sb + aof + (o - 8) * 2048 + ks * 1024);
      else if (o & 1) f[ks].bh[o >> 1] = tr_read(sb + boff[o >> 1] + ks * 4096 + 512);
      else f[ks].bl[o >> 1] = tr_read(sb + boff[o >> 1] + ks * 4096);
    };
    auto bfr = [&](const Fr& c, int j) { return __builtin_shufflevector(c.bl[j], c.bh[j], 0, 1, 2, 3, 4, 5, 6, 7); };
    auto mf1 = [](f32x4& c, bf16x8 b, bf16x8 a, bool z = false) {
      if (z) asm volatile("v_mfma_f32_16x16x32_bf16 %0, %1, %2, 0" : "=&v"(c) : "v"(b), "v"(a));
      else asm volatile("v_mfma_f32_16x16x32_bf16 %0, %1, %2, %0" : "+v"(c) : "v"(b), "v"(a));
    };
    auto mf2 = [](f32x4& c, bf16x8 b, bf16x8 a, bf16x8 b2, bf16x8 a2, bool z) {
      if (z) asm volatile("v_mfma_f32_16x16x32_bf16 %0, %1, %2, 0\n\tv_mfma_f32_16x16x32_bf16 %0, %3, %4, %0" : "=&v"(c) : "v"(b), "v"(a), "v"(b2), "v"(a2));
      else asm volatile("v_mfma_f32_16x16x32_bf16 %0, %1, %2, %0\n\tv_mfma_f32_16x16x32_bf16 %0, %3, %4, %0" : "+v"(c) : "v"(b), "v"(a), "v"(b2), "v"(a2));
    };
    auto pairs = [&](f32x4 (*ac)[4], const Fr* c, int nq, int mx, int nf, auto fill, bool z = false) {
#pragma unroll
      for (int q = 0; q < nq; ++q) {
        const int mf = (q >> 2) ^ mx, j = (q & 4) ? 3 - (q & 3) : q & 3, ks = q & 1;
        SB();
        if (2 * q >= nf) mf2(ac[mf][j], bfr(c[ks], j), c[ks].a[mf], bfr(c[ks ^ 1], j), c[ks ^ 1].a[mf], z);
        else {
          mf1(ac[mf][j], bfr(c[ks], j), c[ks].a[mf], z);
          SB();
          fill(2 * q);
          SB();
          mf1(ac[mf][j], bfr(c[ks ^ 1], j), c[ks ^ 1].a[mf]);
          SB();
          fill(2 * q + 1);
        }
      }
      SB();
    };
    auto flip = [&]() {
      aof ^= 32768; boff[0] ^= 32768; boff[1] ^= 32768; boff[2] ^= 32768; boff[3] ^= 32768;
      asm volatile("" : "+v"(aof), "+v"(boff[0]), "+v"(boff[1]), "+v"(boff[2]), "+v"(boff[3]));
    };
    auto bar = [&]() { __builtin_amdgcn_s_waitcnt(0xC07F); BAR(); };
    auto ystg = [&](const Fr* c, Fr* n, int odd, bool z = false) {
      pairs(acc, c, 16, 0, odd ? 30 : 24, [&](int t) {
        if (t < 24) rd(n, odd, t);
        else if (t == 24) asm volatile("s_nop 0");
        else { int& a = t == 29 ? aof : boff[t - 25]; a ^= 32768; asm volatile("" : "+v"(a)); }
      }, z);
    };
    auto stg = [&](const Fr* c, Fr* n, int odd, bool z = false) { pairs(sacc, c, 8, 0, 10, [&](int t) { rd(n, odd, 2 * t); rd(n, odd, 2 * t + 1); }, z); };
    asm volatile("" : "+v"(aof), "+v"(boff[0]), "+v"(boff[1]), "+v"(boff[2]), "+v"(boff[3]));
    await(0xffffu, 0x404u);
    auto lda = [&](int i) {
      asm volatile("s_mov_b32 m0, %[l]\n\ts_nop 0\n\tglobal_load_lds_dwordx4 %[va], %[a] sc1\n\tglobal_load_lds_dwordx4 %[va], %[a] offset:1024 sc1\n\t"
                   "global_load_lds_dwordx4 %[va], %[a] offset:2048 sc1\n\tglobal_load_lds_dwordx4 %[va], %[a] offset:3072 sc1"
                   :: [l] "s"(shm_base + G2_A + (i + 1) * 16384 + (p ^ role) * 4096), [a] "s"(sa + i * 16384), [va] "v"(va) : "memory", "m0");
    };
    need(0x00330033ull << 32, false);
    TS(4);
    lda(0);
    lda(1);
    need(0x00ff00ffull << 32, false);
    TS(0);
    lda(2);
    WAIT_V(8);
    signal(1u << 24);
    await(0xff000000u, 4u << 24);
    __builtin_amdgcn_s_waitcnt(0xC07F);
    SB();
#pragma unroll
    for (int t = 0; t < 24; ++t) rd(fa, 1, t);
    flip();
#define CPRIO 0
    __builtin_amdgcn_s_setprio(CPRIO);
    WAIT_V(4);
    TS(5);
    peek();
    bar();
    TS(7);
    ystg(fa, fb, 0, true);
    WAIT_V(1);
    bar();
    ystg(fb, fa, 1);
    WAIT_V(0);
    check(0x33003300ull << 32, false);
    constexpr int PAR = (NY - DLT + 1) & 1;
#pragma unroll 1
    for (int g = 2; g < ny - 2; g += 2) {
      bar();
      ystg(fa, fb, 0);
      bar();
      ystg(fb, fa, 1);
    }
    if (PAR) {
      bar();
      ystg(fa, fb, 0);
    }
    Fr* const f0 = PAR ? fb : fa;
    Fr* const f1 = PAR ? fa : fb;
    bar();
    {
      const __amdgpu_buffer_rsrc_t rs = __builtin_amdgcn_make_buffer_rsrc((void*)((char*)slab + (size_t)bid * 16384), 0, 16384, 0x00020000);
      u32x4 pk;
      pairs(acc, f0, 16, 2, 32, [&](int t) {
        if (t < 20) rd(f1, PAR, t);
        else {
          const int f = (t - 20) / 3, e = (t - 20) % 3;
          if (e < 2) { const f32x4 a = acc[2 + (f >> 1)][(f & 1) * 2 + e]; pk[2 * e] = pack2(a[0], a[1]); pk[2 * e + 1] = pack2(a[2], a[3]); }
          else __builtin_amdgcn_raw_buffer_store_b128(pk, rs, vo + f * 1024, 0, 16);
        }
      });
    }
    TS(2);
    static_assert(GETJ % 2 == 1 && GETJ >= 3 && GETJ <= 7, "");
    f32x4 xf[2][4];
#pragma unroll
    for (int j = 0; j < GETJ + 5; j += 2) {
      if (PAR) flip();
      bar();
      stg(f1, f0, PAR ^ 1, j == 0);
      if (!PAR) flip();
      bar();
      u32x2 xv[2];
      pairs(sacc, f0, 8, 0, 12, [&](int t) {
        if (t < 10) { rd(f1, PAR, 2 * t); rd(f1, PAR, 2 * t + 1); }
        else if (j < 8) xv[t - 10] = *(const __attribute__((address_space(3))) u32x2*)(px + (j >> 1) * 1024 + (t - 10) * 256);
      });
      if (j < 8) {
#pragma unroll
        for (int e = 0; e < 2; ++e) xf[j >> 2][(j & 2) + e] = f32x4{lo_f(xv[e][0]), hi_f(xv[e][0]), lo_f(xv[e][1]), hi_f(xv[e][1])};
        asm volatile("" : "+v"(xf[j >> 2][j & 2]), "+v"(xf[j >> 2][(j & 2) + 1]));
        SB();
      }
    }
    if (PAR) flip();
    WAIT_V(0);
    bar();
    u32x4 pv[4];
    {
      lds_cptr pp_ = shm3 + G2_X + 16384 + vo;
      pairs(sacc, f1, 8, 0, 12, [&](int t) {
        if (t < 2) {
#pragma unroll
          for (int f = 2 * t; f < 2 * t + 2; ++f) pv[f] = *(const __attribute__((address_space(3))) u32x4*)(pp_ + f * 1024);
        } else { rd(f0, PAR ^ 1, 2 * t - 4); rd(f0, PAR ^ 1, 2 * t - 3); }
      });
      if (__builtin_expect(blank(pv) || !cok, 0)) {
        const __amdgpu_buffer_rsrc_t rp = __builtin_amdgcn_make_buffer_rsrc((void*)((char*)slab + (size_t)(bid ^ 5) * 16384), 0, 16384, 0x00020000);
        while (!cok && !__all(fresh(__hip_atomic_load(done2 + (bid ^ 5) * 8, RLX_AGENT), t_own))) {
          notify(0, 32);
          if (lane == 0) __hip_atomic_store(done2 + bid * 8, __builtin_amdgcn_s_memrealtime(), RLX_AGENT);
          __builtin_amdgcn_s_sleep(8);
        }
        do {
          asm volatile("" ::: "memory");
#pragma unroll
          for (int f = 0; f < 4; ++f) pv[f] = __builtin_bit_cast(u32x4, __builtin_amdgcn_raw_buffer_load_b128(rp, vo + f * 1024, 0, 16));
        } while (blank(pv));
      }
    }
    auto silu = [&](int g0, int g1) {
#pragma unroll
      for (int f = g0; f < g1; ++f) {
        const int mf = f >> 2, j = f & 3;
        const u32x4 pw = pv[mf * 2 + (j >> 1)];
        const f32x4 ya = acc[mf][j] + f32x4{lo_f(pw[2 * (j & 1)]), hi_f(pw[2 * (j & 1)]), lo_f(pw[2 * (j & 1) + 1]), hi_f(pw[2 * (j & 1) + 1])};
#pragma unroll
        for (int e = 0; e < 2; ++e) {
          const f32x2 y2 = {ya[2 * e], ya[2 * e + 1]};
          const f32x2 ar = y2 * f32x2{-1.4426950408889634f, -1.4426950408889634f};
          const f32x2 dn = f32x2{__builtin_amdgcn_exp2f(ar[0]), __builtin_amdgcn_exp2f(ar[1])} + f32x2{1.0f, 1.0f};
          const f32x2 t = y2 * f32x2{__builtin_amdgcn_rcpf(dn[0]), __builtin_amdgcn_rcpf(dn[1])};
          acc[mf][j][2 * e] = t[0]; acc[mf][j][2 * e + 1] = t[1];
        }
        asm volatile("" : "+v"(acc[mf][j]));
      }
      SB();
    };
#define SPT 0, 0, 1, 2, 4, 6, 8, 8
    constexpr int spt[] = {SPT};
    silu(0, spt[1]);
#pragma unroll
    for (int j = GETJ + 6; j < 15; ++j) {
      if ((j ^ PAR) & 1) flip();
      bar();
      if (j & 1) stg(f0, f1, PAR); else stg(f1, f0, PAR ^ 1);
      silu(spt[j - GETJ - 5], j == 14 ? 8 : spt[j - GETJ - 4]);
    }
    pairs(sacc, f0, 8, 0, 0, [](int) {});
    asm volatile("s_nop 7\n\ts_nop 3" : "+v"(sacc[1][0]), "+v"(sacc[1][1]), "+v"(sacc[1][2]), "+v"(sacc[1][3]));
    SB();
    TS(1);
    {
      const __amdgpu_buffer_rsrc_t ro = __builtin_amdgcn_make_buffer_rsrc((void*)out, 0, S_ * D_ * 2, 0x00020000);
#pragma unroll
      for (int f = 0; f < 4; ++f) {
        const int mf = f >> 1, j = (f & 1) * 2;
        const f32x4 a = __builtin_elementwise_fma(sacc[mf][j], acc[mf][j], xf[mf][j]), b = __builtin_elementwise_fma(sacc[mf][j + 1], acc[mf][j + 1], xf[mf][j + 1]);
        const auto r0 = __builtin_amdgcn_permlane16_swap(pack2(a[0], a[1]), pack2(b[0], b[1]), false, false);
        const auto r1 = __builtin_amdgcn_permlane16_swap(pack2(a[2], a[3]), pack2(b[2], b[3]), false, false);
        __builtin_amdgcn_raw_buffer_store_b128(u32x4{r0[0], r1[0], r0[1], r1[1]}, ro, vx + mf * 2048 + j * 32, 0, 2);
      }
    }
    TS(3);
  }
  if (bid == 73 && wid == 0) { unsigned d_ = ts_r[15] - ts_r[3]; if (d_ > 6000u) d_ = 0; while (__builtin_amdgcn_s_memrealtime() - t_own < 6000ull + d_) {} }
  }
  asm volatile(".Lcode_end:");
}

extern "C" void kernel_launch(void* const* d_in, const int* in_sizes, int n_in, void* d_out, int out_size, void* d_ws,
                              size_t ws_size, hipStream_t stream) {
  const u16* A_x = (const u16*)d_in[0];
  const u16* A_wqkv = (const u16*)d_in[1];
  const u16* A_wo = (const u16*)d_in[2];
  const u16* B_wg = (const u16*)d_in[4];
  const u16* B_wu = (const u16*)d_in[5];
  const u16* B_wd = (const u16*)d_in[6];
  u16* out = (u16*)d_out;
  fused_kernel<<<256, 512, 0, stream>>>(A_x, A_wqkv, B_wg, B_wu, B_wd, A_wo, out, (char*)d_ws);
}
```

```cpp
#define ALL_TS 1
#define TSMASK 8224
#include <hip/hip_runtime.h>
#include <hip/hip_bf16.h>
#include <stdint.h>
#include <type_traits>

typedef unsigned short u16;
using bf16x8 = __attribute__((ext_vector_type(8))) short;
using bf16x4 = __attribute__((ext_vector_type(4))) short;
using f32x4 = __attribute__((ext_vector_type(4))) float;
using u32x2 = __attribute__((ext_vector_type(2))) unsigned;
using u32x4 = __attribute__((ext_vector_type(4))) unsigned;
typedef __attribute__((address_space(3))) const char* lds_cptr;

constexpr int S_ = 2048, D_ = 1024, F_ = 3584;

__device__ __forceinline__ float bf2f(u16 h) { return __uint_as_float((unsigned)h << 16); }
__device__ __forceinline__ u16 f2bf(float f) {
  unsigned u = __float_as_uint(f);
  u += 0x7fffu + ((u >> 16) & 1u);
  return (u16)(u >> 16);
}
__device__ __forceinline__ float rbf(float f) { return bf2f(f2bf(f)); }
typedef __attribute__((ext_vector_type(2))) __bf16 bf16x2_t;
typedef __attribute__((ext_vector_type(2))) float f32x2;
__device__ __forceinline__ unsigned pack2(float a, float b) {
  bf16x2_t r = __builtin_convertvector((f32x2){a, b}, bf16x2_t);
  return __builtin_bit_cast(unsigned, r);
}
__device__ __forceinline__ float lo_f(unsigned p) { return __uint_as_float(p << 16); }
__device__ __forceinline__ float hi_f(unsigned p) { return __uint_as_float(p & 0xffff0000u); }

#define TSDECL unsigned ts_r[16] = {0, 0, 0, 0, 0, 0, 0, 0, 0, 0, 0, 0, 0, 0, 0, 0}
#define TSS(i) do { if ((TSMASK >> (i)) & 1) { unsigned long long t_; asm volatile("s_memtime %0\n\ts_waitcnt lgkmcnt(0)" : "=s"(t_)); ts_r[i] = (unsigned)t_; } } while (0)
#define TSFLUSH() do { if ((threadIdx.x & 63) == 0) { for (int i_ = 0; i_ < 16; ++i_) flags[(blockIdx.x * 8 + (threadIdx.x >> 6)) * 16 + i_] = ts_r[i_]; } } while (0)
#define TS1(i) TSS(i)
#define TS(i) TSS(8 + (i))
#define PIN_S(v) asm volatile("" : "+s"(v))
#define WAIT_V(n) asm volatile("s_waitcnt vmcnt(%0)" ::"n"(n) : "memory")
#define WAIT_L(n) asm volatile("s_waitcnt lgkmcnt(%0)" ::"n"(n) : "memory")
#define BARL() do { WAIT_L(0); BAR(); } while (0)
#define BAR() do { asm volatile("" ::: "memory"); __builtin_amdgcn_s_barrier(); asm volatile("" ::: "memory"); } while (0)

__device__ __forceinline__ bf16x4 tr_read(lds_cptr p) {
  return __builtin_amdgcn_ds_read_tr16_b64_v4i16((__attribute__((address_space(3))) bf16x4*)p);
}
__device__ __forceinline__ bf16x8 tr_frag(lds_cptr p) {
  bf16x4 lo = tr_read(p), hi = tr_read(p + 512);
  bf16x8 r;
  r[0] = lo[0]; r[1] = lo[1]; r[2] = lo[2]; r[3] = lo[3];
  r[4] = hi[0]; r[5] = hi[1]; r[6] = hi[2]; r[7] = hi[3];
  return r;
}
__device__ __forceinline__ void glds16(const void* gsrc, unsigned lds_dst) {
  unsigned keep;
  asm volatile("s_mov_b32 %0, m0\n\ts_mov_b32 m0, %2\n\ts_nop 0\n\tglobal_load_lds_dwordx4 %1, off\n\ts_mov_b32 m0, %0"
               : "=&s"(keep) : "v"(gsrc), "s"(lds_dst) : "memory");
}
#define GLDS(gptr, laddr) glds16((const void*)(gptr), (unsigned)(laddr))
__device__ __forceinline__ void glds16_sc1(const void* gsrc, unsigned lds_dst) {
  unsigned keep;
  asm volatile("s_mov_b32 %0, m0\n\ts_mov_b32 m0, %2\n\ts_nop 0\n\tglobal_load_lds_dwordx4 %1, off sc1\n\ts_mov_b32 m0, %0"
               : "=&s"(keep) : "v"(gsrc), "s"(lds_dst) : "memory");
}
#define GLDS_SC1(gptr, laddr) glds16_sc1((const void*)(gptr), (unsigned)(laddr))
__device__ __forceinline__ void glds16_nc(const void* gsrc, unsigned lds_dst) {
  unsigned keep;
  asm volatile("s_mov_b32 %0, m0\n\ts_mov_b32 m0, %2\n\ts_nop 0\n\tglobal_load_lds_dwordx4 %1, off\n\ts_mov_b32 m0, %0"
               : "=&s"(keep) : "v"(gsrc), "s"(lds_dst));
}
#define GLDS_NC(gptr, laddr) glds16_nc((const void*)(gptr), (unsigned)(laddr))
__device__ __forceinline__ void glds16_s(const void* sbase, unsigned voff, unsigned lds_dst) {
  asm volatile("s_mov_b32 m0, %2\n\ts_nop 0\n\tglobal_load_lds_dwordx4 %0, %1" :: "v"(voff), "s"(sbase), "s"(lds_dst) : "m0");
}

__device__ __forceinline__ void glds16_s1(const void* sbase, unsigned voff, unsigned lds_dst) {
  asm volatile("s_mov_b32 m0, %2\n\ts_nop 0\n\tglobal_load_lds_dwordx4 %0, %1 sc1" :: "v"(voff), "s"(sbase), "s"(lds_dst) : "memory", "m0");
}
__device__ __forceinline__ void glds16_v(const void* p, unsigned lds_dst) {
  asm volatile("s_mov_b32 m0, %1\n\ts_nop 0\n\tglobal_load_lds_dwordx4 %0, off" :: "v"(p), "s"(lds_dst) : "m0");
}
template <int OFF = 0>
__device__ __forceinline__ void gst16(const void* sbase, unsigned voff, u32x4 v) {
  asm volatile("global_store_dwordx4 %0, %1, %2 offset:%3 sc1\n\ts_nop 1" :: "v"(voff), "v"(v), "s"(sbase), "n"(OFF) : "memory");
}

typedef unsigned long long u64;
__device__ __forceinline__ bool fresh(u64 stamp, u64 t_own) {
  const u64 b = t_own + 100ull;
  const unsigned lo = (unsigned)stamp - (unsigned)b, hi = (unsigned)(stamp >> 32) - (unsigned)(b >> 32) - ((unsigned)stamp < (unsigned)b ? 1u : 0u);
  return (hi == 0u) & (lo < (unsigned)(__builtin_amdgcn_s_memrealtime() - t_own));
}
constexpr u64 TIMEOUT_ = 200000;
__device__ __forceinline__ float half_sum(float v) {
  v += __builtin_bit_cast(float, __builtin_amdgcn_update_dpp(0, __builtin_bit_cast(int, v), 0xB1, 0xf, 0xf, false));
  v += __builtin_bit_cast(float, __builtin_amdgcn_update_dpp(0, __builtin_bit_cast(int, v), 0x4E, 0xf, 0xf, false));
  v += __builtin_bit_cast(float, __builtin_amdgcn_update_dpp(0, __builtin_bit_cast(int, v), 0x141, 0xf, 0xf, false));
  v += __builtin_bit_cast(float, __builtin_amdgcn_update_dpp(0, __builtin_bit_cast(int, v), 0x140, 0xf, 0xf, false));
  { auto r = __builtin_amdgcn_permlane16_swap(__float_as_uint(v), __float_as_uint(v), false, false); v = __uint_as_float(r[0]) + __uint_as_float(r[1]); }
  return v;
}
__device__ __forceinline__ unsigned pack_stats(float mean, float var) {
  return (unsigned)f2bf(mean) | ((unsigned)f2bf(sqrtf(rbf(rbf(var) + 1e-5f))) << 16);
}
__device__ __forceinline__ unsigned row_stats_serial(const u16* xr) {
  float s = 0.f, q = 0.f;
#pragma unroll 1
  for (int k = 0; k < D_; ++k) s += bf2f(xr[k]);
  const float mean = s * (1.0f / D_);
#pragma unroll 1
  for (int k = 0; k < D_; ++k) { const float d = bf2f(xr[k]) - mean; q += d * d; }
  return pack_stats(mean, q * (1.0f / D_));
}
constexpr int G1_STAGE = 32768 + 5 * 8192;
#define EPOS 2
#define PA 4
#define PB 8
#define PC 12
#define GETP 4
#define DLT 2
#define PSLEEP 2
#define GETJ 3

constexpr unsigned BLANK = 0x7f817f81u;
__device__ __forceinline__ bool blank(const u32x4* pv) {
  u64 m;
#define CX(i) "v_cmpx_ne_u32 vcc, %1, %" #i "\n\t"
  asm volatile(CX(2) CX(3) CX(4) CX(5) CX(6) CX(7) CX(8) CX(9) CX(10) CX(11) CX(12) CX(13) CX(14) CX(15) CX(16) CX(17)
               "s_mov_b64 %0, exec\n\ts_mov_b64 exec, -1\n\ts_nop 3" : "=s"(m) : "s"(BLANK), "v"(pv[0][0]), "v"(pv[0][1]), "v"(pv[0][2]), "v"(pv[0][3]),
               "v"(pv[1][0]), "v"(pv[1][1]), "v"(pv[1][2]), "v"(pv[1][3]), "v"(pv[2][0]), "v"(pv[2][1]), "v"(pv[2][2]), "v"(pv[2][3]),
               "v"(pv[3][0]), "v"(pv[3][1]), "v"(pv[3][2]), "v"(pv[3][3]) : "vcc");
  return m != ~0ull;
}
#define RLX_AGENT __ATOMIC_RELAXED, __HIP_MEMORY_SCOPE_AGENT

constexpr int G2_A = 65536, G2_X = 131072;
constexpr int G2_NSTAGE = 4;
constexpr size_t MB_ = 1024 * 1024;
__global__ __launch_bounds__(512, 2) __attribute__((aligned(4096))) void fused_kernel(const u16* __restrict__ x, const u16* __restrict__ wqkv,
                                                       const u16* __restrict__ wg, const u16* __restrict__ wu,
                                                       const u16* __restrict__ wd, const u16* __restrict__ wo,
                                                       u16* __restrict__ out, char* __restrict__ ws) {
  u16* const halo = (u16*)(ws + 4 * MB_);
  u16* const abuf = (u16*)(ws + 16 * MB_);
  char* const slab = ws + 64 * MB_;
  unsigned* const flags = (unsigned*)(ws + 96 * MB_);
  u32x4* const rstat = (u32x4*)(ws + 97 * MB_);
  char* const wcopy = ws + 98 * MB_;
  u64* const done1 = (u64*)(ws + 102 * MB_);
  u64* const done2 = (u64*)(ws + 100 * MB_ + 65536);
  u64* const donec = (u64*)(ws + 100 * MB_ + 131072);
  u64* const acks = (u64*)(ws + 100 * MB_ + 196608);
  float* const csum = (float*)(ws + 101 * MB_);
  __shared__ __attribute__((aligned(1024))) char shm[163840];
  constexpr int AUX = 2 * G1_STAGE + 8192;
  {
    u64 pc; int len;
    asm volatile("s_getpc_b64 %0\n.Lcode_beg:\n\ts_mov_b32 %1, .Lcode_end-.Lcode_beg" : "=s"(pc), "=s"(len));
    const int ol = ((blockIdx.x >> 3) + 32 * (threadIdx.x >> 6)) * 1024 + (threadIdx.x & 63) * 16;
    asm volatile("s_mov_b32 m0, %2\n\ts_nop 0\n\tglobal_load_lds_dwordx4 %0, %1"
                 :: "v"(ol < len - 16 ? ol : 0), "s"(pc & ~15ull), "s"((unsigned)(uintptr_t)(lds_cptr)shm + 2 * G1_STAGE + 7168) : "m0");
    __builtin_amdgcn_sched_barrier(0);
  }
  const u64 t_own = __builtin_amdgcn_s_memrealtime();
  TSDECL;
  TS1(7);
  const int tid = threadIdx.x, lane = tid & 63;
  const int wid = __builtin_amdgcn_readfirstlane(tid >> 6);
  const int wm = wid & 3, wc = wid >> 2;
  int bid_ = blockIdx.x;
  PIN_S(bid_);
  const int bid = bid_ & 255, xcd = bid & 7, idx = bid >> 3;
  const int ntile = xcd * 4 + (idx & 3), mtile = idx >> 2;
  const int m0 = mtile * 256;
  const int hg = ntile >> 1;
  const bool tya = (ntile & 1) == 0;
  const int srow = m0 + ntile * 8 + wm * 2;
  lds_cptr shm3 = (lds_cptr)shm;
  const unsigned shm_base = (unsigned)(uintptr_t)shm3;
  const int r8 = tid >> 3, pos = tid & 7;
  const int phys = pos >> 1, half = pos & 1;
  const int fk = ((r8 >> 1) & 1) | (((r8 >> 3) & 1) << 1);
  const int vsrc = phys ^ fk;
  const int qstr = tya ? 3072 * 2 : F_ * 2;
  unsigned voa = (((r8 & 32) + 2 * (r8 & 15) + ((r8 >> 4) & 1)) * D_ + ((pos ^ (r8 & 7)) * 8)) * 2;
  unsigned vog = r8 * (F_ * 2) + vsrc * 32 + half * 16, voq = r8 * qstr + vsrc * 32 + half * 16;
  unsigned vom = r8 * qstr + (vsrc >> (tya ? 0 : 1)) * 32 + half * 16;
  asm volatile("" : "+v"(voa), "+v"(vog), "+v"(voq), "+v"(vom));
  const int kt0 = (hg >> 1) * 7, he = hg & 1;
  unsigned oq = (tya ? hg : kt0 + 2 * he) * 128, og = (kt0 + (tya ? 5 + he : 2 * he + 1)) * 128, om = tya ? 4096 : 512 - 192 * he;
  unsigned d0 = shm_base + wid * 1024 + 32768, ox = m0 * D_ * 2;
  long uq = -32 * qstr;
  PIN_S(oq); PIN_S(og); PIN_S(om); PIN_S(d0); PIN_S(ox); PIN_S(uq);
  __builtin_amdgcn_sched_barrier(0);
  asm volatile("" :: "s"(x), "s"(wqkv), "s"(wg), "s"(wu), "s"(wd), "s"(wo), "s"(out), "s"(ws));
  const char* const ab = (const char*)x + ox;
  int kz = 0;
  asm volatile("" : "+s"(kz));
  u64 pq = (u64)wqkv, pg = (u64)wg;
  PIN_S(pq); PIN_S(pg);
  const char* const qb = (const char*)(tya ? pq : pg) + oq;
  if (wid < 4) {
#pragma unroll
    for (int i = 0; i < 8; ++i) glds16_s(ab + i * 32 * D_ * 2, voa, d0 - 32768 + i * 4096);
  } else {
    glds16_s(qb, voq, d0); glds16_s(qb + uq, voq, d0 - 4096);
  }
  __builtin_amdgcn_sched_barrier(0);
  const char* const gb = (const char*)wg + og;
  const long dgu = (const char*)wu - (const char*)wg, dp1 = tya ? 2048 : dgu;
  const char* const mb0 = qb + om;
  const char* mptr = mb0 + (((vsrc & 1) && !tya) ? dgu : 0) + vom;
  if (wid >= 4) {
    const long ug = kz - 32 * F_ * 2;
    glds16_s(qb + dp1, voq, d0 + 8192); glds16_s(qb + dp1 + uq, voq, d0 + 4096);
    glds16_v(mptr, d0 + 2 * 8192); glds16_v(mptr + uq, d0 + 2 * 8192 - 4096);
    glds16_s(gb, vog, d0 + 3 * 8192); glds16_s(gb + ug, vog, d0 + 3 * 8192 - 4096);
    glds16_s(gb + dgu, vog, d0 + 4 * 8192); glds16_s(gb + dgu + ug, vog, d0 + 4 * 8192 - 4096);
  }
  __builtin_amdgcn_sched_barrier(0);
  f32x4 acc[4][10];

  constexpr int NT = D_ / 64;
  constexpr int CNT = AUX + 4096 + 3584;
  if (tid < 8) *(unsigned*)(shm + CNT + tid * 4) = 0u;
  bf16x8 af0[4], af1[4];
  bf16x4 bl[4], bh[4];
#define SB0() __builtin_amdgcn_sched_barrier(0)
  auto mm = [&](int g, int t, bool two, bool z = false) {
    const int mi = t >> 1, mf = ((g & 1) && g != 9) ? 3 - mi : mi, ks = (t & 1) ^ (mi & 1), s = ((g + 1) & 1) * 2;
    const bf16x8 b0 = __builtin_shufflevector(bl[s + ks], bh[s + ks], 0, 1, 2, 3, 4, 5, 6, 7), a0 = ks ? af1[mf] : af0[mf];
    const bf16x8 b1 = __builtin_shufflevector(bl[s + 1 - ks], bh[s + 1 - ks], 0, 1, 2, 3, 4, 5, 6, 7), a1 = ks ? af0[mf] : af1[mf];
    const bool tr = (g >> 1) == 2;
    if (z && two) asm volatile("v_mfma_f32_16x16x32_bf16 %0, %1, %2, 0\n\tv_mfma_f32_16x16x32_bf16 %0, %3, %4, %0" : "=&v"(acc[mf][g])
                               : "v"(tr ? a0 : b0), "v"(tr ? b0 : a0), "v"(tr ? a1 : b1), "v"(tr ? b1 : a1));
    else if (z && !(t & 1)) asm volatile("v_mfma_f32_16x16x32_bf16 %0, %1, %2, 0" : "=&v"(acc[mf][g]) : "v"(tr ? a0 : b0), "v"(tr ? b0 : a0));
    else if (two) asm volatile("v_mfma_f32_16x16x32_bf16 %0, %1, %2, %0\n\tv_mfma_f32_16x16x32_bf16 %0, %3, %4, %0" : "+v"(acc[mf][g])
                          : "v"(tr ? a0 : b0), "v"(tr ? b0 : a0), "v"(tr ? a1 : b1), "v"(tr ? b1 : a1));
    else asm volatile("v_mfma_f32_16x16x32_bf16 %0, %1, %2, %0" : "+v"(acc[mf][g]) : "v"(tr ? a0 : b0), "v"(tr ? b0 : a0));
  };
  typedef __attribute__((address_space(3))) unsigned lds_u32;
  lds_u32* const cnt = (lds_u32*)(shm3 + CNT);
  bool cok;
  constexpr int NY = 28;
  int role, mt2, nt2;
  struct WS { unsigned vb[2][2]; const char* wb[2]; int sh[2]; };
  auto wsetup = [&](const u16* w, int crow0, int l, bool ck) __attribute__((always_inline)) {
    WS r;
    const unsigned vr = ((l >> 5) & 1) * 16 + (wm & 1) * 8 + ((l >> 3) & 3), vc = (((l >> 1) & 3) ^ ((l >> 4) & 1)) * 32 + (l & 1) * 16;
    u64 cp = (u64)(wcopy + (nt2 >> 2) * 589824 + crow0 * 128);
    asm volatile("" : "+s"(cp));
#pragma unroll
    for (int i = 0; i < 2; ++i) {
      u64 dp = (u64)(w + nt2 * 128 + i * 64);
      asm volatile("" : "+s"(dp));
      const bool slow = ck & ((((unsigned)dp >> 7) & 7) == 3);
      r.sh[i] = slow ? 7 : 11;
      r.wb[i] = (const char*)(slow ? cp : dp) + (crow0 ? (wm >> 1) * 32 << r.sh[i] : 0);
      r.vb[i][0] = (vr << r.sh[i]) + vc; r.vb[i][1] = ((vr + 4) << r.sh[i]) + (vc ^ 64);
    }
    asm volatile("" : "+v"(r.vb[0][0]), "+v"(r.vb[0][1]), "+v"(r.vb[1][0]), "+v"(r.vb[1][1]));
    PIN_S(r.wb[0]); PIN_S(r.wb[1]); PIN_S(r.sh[0]); PIN_S(r.sh[1]);
    return r;
  };
  WS wy, wz;
  int vxq, ws0, ws1;
  const char* sz;
  u32x4 xq[4];
  unsigned vg;
  int st = 3;
  const char *sa, *sw[2];
  auto wsel = [&]() {
    const unsigned r = __builtin_amdgcn_readlane(vg, st);
    sw[0] = wy.wb[0] + (r << wy.sh[0]); sw[1] = wy.wb[1] + (r << wy.sh[1]);
  };
  const int p = wid & 3;
  unsigned sl = 0;
#define G2_W "s_add_u32 m0, %[l], %[ow]\n\ts_nop 0\n\tglobal_load_lds_dwordx4 %[v0], %[w0] sc1\n\t" \
             "s_add_u32 m0, m0, 1024\n\ts_nop 0\n\tglobal_load_lds_dwordx4 %[v1], %[w0] sc1\n\t" \
             "s_add_u32 m0, m0, 7168\n\ts_nop 0\n\tglobal_load_lds_dwordx4 %[v2], %[w1] sc1\n\t" \
             "s_add_u32 m0, m0, 1024\n\ts_nop 0\n\tglobal_load_lds_dwordx4 %[v3], %[w1] sc1"
#define G2_WOP(w) [ow] "s"(shm_base + p * 2048), [w0] "s"(sw[0]), [w1] "s"(sw[1]), [v0] "v"(w.vb[0][0]), [v1] "v"(w.vb[0][1]), [v2] "v"(w.vb[1][0]), [v3] "v"(w.vb[1][1])
  auto tile2 = [&](int t0) __attribute__((always_inline)) {
    int b = bid;
    asm volatile("" : "+s"(b));
    role = 1 - ((b >> 2) & 1);
    mt2 = ((role ? b : b ^ 5) & 7) * 4 + ((b >> 3) & 3), nt2 = b >> 5;
    sa = (const char*)abuf + (size_t)(mt2 * 72 + role * NY + t0) * 16384 + wm * 4096;
    asm volatile("" : "+s"(sa));
  };
  auto prep = [&](bool first) __attribute__((always_inline)) {
    int l = threadIdx.x;
    asm volatile("" : "+v"(l));
    l &= 63;
    if (first) {
      tile2(3);
      vxq = ((mt2 * 128 + (wm >> 1) * 64 + role * 32 + 2 * (l & 15)) * D_ + nt2 * 128 + (wm & 1) * 64) * 2 + ((l >> 4) & 1) * 32 + (l >> 5) * 16;
      const int t = min(l < NY ? l : l - DLT, NY - 1), w = wm >> 1;
      const int o = (0xffaa5500fae504ull >> (2 * t)) & 3;
      int u1 = (0x990990 >> ((2 * t) & 31)) & 3, u2 = 3 + (t & 3);
      asm volatile("" : "+v"(u1), "+v"(u2));
      const int u = t < 12 ? u1 : u2;
      vg = (__umul24((l < NY ? role : 1 - role) * 4 + (((o & 1) << 1) | (o >> 1)), 14) + ((0xb85140a >> (4 * u)) & 15) + (u == 5 ? w : 2 * w)) * 32;
      asm volatile("" : "+v"(vg), "+v"(vxq));
      sz = (const char*)abuf + (size_t)(mt2 * 72 + 56) * 16384 + (wm >> 1) * 8192 + role * 4096 + (wm & 1) * 2048;
      PIN_S(sz);
    }
    wy = wsetup(wd, 0, l, first);
    wz = wsetup(wo, F_, l, first);
    ws0 = 64 << wz.sh[0]; PIN_S(ws0);
    ws1 = 64 << wz.sh[1]; PIN_S(ws1);
    SB0();
  };
  auto start2 = [&]() __attribute__((always_inline)) {
    const __amdgpu_buffer_rsrc_t rx = __builtin_amdgcn_make_buffer_rsrc((void*)x, 0, S_ * D_ * 2, 0x00020000);
#pragma unroll
    for (int f = 0; f < 4; ++f) xq[f] = __builtin_bit_cast(u32x4, __builtin_amdgcn_raw_buffer_load_b128(rx, vxq + (f >> 1) * 2048 + (f & 1) * 64, 0, 0));
    cok = __builtin_amdgcn_readfirstlane(cnt[1] & cnt[2]) != 0u;
    if (__builtin_expect(!cok, 0)) prep(false);
    wsel();
    PIN_S(sw[0]); PIN_S(sw[1]);
    SB0();
  };
  auto kloop = [&](auto wmc) {
    constexpr bool LD = decltype(wmc)::value != 0;
    constexpr int DMP = 2 * G1_STAGE;
    const char* pa = nullptr;
    unsigned vob = 0;
    f32x4 ca = {0.f, 0.f, 0.f, 0.f}, cb = {0.f, 0.f, 0.f, 0.f}, cd = ca;
    bf16x8 sel = {0, 0, 0, 0, 0, 0, 0, 0};
    int tro = 0;
    const int psh = (wid == 2 && !tya) ? 1 : 0, pstr = wid < 3 ? qstr : F_ * 2;
    const char* const pbase = wid == 0 ? qb : (wid == 1 ? qb + dp1 : (wid == 2 ? mb0 : gb));
    int lane = threadIdx.x;
    asm volatile("; %1" : "+v"(lane) : "n"(LD ? 1 : 0));
    lane &= 63;
    const int fr = lane & 15, fq = lane >> 4, q4 = fr >> 2, pp = lane & 3;
    const int ff = ((q4 >> 1) & 1) | ((fq & 1) << 1);
    int aoff[2], boff[2];
#pragma unroll
    for (int ks = 0; ks < 2; ++ks) aoff[ks] = (wm * 64 + fr) * 128 + (((ks * 4 + fq) ^ (fr & 7)) * 16);
#pragma unroll
    for (int j = 0; j < 2; ++j) boff[j] = 32768 + (8 * fq + q4) * 128 + (((2 * wc + j) ^ ff) * 32) + pp * 8;
    unsigned vol = lane * 16;
    float sm = 0.f, sq = 0.f;
    const char* pf = (const char*)wd + (bid * 4 + wid) * 7168;
    long pstep = 64 * pstr;
    if (!LD) {
      const int rs = 256 + mtile * 8 + (lane >> 3), vs = ((lane & 7) >> 1) ^ (((rs >> 1) & 1) | ((mtile & 1) << 1));
      pa = pbase + ((psh & vs) ? dgu : 0) + (rs * pstr + (vs >> psh) * 32 + (lane & 1) * 16);
      vob = rs * (F_ * 2) + vs * 32 + (lane & 1) * 16;
      tro = DMP + wid * 1024 + q4 * 128 + ((fq ^ ((q4 >> 1) | ((mtile & 1) << 1))) * 32) + pp * 8;
      const short one = fr == fq ? 0x3f80 : 0;
      sel = bf16x8{one, one, one, one, one, one, one, one};
      asm volatile("" : "+v"(tro), "+v"(vol), "+v"(sel), "+v"(ca), "+v"(cb), "+v"(sm), "+v"(sq));
      PIN_S(pf); PIN_S(pstep);
    }
    auto accum = [&](lds_cptr p, int ob) {
      ca = __builtin_amdgcn_mfma_f32_16x16x32_bf16(tr_frag(p), sel, ca, 0, 0, 0);
      if (wid == 0) cb = __builtin_amdgcn_mfma_f32_16x16x32_bf16(tr_frag(p + ob), sel, cb, 0, 0, 0);
    };
    auto xrow = [&](int i) {
      u64 a = (u64)(x + (size_t)srow * D_ + i * 256), b = (u64)(wo + (bid * 4 + wid) * 1024);
      PIN_S(a); PIN_S(b);
      asm volatile("global_load_dwordx4 %0, %1, %2" : "+v"(cd) : "v"(vol + (vol & (i < 4 ? 512 : 0)) * 3), "s"(i < 4 ? a : b) : "memory");
    };
    const int q32 = 32 * qstr;
    const char *sq0 = qb + q32, *sq1 = sq0 + dp1, *sg0 = gb + 32 * F_ * 2, *sg1 = sg0 + dgu, *sx = ab + (long)(kz + 128 - 32 * D_ * 2);
    u32x2 mp[2] = {__builtin_bit_cast(u32x2, mptr + q32), __builtin_bit_cast(u32x2, mptr + 2 * q32)};
    u64 cy[2];
    unsigned dst = shm_base + G1_STAGE + wm * 1024;
    int dbuf = G1_STAGE;
    auto stage = [&](int i0, int i1) __attribute__((always_inline)) {
#pragma unroll
        for (int i = i0; i < i1; ++i) {
          const int q = i >> 1, h = i & 1;
          const unsigned ld = dst + h * 4096 + (q < 5 ? 32768 + q * 8192 : (q - 5) * 8192);
          if (q < 2) glds16_s((q ? sq1 : sq0) + h * q32, voq, ld);
          else if (q == 2) glds16_v(__builtin_bit_cast(const char*, mp[h]), ld);
          else if (q < 5) glds16_s((q == 4 ? sg1 : sg0) + h * 32 * F_ * 2, vog, ld);
          else glds16_s(sx + ((q - 5) * 64 + h * 32) * D_ * 2, voa, ld);
        }
    };
    u64 cy0 = 0, rt0 = 0;
#pragma unroll
    for (int a = 0; a < 4; ++a) { acc[a][9] = f32x4{0.f, 0.f, 0.f, 0.f}; asm volatile("" : "+v"(acc[a][9])); }
    if (!LD) {
      xrow(0);
      WAIT_V(1);
    }
    asm volatile("" : "+v"(aoff[0]), "+v"(aoff[1]), "+v"(boff[0]), "+v"(boff[1]));
    bf16x8 kp[2];
    auto body = [&](bool upd, bool keep = false, bool first = false) __attribute__((always_inline)) {
      lds_cptr sb = shm3;
      auto rdb = [&](int g, int i) {
        lds_cptr p = sb + boff[g & 1] + (g >> 1) * 8192 + (i >> 1) * 4096;
        const int s = ((g + 1) & 1) * 2 + (i >> 1);
        if (i & 1) bh[s] = tr_read(p + 512); else bl[s] = tr_read(p);
      };
      auto rda = [&](int ks, int mf) { (ks ? af1 : af0)[mf] = *(const __attribute__((address_space(3))) bf16x8*)(sb + aoff[ks] + mf * 2048); };
      SB0();
      __builtin_amdgcn_s_waitcnt(0xC07F);
#pragma unroll
      for (int gg = 0; gg < 10; ++gg) {
        const int g = (gg + 9) % 10;
#pragma unroll
        for (int t = 0; t < 8; ++t) {
          const bool two = t >= 4 && !(upd && g >= 5 && g <= 8);
          SB0();
          if ((!two || (t & 1)) && !(first && g == 9)) mm(g, two ? t - 1 : t, two, first);
          SB0();
          if (first && !LD && (g == 3 || g == 7) && t == 0) BAR();
          if (g == 9) {
            if (t == 0 || t == 2) { rdb(0, t); rdb(0, t + 1); }
            if (t & 1) { rda((t >> 1) & 1, t >> 1); rda(((t >> 1) & 1) ^ 1, t >> 1); }
          } else if (t < 4) rdb(g + 1, t);
          else if (keep && g == 7) {
            if (t == 5) { WAIT_V(0); kp[0] = tr_frag(shm3 + tro); }
            if (t == 7) kp[1] = tr_frag(shm3 + tro + 4096);
          } else if (upd) {
            if (g == 5 && t == 4) { sq0 += 2 * q32; PIN_S(sq0); }
            if (g == 5 && t == 5) { sq1 += 2 * q32; PIN_S(sq1); }
            if (g == 5 && t == 6) { sg0 += 64 * F_ * 2; PIN_S(sg0); }
            if (g == 6 && t == 4) { sg1 += 64 * F_ * 2; PIN_S(sg1); }
            if (g == 6 && t == 5) { sx += 128; PIN_S(sx); }
            if (g == 6 && t == 6) { dst -= dbuf; PIN_S(dst); }
            if (g == 7 && t == 4) asm volatile("v_add_co_u32 %0, %2, %4, %0\n\tv_add_co_u32 %1, %3, %4, %1" : "+v"(mp[0][0]), "+v"(mp[1][0]), "=&s"(cy[0]), "=&s"(cy[1]) : "s"(2 * q32));
            if (g == 7 && t == 6) asm volatile("v_addc_co_u32 %0, vcc, 0, %0, %2\n\tv_addc_co_u32 %1, vcc, 0, %1, %3" : "+v"(mp[0][1]), "+v"(mp[1][1]) : "s"(cy[0]), "s"(cy[1]) : "vcc");
            if (g == 8 && t == 4) { aoff[0] += dbuf; aoff[1] += dbuf; asm volatile("" : "+v"(aoff[0]), "+v"(aoff[1])); }
            if (g == 8 && t == 5) { boff[0] += dbuf; boff[1] += dbuf; asm volatile("" : "+v"(boff[0]), "+v"(boff[1])); }
            if (g == 8 && t == 6) { dbuf = -dbuf; PIN_S(dbuf); }
          }
        }
      }
      SB0();
    };
    TS1(0);
#define ARRP(o, c) do {} while (0)
    if (LD) {
      stage(0, PA);
      WAIT_V(6 + PA);
      TS1(5);
      BARL();
      stage(PA, PB);
      WAIT_V(2 + PB);
      BAR();
      stage(PB, PC);
      WAIT_V(PC);
      BAR();
      stage(PC, 18);
      body(true, false, true);
      WAIT_V(0);
      BARL();
#pragma clang loop unroll(disable)
      for (int kt = 1; kt < NT - 1; ++kt) {
        if (kt == 1) TS(5);
        if (__builtin_expect(kt == 5 + wid, 0)) prep(true);
        stage(0, 18);
        body(true);
        WAIT_V(0);
        BARL();
        SB0();
      }
      start2();
      body(false);
    } else {
      char* wsx;
      auto tile = [&](int kt, bool keep) __attribute__((always_inline)) {
        SB0();
        ARRP(wid * 64 + kt * 4, true);
        BARL();
        body(false, keep);
        ARRP(320 + kt * 4 + 4, wid == 0);
        if (keep) asm volatile("" : "+v"(kp[0]), "+v"(kp[1])); else WAIT_V(0);
      };
      auto next = [&]() __attribute__((always_inline)) {
        aoff[0] += dbuf; aoff[1] += dbuf; boff[0] += dbuf; boff[1] += dbuf;
        dbuf = -dbuf;
        asm volatile("" : "+v"(aoff[0]), "+v"(aoff[1]), "+v"(boff[0]), "+v"(boff[1]), "+v"(vol), "+v"(tro) :: "memory");
        wsx = ws;
        PIN_S(wsx);
      };
      auto share = [&]() {
        glds16_v(pa, shm_base + DMP + wid * 1024);
        if (wid == 0) glds16_s(gb + dgu, vob, shm_base + DMP + 4096);
      };
      auto kept = [&]() {
        ca = __builtin_amdgcn_mfma_f32_16x16x32_bf16(kp[0], sel, ca, 0, 0, 0);
        if (wid == 0) cb = __builtin_amdgcn_mfma_f32_16x16x32_bf16(kp[1], sel, cb, 0, 0, 0);
      };
      auto cpub = [&](u32x4 a, u32x4 b) {
        if ((vol & 0xc0) == 0) {
          const char* const cs = wsx + 101 * MB_ + ntile * 10240 + mtile * 256;
          const unsigned co = (vol & 0x30) * 4 + (vol >> 8) * 16;
          gst16(cs + wid * 2048, co, a);
          if (wid == 0) gst16(cs + 8192, co, b);
        }
      };
      auto side0 = [&](int kt) __attribute__((always_inline)) {
        if (kt < 2) {
          glds16_v(pa - 2 * pstep, shm_base + DMP + 7168);
          if (wid == 0) glds16_s(gb + dgu, vob - 128 * F_ * 2, shm_base + DMP + 7168);
        }
        share();
        u32x4 xw;
        asm volatile("v_mov_b32 %0, %4\n\tv_mov_b32 %1, %5\n\tv_mov_b32 %2, %6\n\tv_mov_b32 %3, %7" : "=&v"(xw[0]), "=&v"(xw[1]), "=&v"(xw[2]), "=&v"(xw[3])
                     : "v"(cd[0]), "v"(cd[1]), "v"(cd[2]), "v"(cd[3]));
        xrow(kt + 1);
        next();
        if (kt) kept();
        pa += pstep; vob += 64 * F_ * 2;
        accum(shm3 + tro + ((kt & 1) * G1_STAGE + 32768 + mtile * 1024 + wid * 7168 - DMP), 4 * 8192);
#pragma unroll
        for (int i = 0; i < 4; ++i) {
          float lo, hi;
          asm volatile("v_lshlrev_b32 %2, 16, %4\n\tv_and_b32 %3, 0xffff0000, %4\n\tv_add_f32 %0, %0, %2\n\tv_fmac_f32 %1, %2, %2\n\t"
                       "v_add_f32 %0, %0, %3\n\tv_fmac_f32 %1, %3, %3" : "+v"(sm), "+v"(sq), "=&v"(lo), "=&v"(hi) : "v"(xw[i]));
        }
      };
      BARL();
      body(false, false, true);
      WAIT_V(0);
      side0(0);
#pragma clang loop unroll(disable)
      for (int kt = 1; kt < 4; ++kt) {
        tile(kt, true);
        side0(kt);
      }
#pragma clang loop unroll(disable)
      for (int kt = 4; kt < 12; ++kt) {
        tile(kt, true);
        share();
        glds16_s(pf, vol, shm_base + DMP + 7168);
        next();
        kept();
        pa += pstep; vob += 64 * F_ * 2;
        pf = kt == 10 ? (const char*)wo + (bid * 4 + wid) * 2048 + 1024 : pf + 1024;
        if (kt < 8) {
          const unsigned q1 = bid * 4 + wid + (kt - 5) * 1024, q0 = q1 - 1024;
          if (q0 < 1152) gst16(wsx + 98 * MB_ + (q0 & 1) * 589824 + (q0 >> 1) * 1024, vol, *(const u32x4*)(shm + AUX + wid * 1024 + vol));
          if (q1 < 1152) {
            const char* src = (const char*)((q1 >> 1) < 448 ? wd + (size_t)(q1 >> 1) * 8 * D_ : wo + (size_t)((q1 >> 1) - 448) * 8 * D_);
            src += ((3 - ((unsigned)(uintptr_t)src >> 7)) & 7) * 128 + (q1 & 1) * 1024;
            glds16_s(src, (vol >> 7) * 2048 + (vol & 0x70), shm_base + AUX + wid * 1024);
          }
          u32x4 bk;
          asm volatile("v_mov_b32 %0, %1" : "=v"(bk[0]) : "s"(BLANK));
          bk[1] = bk[0]; bk[2] = bk[0]; bk[3] = bk[0];
          gst16(wsx + 64 * MB_ + bid * 16384 + wid * 4096 + (kt - 4) * 1024, vol, bk);
          if (tya && (kt == 4 || kt == 7)) gst16(wsx + 4 * MB_ + (mtile * 32 + ntile) * 4096 + wid * 2048 + (kt & 1) * 1024, vol, bk);
          if (kt == 4) cpub(bk, bk);
        } else {
          if (kt == 8) {
            const float ml = sm * (1.0f / 32), mean = half_sum(sm) * (1.0f / D_), dm = ml - mean;
            const float q = __builtin_fmaf(dm * 32.0f, dm, __builtin_fmaf(-ml, sm, sq));
            const unsigned ps = pack_stats(mean, fmaxf(half_sum(q) * (1.0f / D_), 0.f));
            const u64 now = __builtin_amdgcn_s_memrealtime();
            if ((vol & 0x1f0) == 0) gst16(wsx + 97 * MB_ + srow * 16, vol >> 5, u32x4{ps, (unsigned)now, ps, (unsigned)(now >> 32)});
          }
          if (kt == 9) {
            if (wid == 0) {
              u64 now;
              asm volatile("s_memrealtime %0\n\ts_waitcnt lgkmcnt(0)" : "=s"(now) :: "memory");
              if (vol == 0) asm volatile("global_store_dwordx2 %0, %1, %2 sc1" :: "v"(vol), "v"(now), "s"(wsx + 100 * MB_ + 131072 + bid * 8) : "memory");
            }
            if (wid == 2) glds16_s1(wsx + 102 * MB_ + (bid & ~1) * 512, vol, shm_base + DMP + 7168);
          }
        }
      }
      asm volatile("" :: "v"(cd));
      if (wid >= 2) glds16_s1(wsx + 100 * MB_ + 131072 + (wid - 2) * 1024, vol, shm_base + DMP + 3072 + wid * 1024);
      tile(12, true);
      next();
      kept();
      const int lane = vol >> 4;
      glds16_s1(wsx + 97 * MB_ + (m0 + wid * 64) * 16, ((lane & 32) + 2 * (lane & 15) + ((lane >> 4) & 1)) * 16, shm_base + AUX + wid * 1024);
      asm volatile("s_nop 7\n\ts_nop 3" : "+v"(ca), "+v"(cb));
      cpub(__builtin_bit_cast(u32x4, ca), __builtin_bit_cast(u32x4, cb));
      if (wid >= 2) {
        u32x4 c4 = *(const u32x4*)(shm + DMP + 3072 + wid * 1024 + vol);
        bool ok = __all(fresh(((u64)c4[1] << 32) | c4[0], t_own) & fresh(((u64)c4[3] << 32) | c4[2], t_own));
        while (__builtin_expect(!ok, 0) && __builtin_amdgcn_s_memrealtime() - t_own < TIMEOUT_ / 10) {
          const __amdgpu_buffer_rsrc_t rs = __builtin_amdgcn_make_buffer_rsrc((void*)(wsx + 100 * MB_ + 131072), 0, 2048, 0x00020000);
          asm volatile("" ::: "memory");
          c4 = __builtin_bit_cast(u32x4, __builtin_amdgcn_raw_buffer_load_b128(rs, (wid - 2) * 1024 + vol, 0, 16));
          ok = __all(fresh(((u64)c4[1] << 32) | c4[0], t_own) & fresh(((u64)c4[3] << 32) | c4[2], t_own));
        }
        *(unsigned*)(shm + CNT - 4 + wid * 4) = ok ? 1u : 0u;
      }
      tile(13, false);
      next();
      u32x4 cw[4];
      auto cgather = [&]() {
        const __amdgpu_buffer_rsrc_t rc = __builtin_amdgcn_make_buffer_rsrc((void*)(wsx + 101 * MB_ + ntile * 10240), 0, 5 * 2048, 0x00020000);
        const unsigned vcs = (vol < 512 ? wid * 2048 : 8192) + (vol & 256) * 4 + (vol & 0xf0);
#pragma unroll
        for (int i = 0; i < 4; ++i) cw[i] = __builtin_bit_cast(u32x4, __builtin_amdgcn_raw_buffer_load_b128(rc, vcs + i * 256, 0, 16));
      };
      {
        const int ln = wid * 64 + (vol >> 4);
        u32x4 gr = *(const u32x4*)(shm + AUX + ln * 16);
        if (__builtin_expect(!__all((gr[0] == gr[2]) & fresh(((u64)gr[3] << 32) | gr[1], t_own)), 0)) {
          const __amdgpu_buffer_rsrc_t rs = __builtin_amdgcn_make_buffer_rsrc((void*)(wsx + 97 * MB_ + m0 * 16), 0, 4096, 0x00020000);
          const int prow = (ln & 0xe0) + 2 * (ln & 15) + ((ln >> 4) & 1);
          for (;;) {
            gr = __builtin_bit_cast(u32x4, __builtin_amdgcn_raw_buffer_load_b128(rs, prow * 16, 0, 16));
            if (__all((gr[0] == gr[2]) & fresh(((u64)gr[3] << 32) | gr[1], t_own))) break;
            if (__builtin_amdgcn_s_memrealtime() - t_own > TIMEOUT_) { gr[0] = row_stats_serial(x + (size_t)(m0 + prow) * D_); break; }
            __builtin_amdgcn_s_sleep(2);
          }
        }
        cgather();
        const float nm = -lo_f(gr[0]), dn = hi_f(gr[0]), r = __builtin_amdgcn_rcpf(dn);
        auto mul = [](float a, float b) { float c; asm("v_mul_f32 %0, %1, %2" : "=v"(c) : "v"(a), "v"(b)); return c; };
        const f32x4 fc = {nm, -2.3022081986f * r, mul(mul(mul(-0.10294324f, r), r), r), mul(dn, dn)};
        *(f32x4*)(shm + AUX + ln * 16) = fc;
        float* const tw = (float*)(shm + 2 * G1_STAGE + 4096 + wid * 768) + ln;
#pragma unroll
        for (int i = 0; i < 4; ++i) tw[i * 64] = fc[i];
        ((float*)(shm + AUX + 4096))[384 + ln] = r;
        cok = __builtin_amdgcn_readfirstlane(cnt[1] & cnt[2]) != 0u;
        tile2(0);
        wy = wsetup(wd, 0, vol >> 4, cok);
      }
      tile(14, false);
      next();
      {
        while (__builtin_expect(blank(cw) && cok, 0)) {
          __builtin_amdgcn_s_sleep(2);
          asm volatile("" ::: "memory");
          cgather();
        }
        f32x4 t;
#pragma unroll
        for (int i = 0; i < 4; ++i) {
          float a, b;
          asm("v_add_f32 %0, %2, %3\n\tv_add_f32 %1, %4, %5\n\tv_add_f32 %0, %0, %1" : "=&v"(a), "=&v"(b) : "v"(cw[0][i]), "v"(cw[1][i]), "v"(cw[2][i]), "v"(cw[3][i]));
          const auto r2 = __builtin_amdgcn_permlane16_swap(__float_as_uint(a), __float_as_uint(a), false, false);
          asm("v_add_f32 %0, %1, %2" : "=v"(t[i]) : "v"(r2[0]), "v"(r2[1]));
        }
        const int l = vol >> 4;
        if (__builtin_expect(!cok, 0)) {
          const int gq = (l >> 2) & 3;
          const char* pc = l < 32 ? pbase + ((psh & gq) ? dgu : 0) + (gq >> psh) * 32 + (l & 3) * 8 : gb + dgu + (l & 15) * 8;
          t = f32x4{0.f, 0.f, 0.f, 0.f};
#pragma unroll 1
          for (int k = 0; k < D_; ++k) { const u32x2 w = *(const __attribute__((address_space(1))) u32x2*)(pc + (size_t)k * (l < 32 ? pstr : F_ * 2)); t += f32x4{lo_f(w[0]), hi_f(w[0]), lo_f(w[1]), hi_f(w[1])}; }
        }
        if (l < 32 || wid == 0)
          *(f32x4*)((float*)(shm + AUX + 4096) + ((l >> 3) & 1) * 192 + (l < 32 ? wid : 4) * 32 + (l & 7) * 4) = t;
        __builtin_amdgcn_s_setprio(2);
      }
      SB0();
      ARRP(wid * 64 + 60, true);
      BARL();
      body(false);
    }
#pragma unroll
    for (int t = 0; t < 8; t += 2) mm(9, t, true);
    asm volatile("s_nop 7\n\ts_nop 3");
    SB0();
    TS1(3);
    if (!LD) {
#pragma unroll
      for (int i = 0; i < 3; ++i) {
        const unsigned r = ((role * 4 + (i & 1) * 2) * 14 + (i < 2 ? 10 : 0) + (wm & 2)) * 32;
        sw[0] = wy.wb[0] + (r << wy.sh[0]); sw[1] = wy.wb[1] + (r << wy.sh[1]);
        asm volatile(G2_W :: [l] "s"((i + 1) * 16384), G2_WOP(wy) : "memory", "scc", "m0");
      }
    } else {
      __builtin_amdgcn_s_setprio(3);
      WAIT_V(0);
    }
  };
  if (wid >= 4) kloop(std::integral_constant<int, 1>{});
  else kloop(std::integral_constant<int, 0>{});
  __builtin_amdgcn_sched_barrier(0);
  int tz = threadIdx.x;
  asm volatile("" : "+v"(tz));
  __builtin_amdgcn_sched_barrier(0);
  const int li = tz & 15, lq = (tz >> 4) & 3, ln = tz & 63;
  const __amdgpu_buffer_rsrc_t ra = __builtin_amdgcn_make_buffer_rsrc((void*)abuf, 0, 16 * 72 * 16384, 0x00020000);
  float* const cfin = (float*)(shm + AUX + 4096);
  float* const svr = cfin + 384;
  float* const tb = (float*)(shm + 2 * G1_STAGE + 4096 + wm * 1024);
  auto signal = [&](unsigned v) {
    unsigned a, b;
    asm volatile("v_mov_b32 %0, %2\n\tv_mov_b32 %1, %3\n\ts_mov_b64 exec, 1\n\tds_add_u32 %0, %1\n\ts_mov_b64 exec, -1"
                 : "=&v"(a), "=&v"(b) : "s"(shm_base + CNT), "s"(v) : "memory");
  };
  auto await = [&](unsigned mask, unsigned val) {
    unsigned a, t;
    asm volatile("v_mov_b32 %0, %2\n1:\n\tds_read_b32 %1, %0\n\ts_waitcnt lgkmcnt(0)\n\tv_readfirstlane_b32 vcc_lo, %1\n\ts_and_b32 vcc_lo, vcc_lo, %3\n\t"
                 "s_cmp_eq_u32 vcc_lo, %4\n\ts_cbranch_scc1 2f\n\ts_sleep 1\n\ts_branch 1b\n2:" : "=&v"(a), "=&v"(t) : "s"(shm_base + CNT), "s"(mask), "s"(val) : "memory", "scc", "vcc");
  };
  auto client = [&](int l) { const int m128 = mtile * 2 + ((l >> 4) & 1); return (((l & 7) * 4 + (m128 & 3)) * 8) + ((m128 >> 2) ^ ((l & 8) ? 5 : 0)); };
  auto notify = [&](int l0, int nl) __attribute__((always_inline)) {
    u64 now;
    asm volatile("s_memrealtime %0\n\ts_waitcnt lgkmcnt(0)" : "=s"(now) :: "memory");
    int l = threadIdx.x;
    asm volatile("" : "+v"(l));
    l &= 63;
    const __amdgpu_buffer_rsrc_t rd1 = __builtin_amdgcn_make_buffer_rsrc((void*)(done1 + ntile), 0, 256 * 512, 0x00020000);
    if (l < nl) __builtin_amdgcn_raw_buffer_store_b64(u32x2{(unsigned)now, (unsigned)(now >> 32)}, rd1, client(l0 + l) * 512, 0, 16);
  };
  const int xo = ((xcd & 1) << 1) | ((xcd >> 1) & 1), te = (xcd >> 2) * 28 + 6 * (xo >> 1) + (xo & 1), tl = (xcd >> 2) * 28 + 12 + 4 * xo;
  const u64 te0 = __builtin_amdgcn_s_memrealtime();
  const __amdgpu_buffer_rsrc_t re = __builtin_amdgcn_make_buffer_rsrc((void*)(done1 + client((wm >> 1) * 16 + 8 - (xcd & 4) * 2) * 64 + 32 + (ntile & 15) * 2 + (wm & 1)),
                                                                      0, 7 * 16384 + 8, 0x00020000);
  auto early = [&]() {
    __builtin_amdgcn_raw_buffer_store_b64(u32x2{(unsigned)te0, (unsigned)(te0 >> 32)}, re, ln * 16384, 0, 16);
  };
  signal(1u << (8 * wc));
  if (wc) {
    await(0xffffu, 0x404u);
#pragma unroll
    for (int f = 0; f < 4; ++f) *(u32x4*)(shm + G2_X + wm * 4096 + f * 1024 + ln * 16) = xq[f];
    SB0();
    __builtin_amdgcn_s_setprio(1);
  }
  TS1(4);
  f32x4 rf[4];
#pragma unroll
  for (int mf = 0; mf < 4; ++mf) rf[mf] = *(const f32x4*)(shm + AUX + (wm * 64 + mf * 16 + li) * 16);
  const float* const cl = cfin + wc * 192 + lq * 4;
  const int sbase = ((mtile * 2 + (wm >> 1)) * 72 * 8 + (wm & 1) * 4) * 2048;
  auto putf = [&](int kg, int mf, u32x4 v) {
    __builtin_amdgcn_raw_buffer_store_b128(v, ra, ln * 16 + sbase + ((kg >> 1) * 8 + mf) * 2048 + (kg & 1) * 1024, 0, 16);
  };
  auto gelp = [&](f32x2 g, f32x2 u, f32x2 cg, f32x2 cu, f32x2 m2, f32x2 k12, f32x2 k22, f32x2 d2) {
    const f32x2 tg = __builtin_elementwise_fma(m2, cg, g), tu = __builtin_elementwise_fma(m2, cu, u);
    const f32x2 arg = tg * __builtin_elementwise_fma(tg * tg, k22, k12);
    const f32x2 dn = __builtin_elementwise_fma(f32x2{__builtin_amdgcn_exp2f(arg[0]), __builtin_amdgcn_exp2f(arg[1])}, d2, d2);
    const f32x2 r = (tg * tu) * f32x2{__builtin_amdgcn_rcpf(dn[0]), __builtin_amdgcn_rcpf(dn[1])};
    return pack2(r[0], r[1]);
  };
  auto gel2 = [&](int p, int kg, int mf0 = 0, int mf1 = 4) {
    f32x4 cg[2], cu[2];
#pragma unroll
    for (int j = 0; j < 2; ++j) { cg[j] = *(const f32x4*)(cl + p * 32 + j * 16); cu[j] = *(const f32x4*)(cl + p * 32 + 32 + j * 16); }
#pragma unroll
    for (int mf = mf0; mf < mf1; ++mf) {
      const f32x2 m2 = {rf[mf][0], rf[mf][0]}, k12 = {rf[mf][1], rf[mf][1]}, k22 = {rf[mf][2], rf[mf][2]}, d2 = {rf[mf][3], rf[mf][3]};
      u32x4 o;
#pragma unroll
      for (int i = 0; i < 4; ++i) {
        const int j = i >> 1, e = (i & 1) * 2;
        const f32x4 g = acc[mf][p * 2 + j], u = acc[mf][p * 2 + 2 + j];
        o[i] = gelp(f32x2{g[e], g[e + 1]}, f32x2{u[e], u[e + 1]}, f32x2{cg[j][e], cg[j][e + 1]}, f32x2{cu[j][e], cu[j][e + 1]}, m2, k12, k22, d2);
      }
      putf(kg, mf, o);
    }
  };
  int tsel = ntile & 1;
  asm volatile("" : "+s"(tsel));
  if (tsel != 0) {
    const int tq = (wc ? tl : te + 2 + (xo & 1)) + he;
    gel2(0, 2 * tq);
    gel2(3, 2 * tq + 1);
    const float cg = cfin[wc * 192 + 64 + li], cu = cfin[wc * 192 + 80 + li];
#pragma unroll
    for (int mf = 0; mf < 4; ++mf) {
      f32x4 t[4];
#pragma unroll
      for (int i = 0; i < 4; ++i) t[i] = *(const f32x4*)(tb + i * 64 + mf * 16 + lq * 4);
      const f32x4 g = acc[mf][4], u = acc[mf][5];
      u32x2 o;
#pragma unroll
      for (int e = 0; e < 4; e += 2)
        o[e >> 1] = gelp(f32x2{g[e], g[e + 1]}, f32x2{u[e], u[e + 1]}, f32x2{cg, cg}, f32x2{cu, cu}, f32x2{t[0][e], t[0][e + 1]},
                         f32x2{t[1][e], t[1][e + 1]}, f32x2{t[2][e], t[2][e + 1]}, f32x2{t[3][e], t[3][e + 1]});
      *(u32x2*)(shm + wm * 4096 + mf * 1024 + wc * 512 + li * 32 + lq * 8) = o;
      if (mf == EPOS && wc == 0) {
        TS1(1);
        WAIT_V(0);
        TS1(6);
        early();
      }
    }
    if (wc == 0) {
      signal(0x10000u << wid);
    } else {
      await(0x1000u << wid, 0x1000u << wid);
#pragma unroll
      for (int mf = 0; mf < 4; ++mf) putf(2 * (tl + 2) + he, mf, __builtin_bit_cast(u32x4, tr_frag(shm3 + wm * 4096 + mf * 1024 + ln * 8)));
    }
  } else {
    float rd[4];
#pragma unroll
    for (int mf = 0; mf < 4; ++mf) rd[mf] = svr[wm * 64 + mf * 16 + li];
    auto cvq = [&](int p, int mf, const f32x4& c0, const f32x4& c1) {
      u32x4 o;
#pragma unroll
      for (int j = 0; j < 2; ++j) {
        const f32x4 a = acc[mf][p * 2 + j], c4 = j ? c1 : c0;
        const f32x2 m2 = {rf[mf][0], rf[mf][0]}, r2 = {rd[mf], rd[mf]};
        f32x2 t0 = __builtin_elementwise_fma(m2, f32x2{c4[0], c4[1]}, f32x2{a[0], a[1]});
        f32x2 t1 = __builtin_elementwise_fma(m2, f32x2{c4[2], c4[3]}, f32x2{a[2], a[3]});
        if (p) { t0 *= r2; t1 *= r2; }
        o[2 * j] = pack2(t0[0], t0[1]); o[2 * j + 1] = pack2(t1[0], t1[1]);
      }
      return o;
    };
    const float cv0 = cfin[wc * 192 + 64 + li], cv1 = cfin[wc * 192 + 80 + li];
    auto cvv = [&](int mf) {
      const f32x4 n4 = *(const f32x4*)(tb + mf * 16 + lq * 4), r4 = *(const f32x4*)(svr + wm * 64 + mf * 16 + lq * 4);
      u32x4 o;
#pragma unroll
      for (int j = 0; j < 2; ++j) {
        const f32x4 a = acc[mf][4 + j];
        const f32x2 c2 = {j ? cv1 : cv0, j ? cv1 : cv0};
        const f32x2 t0 = __builtin_elementwise_fma(f32x2{n4[0], n4[1]}, c2, f32x2{a[0], a[1]}) * f32x2{r4[0], r4[1]};
        const f32x2 t1 = __builtin_elementwise_fma(f32x2{n4[2], n4[3]}, c2, f32x2{a[2], a[3]}) * f32x2{r4[2], r4[3]};
        o[2 * j] = pack2(t0[0], t0[1]); o[2 * j + 1] = pack2(t1[0], t1[1]);
      }
      return o;
    };
    u32x4 kx[4], vx[4], qx[4], hx[4];
    const bf16x4 ones = {0x3f80, 0x3f80, 0x3f80, 0x3f80};
    const f32x4 ck0 = *(const f32x4*)(cl + 32), ck1 = *(const f32x4*)(cl + 48);
    kx[2] = cvq(1, 2, ck0, ck1); kx[3] = cvq(1, 3, ck0, ck1);
    vx[2] = cvv(2); vx[3] = cvv(3);
    auto exo = [](int e) { return e * 4096 + (e >> 2) * (G2_A - 16384); };
    const bool needh = wm == 0 && mtile > 0;
    const __amdgpu_buffer_rsrc_t rh = __builtin_amdgcn_make_buffer_rsrc((void*)((char*)halo + ((needh ? mtile - 1 : mtile) * 32 + ntile + wc) * 4096), 0,
                                                                        needh || wm == 3 ? 4096 : 0, 0x00020000);
    if (wm < 3) {
      char* const ex = shm + exo(wc * 3 + wm) + ln * 16;
      *(u32x4*)ex = kx[2]; *(u32x4*)(ex + 1024) = kx[3]; *(u32x4*)(ex + 2048) = vx[2]; *(u32x4*)(ex + 3072) = vx[3];
      signal(0x10000u << wid);
    } else {
#pragma unroll
      for (int r = 0; r < 4; ++r) __builtin_amdgcn_raw_buffer_store_b128(r < 2 ? kx[2 + r] : vx[r], rh, ln * 16 + r * 1024, 0, 16);
    }
    auto hload = [&]() {
#pragma unroll
      for (int r = 0; r < 4; ++r) hx[r] = __builtin_bit_cast(u32x4, __builtin_amdgcn_raw_buffer_load_b128(rh, ln * 16 + r * 1024, 0, 16));
    };
    SB0();
    gel2(3, 2 * (wc ? tl + 3 : te) + he);
    SB0();
    if (wm == 0) hload();
    SB0();
    kx[0] = cvq(1, 0, ck0, ck1); kx[1] = cvq(1, 1, ck0, ck1);
    vx[0] = cvv(0); vx[1] = cvv(1);
    {
      const f32x4 cq0 = *(const f32x4*)(cl), cq1 = *(const f32x4*)(cl + 16);
#pragma unroll
      for (int mf = 0; mf < 4; ++mf) qx[mf] = cvq(0, mf, cq0, cq1);
    }
    f32x4 mk0, mk1;
    u32x2 pm;
#pragma unroll
    for (int j = 0; j < 4; ++j) {
      const int d = 4 * lq + j - li;
      mk0[j] = d >= 9 ? 0.f : -1.0e30f;
      mk1[j] = (unsigned)(d + 7) <= 7u ? 0.f : -1.0e30f;
    }
#pragma unroll
    for (int j = 0; j < 2; ++j) pm[j] = (4 * lq + 2 * j > li ? 0xffffu : 0u) | (4 * lq + 2 * j + 1 > li ? 0xffff0000u : 0u);
    auto max3 = [](float a, float b, float c) { float r; asm("v_max3_f32 %0, %1, %2, %3" : "=v"(r) : "v"(a), "v"(b), "v"(c)); return r; };
    auto max2 = [](float a, float b) { float r; asm("v_max_f32 %0, %1, %2" : "=v"(r) : "v"(a), "v"(b)); return r; };
    asm volatile("" : "+v"(kx[0]), "+v"(kx[1]), "+v"(vx[0]), "+v"(vx[1]), "+v"(qx[0]), "+v"(qx[1]), "+v"(qx[2]), "+v"(qx[3]), "+v"(mk0), "+v"(mk1), "+v"(pm));
    TS1(1);
    if (wc == 0) {
      if (wm == 0) WAIT_V(4); else WAIT_V(0);
      early();
    }
    if (wm > 0) {
      await(0x8000u << wid, 0x8000u << wid);
#pragma unroll
      for (int i = 0; i < 4; ++i) hx[i] = *(const u32x4*)(shm + exo(wc * 3 + wm - 1) + i * 1024 + ln * 16);
    } else if (__builtin_expect(cok || !needh, 1)) {
      while (__builtin_expect(blank(hx), 0)) {
        __builtin_amdgcn_s_sleep(1);
        asm volatile("" ::: "memory");
        hload();
      }
    } else {
      await(0xffffu, 0x404u);
      char* const fb = shm + exo(6 + wc) + ln * 16;
#pragma unroll 1
      for (int i = 0; i < 32; ++i) {
        const int rec = i >> 3, e = i & 7;
        const int row = m0 - 32 + 2 * (rec < 2 ? li : lq * 4 + (e & 3)) + (rec & 1);
        const int col = (rec < 2 ? D_ + lq * 4 + (e & 3) : 2 * D_ + li) + (ntile + wc) * 32 + (e >> 2) * 16;
        const u16* xr = x + (size_t)row * D_;
        const unsigned stt = row_stats_serial(xr);
        const float mb = lo_f(stt);
        float a = 0.f;
#pragma unroll 1
        for (int k = 0; k < D_; ++k) a += (bf2f(xr[k]) - mb) * bf2f(wqkv[(size_t)k * 3072 + col]);
        *(u16*)(fb + rec * 1024 + e * 2) = f2bf(a / hi_f(stt));
      }
#pragma unroll
      for (int i = 0; i < 4; ++i) hx[i] = *(const u32x4*)(fb + i * 1024);
    }
    TS1(6);
    {
      const f32x4 z = {0.f, 0.f, 0.f, 0.f};
      const float mz = mtile == 0 && wm == 0 ? -1.0e30f : 0.f;
      const f32x4 mkf = mk0 + f32x4{mz, mz, mz, mz};
      f32x4 s0[4], s1[4];
#pragma unroll
      for (int i = 0; i < 4; ++i) {
        const int n = i ^ 2;
        const bf16x8 qf = __builtin_bit_cast(bf16x8, qx[n]);
        s1[n] = __builtin_amdgcn_mfma_f32_16x16x32_bf16(__builtin_bit_cast(bf16x8, kx[n]), qf, mk1, 0, 0, 0);
        s0[n] = __builtin_amdgcn_mfma_f32_16x16x32_bf16(__builtin_bit_cast(bf16x8, n < 2 ? hx[n] : kx[n - 2]), qf, n < 2 ? mkf : mk0, 0, 0, 0);
      }
      SB0();
      asm volatile("s_nop 7\n\ts_nop 3" : "+v"(s0[0]), "+v"(s1[0]), "+v"(s0[1]), "+v"(s1[1]), "+v"(s0[2]), "+v"(s1[2]), "+v"(s0[3]), "+v"(s1[3]));
      float mx[4];
#pragma unroll
      for (int n = 0; n < 4; ++n)
#pragma unroll
        for (int j = 0; j < 4; ++j) s0[n][j] = max2(s0[n][j], s1[n][j]);
#pragma unroll
      for (int n = 0; n < 4; ++n) mx[n] = max3(s0[n][0], s0[n][1], s0[n][2]);
#pragma unroll
      for (int n = 0; n < 4; ++n) mx[n] = max2(mx[n], s0[n][3]);
#pragma unroll
      for (int n = 0; n < 4; ++n) { auto r = __builtin_amdgcn_permlane16_swap(__float_as_uint(mx[n]), __float_as_uint(mx[n]), false, false);
        mx[n] = max2(__uint_as_float(r[0]), __uint_as_float(r[1])); }
#pragma unroll
      for (int n = 0; n < 4; ++n) { auto r = __builtin_amdgcn_permlane32_swap(__float_as_uint(mx[n]), __float_as_uint(mx[n]), false, false);
        mx[n] = max2(__uint_as_float(r[0]), __uint_as_float(r[1])); }
      SB0();
      constexpr float cs2 = 0.17677669529663687f * 1.4426950408889634f;
      f32x4 os[4], o0[4], o1[4];
#pragma unroll
      for (int n = 0; n < 4; ++n) {
        const float c = cs2 * rd[n], nm = -mx[n] * c;
        const f32x4 ar = __builtin_elementwise_fma(s0[n], f32x4{c, c, c, c}, f32x4{nm, nm, nm, nm});
        const u32x2 pk = {pack2(__builtin_amdgcn_exp2f(ar[0]), __builtin_amdgcn_exp2f(ar[1])), pack2(__builtin_amdgcn_exp2f(ar[2]), __builtin_amdgcn_exp2f(ar[3]))};
        const bf16x4 pa = __builtin_bit_cast(bf16x4, pk), p0 = __builtin_bit_cast(bf16x4, pk & pm), p1 = __builtin_bit_cast(bf16x4, pk & ~pm);
        const u32x4 vp = n < 2 ? hx[2 + n] : vx[n - 2];
        auto half = [](const u32x4& v, int h) { return __builtin_bit_cast(bf16x4, u32x2{v[2 * h], v[2 * h + 1]}); };
        os[n] = __builtin_amdgcn_mfma_f32_16x16x16bf16_1k(ones, pa, z, 0, 0, 0);
        o0[n] = __builtin_amdgcn_mfma_f32_16x16x16bf16_1k(half(vx[n], 0), p1, __builtin_amdgcn_mfma_f32_16x16x16bf16_1k(half(vp, 0), p0, z, 0, 0, 0), 0, 0, 0);
        o1[n] = __builtin_amdgcn_mfma_f32_16x16x16bf16_1k(half(vx[n], 1), p1, __builtin_amdgcn_mfma_f32_16x16x16bf16_1k(half(vp, 1), p0, z, 0, 0, 0), 0, 0, 0);
      }
#pragma unroll
      for (int n = 0; n < 4; ++n) {
        const float inv = __builtin_amdgcn_rcpf(os[n][0]);
        const f32x4 n0 = o0[n] * inv, n1 = o1[n] * inv;
        putf(112 + ntile + wc, n, u32x4{pack2(n0[0], n0[1]), pack2(n0[2], n0[3]), pack2(n1[0], n1[1]), pack2(n1[2], n1[3])});
      }
    }
  }
  TS1(2);
  {
  __builtin_amdgcn_sched_barrier(0);
  int tid = threadIdx.x;
  asm volatile("" : "+v"(tid));
  __builtin_amdgcn_sched_barrier(0);
  const int lane = tid & 63;
  const int wm = wid >> 1, wn = wid & 1;
  const int m0 = mt2 * 128, n0 = nt2 * 128;
  constexpr int PD = G2_NSTAGE;
  const int ny = (xcd & 1) ? NY - DLT : NY + DLT;
  const unsigned va = lane * 16;
  auto stage_y = [&]() {
    asm volatile("s_add_u32 m0, %[l], %[oa]\n\ts_nop 0\n\tglobal_load_lds_dwordx4 %[va], %[a] sc1\n\tglobal_load_lds_dwordx4 %[va], %[a] offset:1024 sc1\n\t"
                 "global_load_lds_dwordx4 %[va], %[a] offset:2048 sc1\n\tglobal_load_lds_dwordx4 %[va], %[a] offset:3072 sc1\n\t" G2_W
                 :: [l] "s"(sl), [oa] "s"(shm_base + G2_A + (p ^ role) * 4096), [a] "s"(sa), [va] "v"(va), G2_WOP(wy) : "memory", "scc", "m0");
    ++st;
    sa += 16384;
    sl = (sl + 16384) & 65535;
    wsel();
  };
  u64 ready = 0;
  const u64* const mbox = &done1[bid * 64 + lane];
  auto seen = [&](u64 v) __attribute__((always_inline)) {
    const u64 r = __ballot(fresh(v, t_own));
    u64 d;
    asm("s_bitreplicate_b64_b32 %0, %1" : "=s"(d) : "s"((unsigned)r >> (16 * role)));
    ready |= r | (d << 32);
  };
  auto need = [&](u64 mask, bool renew) __attribute__((always_inline)) {
    if ((ready & mask) != mask) {
      seen(__hip_atomic_load(mbox, RLX_AGENT));
      while ((ready & mask) != mask) {
        __builtin_amdgcn_s_sleep(PSLEEP);
        if (renew && !cok) notify(0, 32);
        seen(__hip_atomic_load(mbox, RLX_AGENT));
      }
    }
    asm volatile("" ::: "memory");
  };
  u64 fl = 0;
  auto peek = [&]() { asm volatile("global_load_dwordx2 %0, %1, %2 sc1" : "+v"(fl) : "v"(va >> 1), "s"(done1 + bid * 64) : "memory"); };
  auto check = [&](u64 mask, bool renew) __attribute__((always_inline)) {
    asm volatile("" : "+v"(fl));
    seen(fl);
    if (__builtin_expect((ready & mask) != mask, 0)) need(mask, renew);
  };
  if (wid >= 4) {
    TS(4);
    __builtin_amdgcn_s_setprio(0);
    BARL();
    stage_y();
    stage_y();
    BAR();
    if (p == 1) peek();
    stage_y();
    WAIT_V(16);
    BAR();
    if (p == 0) notify(0, 32);
#define G2_A2 "global_load_lds_dwordx4 %[va], %[a] sc1\n\tglobal_load_lds_dwordx4 %[va], %[a] offset:1024 sc1"
#define G2_A4 G2_A2 "\n\tglobal_load_lds_dwordx4 %[va], %[a] offset:2048 sc1\n\tglobal_load_lds_dwordx4 %[va], %[a] offset:3072 sc1"
#define G2_SYNC "\n\ts_mov_b32 m0, %[la]\n\ts_waitcnt vmcnt(%[n])\n\ts_barrier\n\t"
    const unsigned oy = shm_base + G2_A + (p ^ role) * 4096, os = shm_base + G2_A + (p >> 1) * 8192 + (p & 1) * 2048;
    asm volatile("s_mov_b32 m0, %[la]\n\ts_nop 0\n\t" G2_A4 :: [la] "s"(sl + oy), [a] "s"(sa), [va] "v"(va) : "memory", "m0");
    auto step = [&](long adv = 16384) __attribute__((always_inline)) {
      wsel();
      ++st;
      sa += adv;
      const unsigned sn = (sl + 16384) & 65535;
      asm volatile(G2_W G2_SYNC G2_A4 :: [l] "s"(sl), [la] "s"(sn + oy), [n] "n"(16), [a] "s"(sa), [va] "v"(va), G2_WOP(wy) : "memory", "scc", "m0");
      sl = sn;
    };
    auto chk1 = [&](u64 mask) __attribute__((always_inline)) { if (p == 1) { WAIT_V(16); check(mask, true); } };
    step();
    chk1(0xcc00cc00ull << 32);
    step(); step();
    if (p == 1) peek();
    step(); step();
    chk1(0x0f0fu << (16 * role));
#pragma unroll 1
    for (int g = 7; g < 13; ++g) step();
    if (p == 1) peek();
    step(); step();
    chk1((0xffffu << (16 * role)) | 0xf555f555u);
#pragma unroll 1
    for (int g = 15; g < min(ny, NY) - 5; ++g) step();
    if (ny > NY) {
      step(((1 - 2 * role) * NY + 1 - DLT) * 16384);
#pragma unroll
      for (int i = 1; i < DLT; ++i) step();
    }
    wsel();
    sa = sz;
    {
      const unsigned sn = (sl + 16384) & 65535;
      asm volatile(G2_W G2_SYNC G2_A2 :: [l] "s"(sl), [la] "s"(sn + os), [n] "n"(16), [a] "s"(sa), [va] "v"(va), G2_WOP(wy) : "memory", "scc", "m0");
      sl = sn;
    }
    sw[0] = wz.wb[0]; sw[1] = wz.wb[1];
#pragma unroll
    for (int j = -3; j < 13; ++j) {
      if (j == GETP + 1 || j == GETP + 2)
        asm volatile("s_mov_b32 m0, %[l]\n\ts_nop 0\n\tglobal_load_lds_dwordx4 %[va], %[a] offset:%[o] sc1\n\tglobal_load_lds_dwordx4 %[va], %[a] offset:%[o]+1024 sc1"
                     :: [l] "s"(shm_base + G2_X + 16384 + p * 4096), [a] "s"((const char*)slab + (size_t)(bid ^ 5) * 16384 + p * 4096), [va] "v"(va),
                        [o] "n"((j - GETP - 1) * 2048) : "memory", "m0");
      sa += 16384;
      const unsigned sn = (sl + 16384) & 65535;
      constexpr int G = GETP;
      static_assert(GETP <= GETJ + 1, "");
      if (j < 12) asm volatile(G2_W G2_SYNC G2_A2 :: [l] "s"(sl), [la] "s"(sn + os), [n] "n"((j == -3 ? 14 : 12) + (j > G && j < G + 3 ? 2 : 0) + (j > G + 1 && j < G + 4 ? 2 : 0)),
                               [a] "s"(sa), [va] "v"(va), G2_WOP(wz) : "memory", "scc", "m0");
      else asm volatile(G2_W :: [l] "s"(sl), G2_WOP(wz) : "memory", "scc", "m0");
      sl = sn;
      sw[0] += ws0; sw[1] += ws1;
    }
    WAIT_V(12);
    BAR();
    WAIT_V(6);
    BAR();
    WAIT_V(0);
    BAR();
    asm volatile("" : "+v"(fl));
    if (tid < 258) __hip_atomic_store(lane ? done2 + bid * 8 : &acks[bid], __builtin_amdgcn_s_memrealtime(), RLX_AGENT);
    if (__builtin_expect(!cok, 0) && p == 0) {
      const bool mine = lane <= 32;
      const u64* const pa = &acks[lane < 32 ? client(lane) : bid ^ 5];
      while (!__all(!mine || fresh(__hip_atomic_load(pa, RLX_AGENT), t_own))) {
        notify(0, 32);
        if (lane == 0) __hip_atomic_store(done2 + bid * 8, __builtin_amdgcn_s_memrealtime(), RLX_AGENT);
        __builtin_amdgcn_s_sleep(32);
      }
    }
  } else {
    TS(6);
    f32x4 acc[4][4], sacc[2][4];
    const int fr = lane & 15, fq = lane >> 4, q4 = fr >> 2, pp = lane & 3;
    const int ff = ((q4 >> 1) & 1) | ((fq & 1) << 1);
    int aof = G2_A + wm * 8192 + lane * 16;
    int boff[4];
#pragma unroll
    for (int j = 0; j < 4; ++j) boff[j] = wn * 8192 + (8 * fq + q4) * 128 + ((j ^ ff) * 32) + pp * 8;
    int vo = wid * 4096 + lane * 16;
    int vx = ((m0 + wm * 64 + role * 32 + 2 * fr) * D_ + n0 + wn * 64) * 2 + (fq & 1) * 32 + (fq >> 1) * 16;
    lds_cptr px = shm3 + G2_X + wid * 4096 + (fq >> 1) * 512 + fr * 16 + (fq & 1) * 8;
    asm volatile("" : "+v"(vo), "+v"(vx), "+v"(px));
#define SB() __builtin_amdgcn_sched_barrier(0)
    struct Fr { bf16x8 a[4]; bf16x4 bl[4], bh[4]; };
    Fr fa[2], fb[2];
    auto rd = [&](Fr* f, int odd, int t) {
      constexpr int ord[12] = {0, 1, 8, 2, 3, 4, 5, 6, 7, 9, 10, 11};
      constexpr int seq[24] = {0, 1, 2, 12, 13, 14, 15, 16, 3, 4, 5, 6, 17, 18, 19, 20, 7, 8, 9, 21, 10, 22, 11, 23};
      lds_cptr sb = shm3 + odd * 16384;
      const int ks = seq[t] / 12, o = ord[seq[t] % 12];
      if (o >= 8) f[ks].a[o - 8] = *(const __attribute__((address_space(3))) bf16x8*)(sb + aof + (o - 8) * 2048 + ks * 1024);
      else if (o & 1) f[ks].bh[o >> 1] = tr_read(sb + boff[o >> 1] + ks * 4096 + 512);
      else f[ks].bl[o >> 1] = tr_read(sb + boff[o >> 1] + ks * 4096);
    };
    auto bfr = [&](const Fr& c, int j) { return __builtin_shufflevector(c.bl[j], c.bh[j], 0, 1, 2, 3, 4, 5, 6, 7); };
    auto mf1 = [](f32x4& c, bf16x8 b, bf16x8 a, bool z = false) {
      if (z) asm volatile("v_mfma_f32_16x16x32_bf16 %0, %1, %2, 0" : "=&v"(c) : "v"(b), "v"(a));
      else asm volatile("v_mfma_f32_16x16x32_bf16 %0, %1, %2, %0" : "+v"(c) : "v"(b), "v"(a));
    };
    auto mf2 = [](f32x4& c, bf16x8 b, bf16x8 a, bf16x8 b2, bf16x8 a2, bool z) {
      if (z) asm volatile("v_mfma_f32_16x16x32_bf16 %0, %1, %2, 0\n\tv_mfma_f32_16x16x32_bf16 %0, %3, %4, %0" : "=&v"(c) : "v"(b), "v"(a), "v"(b2), "v"(a2));
      else asm volatile("v_mfma_f32_16x16x32_bf16 %0, %1, %2, %0\n\tv_mfma_f32_16x16x32_bf16 %0, %3, %4, %0" : "+v"(c) : "v"(b), "v"(a), "v"(b2), "v"(a2));
    };
    auto pairs = [&](f32x4 (*ac)[4], const Fr* c, int nq, int mx, int nf, auto fill, bool z = false) {
#pragma unroll
      for (int q = 0; q < nq; ++q) {
        const int mf = (q >> 2) ^ mx, j = (q & 4) ? 3 - (q & 3) : q & 3, ks = q & 1;
        SB();
        if (2 * q >= nf) mf2(ac[mf][j], bfr(c[ks], j), c[ks].a[mf], bfr(c[ks ^ 1], j), c[ks ^ 1].a[mf], z);
        else {
          mf1(ac[mf][j], bfr(c[ks], j), c[ks].a[mf], z);
          SB();
          fill(2 * q);
          SB();
          mf1(ac[mf][j], bfr(c[ks ^ 1], j), c[ks ^ 1].a[mf]);
          SB();
          fill(2 * q + 1);
        }
      }
      SB();
    };
    auto flip = [&]() {
      aof ^= 32768; boff[0] ^= 32768; boff[1] ^= 32768; boff[2] ^= 32768; boff[3] ^= 32768;
      asm volatile("" : "+v"(aof), "+v"(boff[0]), "+v"(boff[1]), "+v"(boff[2]), "+v"(boff[3]));
    };
    auto bar = [&]() { __builtin_amdgcn_s_waitcnt(0xC07F); BAR(); };
    auto ystg = [&](const Fr* c, Fr* n, int odd, bool z = false) {
      pairs(acc, c, 16, 0, odd ? 30 : 24, [&](int t) {
        if (t < 24) rd(n, odd, t);
        else if (t == 24) asm volatile("s_nop 0");
        else { int& a = t == 29 ? aof : boff[t - 25]; a ^= 32768; asm volatile("" : "+v"(a)); }
      }, z);
    };
    auto stg = [&](const Fr* c, Fr* n, int odd, bool z = false) { pairs(sacc, c, 8, 0, 10, [&](int t) { rd(n, odd, 2 * t); rd(n, odd, 2 * t + 1); }, z); };
    asm volatile("" : "+v"(aof), "+v"(boff[0]), "+v"(boff[1]), "+v"(boff[2]), "+v"(boff[3]));
    await(0xffffu, 0x404u);
    auto lda = [&](int i) {
      asm volatile("s_mov_b32 m0, %[l]\n\ts_nop 0\n\tglobal_load_lds_dwordx4 %[va], %[a] sc1\n\tglobal_load_lds_dwordx4 %[va], %[a] offset:1024 sc1\n\t"
                   "global_load_lds_dwordx4 %[va], %[a] offset:2048 sc1\n\tglobal_load_lds_dwordx4 %[va], %[a] offset:3072 sc1"
                   :: [l] "s"(shm_base + G2_A + (i + 1) * 16384 + (p ^ role) * 4096), [a] "s"(sa + i * 16384), [va] "v"(va) : "memory", "m0");
    };
    need(0x00330033ull << 32, false);
    TS(4);
    lda(0);
    lda(1);
    need(0x00ff00ffull << 32, false);
    TS(0);
    lda(2);
    WAIT_V(8);
    signal(1u << 24);
    await(0xff000000u, 4u << 24);
    __builtin_amdgcn_s_waitcnt(0xC07F);
    SB();
#pragma unroll
    for (int t = 0; t < 24; ++t) rd(fa, 1, t);
    flip();
#define CPRIO 0
    __builtin_amdgcn_s_setprio(CPRIO);
    WAIT_V(4);
    peek();
    bar();
    TS(7);
    ystg(fa, fb, 0, true);
    WAIT_V(1);
    bar();
    ystg(fb, fa, 1);
    WAIT_V(0);
    check(0x33003300ull << 32, false);
    constexpr int PAR = (NY - DLT + 1) & 1;
#pragma unroll 1
    for (int g = 2; g < ny - 2; g += 2) {
      bar();
      ystg(fa, fb, 0);
      bar();
      ystg(fb, fa, 1);
    }
    if (PAR) {
      bar();
      ystg(fa, fb, 0);
    }
    Fr* const f0 = PAR ? fb : fa;
    Fr* const f1 = PAR ? fa : fb;
    bar();
    {
      const __amdgpu_buffer_rsrc_t rs = __builtin_amdgcn_make_buffer_rsrc((void*)((char*)slab + (size_t)bid * 16384), 0, 16384, 0x00020000);
      u32x4 pk;
      pairs(acc, f0, 16, 2, 32, [&](int t) {
        if (t < 20) rd(f1, PAR, t);
        else {
          const int f = (t - 20) / 3, e = (t - 20) % 3;
          if (e < 2) { const f32x4 a = acc[2 + (f >> 1)][(f & 1) * 2 + e]; pk[2 * e] = pack2(a[0], a[1]); pk[2 * e + 1] = pack2(a[2], a[3]); }
          else __builtin_amdgcn_raw_buffer_store_b128(pk, rs, vo + f * 1024, 0, 16);
        }
      });
    }
    TS(2);
    static_assert(GETJ % 2 == 1 && GETJ >= 3 && GETJ <= 7, "");
    f32x4 xf[2][4];
#pragma unroll
    for (int j = 0; j < GETJ + 5; j += 2) {
      if (PAR) flip();
      bar();
      stg(f1, f0, PAR ^ 1, j == 0);
      if (!PAR) flip();
      bar();
      u32x2 xv[2];
      pairs(sacc, f0, 8, 0, 12, [&](int t) {
        if (t < 10) { rd(f1, PAR, 2 * t); rd(f1, PAR, 2 * t + 1); }
        else if (j < 8) xv[t - 10] = *(const __attribute__((address_space(3))) u32x2*)(px + (j >> 1) * 1024 + (t - 10) * 256);
      });
      if (j < 8) {
#pragma unroll
        for (int e = 0; e < 2; ++e) xf[j >> 2][(j & 2) + e] = f32x4{lo_f(xv[e][0]), hi_f(xv[e][0]), lo_f(xv[e][1]), hi_f(xv[e][1])};
        asm volatile("" : "+v"(xf[j >> 2][j & 2]), "+v"(xf[j >> 2][(j & 2) + 1]));
        SB();
      }
    }
    if (PAR) flip();
    WAIT_V(0);
    bar();
    u32x4 pv[4];
    {
      lds_cptr pp_ = shm3 + G2_X + 16384 + vo;
      pairs(sacc, f1, 8, 0, 12, [&](int t) {
        if (t < 2) {
#pragma unroll
          for (int f = 2 * t; f < 2 * t + 2; ++f) pv[f] = *(const __attribute__((address_space(3))) u32x4*)(pp_ + f * 1024);
        } else { rd(f0, PAR ^ 1, 2 * t - 4); rd(f0, PAR ^ 1, 2 * t - 3); }
      });
      if (__builtin_expect(blank(pv) || !cok, 0)) {
        const __amdgpu_buffer_rsrc_t rp = __builtin_amdgcn_make_buffer_rsrc((void*)((char*)slab + (size_t)(bid ^ 5) * 16384), 0, 16384, 0x00020000);
        while (!cok && !__all(fresh(__hip_atomic_load(done2 + (bid ^ 5) * 8, RLX_AGENT), t_own))) {
          notify(0, 32);
          if (lane == 0) __hip_atomic_store(done2 + bid * 8, __builtin_amdgcn_s_memrealtime(), RLX_AGENT);
          __builtin_amdgcn_s_sleep(8);
        }
        do {
          asm volatile("" ::: "memory");
#pragma unroll
          for (int f = 0; f < 4; ++f) pv[f] = __builtin_bit_cast(u32x4, __builtin_amdgcn_raw_buffer_load_b128(rp, vo + f * 1024, 0, 16));
        } while (blank(pv));
      }
    }
    auto silu = [&](int g0, int g1) {
#pragma unroll
      for (int f = g0; f < g1; ++f) {
        const int mf = f >> 2, j = f & 3;
        const u32x4 pw = pv[mf * 2 + (j >> 1)];
        const f32x4 ya = acc[mf][j] + f32x4{lo_f(pw[2 * (j & 1)]), hi_f(pw[2 * (j & 1)]), lo_f(pw[2 * (j & 1) + 1]), hi_f(pw[2 * (j & 1) + 1])};
#pragma unroll
        for (int e = 0; e < 2; ++e) {
          const f32x2 y2 = {ya[2 * e], ya[2 * e + 1]};
          const f32x2 ar = y2 * f32x2{-1.4426950408889634f, -1.4426950408889634f};
          const f32x2 dn = f32x2{__builtin_amdgcn_exp2f(ar[0]), __builtin_amdgcn_exp2f(ar[1])} + f32x2{1.0f, 1.0f};
          const f32x2 t = y2 * f32x2{__builtin_amdgcn_rcpf(dn[0]), __builtin_amdgcn_rcpf(dn[1])};
          acc[mf][j][2 * e] = t[0]; acc[mf][j][2 * e + 1] = t[1];
        }
        asm volatile("" : "+v"(acc[mf][j]));
      }
      SB();
    };
#define SPT 0, 0, 1, 2, 4, 6, 8, 8
    constexpr int spt[] = {SPT};
    silu(0, spt[1]);
#pragma unroll
    for (int j = GETJ + 6; j < 15; ++j) {
      if ((j ^ PAR) & 1) flip();
      bar();
      if (j & 1) stg(f0, f1, PAR); else stg(f1, f0, PAR ^ 1);
      silu(spt[j - GETJ - 5], j == 14 ? 8 : spt[j - GETJ - 4]);
    }
    pairs(sacc, f0, 8, 0, 0, [](int) {});
    asm volatile("s_nop 7\n\ts_nop 3" : "+v"(sacc[1][0]), "+v"(sacc[1][1]), "+v"(sacc[1][2]), "+v"(sacc[1][3]));
    SB();
    TS(1);
    {
      const __amdgpu_buffer_rsrc_t ro = __builtin_amdgcn_make_buffer_rsrc((void*)out, 0, S_ * D_ * 2, 0x00020000);
#pragma unroll
      for (int f = 0; f < 4; ++f) {
        const int mf = f >> 1, j = (f & 1) * 2;
        const f32x4 a = __builtin_elementwise_fma(sacc[mf][j], acc[mf][j], xf[mf][j]), b = __builtin_elementwise_fma(sacc[mf][j + 1], acc[mf][j + 1], xf[mf][j + 1]);
        const auto r0 = __builtin_amdgcn_permlane16_swap(pack2(a[0], a[1]), pack2(b[0], b[1]), false, false);
        const auto r1 = __builtin_amdgcn_permlane16_swap(pack2(a[2], a[3]), pack2(b[2], b[3]), false, false);
        __builtin_amdgcn_raw_buffer_store_b128(u32x4{r0[0], r1[0], r0[1], r1[1]}, ro, vx + mf * 2048 + j * 32, 0, 2);
      }
    }
    TS(3);
  }
  if (bid == 73 && wid == 4) { unsigned d_ = (ts_r[13] - ts_r[5]) / 5u; if (d_ > 9000u) d_ = 0; while (__builtin_amdgcn_s_memrealtime() - t_own < 6000ull + d_) {} }
  }
  asm volatile(".Lcode_end:");
}

extern "C" void kernel_launch(void* const* d_in, const int* in_sizes, int n_in, void* d_out, int out_size, void* d_ws,
                              size_t ws_size, hipStream_t stream) {
  const u16* A_x = (const u16*)d_in[0];
  const u16* A_wqkv = (const u16*)d_in[1];
  const u16* A_wo = (const u16*)d_in[2];
  const u16* B_wg = (const u16*)d_in[4];
  const u16* B_wu = (const u16*)d_in[5];
  const u16* B_wd = (const u16*)d_in[6];
  u16* out = (u16*)d_out;
  fused_kernel<<<256, 512, 0, stream>>>(A_x, A_wqkv, B_wg, B_wu, B_wd, A_wo, out, (char*)d_ws);
}
```

```cpp
#define SH 3
#define ALL_TS 1
#define TSMASK 8192
#include <hip/hip_runtime.h>
#include <hip/hip_bf16.h>
#include <stdint.h>
#include <type_traits>

typedef unsigned short u16;
using bf16x8 = __attribute__((ext_vector_type(8))) short;
using bf16x4 = __attribute__((ext_vector_type(4))) short;
using f32x4 = __attribute__((ext_vector_type(4))) float;
using u32x2 = __attribute__((ext_vector_type(2))) unsigned;
using u32x4 = __attribute__((ext_vector_type(4))) unsigned;
typedef __attribute__((address_space(3))) const char* lds_cptr;

constexpr int S_ = 2048, D_ = 1024, F_ = 3584;

__device__ __forceinline__ float bf2f(u16 h) { return __uint_as_float((unsigned)h << 16); }
__device__ __forceinline__ u16 f2bf(float f) {
  unsigned u = __float_as_uint(f);
  u += 0x7fffu + ((u >> 16) & 1u);
  return (u16)(u >> 16);
}
__device__ __forceinline__ float rbf(float f) { return bf2f(f2bf(f)); }
typedef __attribute__((ext_vector_type(2))) __bf16 bf16x2_t;
typedef __attribute__((ext_vector_type(2))) float f32x2;
__device__ __forceinline__ unsigned pack2(float a, float b) {
  bf16x2_t r = __builtin_convertvector((f32x2){a, b}, bf16x2_t);
  return __builtin_bit_cast(unsigned, r);
}
__device__ __forceinline__ float lo_f(unsigned p) { return __uint_as_float(p << 16); }
__device__ __forceinline__ float hi_f(unsigned p) { return __uint_as_float(p & 0xffff0000u); }

#define TSDECL unsigned ts_r[16] = {0, 0, 0, 0, 0, 0, 0, 0, 0, 0, 0, 0, 0, 0, 0, 0}
#define TSS(i) do { if ((TSMASK >> (i)) & 1) { unsigned long long t_; asm volatile("s_memrealtime %0\n\ts_waitcnt lgkmcnt(0)" : "=s"(t_)); ts_r[i] = (unsigned)t_; } } while (0)
#define TSFLUSH() do { if ((threadIdx.x & 63) == 0) { for (int i_ = 0; i_ < 16; ++i_) flags[(blockIdx.x * 8 + (threadIdx.x >> 6)) * 16 + i_] = ts_r[i_]; } } while (0)
#define TS1(i) TSS(i)
#define TS(i) TSS(8 + (i))
#define PIN_S(v) asm volatile("" : "+s"(v))
#define WAIT_V(n) asm volatile("s_waitcnt vmcnt(%0)" ::"n"(n) : "memory")
#define WAIT_L(n) asm volatile("s_waitcnt lgkmcnt(%0)" ::"n"(n) : "memory")
#define BARL() do { WAIT_L(0); BAR(); } while (0)
#define BAR() do { asm volatile("" ::: "memory"); __builtin_amdgcn_s_barrier(); asm volatile("" ::: "memory"); } while (0)

__device__ __forceinline__ bf16x4 tr_read(lds_cptr p) {
  return __builtin_amdgcn_ds_read_tr16_b64_v4i16((__attribute__((address_space(3))) bf16x4*)p);
}
__device__ __forceinline__ bf16x8 tr_frag(lds_cptr p) {
  bf16x4 lo = tr_read(p), hi = tr_read(p + 512);
  bf16x8 r;
  r[0] = lo[0]; r[1] = lo[1]; r[2] = lo[2]; r[3] = lo[3];
  r[4] = hi[0]; r[5] = hi[1]; r[6] = hi[2]; r[7] = hi[3];
  return r;
}
__device__ __forceinline__ void glds16(const void* gsrc, unsigned lds_dst) {
  unsigned keep;
  asm volatile("s_mov_b32 %0, m0\n\ts_mov_b32 m0, %2\n\ts_nop 0\n\tglobal_load_lds_dwordx4 %1, off\n\ts_mov_b32 m0, %0"
               : "=&s"(keep) : "v"(gsrc), "s"(lds_dst) : "memory");
}
#define GLDS(gptr, laddr) glds16((const void*)(gptr), (unsigned)(laddr))
__device__ __forceinline__ void glds16_sc1(const void* gsrc, unsigned lds_dst) {
  unsigned keep;
  asm volatile("s_mov_b32 %0, m0\n\ts_mov_b32 m0, %2\n\ts_nop 0\n\tglobal_load_lds_dwordx4 %1, off sc1\n\ts_mov_b32 m0, %0"
               : "=&s"(keep) : "v"(gsrc), "s"(lds_dst) : "memory");
}
#define GLDS_SC1(gptr, laddr) glds16_sc1((const void*)(gptr), (unsigned)(laddr))
__device__ __forceinline__ void glds16_nc(const void* gsrc, unsigned lds_dst) {
  unsigned keep;
  asm volatile("s_mov_b32 %0, m0\n\ts_mov_b32 m0, %2\n\ts_nop 0\n\tglobal_load_lds_dwordx4 %1, off\n\ts_mov_b32 m0, %0"
               : "=&s"(keep) : "v"(gsrc), "s"(lds_dst));
}
#define GLDS_NC(gptr, laddr) glds16_nc((const void*)(gptr), (unsigned)(laddr))
__device__ __forceinline__ void glds16_s(const void* sbase, unsigned voff, unsigned lds_dst) {
  asm volatile("s_mov_b32 m0, %2\n\ts_nop 0\n\tglobal_load_lds_dwordx4 %0, %1" :: "v"(voff), "s"(sbase), "s"(lds_dst) : "m0");
}

__device__ __forceinline__ void glds16_s1(const void* sbase, unsigned voff, unsigned lds_dst) {
  asm volatile("s_mov_b32 m0, %2\n\ts_nop 0\n\tglobal_load_lds_dwordx4 %0, %1 sc1" :: "v"(voff), "s"(sbase), "s"(lds_dst) : "memory", "m0");
}
__device__ __forceinline__ void glds16_v(const void* p, unsigned lds_dst) {
  asm volatile("s_mov_b32 m0, %1\n\ts_nop 0\n\tglobal_load_lds_dwordx4 %0, off" :: "v"(p), "s"(lds_dst) : "m0");
}
template <int OFF = 0>
__device__ __forceinline__ void gst16(const void* sbase, unsigned voff, u32x4 v) {
  asm volatile("global_store_dwordx4 %0, %1, %2 offset:%3 sc1\n\ts_nop 1" :: "v"(voff), "v"(v), "s"(sbase), "n"(OFF) : "memory");
}

typedef unsigned long long u64;
__device__ __forceinline__ bool fresh(u64 stamp, u64 t_own) {
  const u64 b = t_own + 100ull;
  const unsigned lo = (unsigned)stamp - (unsigned)b, hi = (unsigned)(stamp >> 32) - (unsigned)(b >> 32) - ((unsigned)stamp < (unsigned)b ? 1u : 0u);
  return (hi == 0u) & (lo < (unsigned)(__builtin_amdgcn_s_memrealtime() - t_own));
}
constexpr u64 TIMEOUT_ = 200000;
__device__ __forceinline__ float half_sum(float v) {
  v += __builtin_bit_cast(float, __builtin_amdgcn_update_dpp(0, __builtin_bit_cast(int, v), 0xB1, 0xf, 0xf, false));
  v += __builtin_bit_cast(float, __builtin_amdgcn_update_dpp(0, __builtin_bit_cast(int, v), 0x4E, 0xf, 0xf, false));
  v += __builtin_bit_cast(float, __builtin_amdgcn_update_dpp(0, __builtin_bit_cast(int, v), 0x141, 0xf, 0xf, false));
  v += __builtin_bit_cast(float, __builtin_amdgcn_update_dpp(0, __builtin_bit_cast(int, v), 0x140, 0xf, 0xf, false));
  { auto r = __builtin_amdgcn_permlane16_swap(__float_as_uint(v), __float_as_uint(v), false, false); v = __uint_as_float(r[0]) + __uint_as_float(r[1]); }
  return v;
}
__device__ __forceinline__ unsigned pack_stats(float mean, float var) {
  return (unsigned)f2bf(mean) | ((unsigned)f2bf(sqrtf(rbf(rbf(var) + 1e-5f))) << 16);
}
__device__ __forceinline__ unsigned row_stats_serial(const u16* xr) {
  float s = 0.f, q = 0.f;
#pragma unroll 1
  for (int k = 0; k < D_; ++k) s += bf2f(xr[k]);
  const float mean = s * (1.0f / D_);
#pragma unroll 1
  for (int k = 0; k < D_; ++k) { const float d = bf2f(xr[k]) - mean; q += d * d; }
  return pack_stats(mean, q * (1.0f / D_));
}
constexpr int G1_STAGE = 32768 + 5 * 8192;
#define EPOS 2
#define PA 4
#define PB 8
#define PC 12
#define GETP 4
#define DLT 2
#define PSLEEP 2
#define GETJ 3

constexpr unsigned BLANK = 0x7f817f81u;
__device__ __forceinline__ bool blank(const u32x4* pv) {
  u64 m;
#define CX(i) "v_cmpx_ne_u32 vcc, %1, %" #i "\n\t"
  asm volatile(CX(2) CX(3) CX(4) CX(5) CX(6) CX(7) CX(8) CX(9) CX(10) CX(11) CX(12) CX(13) CX(14) CX(15) CX(16) CX(17)
               "s_mov_b64 %0, exec\n\ts_mov_b64 exec, -1\n\ts_nop 3" : "=s"(m) : "s"(BLANK), "v"(pv[0][0]), "v"(pv[0][1]), "v"(pv[0][2]), "v"(pv[0][3]),
               "v"(pv[1][0]), "v"(pv[1][1]), "v"(pv[1][2]), "v"(pv[1][3]), "v"(pv[2][0]), "v"(pv[2][1]), "v"(pv[2][2]), "v"(pv[2][3]),
               "v"(pv[3][0]), "v"(pv[3][1]), "v"(pv[3][2]), "v"(pv[3][3]) : "vcc");
  return m != ~0ull;
}
#define RLX_AGENT __ATOMIC_RELAXED, __HIP_MEMORY_SCOPE_AGENT

constexpr int G2_A = 65536, G2_X = 131072;
constexpr int G2_NSTAGE = 4;
constexpr size_t MB_ = 1024 * 1024;
__global__ __launch_bounds__(512, 2) __attribute__((aligned(4096))) void fused_kernel(const u16* __restrict__ x, const u16* __restrict__ wqkv,
                                                       const u16* __restrict__ wg, const u16* __restrict__ wu,
                                                       const u16* __restrict__ wd, const u16* __restrict__ wo,
                                                       u16* __restrict__ out, char* __restrict__ ws) {
  u16* const halo = (u16*)(ws + 4 * MB_);
  u16* const abuf = (u16*)(ws + 16 * MB_);
  char* const slab = ws + 64 * MB_;
  unsigned* const flags = (unsigned*)(ws + 96 * MB_);
  u32x4* const rstat = (u32x4*)(ws + 97 * MB_);
  char* const wcopy = ws + 98 * MB_;
  u64* const done1 = (u64*)(ws + 102 * MB_);
  u64* const done2 = (u64*)(ws + 100 * MB_ + 65536);
  u64* const donec = (u64*)(ws + 100 * MB_ + 131072);
  u64* const acks = (u64*)(ws + 100 * MB_ + 196608);
  float* const csum = (float*)(ws + 101 * MB_);
  __shared__ __attribute__((aligned(1024))) char shm[163840];
  constexpr int AUX = 2 * G1_STAGE + 8192;
  {
    u64 pc; int len;
    asm volatile("s_getpc_b64 %0\n.Lcode_beg:\n\ts_mov_b32 %1, .Lcode_end-.Lcode_beg" : "=s"(pc), "=s"(len));
    const int ol = ((blockIdx.x >> 3) + 32 * (threadIdx.x >> 6)) * 1024 + (threadIdx.x & 63) * 16;
    asm volatile("s_mov_b32 m0, %2\n\ts_nop 0\n\tglobal_load_lds_dwordx4 %0, %1"
                 :: "v"(ol < len - 16 ? ol : 0), "s"(pc & ~15ull), "s"((unsigned)(uintptr_t)(lds_cptr)shm + 2 * G1_STAGE + 7168) : "m0");
    __builtin_amdgcn_sched_barrier(0);
  }
  const u64 t_own = __builtin_amdgcn_s_memrealtime();
  TSDECL;
  TS1(7);
  const int tid = threadIdx.x, lane = tid & 63;
  const int wid = __builtin_amdgcn_readfirstlane(tid >> 6);
  const int wm = wid & 3, wc = wid >> 2;
  int bid_ = blockIdx.x;
  PIN_S(bid_);
  const int bid = bid_ & 255, xcd = bid & 7, idx = bid >> 3;
  const int ntile = xcd * 4 + (idx & 3), mtile = idx >> 2;
  const int m0 = mtile * 256;
  const int hg = ntile >> 1;
  const bool tya = (ntile & 1) == 0;
  const int srow = m0 + ntile * 8 + wm * 2;
  lds_cptr shm3 = (lds_cptr)shm;
  const unsigned shm_base = (unsigned)(uintptr_t)shm3;
  const int r8 = tid >> 3, pos = tid & 7;
  const int phys = pos >> 1, half = pos & 1;
  const int fk = ((r8 >> 1) & 1) | (((r8 >> 3) & 1) << 1);
  const int vsrc = phys ^ fk;
  const int qstr = tya ? 3072 * 2 : F_ * 2;
  unsigned voa = (((r8 & 32) + 2 * (r8 & 15) + ((r8 >> 4) & 1)) * D_ + ((pos ^ (r8 & 7)) * 8)) * 2;
  unsigned vog = r8 * (F_ * 2) + vsrc * 32 + half * 16, voq = r8 * qstr + vsrc * 32 + half * 16;
  unsigned vom = r8 * qstr + (vsrc >> (tya ? 0 : 1)) * 32 + half * 16;
  asm volatile("" : "+v"(voa), "+v"(vog), "+v"(voq), "+v"(vom));
  const int kt0 = (hg >> 1) * 7, he = hg & 1;
  unsigned oq = (tya ? hg : kt0 + 2 * he) * 128, og = (kt0 + (tya ? 5 + he : 2 * he + 1)) * 128, om = tya ? 4096 : 512 - 192 * he;
  unsigned d0 = shm_base + wid * 1024 + 32768, ox = m0 * D_ * 2;
  long uq = -32 * qstr;
  PIN_S(oq); PIN_S(og); PIN_S(om); PIN_S(d0); PIN_S(ox); PIN_S(uq);
  __builtin_amdgcn_sched_barrier(0);
  asm volatile("" :: "s"(x), "s"(wqkv), "s"(wg), "s"(wu), "s"(wd), "s"(wo), "s"(out), "s"(ws));
  const char* const ab = (const char*)x + ox;
  int kz = 0;
  asm volatile("" : "+s"(kz));
  u64 pq = (u64)wqkv, pg = (u64)wg;
  PIN_S(pq); PIN_S(pg);
  const char* const qb = (const char*)(tya ? pq : pg) + oq;
  if (wid < 4) {
#pragma unroll
    for (int i = 0; i < 8; ++i) glds16_s(ab + i * 32 * D_ * 2, voa, d0 - 32768 + i * 4096);
  } else {
    glds16_s(qb, voq, d0); glds16_s(qb + uq, voq, d0 - 4096);
  }
  __builtin_amdgcn_sched_barrier(0);
  const char* const gb = (const char*)wg + og;
  const long dgu = (const char*)wu - (const char*)wg, dp1 = tya ? 2048 : dgu;
  const char* const mb0 = qb + om;
  const char* mptr = mb0 + (((vsrc & 1) && !tya) ? dgu : 0) + vom;
  if (wid >= 4) {
    const long ug = kz - 32 * F_ * 2;
    glds16_s(qb + dp1, voq, d0 + 8192); glds16_s(qb + dp1 + uq, voq, d0 + 4096);
    glds16_v(mptr, d0 + 2 * 8192); glds16_v(mptr + uq, d0 + 2 * 8192 - 4096);
    glds16_s(gb, vog, d0 + 3 * 8192); glds16_s(gb + ug, vog, d0 + 3 * 8192 - 4096);
    glds16_s(gb + dgu, vog, d0 + 4 * 8192); glds16_s(gb + dgu + ug, vog, d0 + 4 * 8192 - 4096);
  }
  __builtin_amdgcn_sched_barrier(0);
  f32x4 acc[4][10];

  constexpr int NT = D_ / 64;
  constexpr int CNT = AUX + 4096 + 3584;
  if (tid < 8) *(unsigned*)(shm + CNT + tid * 4) = 0u;
  bf16x8 af0[4], af1[4];
  bf16x4 bl[4], bh[4];
#define SB0() __builtin_amdgcn_sched_barrier(0)
  auto mm = [&](int g, int t, bool two, bool z = false) {
    const int mi = t >> 1, mf = ((g & 1) && g != 9) ? 3 - mi : mi, ks = (t & 1) ^ (mi & 1), s = ((g + 1) & 1) * 2;
    const bf16x8 b0 = __builtin_shufflevector(bl[s + ks], bh[s + ks], 0, 1, 2, 3, 4, 5, 6, 7), a0 = ks ? af1[mf] : af0[mf];
    const bf16x8 b1 = __builtin_shufflevector(bl[s + 1 - ks], bh[s + 1 - ks], 0, 1, 2, 3, 4, 5, 6, 7), a1 = ks ? af0[mf] : af1[mf];
    const bool tr = (g >> 1) == 2;
    if (z && two) asm volatile("v_mfma_f32_16x16x32_bf16 %0, %1, %2, 0\n\tv_mfma_f32_16x16x32_bf16 %0, %3, %4, %0" : "=&v"(acc[mf][g])
                               : "v"(tr ? a0 : b0), "v"(tr ? b0 : a0), "v"(tr ? a1 : b1), "v"(tr ? b1 : a1));
    else if (z && !(t & 1)) asm volatile("v_mfma_f32_16x16x32_bf16 %0, %1, %2, 0" : "=&v"(acc[mf][g]) : "v"(tr ? a0 : b0), "v"(tr ? b0 : a0));
    else if (two) asm volatile("v_mfma_f32_16x16x32_bf16 %0, %1, %2, %0\n\tv_mfma_f32_16x16x32_bf16 %0, %3, %4, %0" : "+v"(acc[mf][g])
                          : "v"(tr ? a0 : b0), "v"(tr ? b0 : a0), "v"(tr ? a1 : b1), "v"(tr ? b1 : a1));
    else asm volatile("v_mfma_f32_16x16x32_bf16 %0, %1, %2, %0" : "+v"(acc[mf][g]) : "v"(tr ? a0 : b0), "v"(tr ? b0 : a0));
  };
  typedef __attribute__((address_space(3))) unsigned lds_u32;
  lds_u32* const cnt = (lds_u32*)(shm3 + CNT);
  bool cok;
  constexpr int NY = 28;
  int role, mt2, nt2;
  struct WS { unsigned vb[2][2]; const char* wb[2]; int sh[2]; };
  auto wsetup = [&](const u16* w, int crow0, int l, bool ck) __attribute__((always_inline)) {
    WS r;
    const unsigned vr = ((l >> 5) & 1) * 16 + (wm & 1) * 8 + ((l >> 3) & 3), vc = (((l >> 1) & 3) ^ ((l >> 4) & 1)) * 32 + (l & 1) * 16;
    u64 cp = (u64)(wcopy + (nt2 >> 2) * 589824 + crow0 * 128);
    asm volatile("" : "+s"(cp));
#pragma unroll
    for (int i = 0; i < 2; ++i) {
      u64 dp = (u64)(w + nt2 * 128 + i * 64);
      asm volatile("" : "+s"(dp));
      const bool slow = ck & ((((unsigned)dp >> 7) & 7) == 3);
      r.sh[i] = slow ? 7 : 11;
      r.wb[i] = (const char*)(slow ? cp : dp) + (crow0 ? (wm >> 1) * 32 << r.sh[i] : 0);
      r.vb[i][0] = (vr << r.sh[i]) + vc; r.vb[i][1] = ((vr + 4) << r.sh[i]) + (vc ^ 64);
    }
    asm volatile("" : "+v"(r.vb[0][0]), "+v"(r.vb[0][1]), "+v"(r.vb[1][0]), "+v"(r.vb[1][1]));
    PIN_S(r.wb[0]); PIN_S(r.wb[1]); PIN_S(r.sh[0]); PIN_S(r.sh[1]);
    return r;
  };
  WS wy, wz;
  int vxq, ws0, ws1;
  const char* sz;
  u32x4 xq[4];
  unsigned vg;
  int st = 3;
  const char *sa, *sw[2];
  auto wsel = [&]() {
    const unsigned r = __builtin_amdgcn_readlane(vg, st);
    sw[0] = wy.wb[0] + (r << wy.sh[0]); sw[1] = wy.wb[1] + (r << wy.sh[1]);
  };
  const int p = wid & 3;
  unsigned sl = 0;
#define G2_W "s_add_u32 m0, %[l], %[ow]\n\ts_nop 0\n\tglobal_load_lds_dwordx4 %[v0], %[w0] sc1\n\t" \
             "s_add_u32 m0, m0, 1024\n\ts_nop 0\n\tglobal_load_lds_dwordx4 %[v1], %[w0] sc1\n\t" \
             "s_add_u32 m0, m0, 7168\n\ts_nop 0\n\tglobal_load_lds_dwordx4 %[v2], %[w1] sc1\n\t" \
             "s_add_u32 m0, m0, 1024\n\ts_nop 0\n\tglobal_load_lds_dwordx4 %[v3], %[w1] sc1"
#define G2_WOP(w) [ow] "s"(shm_base + p * 2048), [w0] "s"(sw[0]), [w1] "s"(sw[1]), [v0] "v"(w.vb[0][0]), [v1] "v"(w.vb[0][1]), [v2] "v"(w.vb[1][0]), [v3] "v"(w.vb[1][1])
  auto tile2 = [&](int t0) __attribute__((always_inline)) {
    int b = bid;
    asm volatile("" : "+s"(b));
    role = 1 - ((b >> 2) & 1);
    mt2 = ((role ? b : b ^ 5) & 7) * 4 + ((b >> 3) & 3), nt2 = b >> 5;
    sa = (const char*)abuf + (size_t)(mt2 * 72 + role * NY + t0) * 16384 + wm * 4096;
    asm volatile("" : "+s"(sa));
  };
  auto prep = [&](bool first) __attribute__((always_inline)) {
    int l = threadIdx.x;
    asm volatile("" : "+v"(l));
    l &= 63;
    if (first) {
      tile2(3);
      vxq = ((mt2 * 128 + (wm >> 1) * 64 + role * 32 + 2 * (l & 15)) * D_ + nt2 * 128 + (wm & 1) * 64) * 2 + ((l >> 4) & 1) * 32 + (l >> 5) * 16;
      const int t = min(l < NY ? l : l - DLT, NY - 1), w = wm >> 1;
      const int o = (0xffaa5500fae504ull >> (2 * t)) & 3;
      int u1 = (0x990990 >> ((2 * t) & 31)) & 3, u2 = 3 + (t & 3);
      asm volatile("" : "+v"(u1), "+v"(u2));
      const int u = t < 12 ? u1 : u2;
      vg = (__umul24((l < NY ? role : 1 - role) * 4 + (((o & 1) << 1) | (o >> 1)), 14) + ((0xb85140a >> (4 * u)) & 15) + (u == 5 ? w : 2 * w)) * 32;
      asm volatile("" : "+v"(vg), "+v"(vxq));
      sz = (const char*)abuf + (size_t)(mt2 * 72 + 56) * 16384 + (wm >> 1) * 8192 + role * 4096 + (wm & 1) * 2048;
      PIN_S(sz);
    }
    wy = wsetup(wd, 0, l, first);
    wz = wsetup(wo, F_, l, first);
    ws0 = 64 << wz.sh[0]; PIN_S(ws0);
    ws1 = 64 << wz.sh[1]; PIN_S(ws1);
    SB0();
  };
  auto start2 = [&]() __attribute__((always_inline)) {
    const __amdgpu_buffer_rsrc_t rx = __builtin_amdgcn_make_buffer_rsrc((void*)x, 0, S_ * D_ * 2, 0x00020000);
#pragma unroll
    for (int f = 0; f < 4; ++f) xq[f] = __builtin_bit_cast(u32x4, __builtin_amdgcn_raw_buffer_load_b128(rx, vxq + (f >> 1) * 2048 + (f & 1) * 64, 0, 0));
    cok = __builtin_amdgcn_readfirstlane(cnt[1] & cnt[2]) != 0u;
    if (__builtin_expect(!cok, 0)) prep(false);
    wsel();
    PIN_S(sw[0]); PIN_S(sw[1]);
    SB0();
  };
  auto kloop = [&](auto wmc) {
    constexpr bool LD = decltype(wmc)::value != 0;
    constexpr int DMP = 2 * G1_STAGE;
    const char* pa = nullptr;
    unsigned vob = 0;
    f32x4 ca = {0.f, 0.f, 0.f, 0.f}, cb = {0.f, 0.f, 0.f, 0.f}, cd = ca;
    bf16x8 sel = {0, 0, 0, 0, 0, 0, 0, 0};
    int tro = 0;
    const int psh = (wid == 2 && !tya) ? 1 : 0, pstr = wid < 3 ? qstr : F_ * 2;
    const char* const pbase = wid == 0 ? qb : (wid == 1 ? qb + dp1 : (wid == 2 ? mb0 : gb));
    int lane = threadIdx.x;
    asm volatile("; %1" : "+v"(lane) : "n"(LD ? 1 : 0));
    lane &= 63;
    const int fr = lane & 15, fq = lane >> 4, q4 = fr >> 2, pp = lane & 3;
    const int ff = ((q4 >> 1) & 1) | ((fq & 1) << 1);
    int aoff[2], boff[2];
#pragma unroll
    for (int ks = 0; ks < 2; ++ks) aoff[ks] = (wm * 64 + fr) * 128 + (((ks * 4 + fq) ^ (fr & 7)) * 16);
#pragma unroll
    for (int j = 0; j < 2; ++j) boff[j] = 32768 + (8 * fq + q4) * 128 + (((2 * wc + j) ^ ff) * 32) + pp * 8;
    unsigned vol = lane * 16;
    float sm = 0.f, sq = 0.f;
    const char* pf = (const char*)wd + (bid * 4 + wid) * 7168;
    long pstep = 64 * pstr;
    if (!LD) {
      const int rs = 256 + mtile * 8 + (lane >> 3), vs = ((lane & 7) >> 1) ^ (((rs >> 1) & 1) | ((mtile & 1) << 1));
      pa = pbase + ((psh & vs) ? dgu : 0) + (rs * pstr + (vs >> psh) * 32 + (lane & 1) * 16);
      vob = rs * (F_ * 2) + vs * 32 + (lane & 1) * 16;
      tro = DMP + wid * 1024 + q4 * 128 + ((fq ^ ((q4 >> 1) | ((mtile & 1) << 1))) * 32) + pp * 8;
      const short one = fr == fq ? 0x3f80 : 0;
      sel = bf16x8{one, one, one, one, one, one, one, one};
      asm volatile("" : "+v"(tro), "+v"(vol), "+v"(sel), "+v"(ca), "+v"(cb), "+v"(sm), "+v"(sq));
      PIN_S(pf); PIN_S(pstep);
    }
    auto accum = [&](lds_cptr p, int ob) {
      ca = __builtin_amdgcn_mfma_f32_16x16x32_bf16(tr_frag(p), sel, ca, 0, 0, 0);
      if (wid == 0) cb = __builtin_amdgcn_mfma_f32_16x16x32_bf16(tr_frag(p + ob), sel, cb, 0, 0, 0);
    };
    auto xrow = [&](int i) {
      u64 a = (u64)(x + (size_t)srow * D_ + i * 256), b = (u64)(wo + (bid * 4 + wid) * 1024);
      PIN_S(a); PIN_S(b);
      asm volatile("global_load_dwordx4 %0, %1, %2" : "+v"(cd) : "v"(vol + (vol & (i < 4 ? 512 : 0)) * 3), "s"(i < 4 ? a : b) : "memory");
    };
    const int q32 = 32 * qstr;
    const char *sq0 = qb + q32, *sq1 = sq0 + dp1, *sg0 = gb + 32 * F_ * 2, *sg1 = sg0 + dgu, *sx = ab + (long)(kz + 128 - 32 * D_ * 2);
    u32x2 mp[2] = {__builtin_bit_cast(u32x2, mptr + q32), __builtin_bit_cast(u32x2, mptr + 2 * q32)};
    u64 cy[2];
    unsigned dst = shm_base + G1_STAGE + wm * 1024;
    int dbuf = G1_STAGE;
    auto stage = [&](int i0, int i1) __attribute__((always_inline)) {
#pragma unroll
        for (int i = i0; i < i1; ++i) {
          const int q = i >> 1, h = i & 1;
          const unsigned ld = dst + h * 4096 + (q < 5 ? 32768 + q * 8192 : (q - 5) * 8192);
          if (q < 2) glds16_s((q ? sq1 : sq0) + h * q32, voq, ld);
          else if (q == 2) glds16_v(__builtin_bit_cast(const char*, mp[h]), ld);
          else if (q < 5) glds16_s((q == 4 ? sg1 : sg0) + h * 32 * F_ * 2, vog, ld);
          else glds16_s(sx + ((q - 5) * 64 + h * 32) * D_ * 2, voa, ld);
        }
    };
    u64 cy0 = 0, rt0 = 0;
#pragma unroll
    for (int a = 0; a < 4; ++a) { acc[a][9] = f32x4{0.f, 0.f, 0.f, 0.f}; asm volatile("" : "+v"(acc[a][9])); }
    if (!LD) {
      if (SH & 1) {
        glds16_v(pa - 4 * pstep, shm_base + DMP + 7168);
        if (wid == 0) glds16_s(gb + dgu, vob - 256 * F_ * 2, shm_base + DMP + 7168);
      }
      if (SH & 2) {
        glds16_v(pa - 3 * pstep, shm_base + DMP + 7168);
        if (wid == 0) glds16_s(gb + dgu, vob - 192 * F_ * 2, shm_base + DMP + 7168);
      }
      xrow(0);
      if (wid == 0) WAIT_V(1 + 2 * ((SH & 1) + (SH >> 1))); else WAIT_V(1 + (SH & 1) + (SH >> 1));
    }
    asm volatile("" : "+v"(aoff[0]), "+v"(aoff[1]), "+v"(boff[0]), "+v"(boff[1]));
    bf16x8 kp[2];
    auto body = [&](bool upd, bool keep = false, bool first = false) __attribute__((always_inline)) {
      lds_cptr sb = shm3;
      auto rdb = [&](int g, int i) {
        lds_cptr p = sb + boff[g & 1] + (g >> 1) * 8192 + (i >> 1) * 4096;
        const int s = ((g + 1) & 1) * 2 + (i >> 1);
        if (i & 1) bh[s] = tr_read(p + 512); else bl[s] = tr_read(p);
      };
      auto rda = [&](int ks, int mf) { (ks ? af1 : af0)[mf] = *(const __attribute__((address_space(3))) bf16x8*)(sb + aoff[ks] + mf * 2048); };
      SB0();
      __builtin_amdgcn_s_waitcnt(0xC07F);
#pragma unroll
      for (int gg = 0; gg < 10; ++gg) {
        const int g = (gg + 9) % 10;
#pragma unroll
        for (int t = 0; t < 8; ++t) {
          const bool two = t >= 4 && !(upd && g >= 5 && g <= 8);
          SB0();
          if ((!two || (t & 1)) && !(first && g == 9)) mm(g, two ? t - 1 : t, two, first);
          SB0();
          if (first && !LD && (g == 3 || g == 7) && t == 0) BAR();
          if (g == 9) {
            if (t == 0 || t == 2) { rdb(0, t); rdb(0, t + 1); }
            if (t & 1) { rda((t >> 1) & 1, t >> 1); rda(((t >> 1) & 1) ^ 1, t >> 1); }
          } else if (t < 4) rdb(g + 1, t);
          else if (keep && g == 7) {
            if (t == 5) { WAIT_V(0); kp[0] = tr_frag(shm3 + tro); }
            if (t == 7) kp[1] = tr_frag(shm3 + tro + 4096);
          } else if (upd) {
            if (g == 5 && t == 4) { sq0 += 2 * q32; PIN_S(sq0); }
            if (g == 5 && t == 5) { sq1 += 2 * q32; PIN_S(sq1); }
            if (g == 5 && t == 6) { sg0 += 64 * F_ * 2; PIN_S(sg0); }
            if (g == 6 && t == 4) { sg1 += 64 * F_ * 2; PIN_S(sg1); }
            if (g == 6 && t == 5) { sx += 128; PIN_S(sx); }
            if (g == 6 && t == 6) { dst -= dbuf; PIN_S(dst); }
            if (g == 7 && t == 4) asm volatile("v_add_co_u32 %0, %2, %4, %0\n\tv_add_co_u32 %1, %3, %4, %1" : "+v"(mp[0][0]), "+v"(mp[1][0]), "=&s"(cy[0]), "=&s"(cy[1]) : "s"(2 * q32));
            if (g == 7 && t == 6) asm volatile("v_addc_co_u32 %0, vcc, 0, %0, %2\n\tv_addc_co_u32 %1, vcc, 0, %1, %3" : "+v"(mp[0][1]), "+v"(mp[1][1]) : "s"(cy[0]), "s"(cy[1]) : "vcc");
            if (g == 8 && t == 4) { aoff[0] += dbuf; aoff[1] += dbuf; asm volatile("" : "+v"(aoff[0]), "+v"(aoff[1])); }
            if (g == 8 && t == 5) { boff[0] += dbuf; boff[1] += dbuf; asm volatile("" : "+v"(boff[0]), "+v"(boff[1])); }
            if (g == 8 && t == 6) { dbuf = -dbuf; PIN_S(dbuf); }
          }
        }
      }
      SB0();
    };
    TS1(0);
#define ARRP(o, c) do {} while (0)
    if (LD) {
      stage(0, PA);
      WAIT_V(6 + PA);
      TS1(5);
      BARL();
      stage(PA, PB);
      WAIT_V(2 + PB);
      BAR();
      stage(PB, PC);
      WAIT_V(PC);
      BAR();
      stage(PC, 18);
      body(true, false, true);
      WAIT_V(0);
      BARL();
#pragma clang loop unroll(disable)
      for (int kt = 1; kt < NT - 1; ++kt) {
        if (kt == 2) TS(5);
        if (__builtin_expect(kt == 5 + wid, 0)) prep(true);
        stage(0, 18);
        body(true);
        WAIT_V(0);
        BARL();
        SB0();
      }
      start2();
      body(false);
    } else {
      char* wsx;
      auto tile = [&](int kt, bool keep) __attribute__((always_inline)) {
        SB0();
        ARRP(wid * 64 + kt * 4, true);
        BARL();
        body(false, keep);
        ARRP(320 + kt * 4 + 4, wid == 0);
        if (keep) asm volatile("" : "+v"(kp[0]), "+v"(kp[1])); else WAIT_V(0);
      };
      auto next = [&]() __attribute__((always_inline)) {
        aoff[0] += dbuf; aoff[1] += dbuf; boff[0] += dbuf; boff[1] += dbuf;
        dbuf = -dbuf;
        asm volatile("" : "+v"(aoff[0]), "+v"(aoff[1]), "+v"(boff[0]), "+v"(boff[1]), "+v"(vol), "+v"(tro) :: "memory");
        wsx = ws;
        PIN_S(wsx);
      };
      auto share = [&]() {
        glds16_v(pa, shm_base + DMP + wid * 1024);
        if (wid == 0) glds16_s(gb + dgu, vob, shm_base + DMP + 4096);
      };
      auto kept = [&]() {
        ca = __builtin_amdgcn_mfma_f32_16x16x32_bf16(kp[0], sel, ca, 0, 0, 0);
        if (wid == 0) cb = __builtin_amdgcn_mfma_f32_16x16x32_bf16(kp[1], sel, cb, 0, 0, 0);
      };
      auto cpub = [&](u32x4 a, u32x4 b) {
        if ((vol & 0xc0) == 0) {
          const char* const cs = wsx + 101 * MB_ + ntile * 10240 + mtile * 256;
          const unsigned co = (vol & 0x30) * 4 + (vol >> 8) * 16;
          gst16(cs + wid * 2048, co, a);
          if (wid == 0) gst16(cs + 8192, co, b);
        }
      };
      auto side0 = [&](int kt) __attribute__((always_inline)) {
        if (kt < 2) {
          glds16_v(pa - 2 * pstep, shm_base + DMP + 7168);
          if (wid == 0) glds16_s(gb + dgu, vob - 128 * F_ * 2, shm_base + DMP + 7168);
        }
        share();
        u32x4 xw;
        asm volatile("v_mov_b32 %0, %4\n\tv_mov_b32 %1, %5\n\tv_mov_b32 %2, %6\n\tv_mov_b32 %3, %7" : "=&v"(xw[0]), "=&v"(xw[1]), "=&v"(xw[2]), "=&v"(xw[3])
                     : "v"(cd[0]), "v"(cd[1]), "v"(cd[2]), "v"(cd[3]));
        xrow(kt + 1);
        next();
        if (kt) kept();
        pa += pstep; vob += 64 * F_ * 2;
        accum(shm3 + tro + ((kt & 1) * G1_STAGE + 32768 + mtile * 1024 + wid * 7168 - DMP), 4 * 8192);
#pragma unroll
        for (int i = 0; i < 4; ++i) {
          float lo, hi;
          asm volatile("v_lshlrev_b32 %2, 16, %4\n\tv_and_b32 %3, 0xffff0000, %4\n\tv_add_f32 %0, %0, %2\n\tv_fmac_f32 %1, %2, %2\n\t"
                       "v_add_f32 %0, %0, %3\n\tv_fmac_f32 %1, %3, %3" : "+v"(sm), "+v"(sq), "=&v"(lo), "=&v"(hi) : "v"(xw[i]));
        }
      };
      BARL();
      body(false, false, true);
      WAIT_V(0);
      side0(0);
#pragma clang loop unroll(disable)
      for (int kt = 1; kt < 4; ++kt) {
        tile(kt, true);
        side0(kt);
      }
#pragma clang loop unroll(disable)
      for (int kt = 4; kt < 12; ++kt) {
        tile(kt, true);
        share();
        glds16_s(pf, vol, shm_base + DMP + 7168);
        next();
        kept();
        pa += pstep; vob += 64 * F_ * 2;
        pf = kt == 10 ? (const char*)wo + (bid * 4 + wid) * 2048 + 1024 : pf + 1024;
        if (kt < 8) {
          const unsigned q1 = bid * 4 + wid + (kt - 5) * 1024, q0 = q1 - 1024;
          if (q0 < 1152) gst16(wsx + 98 * MB_ + (q0 & 1) * 589824 + (q0 >> 1) * 1024, vol, *(const u32x4*)(shm + AUX + wid * 1024 + vol));
          if (q1 < 1152) {
            const char* src = (const char*)((q1 >> 1) < 448 ? wd + (size_t)(q1 >> 1) * 8 * D_ : wo + (size_t)((q1 >> 1) - 448) * 8 * D_);
            src += ((3 - ((unsigned)(uintptr_t)src >> 7)) & 7) * 128 + (q1 & 1) * 1024;
            glds16_s(src, (vol >> 7) * 2048 + (vol & 0x70), shm_base + AUX + wid * 1024);
          }
          u32x4 bk;
          asm volatile("v_mov_b32 %0, %1" : "=v"(bk[0]) : "s"(BLANK));
          bk[1] = bk[0]; bk[2] = bk[0]; bk[3] = bk[0];
          gst16(wsx + 64 * MB_ + bid * 16384 + wid * 4096 + (kt - 4) * 1024, vol, bk);
          if (tya && (kt == 4 || kt == 7)) gst16(wsx + 4 * MB_ + (mtile * 32 + ntile) * 4096 + wid * 2048 + (kt & 1) * 1024, vol, bk);
          if (kt == 4) cpub(bk, bk);
        } else {
          if (kt == 8) {
            const float ml = sm * (1.0f / 32), mean = half_sum(sm) * (1.0f / D_), dm = ml - mean;
            const float q = __builtin_fmaf(dm * 32.0f, dm, __builtin_fmaf(-ml, sm, sq));
            const unsigned ps = pack_stats(mean, fmaxf(half_sum(q) * (1.0f / D_), 0.f));
            const u64 now = __builtin_amdgcn_s_memrealtime();
            if ((vol & 0x1f0) == 0) gst16(wsx + 97 * MB_ + srow * 16, vol >> 5, u32x4{ps, (unsigned)now, ps, (unsigned)(now >> 32)});
          }
          if (kt == 9) {
            if (wid == 0) {
              u64 now;
              asm volatile("s_memrealtime %0\n\ts_waitcnt lgkmcnt(0)" : "=s"(now) :: "memory");
              if (vol == 0) asm volatile("global_store_dwordx2 %0, %1, %2 sc1" :: "v"(vol), "v"(now), "s"(wsx + 100 * MB_ + 131072 + bid * 8) : "memory");
            }
            if (wid == 2) glds16_s1(wsx + 102 * MB_ + (bid & ~1) * 512, vol, shm_base + DMP + 7168);
          }
        }
      }
      asm volatile("" :: "v"(cd));
      if (wid >= 2) glds16_s1(wsx + 100 * MB_ + 131072 + (wid - 2) * 1024, vol, shm_base + DMP + 3072 + wid * 1024);
      tile(12, true);
      next();
      kept();
      const int lane = vol >> 4;
      glds16_s1(wsx + 97 * MB_ + (m0 + wid * 64) * 16, ((lane & 32) + 2 * (lane & 15) + ((lane >> 4) & 1)) * 16, shm_base + AUX + wid * 1024);
      asm volatile("s_nop 7\n\ts_nop 3" : "+v"(ca), "+v"(cb));
      cpub(__builtin_bit_cast(u32x4, ca), __builtin_bit_cast(u32x4, cb));
      if (wid >= 2) {
        u32x4 c4 = *(const u32x4*)(shm + DMP + 3072 + wid * 1024 + vol);
        bool ok = __all(fresh(((u64)c4[1] << 32) | c4[0], t_own) & fresh(((u64)c4[3] << 32) | c4[2], t_own));
        while (__builtin_expect(!ok, 0) && __builtin_amdgcn_s_memrealtime() - t_own < TIMEOUT_ / 10) {
          const __amdgpu_buffer_rsrc_t rs = __builtin_amdgcn_make_buffer_rsrc((void*)(wsx + 100 * MB_ + 131072), 0, 2048, 0x00020000);
          asm volatile("" ::: "memory");
          c4 = __builtin_bit_cast(u32x4, __builtin_amdgcn_raw_buffer_load_b128(rs, (wid - 2) * 1024 + vol, 0, 16));
          ok = __all(fresh(((u64)c4[1] << 32) | c4[0], t_own) & fresh(((u64)c4[3] << 32) | c4[2], t_own));
        }
        *(unsigned*)(shm + CNT - 4 + wid * 4) = ok ? 1u : 0u;
      }
      tile(13, false);
      next();
      u32x4 cw[4];
      auto cgather = [&]() {
        const __amdgpu_buffer_rsrc_t rc = __builtin_amdgcn_make_buffer_rsrc((void*)(wsx + 101 * MB_ + ntile * 10240), 0, 5 * 2048, 0x00020000);
        const unsigned vcs = (vol < 512 ? wid * 2048 : 8192) + (vol & 256) * 4 + (vol & 0xf0);
#pragma unroll
        for (int i = 0; i < 4; ++i) cw[i] = __builtin_bit_cast(u32x4, __builtin_amdgcn_raw_buffer_load_b128(rc, vcs + i * 256, 0, 16));
      };
      {
        const int ln = wid * 64 + (vol >> 4);
        u32x4 gr = *(const u32x4*)(shm + AUX + ln * 16);
        if (__builtin_expect(!__all((gr[0] == gr[2]) & fresh(((u64)gr[3] << 32) | gr[1], t_own)), 0)) {
          const __amdgpu_buffer_rsrc_t rs = __builtin_amdgcn_make_buffer_rsrc((void*)(wsx + 97 * MB_ + m0 * 16), 0, 4096, 0x00020000);
          const int prow = (ln & 0xe0) + 2 * (ln & 15) + ((ln >> 4) & 1);
          for (;;) {
            gr = __builtin_bit_cast(u32x4, __builtin_amdgcn_raw_buffer_load_b128(rs, prow * 16, 0, 16));
            if (__all((gr[0] == gr[2]) & fresh(((u64)gr[3] << 32) | gr[1], t_own))) break;
            if (__builtin_amdgcn_s_memrealtime() - t_own > TIMEOUT_) { gr[0] = row_stats_serial(x + (size_t)(m0 + prow) * D_); break; }
            __builtin_amdgcn_s_sleep(2);
          }
        }
        cgather();
        const float nm = -lo_f(gr[0]), dn = hi_f(gr[0]), r = __builtin_amdgcn_rcpf(dn);
        auto mul = [](float a, float b) { float c; asm("v_mul_f32 %0, %1, %2" : "=v"(c) : "v"(a), "v"(b)); return c; };
        const f32x4 fc = {nm, -2.3022081986f * r, mul(mul(mul(-0.10294324f, r), r), r), mul(dn, dn)};
        *(f32x4*)(shm + AUX + ln * 16) = fc;
        float* const tw = (float*)(shm + 2 * G1_STAGE + 4096 + wid * 768) + ln;
#pragma unroll
        for (int i = 0; i < 4; ++i) tw[i * 64] = fc[i];
        ((float*)(shm + AUX + 4096))[384 + ln] = r;
        cok = __builtin_amdgcn_readfirstlane(cnt[1] & cnt[2]) != 0u;
        tile2(0);
        wy = wsetup(wd, 0, vol >> 4, cok);
      }
      tile(14, false);
      next();
      {
        while (__builtin_expect(blank(cw) && cok, 0)) {
          __builtin_amdgcn_s_sleep(2);
          asm volatile("" ::: "memory");
          cgather();
        }
        f32x4 t;
#pragma unroll
        for (int i = 0; i < 4; ++i) {
          float a, b;
          asm("v_add_f32 %0, %2, %3\n\tv_add_f32 %1, %4, %5\n\tv_add_f32 %0, %0, %1" : "=&v"(a), "=&v"(b) : "v"(cw[0][i]), "v"(cw[1][i]), "v"(cw[2][i]), "v"(cw[3][i]));
          const auto r2 = __builtin_amdgcn_permlane16_swap(__float_as_uint(a), __float_as_uint(a), false, false);
          asm("v_add_f32 %0, %1, %2" : "=v"(t[i]) : "v"(r2[0]), "v"(r2[1]));
        }
        const int l = vol >> 4;
        if (__builtin_expect(!cok, 0)) {
          const int gq = (l >> 2) & 3;
          const char* pc = l < 32 ? pbase + ((psh & gq) ? dgu : 0) + (gq >> psh) * 32 + (l & 3) * 8 : gb + dgu + (l & 15) * 8;
          t = f32x4{0.f, 0.f, 0.f, 0.f};
#pragma unroll 1
          for (int k = 0; k < D_; ++k) { const u32x2 w = *(const __attribute__((address_space(1))) u32x2*)(pc + (size_t)k * (l < 32 ? pstr : F_ * 2)); t += f32x4{lo_f(w[0]), hi_f(w[0]), lo_f(w[1]), hi_f(w[1])}; }
        }
        if (l < 32 || wid == 0)
          *(f32x4*)((float*)(shm + AUX + 4096) + ((l >> 3) & 1) * 192 + (l < 32 ? wid : 4) * 32 + (l & 7) * 4) = t;
        __builtin_amdgcn_s_setprio(2);
      }
      SB0();
      ARRP(wid * 64 + 60, true);
      BARL();
      body(false);
    }
#pragma unroll
    for (int t = 0; t < 8; t += 2) mm(9, t, true);
    asm volatile("s_nop 7\n\ts_nop 3");
    SB0();
    TS1(3);
    if (!LD) {
#pragma unroll
      for (int i = 0; i < 3; ++i) {
        const unsigned r = ((role * 4 + (i & 1) * 2) * 14 + (i < 2 ? 10 : 0) + (wm & 2)) * 32;
        sw[0] = wy.wb[0] + (r << wy.sh[0]); sw[1] = wy.wb[1] + (r << wy.sh[1]);
        asm volatile(G2_W :: [l] "s"((i + 1) * 16384), G2_WOP(wy) : "memory", "scc", "m0");
      }
    } else {
      __builtin_amdgcn_s_setprio(3);
      WAIT_V(0);
    }
  };
  if (wid >= 4) kloop(std::integral_constant<int, 1>{});
  else kloop(std::integral_constant<int, 0>{});
  __builtin_amdgcn_sched_barrier(0);
  int tz = threadIdx.x;
  asm volatile("" : "+v"(tz));
  __builtin_amdgcn_sched_barrier(0);
  const int li = tz & 15, lq = (tz >> 4) & 3, ln = tz & 63;
  const __amdgpu_buffer_rsrc_t ra = __builtin_amdgcn_make_buffer_rsrc((void*)abuf, 0, 16 * 72 * 16384, 0x00020000);
  float* const cfin = (float*)(shm + AUX + 4096);
  float* const svr = cfin + 384;
  float* const tb = (float*)(shm + 2 * G1_STAGE + 4096 + wm * 1024);
  auto signal = [&](unsigned v) {
    unsigned a, b;
    asm volatile("v_mov_b32 %0, %2\n\tv_mov_b32 %1, %3\n\ts_mov_b64 exec, 1\n\tds_add_u32 %0, %1\n\ts_mov_b64 exec, -1"
                 : "=&v"(a), "=&v"(b) : "s"(shm_base + CNT), "s"(v) : "memory");
  };
  auto await = [&](unsigned mask, unsigned val) {
    unsigned a, t;
    asm volatile("v_mov_b32 %0, %2\n1:\n\tds_read_b32 %1, %0\n\ts_waitcnt lgkmcnt(0)\n\tv_readfirstlane_b32 vcc_lo, %1\n\ts_and_b32 vcc_lo, vcc_lo, %3\n\t"
                 "s_cmp_eq_u32 vcc_lo, %4\n\ts_cbranch_scc1 2f\n\ts_sleep 1\n\ts_branch 1b\n2:" : "=&v"(a), "=&v"(t) : "s"(shm_base + CNT), "s"(mask), "s"(val) : "memory", "scc", "vcc");
  };
  auto client = [&](int l) { const int m128 = mtile * 2 + ((l >> 4) & 1); return (((l & 7) * 4 + (m128 & 3)) * 8) + ((m128 >> 2) ^ ((l & 8) ? 5 : 0)); };
  auto notify = [&](int l0, int nl) __attribute__((always_inline)) {
    u64 now;
    asm volatile("s_memrealtime %0\n\ts_waitcnt lgkmcnt(0)" : "=s"(now) :: "memory");
    int l = threadIdx.x;
    asm volatile("" : "+v"(l));
    l &= 63;
    const __amdgpu_buffer_rsrc_t rd1 = __builtin_amdgcn_make_buffer_rsrc((void*)(done1 + ntile), 0, 256 * 512, 0x00020000);
    if (l < nl) __builtin_amdgcn_raw_buffer_store_b64(u32x2{(unsigned)now, (unsigned)(now >> 32)}, rd1, client(l0 + l) * 512, 0, 16);
  };
  const int xo = ((xcd & 1) << 1) | ((xcd >> 1) & 1), te = (xcd >> 2) * 28 + 6 * (xo >> 1) + (xo & 1), tl = (xcd >> 2) * 28 + 12 + 4 * xo;
  const u64 te0 = __builtin_amdgcn_s_memrealtime();
  const __amdgpu_buffer_rsrc_t re = __builtin_amdgcn_make_buffer_rsrc((void*)(done1 + client((wm >> 1) * 16 + 8 - (xcd & 4) * 2) * 64 + 32 + (ntile & 15) * 2 + (wm & 1)),
                                                                      0, 7 * 16384 + 8, 0x00020000);
  auto early = [&]() {
    __builtin_amdgcn_raw_buffer_store_b64(u32x2{(unsigned)te0, (unsigned)(te0 >> 32)}, re, ln * 16384, 0, 16);
  };
  signal(1u << (8 * wc));
  if (wc) {
    await(0xffffu, 0x404u);
#pragma unroll
    for (int f = 0; f < 4; ++f) *(u32x4*)(shm + G2_X + wm * 4096 + f * 1024 + ln * 16) = xq[f];
    SB0();
    __builtin_amdgcn_s_setprio(1);
  }
  TS1(4);
  f32x4 rf[4];
#pragma unroll
  for (int mf = 0; mf < 4; ++mf) rf[mf] = *(const f32x4*)(shm + AUX + (wm * 64 + mf * 16 + li) * 16);
  const float* const cl = cfin + wc * 192 + lq * 4;
  const int sbase = ((mtile * 2 + (wm >> 1)) * 72 * 8 + (wm & 1) * 4) * 2048;
  auto putf = [&](int kg, int mf, u32x4 v) {
    __builtin_amdgcn_raw_buffer_store_b128(v, ra, ln * 16 + sbase + ((kg >> 1) * 8 + mf) * 2048 + (kg & 1) * 1024, 0, 16);
  };
  auto gelp = [&](f32x2 g, f32x2 u, f32x2 cg, f32x2 cu, f32x2 m2, f32x2 k12, f32x2 k22, f32x2 d2) {
    const f32x2 tg = __builtin_elementwise_fma(m2, cg, g), tu = __builtin_elementwise_fma(m2, cu, u);
    const f32x2 arg = tg * __builtin_elementwise_fma(tg * tg, k22, k12);
    const f32x2 dn = __builtin_elementwise_fma(f32x2{__builtin_amdgcn_exp2f(arg[0]), __builtin_amdgcn_exp2f(arg[1])}, d2, d2);
    const f32x2 r = (tg * tu) * f32x2{__builtin_amdgcn_rcpf(dn[0]), __builtin_amdgcn_rcpf(dn[1])};
    return pack2(r[0], r[1]);
  };
  auto gel2 = [&](int p, int kg, int mf0 = 0, int mf1 = 4) {
    f32x4 cg[2], cu[2];
#pragma unroll
    for (int j = 0; j < 2; ++j) { cg[j] = *(const f32x4*)(cl + p * 32 + j * 16); cu[j] = *(const f32x4*)(cl + p * 32 + 32 + j * 16); }
#pragma unroll
    for (int mf = mf0; mf < mf1; ++mf) {
      const f32x2 m2 = {rf[mf][0], rf[mf][0]}, k12 = {rf[mf][1], rf[mf][1]}, k22 = {rf[mf][2], rf[mf][2]}, d2 = {rf[mf][3], rf[mf][3]};
      u32x4 o;
#pragma unroll
      for (int i = 0; i < 4; ++i) {
        const int j = i >> 1, e = (i & 1) * 2;
        const f32x4 g = acc[mf][p * 2 + j], u = acc[mf][p * 2 + 2 + j];
        o[i] = gelp(f32x2{g[e], g[e + 1]}, f32x2{u[e], u[e + 1]}, f32x2{cg[j][e], cg[j][e + 1]}, f32x2{cu[j][e], cu[j][e + 1]}, m2, k12, k22, d2);
      }
      putf(kg, mf, o);
    }
  };
  int tsel = ntile & 1;
  asm volatile("" : "+s"(tsel));
  if (tsel != 0) {
    const int tq = (wc ? tl : te + 2 + (xo & 1)) + he;
    gel2(0, 2 * tq);
    gel2(3, 2 * tq + 1);
    const float cg = cfin[wc * 192 + 64 + li], cu = cfin[wc * 192 + 80 + li];
#pragma unroll
    for (int mf = 0; mf < 4; ++mf) {
      f32x4 t[4];
#pragma unroll
      for (int i = 0; i < 4; ++i) t[i] = *(const f32x4*)(tb + i * 64 + mf * 16 + lq * 4);
      const f32x4 g = acc[mf][4], u = acc[mf][5];
      u32x2 o;
#pragma unroll
      for (int e = 0; e < 4; e += 2)
        o[e >> 1] = gelp(f32x2{g[e], g[e + 1]}, f32x2{u[e], u[e + 1]}, f32x2{cg, cg}, f32x2{cu, cu}, f32x2{t[0][e], t[0][e + 1]},
                         f32x2{t[1][e], t[1][e + 1]}, f32x2{t[2][e], t[2][e + 1]}, f32x2{t[3][e], t[3][e + 1]});
      *(u32x2*)(shm + wm * 4096 + mf * 1024 + wc * 512 + li * 32 + lq * 8) = o;
      if (mf == EPOS && wc == 0) {
        TS1(1);
        WAIT_V(0);
        TS1(6);
        early();
      }
    }
    if (wc == 0) {
      signal(0x10000u << wid);
    } else {
      await(0x1000u << wid, 0x1000u << wid);
#pragma unroll
      for (int mf = 0; mf < 4; ++mf) putf(2 * (tl + 2) + he, mf, __builtin_bit_cast(u32x4, tr_frag(shm3 + wm * 4096 + mf * 1024 + ln * 8)));
    }
  } else {
    float rd[4];
#pragma unroll
    for (int mf = 0; mf < 4; ++mf) rd[mf] = svr[wm * 64 + mf * 16 + li];
    auto cvq = [&](int p, int mf, const f32x4& c0, const f32x4& c1) {
      u32x4 o;
#pragma unroll
      for (int j = 0; j < 2; ++j) {
        const f32x4 a = acc[mf][p * 2 + j], c4 = j ? c1 : c0;
        const f32x2 m2 = {rf[mf][0], rf[mf][0]}, r2 = {rd[mf], rd[mf]};
        f32x2 t0 = __builtin_elementwise_fma(m2, f32x2{c4[0], c4[1]}, f32x2{a[0], a[1]});
        f32x2 t1 = __builtin_elementwise_fma(m2, f32x2{c4[2], c4[3]}, f32x2{a[2], a[3]});
        if (p) { t0 *= r2; t1 *= r2; }
        o[2 * j] = pack2(t0[0], t0[1]); o[2 * j + 1] = pack2(t1[0], t1[1]);
      }
      return o;
    };
    const float cv0 = cfin[wc * 192 + 64 + li], cv1 = cfin[wc * 192 + 80 + li];
    auto cvv = [&](int mf) {
      const f32x4 n4 = *(const f32x4*)(tb + mf * 16 + lq * 4), r4 = *(const f32x4*)(svr + wm * 64 + mf * 16 + lq * 4);
      u32x4 o;
#pragma unroll
      for (int j = 0; j < 2; ++j) {
        const f32x4 a = acc[mf][4 + j];
        const f32x2 c2 = {j ? cv1 : cv0, j ? cv1 : cv0};
        const f32x2 t0 = __builtin_elementwise_fma(f32x2{n4[0], n4[1]}, c2, f32x2{a[0], a[1]}) * f32x2{r4[0], r4[1]};
        const f32x2 t1 = __builtin_elementwise_fma(f32x2{n4[2], n4[3]}, c2, f32x2{a[2], a[3]}) * f32x2{r4[2], r4[3]};
        o[2 * j] = pack2(t0[0], t0[1]); o[2 * j + 1] = pack2(t1[0], t1[1]);
      }
      return o;
    };
    u32x4 kx[4], vx[4], qx[4], hx[4];
    const bf16x4 ones = {0x3f80, 0x3f80, 0x3f80, 0x3f80};
    const f32x4 ck0 = *(const f32x4*)(cl + 32), ck1 = *(const f32x4*)(cl + 48);
    kx[2] = cvq(1, 2, ck0, ck1); kx[3] = cvq(1, 3, ck0, ck1);
    vx[2] = cvv(2); vx[3] = cvv(3);
    auto exo = [](int e) { return e * 4096 + (e >> 2) * (G2_A - 16384); };
    const bool needh = wm == 0 && mtile > 0;
    const __amdgpu_buffer_rsrc_t rh = __builtin_amdgcn_make_buffer_rsrc((void*)((char*)halo + ((needh ? mtile - 1 : mtile) * 32 + ntile + wc) * 4096), 0,
                                                                        needh || wm == 3 ? 4096 : 0, 0x00020000);
    if (wm < 3) {
      char* const ex = shm + exo(wc * 3 + wm) + ln * 16;
      *(u32x4*)ex = kx[2]; *(u32x4*)(ex + 1024) = kx[3]; *(u32x4*)(ex + 2048) = vx[2]; *(u32x4*)(ex + 3072) = vx[3];
      signal(0x10000u << wid);
    } else {
#pragma unroll
      for (int r = 0; r < 4; ++r) __builtin_amdgcn_raw_buffer_store_b128(r < 2 ? kx[2 + r] : vx[r], rh, ln * 16 + r * 1024, 0, 16);
    }
    auto hload = [&]() {
#pragma unroll
      for (int r = 0; r < 4; ++r) hx[r] = __builtin_bit_cast(u32x4, __builtin_amdgcn_raw_buffer_load_b128(rh, ln * 16 + r * 1024, 0, 16));
    };
    SB0();
    gel2(3, 2 * (wc ? tl + 3 : te) + he);
    SB0();
    if (wm == 0) hload();
    SB0();
    kx[0] = cvq(1, 0, ck0, ck1); kx[1] = cvq(1, 1, ck0, ck1);
    vx[0] = cvv(0); vx[1] = cvv(1);
    {
      const f32x4 cq0 = *(const f32x4*)(cl), cq1 = *(const f32x4*)(cl + 16);
#pragma unroll
      for (int mf = 0; mf < 4; ++mf) qx[mf] = cvq(0, mf, cq0, cq1);
    }
    f32x4 mk0, mk1;
    u32x2 pm;
#pragma unroll
    for (int j = 0; j < 4; ++j) {
      const int d = 4 * lq + j - li;
      mk0[j] = d >= 9 ? 0.f : -1.0e30f;
      mk1[j] = (unsigned)(d + 7) <= 7u ? 0.f : -1.0e30f;
    }
#pragma unroll
    for (int j = 0; j < 2; ++j) pm[j] = (4 * lq + 2 * j > li ? 0xffffu : 0u) | (4 * lq + 2 * j + 1 > li ? 0xffff0000u : 0u);
    auto max3 = [](float a, float b, float c) { float r; asm("v_max3_f32 %0, %1, %2, %3" : "=v"(r) : "v"(a), "v"(b), "v"(c)); return r; };
    auto max2 = [](float a, float b) { float r; asm("v_max_f32 %0, %1, %2" : "=v"(r) : "v"(a), "v"(b)); return r; };
    asm volatile("" : "+v"(kx[0]), "+v"(kx[1]), "+v"(vx[0]), "+v"(vx[1]), "+v"(qx[0]), "+v"(qx[1]), "+v"(qx[2]), "+v"(qx[3]), "+v"(mk0), "+v"(mk1), "+v"(pm));
    TS1(1);
    if (wc == 0) {
      if (wm == 0) WAIT_V(4); else WAIT_V(0);
      early();
    }
    if (wm > 0) {
      await(0x8000u << wid, 0x8000u << wid);
#pragma unroll
      for (int i = 0; i < 4; ++i) hx[i] = *(const u32x4*)(shm + exo(wc * 3 + wm - 1) + i * 1024 + ln * 16);
    } else if (__builtin_expect(cok || !needh, 1)) {
      while (__builtin_expect(blank(hx), 0)) {
        __builtin_amdgcn_s_sleep(1);
        asm volatile("" ::: "memory");
        hload();
      }
    } else {
      await(0xffffu, 0x404u);
      char* const fb = shm + exo(6 + wc) + ln * 16;
#pragma unroll 1
      for (int i = 0; i < 32; ++i) {
        const int rec = i >> 3, e = i & 7;
        const int row = m0 - 32 + 2 * (rec < 2 ? li : lq * 4 + (e & 3)) + (rec & 1);
        const int col = (rec < 2 ? D_ + lq * 4 + (e & 3) : 2 * D_ + li) + (ntile + wc) * 32 + (e >> 2) * 16;
        const u16* xr = x + (size_t)row * D_;
        const unsigned stt = row_stats_serial(xr);
        const float mb = lo_f(stt);
        float a = 0.f;
#pragma unroll 1
        for (int k = 0; k < D_; ++k) a += (bf2f(xr[k]) - mb) * bf2f(wqkv[(size_t)k * 3072 + col]);
        *(u16*)(fb + rec * 1024 + e * 2) = f2bf(a / hi_f(stt));
      }
#pragma unroll
      for (int i = 0; i < 4; ++i) hx[i] = *(const u32x4*)(fb + i * 1024);
    }
    TS1(6);
    {
      const f32x4 z = {0.f, 0.f, 0.f, 0.f};
      const float mz = mtile == 0 && wm == 0 ? -1.0e30f : 0.f;
      const f32x4 mkf = mk0 + f32x4{mz, mz, mz, mz};
      f32x4 s0[4], s1[4];
#pragma unroll
      for (int i = 0; i < 4; ++i) {
        const int n = i ^ 2;
        const bf16x8 qf = __builtin_bit_cast(bf16x8, qx[n]);
        s1[n] = __builtin_amdgcn_mfma_f32_16x16x32_bf16(__builtin_bit_cast(bf16x8, kx[n]), qf, mk1, 0, 0, 0);
        s0[n] = __builtin_amdgcn_mfma_f32_16x16x32_bf16(__builtin_bit_cast(bf16x8, n < 2 ? hx[n] : kx[n - 2]), qf, n < 2 ? mkf : mk0, 0, 0, 0);
      }
      SB0();
      asm volatile("s_nop 7\n\ts_nop 3" : "+v"(s0[0]), "+v"(s1[0]), "+v"(s0[1]), "+v"(s1[1]), "+v"(s0[2]), "+v"(s1[2]), "+v"(s0[3]), "+v"(s1[3]));
      float mx[4];
#pragma unroll
      for (int n = 0; n < 4; ++n)
#pragma unroll
        for (int j = 0; j < 4; ++j) s0[n][j] = max2(s0[n][j], s1[n][j]);
#pragma unroll
      for (int n = 0; n < 4; ++n) mx[n] = max3(s0[n][0], s0[n][1], s0[n][2]);
#pragma unroll
      for (int n = 0; n < 4; ++n) mx[n] = max2(mx[n], s0[n][3]);
#pragma unroll
      for (int n = 0; n < 4; ++n) { auto r = __builtin_amdgcn_permlane16_swap(__float_as_uint(mx[n]), __float_as_uint(mx[n]), false, false);
        mx[n] = max2(__uint_as_float(r[0]), __uint_as_float(r[1])); }
#pragma unroll
      for (int n = 0; n < 4; ++n) { auto r = __builtin_amdgcn_permlane32_swap(__float_as_uint(mx[n]), __float_as_uint(mx[n]), false, false);
        mx[n] = max2(__uint_as_float(r[0]), __uint_as_float(r[1])); }
      SB0();
      constexpr float cs2 = 0.17677669529663687f * 1.4426950408889634f;
      f32x4 os[4], o0[4], o1[4];
#pragma unroll
      for (int n = 0; n < 4; ++n) {
        const float c = cs2 * rd[n], nm = -mx[n] * c;
        const f32x4 ar = __builtin_elementwise_fma(s0[n], f32x4{c, c, c, c}, f32x4{nm, nm, nm, nm});
        const u32x2 pk = {pack2(__builtin_amdgcn_exp2f(ar[0]), __builtin_amdgcn_exp2f(ar[1])), pack2(__builtin_amdgcn_exp2f(ar[2]), __builtin_amdgcn_exp2f(ar[3]))};
        const bf16x4 pa = __builtin_bit_cast(bf16x4, pk), p0 = __builtin_bit_cast(bf16x4, pk & pm), p1 = __builtin_bit_cast(bf16x4, pk & ~pm);
        const u32x4 vp = n < 2 ? hx[2 + n] : vx[n - 2];
        auto half = [](const u32x4& v, int h) { return __builtin_bit_cast(bf16x4, u32x2{v[2 * h], v[2 * h + 1]}); };
        os[n] = __builtin_amdgcn_mfma_f32_16x16x16bf16_1k(ones, pa, z, 0, 0, 0);
        o0[n] = __builtin_amdgcn_mfma_f32_16x16x16bf16_1k(half(vx[n], 0), p1, __builtin_amdgcn_mfma_f32_16x16x16bf16_1k(half(vp, 0), p0, z, 0, 0, 0), 0, 0, 0);
        o1[n] = __builtin_amdgcn_mfma_f32_16x16x16bf16_1k(half(vx[n], 1), p1, __builtin_amdgcn_mfma_f32_16x16x16bf16_1k(half(vp, 1), p0, z, 0, 0, 0), 0, 0, 0);
      }
#pragma unroll
      for (int n = 0; n < 4; ++n) {
        const float inv = __builtin_amdgcn_rcpf(os[n][0]);
        const f32x4 n0 = o0[n] * inv, n1 = o1[n] * inv;
        putf(112 + ntile + wc, n, u32x4{pack2(n0[0], n0[1]), pack2(n0[2], n0[3]), pack2(n1[0], n1[1]), pack2(n1[2], n1[3])});
      }
    }
  }
  TS1(2);
  {
  __builtin_amdgcn_sched_barrier(0);
  int tid = threadIdx.x;
  asm volatile("" : "+v"(tid));
  __builtin_amdgcn_sched_barrier(0);
  const int lane = tid & 63;
  const int wm = wid >> 1, wn = wid & 1;
  const int m0 = mt2 * 128, n0 = nt2 * 128;
  constexpr int PD = G2_NSTAGE;
  const int ny = (xcd & 1) ? NY - DLT : NY + DLT;
  const unsigned va = lane * 16;
  auto stage_y = [&]() {
    asm volatile("s_add_u32 m0, %[l], %[oa]\n\ts_nop 0\n\tglobal_load_lds_dwordx4 %[va], %[a] sc1\n\tglobal_load_lds_dwordx4 %[va], %[a] offset:1024 sc1\n\t"
                 "global_load_lds_dwordx4 %[va], %[a] offset:2048 sc1\n\tglobal_load_lds_dwordx4 %[va], %[a] offset:3072 sc1\n\t" G2_W
                 :: [l] "s"(sl), [oa] "s"(shm_base + G2_A + (p ^ role) * 4096), [a] "s"(sa), [va] "v"(va), G2_WOP(wy) : "memory", "scc", "m0");
    ++st;
    sa += 16384;
    sl = (sl + 16384) & 65535;
    wsel();
  };
  u64 ready = 0;
  const u64* const mbox = &done1[bid * 64 + lane];
  auto seen = [&](u64 v) __attribute__((always_inline)) {
    const u64 r = __ballot(fresh(v, t_own));
    u64 d;
    asm("s_bitreplicate_b64_b32 %0, %1" : "=s"(d) : "s"((unsigned)r >> (16 * role)));
    ready |= r | (d << 32);
  };
  auto need = [&](u64 mask, bool renew) __attribute__((always_inline)) {
    if ((ready & mask) != mask) {
      seen(__hip_atomic_load(mbox, RLX_AGENT));
      while ((ready & mask) != mask) {
        __builtin_amdgcn_s_sleep(PSLEEP);
        if (renew && !cok) notify(0, 32);
        seen(__hip_atomic_load(mbox, RLX_AGENT));
      }
    }
    asm volatile("" ::: "memory");
  };
  u64 fl = 0;
  auto peek = [&]() { asm volatile("global_load_dwordx2 %0, %1, %2 sc1" : "+v"(fl) : "v"(va >> 1), "s"(done1 + bid * 64) : "memory"); };
  auto check = [&](u64 mask, bool renew) __attribute__((always_inline)) {
    asm volatile("" : "+v"(fl));
    seen(fl);
    if (__builtin_expect((ready & mask) != mask, 0)) need(mask, renew);
  };
  if (wid >= 4) {
    TS(4);
    __builtin_amdgcn_s_setprio(0);
    BARL();
    stage_y();
    stage_y();
    BAR();
    if (p == 1) peek();
    stage_y();
    WAIT_V(16);
    BAR();
    if (p == 0) notify(0, 32);
#define G2_A2 "global_load_lds_dwordx4 %[va], %[a] sc1\n\tglobal_load_lds_dwordx4 %[va], %[a] offset:1024 sc1"
#define G2_A4 G2_A2 "\n\tglobal_load_lds_dwordx4 %[va], %[a] offset:2048 sc1\n\tglobal_load_lds_dwordx4 %[va], %[a] offset:3072 sc1"
#define G2_SYNC "\n\ts_mov_b32 m0, %[la]\n\ts_waitcnt vmcnt(%[n])\n\ts_barrier\n\t"
    const unsigned oy = shm_base + G2_A + (p ^ role) * 4096, os = shm_base + G2_A + (p >> 1) * 8192 + (p & 1) * 2048;
    asm volatile("s_mov_b32 m0, %[la]\n\ts_nop 0\n\t" G2_A4 :: [la] "s"(sl + oy), [a] "s"(sa), [va] "v"(va) : "memory", "m0");
    auto step = [&](long adv = 16384) __attribute__((always_inline)) {
      wsel();
      ++st;
      sa += adv;
      const unsigned sn = (sl + 16384) & 65535;
      asm volatile(G2_W G2_SYNC G2_A4 :: [l] "s"(sl), [la] "s"(sn + oy), [n] "n"(16), [a] "s"(sa), [va] "v"(va), G2_WOP(wy) : "memory", "scc", "m0");
      sl = sn;
    };
    auto chk1 = [&](u64 mask) __attribute__((always_inline)) { if (p == 1) { WAIT_V(16); check(mask, true); } };
    step();
    chk1(0xcc00cc00ull << 32);
    step(); step();
    if (p == 1) peek();
    step(); step();
    chk1(0x0f0fu << (16 * role));
#pragma unroll 1
    for (int g = 7; g < 13; ++g) step();
    if (p == 1) peek();
    step(); step();
    chk1((0xffffu << (16 * role)) | 0xf555f555u);
#pragma unroll 1
    for (int g = 15; g < min(ny, NY) - 5; ++g) step();
    if (ny > NY) {
      step(((1 - 2 * role) * NY + 1 - DLT) * 16384);
#pragma unroll
      for (int i = 1; i < DLT; ++i) step();
    }
    wsel();
    sa = sz;
    {
      const unsigned sn = (sl + 16384) & 65535;
      asm volatile(G2_W G2_SYNC G2_A2 :: [l] "s"(sl), [la] "s"(sn + os), [n] "n"(16), [a] "s"(sa), [va] "v"(va), G2_WOP(wy) : "memory", "scc", "m0");
      sl = sn;
    }
    sw[0] = wz.wb[0]; sw[1] = wz.wb[1];
#pragma unroll
    for (int j = -3; j < 13; ++j) {
      if (j == GETP + 1 || j == GETP + 2)
        asm volatile("s_mov_b32 m0, %[l]\n\ts_nop 0\n\tglobal_load_lds_dwordx4 %[va], %[a] offset:%[o] sc1\n\tglobal_load_lds_dwordx4 %[va], %[a] offset:%[o]+1024 sc1"
                     :: [l] "s"(shm_base + G2_X + 16384 + p * 4096), [a] "s"((const char*)slab + (size_t)(bid ^ 5) * 16384 + p * 4096), [va] "v"(va),
                        [o] "n"((j - GETP - 1) * 2048) : "memory", "m0");
      sa += 16384;
      const unsigned sn = (sl + 16384) & 65535;
      constexpr int G = GETP;
      static_assert(GETP <= GETJ + 1, "");
      if (j < 12) asm volatile(G2_W G2_SYNC G2_A2 :: [l] "s"(sl), [la] "s"(sn + os), [n] "n"((j == -3 ? 14 : 12) + (j > G && j < G + 3 ? 2 : 0) + (j > G + 1 && j < G + 4 ? 2 : 0)),
                               [a] "s"(sa), [va] "v"(va), G2_WOP(wz) : "memory", "scc", "m0");
      else asm volatile(G2_W :: [l] "s"(sl), G2_WOP(wz) : "memory", "scc", "m0");
      sl = sn;
      sw[0] += ws0; sw[1] += ws1;
    }
    WAIT_V(12);
    BAR();
    WAIT_V(6);
    BAR();
    WAIT_V(0);
    BAR();
    asm volatile("" : "+v"(fl));
    if (tid < 258) __hip_atomic_store(lane ? done2 + bid * 8 : &acks[bid], __builtin_amdgcn_s_memrealtime(), RLX_AGENT);
    if (__builtin_expect(!cok, 0) && p == 0) {
      const bool mine = lane <= 32;
      const u64* const pa = &acks[lane < 32 ? client(lane) : bid ^ 5];
      while (!__all(!mine || fresh(__hip_atomic_load(pa, RLX_AGENT), t_own))) {
        notify(0, 32);
        if (lane == 0) __hip_atomic_store(done2 + bid * 8, __builtin_amdgcn_s_memrealtime(), RLX_AGENT);
        __builtin_amdgcn_s_sleep(32);
      }
    }
  } else {
    TS(6);
    f32x4 acc[4][4], sacc[2][4];
    const int fr = lane & 15, fq = lane >> 4, q4 = fr >> 2, pp = lane & 3;
    const int ff = ((q4 >> 1) & 1) | ((fq & 1) << 1);
    int aof = G2_A + wm * 8192 + lane * 16;
    int boff[4];
#pragma unroll
    for (int j = 0; j < 4; ++j) boff[j] = wn * 8192 + (8 * fq + q4) * 128 + ((j ^ ff) * 32) + pp * 8;
    int vo = wid * 4096 + lane * 16;
    int vx = ((m0 + wm * 64 + role * 32 + 2 * fr) * D_ + n0 + wn * 64) * 2 + (fq & 1) * 32 + (fq >> 1) * 16;
    lds_cptr px = shm3 + G2_X + wid * 4096 + (fq >> 1) * 512 + fr * 16 + (fq & 1) * 8;
    asm volatile("" : "+v"(vo), "+v"(vx), "+v"(px));
#define SB() __builtin_amdgcn_sched_barrier(0)
    struct Fr { bf16x8 a[4]; bf16x4 bl[4], bh[4]; };
    Fr fa[2], fb[2];
    auto rd = [&](Fr* f, int odd, int t) {
      constexpr int ord[12] = {0, 1, 8, 2, 3, 4, 5, 6, 7, 9, 10, 11};
      constexpr int seq[24] = {0, 1, 2, 12, 13, 14, 15, 16, 3, 4, 5, 6, 17, 18, 19, 20, 7, 8, 9, 21, 10, 22, 11, 23};
      lds_cptr sb = shm3 + odd * 16384;
      const int ks = seq[t] / 12, o = ord[seq[t] % 12];
      if (o >= 8) f[ks].a[o - 8] = *(const __attribute__((address_space(3))) bf16x8*)(sb + aof + (o - 8) * 2048 + ks * 1024);
      else if (o & 1) f[ks].bh[o >> 1] = tr_read(sb + boff[o >> 1] + ks * 4096 + 512);
      else f[ks].bl[o >> 1] = tr_read(sb + boff[o >> 1] + ks * 4096);
    };
    auto bfr = [&](const Fr& c, int j) { return __builtin_shufflevector(c.bl[j], c.bh[j], 0, 1, 2, 3, 4, 5, 6, 7); };
    auto mf1 = [](f32x4& c, bf16x8 b, bf16x8 a, bool z = false) {
      if (z) asm volatile("v_mfma_f32_16x16x32_bf16 %0, %1, %2, 0" : "=&v"(c) : "v"(b), "v"(a));
      else asm volatile("v_mfma_f32_16x16x32_bf16 %0, %1, %2, %0" : "+v"(c) : "v"(b), "v"(a));
    };
    auto mf2 = [](f32x4& c, bf16x8 b, bf16x8 a, bf16x8 b2, bf16x8 a2, bool z) {
      if (z) asm volatile("v_mfma_f32_16x16x32_bf16 %0, %1, %2, 0\n\tv_mfma_f32_16x16x32_bf16 %0, %3, %4, %0" : "=&v"(c) : "v"(b), "v"(a), "v"(b2), "v"(a2));
      else asm volatile("v_mfma_f32_16x16x32_bf16 %0, %1, %2, %0\n\tv_mfma_f32_16x16x32_bf16 %0, %3, %4, %0" : "+v"(c) : "v"(b), "v"(a), "v"(b2), "v"(a2));
    };
    auto pairs = [&](f32x4 (*ac)[4], const Fr* c, int nq, int mx, int nf, auto fill, bool z = false) {
#pragma unroll
      for (int q = 0; q < nq; ++q) {
        const int mf = (q >> 2) ^ mx, j = (q & 4) ? 3 - (q & 3) : q & 3, ks = q & 1;
        SB();
        if (2 * q >= nf) mf2(ac[mf][j], bfr(c[ks], j), c[ks].a[mf], bfr(c[ks ^ 1], j), c[ks ^ 1].a[mf], z);
        else {
          mf1(ac[mf][j], bfr(c[ks], j), c[ks].a[mf], z);
          SB();
          fill(2 * q);
          SB();
          mf1(ac[mf][j], bfr(c[ks ^ 1], j), c[ks ^ 1].a[mf]);
          SB();
          fill(2 * q + 1);
        }
      }
      SB();
    };
    auto flip = [&]() {
      aof ^= 32768; boff[0] ^= 32768; boff[1] ^= 32768; boff[2] ^= 32768; boff[3] ^= 32768;
      asm volatile("" : "+v"(aof), "+v"(boff[0]), "+v"(boff[1]), "+v"(boff[2]), "+v"(boff[3]));
    };
    auto bar = [&]() { __builtin_amdgcn_s_waitcnt(0xC07F); BAR(); };
    auto ystg = [&](const Fr* c, Fr* n, int odd, bool z = false) {
      pairs(acc, c, 16, 0, odd ? 30 : 24, [&](int t) {
        if (t < 24) rd(n, odd, t);
        else if (t == 24) asm volatile("s_nop 0");
        else { int& a = t == 29 ? aof : boff[t - 25]; a ^= 32768; asm volatile("" : "+v"(a)); }
      }, z);
    };
    auto stg = [&](const Fr* c, Fr* n, int odd, bool z = false) { pairs(sacc, c, 8, 0, 10, [&](int t) { rd(n, odd, 2 * t); rd(n, odd, 2 * t + 1); }, z); };
    asm volatile("" : "+v"(aof), "+v"(boff[0]), "+v"(boff[1]), "+v"(boff[2]), "+v"(boff[3]));
    await(0xffffu, 0x404u);
    auto lda = [&](int i) {
      asm volatile("s_mov_b32 m0, %[l]\n\ts_nop 0\n\tglobal_load_lds_dwordx4 %[va], %[a] sc1\n\tglobal_load_lds_dwordx4 %[va], %[a] offset:1024 sc1\n\t"
                   "global_load_lds_dwordx4 %[va], %[a] offset:2048 sc1\n\tglobal_load_lds_dwordx4 %[va], %[a] offset:3072 sc1"
                   :: [l] "s"(shm_base + G2_A + (i + 1) * 16384 + (p ^ role) * 4096), [a] "s"(sa + i * 16384), [va] "v"(va) : "memory", "m0");
    };
    need(0x00330033ull << 32, false);
    TS(4);
    lda(0);
    lda(1);
    need(0x00ff00ffull << 32, false);
    TS(0);
    lda(2);
    WAIT_V(8);
    signal(1u << 24);
    await(0xff000000u, 4u << 24);
    __builtin_amdgcn_s_waitcnt(0xC07F);
    SB();
#pragma unroll
    for (int t = 0; t < 24; ++t) rd(fa, 1, t);
    flip();
#define CPRIO 0
    __builtin_amdgcn_s_setprio(CPRIO);
    WAIT_V(4);
    peek();
    bar();
    TS(7);
    ystg(fa, fb, 0, true);
    WAIT_V(1);
    bar();
    ystg(fb, fa, 1);
    WAIT_V(0);
    check(0x33003300ull << 32, false);
    constexpr int PAR = (NY - DLT + 1) & 1;
#pragma unroll 1
    for (int g = 2; g < ny - 2; g += 2) {
      bar();
      ystg(fa, fb, 0);
      bar();
      ystg(fb, fa, 1);
    }
    if (PAR) {
      bar();
      ystg(fa, fb, 0);
    }
    Fr* const f0 = PAR ? fb : fa;
    Fr* const f1 = PAR ? fa : fb;
    bar();
    {
      const __amdgpu_buffer_rsrc_t rs = __builtin_amdgcn_make_buffer_rsrc((void*)((char*)slab + (size_t)bid * 16384), 0, 16384, 0x00020000);
      u32x4 pk;
      pairs(acc, f0, 16, 2, 32, [&](int t) {
        if (t < 20) rd(f1, PAR, t);
        else {
          const int f = (t - 20) / 3, e = (t - 20) % 3;
          if (e < 2) { const f32x4 a = acc[2 + (f >> 1)][(f & 1) * 2 + e]; pk[2 * e] = pack2(a[0], a[1]); pk[2 * e + 1] = pack2(a[2], a[3]); }
          else __builtin_amdgcn_raw_buffer_store_b128(pk, rs, vo + f * 1024, 0, 16);
        }
      });
    }
    TS(2);
    static_assert(GETJ % 2 == 1 && GETJ >= 3 && GETJ <= 7, "");
    f32x4 xf[2][4];
#pragma unroll
    for (int j = 0; j < GETJ + 5; j += 2) {
      if (PAR) flip();
      bar();
      stg(f1, f0, PAR ^ 1, j == 0);
      if (!PAR) flip();
      bar();
      u32x2 xv[2];
      pairs(sacc, f0, 8, 0, 12, [&](int t) {
        if (t < 10) { rd(f1, PAR, 2 * t); rd(f1, PAR, 2 * t + 1); }
        else if (j < 8) xv[t - 10] = *(const __attribute__((address_space(3))) u32x2*)(px + (j >> 1) * 1024 + (t - 10) * 256);
      });
      if (j < 8) {
#pragma unroll
        for (int e = 0; e < 2; ++e) xf[j >> 2][(j & 2) + e] = f32x4{lo_f(xv[e][0]), hi_f(xv[e][0]), lo_f(xv[e][1]), hi_f(xv[e][1])};
        asm volatile("" : "+v"(xf[j >> 2][j & 2]), "+v"(xf[j >> 2][(j & 2) + 1]));
        SB();
      }
    }
    if (PAR) flip();
    WAIT_V(0);
    bar();
    u32x4 pv[4];
    {
      lds_cptr pp_ = shm3 + G2_X + 16384 + vo;
      pairs(sacc, f1, 8, 0, 12, [&](int t) {
        if (t < 2) {
#pragma unroll
          for (int f = 2 * t; f < 2 * t + 2; ++f) pv[f] = *(const __attribute__((address_space(3))) u32x4*)(pp_ + f * 1024);
        } else { rd(f0, PAR ^ 1, 2 * t - 4); rd(f0, PAR ^ 1, 2 * t - 3); }
      });
      if (__builtin_expect(blank(pv) || !cok, 0)) {
        const __amdgpu_buffer_rsrc_t rp = __builtin_amdgcn_make_buffer_rsrc((void*)((char*)slab + (size_t)(bid ^ 5) * 16384), 0, 16384, 0x00020000);
        while (!cok && !__all(fresh(__hip_atomic_load(done2 + (bid ^ 5) * 8, RLX_AGENT), t_own))) {
          notify(0, 32);
          if (lane == 0) __hip_atomic_store(done2 + bid * 8, __builtin_amdgcn_s_memrealtime(), RLX_AGENT);
          __builtin_amdgcn_s_sleep(8);
        }
        do {
          asm volatile("" ::: "memory");
#pragma unroll
          for (int f = 0; f < 4; ++f) pv[f] = __builtin_bit_cast(u32x4, __builtin_amdgcn_raw_buffer_load_b128(rp, vo + f * 1024, 0, 16));
        } while (blank(pv));
      }
    }
    auto silu = [&](int g0, int g1) {
#pragma unroll
      for (int f = g0; f < g1; ++f) {
        const int mf = f >> 2, j = f & 3;
        const u32x4 pw = pv[mf * 2 + (j >> 1)];
        const f32x4 ya = acc[mf][j] + f32x4{lo_f(pw[2 * (j & 1)]), hi_f(pw[2 * (j & 1)]), lo_f(pw[2 * (j & 1) + 1]), hi_f(pw[2 * (j & 1) + 1])};
#pragma unroll
        for (int e = 0; e < 2; ++e) {
          const f32x2 y2 = {ya[2 * e], ya[2 * e + 1]};
          const f32x2 ar = y2 * f32x2{-1.4426950408889634f, -1.4426950408889634f};
          const f32x2 dn = f32x2{__builtin_amdgcn_exp2f(ar[0]), __builtin_amdgcn_exp2f(ar[1])} + f32x2{1.0f, 1.0f};
          const f32x2 t = y2 * f32x2{__builtin_amdgcn_rcpf(dn[0]), __builtin_amdgcn_rcpf(dn[1])};
          acc[mf][j][2 * e] = t[0]; acc[mf][j][2 * e + 1] = t[1];
        }
        asm volatile("" : "+v"(acc[mf][j]));
      }
      SB();
    };
#define SPT 0, 0, 1, 2, 4, 6, 8, 8
    constexpr int spt[] = {SPT};
    silu(0, spt[1]);
#pragma unroll
    for (int j = GETJ + 6; j < 15; ++j) {
      if ((j ^ PAR) & 1) flip();
      bar();
      if (j & 1) stg(f0, f1, PAR); else stg(f1, f0, PAR ^ 1);
      silu(spt[j - GETJ - 5], j == 14 ? 8 : spt[j - GETJ - 4]);
    }
    pairs(sacc, f0, 8, 0, 0, [](int) {});
    asm volatile("s_nop 7\n\ts_nop 3" : "+v"(sacc[1][0]), "+v"(sacc[1][1]), "+v"(sacc[1][2]), "+v"(sacc[1][3]));
    SB();
    TS(1);
    {
      const __amdgpu_buffer_rsrc_t ro = __builtin_amdgcn_make_buffer_rsrc((void*)out, 0, S_ * D_ * 2, 0x00020000);
#pragma unroll
      for (int f = 0; f < 4; ++f) {
        const int mf = f >> 1, j = (f & 1) * 2;
        const f32x4 a = __builtin_elementwise_fma(sacc[mf][j], acc[mf][j], xf[mf][j]), b = __builtin_elementwise_fma(sacc[mf][j + 1], acc[mf][j + 1], xf[mf][j + 1]);
        const auto r0 = __builtin_amdgcn_permlane16_swap(pack2(a[0], a[1]), pack2(b[0], b[1]), false, false);
        const auto r1 = __builtin_amdgcn_permlane16_swap(pack2(a[2], a[3]), pack2(b[2], b[3]), false, false);
        __builtin_amdgcn_raw_buffer_store_b128(u32x4{r0[0], r1[0], r0[1], r1[1]}, ro, vx + mf * 2048 + j * 32, 0, 2);
      }
    }
    TS(3);
  }
  if (bid == 66 && wid == 4) { unsigned d_ = (ts_r[13] - (unsigned)t_own) * 5u; if (d_ > 9000u) d_ = 0; while (__builtin_amdgcn_s_memrealtime() - t_own < 6000ull + d_) {} }
  }
  asm volatile(".Lcode_end:");
}

extern "C" void kernel_launch(void* const* d_in, const int* in_sizes, int n_in, void* d_out, int out_size, void* d_ws,
                              size_t ws_size, hipStream_t stream) {
  const u16* A_x = (const u16*)d_in[0];
  const u16* A_wqkv = (const u16*)d_in[1];
  const u16* A_wo = (const u16*)d_in[2];
  const u16* B_wg = (const u16*)d_in[4];
  const u16* B_wu = (const u16*)d_in[5];
  const u16* B_wd = (const u16*)d_in[6];
  u16* out = (u16*)d_out;
  fused_kernel<<<256, 512, 0, stream>>>(A_x, A_wqkv, B_wg, B_wu, B_wd, A_wo, out, (char*)d_ws);
}
```

```cpp
#define ALL_TS 1
#define TSMASK 8192
#include <hip/hip_runtime.h>
#include <hip/hip_bf16.h>
#include <stdint.h>
#include <type_traits>

typedef unsigned short u16;
using bf16x8 = __attribute__((ext_vector_type(8))) short;
using bf16x4 = __attribute__((ext_vector_type(4))) short;
using f32x4 = __attribute__((ext_vector_type(4))) float;
using u32x2 = __attribute__((ext_vector_type(2))) unsigned;
using u32x4 = __attribute__((ext_vector_type(4))) unsigned;
typedef __attribute__((address_space(3))) const char* lds_cptr;

constexpr int S_ = 2048, D_ = 1024, F_ = 3584;

__device__ __forceinline__ float bf2f(u16 h) { return __uint_as_float((unsigned)h << 16); }
__device__ __forceinline__ u16 f2bf(float f) {
  unsigned u = __float_as_uint(f);
  u += 0x7fffu + ((u >> 16) & 1u);
  return (u16)(u >> 16);
}
__device__ __forceinline__ float rbf(float f) { return bf2f(f2bf(f)); }
typedef __attribute__((ext_vector_type(2))) __bf16 bf16x2_t;
typedef __attribute__((ext_vector_type(2))) float f32x2;
__device__ __forceinline__ unsigned pack2(float a, float b) {
  bf16x2_t r = __builtin_convertvector((f32x2){a, b}, bf16x2_t);
  return __builtin_bit_cast(unsigned, r);
}
__device__ __forceinline__ float lo_f(unsigned p) { return __uint_as_float(p << 16); }
__device__ __forceinline__ float hi_f(unsigned p) { return __uint_as_float(p & 0xffff0000u); }

#define TSDECL unsigned ts_r[16] = {0, 0, 0, 0, 0, 0, 0, 0, 0, 0, 0, 0, 0, 0, 0, 0}
#define TSS(i) do { if ((TSMASK >> (i)) & 1) { unsigned long long t_; asm volatile("s_memrealtime %0\n\ts_waitcnt lgkmcnt(0)" : "=s"(t_)); ts_r[i] = (unsigned)t_; } } while (0)
#define TSFLUSH() do { if ((threadIdx.x & 63) == 0) { for (int i_ = 0; i_ < 16; ++i_) flags[(blockIdx.x * 8 + (threadIdx.x >> 6)) * 16 + i_] = ts_r[i_]; } } while (0)
#define TS1(i) TSS(i)
#define TS(i) TSS(8 + (i))
#define PIN_S(v) asm volatile("" : "+s"(v))
#define WAIT_V(n) asm volatile("s_waitcnt vmcnt(%0)" ::"n"(n) : "memory")
#define WAIT_L(n) asm volatile("s_waitcnt lgkmcnt(%0)" ::"n"(n) : "memory")
#define BARL() do { WAIT_L(0); BAR(); } while (0)
#define BAR() do { asm volatile("" ::: "memory"); __builtin_amdgcn_s_barrier(); asm volatile("" ::: "memory"); } while (0)

__device__ __forceinline__ bf16x4 tr_read(lds_cptr p) {
  return __builtin_amdgcn_ds_read_tr16_b64_v4i16((__attribute__((address_space(3))) bf16x4*)p);
}
__device__ __forceinline__ bf16x8 tr_frag(lds_cptr p) {
  bf16x4 lo = tr_read(p), hi = tr_read(p + 512);
  bf16x8 r;
  r[0] = lo[0]; r[1] = lo[1]; r[2] = lo[2]; r[3] = lo[3];
  r[4] = hi[0]; r[5] = hi[1]; r[6] = hi[2]; r[7] = hi[3];
  return r;
}
__device__ __forceinline__ void glds16(const void* gsrc, unsigned lds_dst) {
  unsigned keep;
  asm volatile("s_mov_b32 %0, m0\n\ts_mov_b32 m0, %2\n\ts_nop 0\n\tglobal_load_lds_dwordx4 %1, off\n\ts_mov_b32 m0, %0"
               : "=&s"(keep) : "v"(gsrc), "s"(lds_dst) : "memory");
}
#define GLDS(gptr, laddr) glds16((const void*)(gptr), (unsigned)(laddr))
__device__ __forceinline__ void glds16_sc1(const void* gsrc, unsigned lds_dst) {
  unsigned keep;
  asm volatile("s_mov_b32 %0, m0\n\ts_mov_b32 m0, %2\n\ts_nop 0\n\tglobal_load_lds_dwordx4 %1, off sc1\n\ts_mov_b32 m0, %0"
               : "=&s"(keep) : "v"(gsrc), "s"(lds_dst) : "memory");
}
#define GLDS_SC1(gptr, laddr) glds16_sc1((const void*)(gptr), (unsigned)(laddr))
__device__ __forceinline__ void glds16_nc(const void* gsrc, unsigned lds_dst) {
  unsigned keep;
  asm volatile("s_mov_b32 %0, m0\n\ts_mov_b32 m0, %2\n\ts_nop 0\n\tglobal_load_lds_dwordx4 %1, off\n\ts_mov_b32 m0, %0"
               : "=&s"(keep) : "v"(gsrc), "s"(lds_dst));
}
#define GLDS_NC(gptr, laddr) glds16_nc((const void*)(gptr), (unsigned)(laddr))
__device__ __forceinline__ void glds16_s(const void* sbase, unsigned voff, unsigned lds_dst) {
  asm volatile("s_mov_b32 m0, %2\n\ts_nop 0\n\tglobal_load_lds_dwordx4 %0, %1" :: "v"(voff), "s"(sbase), "s"(lds_dst) : "m0");
}

__device__ __forceinline__ void glds16_s1(const void* sbase, unsigned voff, unsigned lds_dst) {
  asm volatile("s_mov_b32 m0, %2\n\ts_nop 0\n\tglobal_load_lds_dwordx4 %0, %1 sc1" :: "v"(voff), "s"(sbase), "s"(lds_dst) : "memory", "m0");
}
__device__ __forceinline__ void glds16_v(const void* p, unsigned lds_dst) {
  asm volatile("s_mov_b32 m0, %1\n\ts_nop 0\n\tglobal_load_lds_dwordx4 %0, off" :: "v"(p), "s"(lds_dst) : "m0");
}
template <int OFF = 0>
__device__ __forceinline__ void gst16(const void* sbase, unsigned voff, u32x4 v) {
  asm volatile("global_store_dwordx4 %0, %1, %2 offset:%3 sc1\n\ts_nop 1" :: "v"(voff), "v"(v), "s"(sbase), "n"(OFF) : "memory");
}

typedef unsigned long long u64;
__device__ __forceinline__ bool fresh(u64 stamp, u64 t_own) {
  const u64 b = t_own + 100ull;
  const unsigned lo = (unsigned)stamp - (unsigned)b, hi = (unsigned)(stamp >> 32) - (unsigned)(b >> 32) - ((unsigned)stamp < (unsigned)b ? 1u : 0u);
  return (hi == 0u) & (lo < (unsigned)(__builtin_amdgcn_s_memrealtime() - t_own));
}
constexpr u64 TIMEOUT_ = 200000;
__device__ __forceinline__ float half_sum(float v) {
  v += __builtin_bit_cast(float, __builtin_amdgcn_update_dpp(0, __builtin_bit_cast(int, v), 0xB1, 0xf, 0xf, false));
  v += __builtin_bit_cast(float, __builtin_amdgcn_update_dpp(0, __builtin_bit_cast(int, v), 0x4E, 0xf, 0xf, false));
  v += __builtin_bit_cast(float, __builtin_amdgcn_update_dpp(0, __builtin_bit_cast(int, v), 0x141, 0xf, 0xf, false));
  v += __builtin_bit_cast(float, __builtin_amdgcn_update_dpp(0, __builtin_bit_cast(int, v), 0x140, 0xf, 0xf, false));
  { auto r = __builtin_amdgcn_permlane16_swap(__float_as_uint(v), __float_as_uint(v), false, false); v = __uint_as_float(r[0]) + __uint_as_float(r[1]); }
  return v;
}
__device__ __forceinline__ unsigned pack_stats(float mean, float var) {
  return (unsigned)f2bf(mean) | ((unsigned)f2bf(sqrtf(rbf(rbf(var) + 1e-5f))) << 16);
}
__device__ __forceinline__ unsigned row_stats_serial(const u16* xr) {
  float s = 0.f, q = 0.f;
#pragma unroll 1
  for (int k = 0; k < D_; ++k) s += bf2f(xr[k]);
  const float mean = s * (1.0f / D_);
#pragma unroll 1
  for (int k = 0; k < D_; ++k) { const float d = bf2f(xr[k]) - mean; q += d * d; }
  return pack_stats(mean, q * (1.0f / D_));
}
constexpr int G1_STAGE = 32768 + 5 * 8192;
#define EPOS 2
#define PA 4
#define PB 8
#define PC 12
#define GETP 4
#define DLT 2
#define PSLEEP 2
#define GETJ 3

constexpr unsigned BLANK = 0x7f817f81u;
__device__ __forceinline__ bool blank(const u32x4* pv) {
  u64 m;
#define CX(i) "v_cmpx_ne_u32 vcc, %1, %" #i "\n\t"
  asm volatile(CX(2) CX(3) CX(4) CX(5) CX(6) CX(7) CX(8) CX(9) CX(10) CX(11) CX(12) CX(13) CX(14) CX(15) CX(16) CX(17)
               "s_mov_b64 %0, exec\n\ts_mov_b64 exec, -1\n\ts_nop 3" : "=s"(m) : "s"(BLANK), "v"(pv[0][0]), "v"(pv[0][1]), "v"(pv[0][2]), "v"(pv[0][3]),
               "v"(pv[1][0]), "v"(pv[1][1]), "v"(pv[1][2]), "v"(pv[1][3]), "v"(pv[2][0]), "v"(pv[2][1]), "v"(pv[2][2]), "v"(pv[2][3]),
               "v"(pv[3][0]), "v"(pv[3][1]), "v"(pv[3][2]), "v"(pv[3][3]) : "vcc");
  return m != ~0ull;
}
#define RLX_AGENT __ATOMIC_RELAXED, __HIP_MEMORY_SCOPE_AGENT

constexpr int G2_A = 65536, G2_X = 131072;
constexpr int G2_NSTAGE = 4;
constexpr size_t MB_ = 1024 * 1024;
__global__ __launch_bounds__(512, 2) __attribute__((aligned(4096))) void fused_kernel(const u16* __restrict__ x, const u16* __restrict__ wqkv,
                                                       const u16* __restrict__ wg, const u16* __restrict__ wu,
                                                       const u16* __restrict__ wd, const u16* __restrict__ wo,
                                                       u16* __restrict__ out, char* __restrict__ ws) {
  u16* const halo = (u16*)(ws + 4 * MB_);
  u16* const abuf = (u16*)(ws + 16 * MB_);
  char* const slab = ws + 64 * MB_;
  unsigned* const flags = (unsigned*)(ws + 96 * MB_);
  u32x4* const rstat = (u32x4*)(ws + 97 * MB_);
  char* const wcopy = ws + 98 * MB_;
  u64* const done1 = (u64*)(ws + 102 * MB_);
  u64* const done2 = (u64*)(ws + 100 * MB_ + 65536);
  u64* const donec = (u64*)(ws + 100 * MB_ + 131072);
  u64* const acks = (u64*)(ws + 100 * MB_ + 196608);
  float* const csum = (float*)(ws + 101 * MB_);
  __shared__ __attribute__((aligned(1024))) char shm[163840];
  constexpr int AUX = 2 * G1_STAGE + 8192;
  {
    u64 pc; int len;
    asm volatile("s_getpc_b64 %0\n.Lcode_beg:\n\ts_mov_b32 %1, .Lcode_end-.Lcode_beg" : "=s"(pc), "=s"(len));
    const int ol = ((blockIdx.x >> 3) + 32 * (threadIdx.x >> 6)) * 1024 + (threadIdx.x & 63) * 16;
    asm volatile("s_mov_b32 m0, %2\n\ts_nop 0\n\tglobal_load_lds_dwordx4 %0, %1"
                 :: "v"(ol < len - 16 ? ol : 0), "s"(pc & ~15ull), "s"((unsigned)(uintptr_t)(lds_cptr)shm + 2 * G1_STAGE + 7168) : "m0");
    __builtin_amdgcn_sched_barrier(0);
  }
  const u64 t_own = __builtin_amdgcn_s_memrealtime();
  TSDECL;
  TS1(7);
  const int tid = threadIdx.x, lane = tid & 63;
  const int wid = __builtin_amdgcn_readfirstlane(tid >> 6);
  const int wm = wid & 3, wc = wid >> 2;
  int bid_ = blockIdx.x;
  PIN_S(bid_);
  const int bid = bid_ & 255, xcd = bid & 7, idx = bid >> 3;
  const int ntile = xcd * 4 + (idx & 3), mtile = idx >> 2;
  const int m0 = mtile * 256;
  const int hg = ntile >> 1;
  const bool tya = (ntile & 1) == 0;
  const int srow = m0 + ntile * 8 + wm * 2;
  lds_cptr shm3 = (lds_cptr)shm;
  const unsigned shm_base = (unsigned)(uintptr_t)shm3;
  const int r8 = tid >> 3, pos = tid & 7;
  const int phys = pos >> 1, half = pos & 1;
  const int fk = ((r8 >> 1) & 1) | (((r8 >> 3) & 1) << 1);
  const int vsrc = phys ^ fk;
  const int qstr = tya ? 3072 * 2 : F_ * 2;
  unsigned voa = (((r8 & 32) + 2 * (r8 & 15) + ((r8 >> 4) & 1)) * D_ + ((pos ^ (r8 & 7)) * 8)) * 2;
  unsigned vog = r8 * (F_ * 2) + vsrc * 32 + half * 16, voq = r8 * qstr + vsrc * 32 + half * 16;
  unsigned vom = r8 * qstr + (vsrc >> (tya ? 0 : 1)) * 32 + half * 16;
  asm volatile("" : "+v"(voa), "+v"(vog), "+v"(voq), "+v"(vom));
  const int kt0 = (hg >> 1) * 7, he = hg & 1;
  unsigned oq = (tya ? hg : kt0 + 2 * he) * 128, og = (kt0 + (tya ? 5 + he : 2 * he + 1)) * 128, om = tya ? 4096 : 512 - 192 * he;
  unsigned d0 = shm_base + wid * 1024 + 32768, ox = m0 * D_ * 2;
  long uq = -32 * qstr;
  const int pr = wm * 64 + (mtile - wid) * 8;
  long pq_ = pr * qstr, pg_ = pr * (F_ * 2);
  PIN_S(pq_); PIN_S(pg_);
  PIN_S(oq); PIN_S(og); PIN_S(om); PIN_S(d0); PIN_S(ox); PIN_S(uq);
  __builtin_amdgcn_sched_barrier(0);
  asm volatile("" :: "s"(x), "s"(wqkv), "s"(wg), "s"(wu), "s"(wd), "s"(wo), "s"(out), "s"(ws));
  const char* const ab = (const char*)x + ox;
  int kz = 0;
  asm volatile("" : "+s"(kz));
  u64 pq = (u64)wqkv, pg = (u64)wg;
  PIN_S(pq); PIN_S(pg);
  const char* const qb = (const char*)(tya ? pq : pg) + oq;
  if (wid < 4) {
#pragma unroll
    for (int i = 0; i < 8; ++i) glds16_s(ab + i * 32 * D_ * 2, voa, d0 - 32768 + i * 4096);
  }
  __builtin_amdgcn_sched_barrier(0);
  const char* const gb = (const char*)wg + og;
  const long dgu = (const char*)wu - (const char*)wg, dp1 = tya ? 2048 : dgu;
  const char* const mb0 = qb + om;
  const char* mptr = mb0 + (((vsrc & 1) && !tya) ? dgu : 0) + vom;
#define NT0 2
  if (wid - 4u < NT0) {
    const unsigned jk = shm_base + 2 * G1_STAGE + 7168;
    glds16_s(qb + pq_, voq, jk); glds16_s(qb + dp1 + pq_, voq, jk); glds16_v(mptr + pq_, jk); glds16_s(gb + pg_, vog, jk); glds16_s(gb + dgu + pg_, vog, jk);
  }
  if (wid >= 4) {
    const long ug = kz - 32 * F_ * 2;
    glds16_s(qb, voq, d0); glds16_s(qb + uq, voq, d0 - 4096);
    glds16_s(qb + dp1, voq, d0 + 8192); glds16_s(qb + dp1 + uq, voq, d0 + 4096);
    glds16_v(mptr, d0 + 2 * 8192); glds16_v(mptr + uq, d0 + 2 * 8192 - 4096);
    glds16_s(gb, vog, d0 + 3 * 8192); glds16_s(gb + ug, vog, d0 + 3 * 8192 - 4096);
    glds16_s(gb + dgu, vog, d0 + 4 * 8192); glds16_s(gb + dgu + ug, vog, d0 + 4 * 8192 - 4096);
  }
  __builtin_amdgcn_sched_barrier(0);
  f32x4 acc[4][10];

  constexpr int NT = D_ / 64;
  constexpr int CNT = AUX + 4096 + 3584;
  if (tid < 8) *(unsigned*)(shm + CNT + tid * 4) = 0u;
  bf16x8 af0[4], af1[4];
  bf16x4 bl[4], bh[4];
#define SB0() __builtin_amdgcn_sched_barrier(0)
  auto mm = [&](int g, int t, bool two, bool z = false) {
    const int mi = t >> 1, mf = ((g & 1) && g != 9) ? 3 - mi : mi, ks = (t & 1) ^ (mi & 1), s = ((g + 1) & 1) * 2;
    const bf16x8 b0 = __builtin_shufflevector(bl[s + ks], bh[s + ks], 0, 1, 2, 3, 4, 5, 6, 7), a0 = ks ? af1[mf] : af0[mf];
    const bf16x8 b1 = __builtin_shufflevector(bl[s + 1 - ks], bh[s + 1 - ks], 0, 1, 2, 3, 4, 5, 6, 7), a1 = ks ? af0[mf] : af1[mf];
    const bool tr = (g >> 1) == 2;
    if (z && two) asm volatile("v_mfma_f32_16x16x32_bf16 %0, %1, %2, 0\n\tv_mfma_f32_16x16x32_bf16 %0, %3, %4, %0" : "=&v"(acc[mf][g])
                               : "v"(tr ? a0 : b0), "v"(tr ? b0 : a0), "v"(tr ? a1 : b1), "v"(tr ? b1 : a1));
    else if (z && !(t & 1)) asm volatile("v_mfma_f32_16x16x32_bf16 %0, %1, %2, 0" : "=&v"(acc[mf][g]) : "v"(tr ? a0 : b0), "v"(tr ? b0 : a0));
    else if (two) asm volatile("v_mfma_f32_16x16x32_bf16 %0, %1, %2, %0\n\tv_mfma_f32_16x16x32_bf16 %0, %3, %4, %0" : "+v"(acc[mf][g])
                          : "v"(tr ? a0 : b0), "v"(tr ? b0 : a0), "v"(tr ? a1 : b1), "v"(tr ? b1 : a1));
    else asm volatile("v_mfma_f32_16x16x32_bf16 %0, %1, %2, %0" : "+v"(acc[mf][g]) : "v"(tr ? a0 : b0), "v"(tr ? b0 : a0));
  };
  typedef __attribute__((address_space(3))) unsigned lds_u32;
  lds_u32* const cnt = (lds_u32*)(shm3 + CNT);
  bool cok;
  constexpr int NY = 28;
  int role, mt2, nt2;
  struct WS { unsigned vb[2][2]; const char* wb[2]; int sh[2]; };
  auto wsetup = [&](const u16* w, int crow0, int l, bool ck) __attribute__((always_inline)) {
    WS r;
    const unsigned vr = ((l >> 5) & 1) * 16 + (wm & 1) * 8 + ((l >> 3) & 3), vc = (((l >> 1) & 3) ^ ((l >> 4) & 1)) * 32 + (l & 1) * 16;
    u64 cp = (u64)(wcopy + (nt2 >> 2) * 589824 + crow0 * 128);
    asm volatile("" : "+s"(cp));
#pragma unroll
    for (int i = 0; i < 2; ++i) {
      u64 dp = (u64)(w + nt2 * 128 + i * 64);
      asm volatile("" : "+s"(dp));
      const bool slow = ck & ((((unsigned)dp >> 7) & 7) == 3);
      r.sh[i] = slow ? 7 : 11;
      r.wb[i] = (const char*)(slow ? cp : dp) + (crow0 ? (wm >> 1) * 32 << r.sh[i] : 0);
      r.vb[i][0] = (vr << r.sh[i]) + vc; r.vb[i][1] = ((vr + 4) << r.sh[i]) + (vc ^ 64);
    }
    asm volatile("" : "+v"(r.vb[0][0]), "+v"(r.vb[0][1]), "+v"(r.vb[1][0]), "+v"(r.vb[1][1]));
    PIN_S(r.wb[0]); PIN_S(r.wb[1]); PIN_S(r.sh[0]); PIN_S(r.sh[1]);
    return r;
  };
  WS wy, wz;
  int vxq, ws0, ws1;
  const char* sz;
  u32x4 xq[4];
  unsigned vg;
  int st = 3;
  const char *sa, *sw[2];
  auto wsel = [&]() {
    const unsigned r = __builtin_amdgcn_readlane(vg, st);
    sw[0] = wy.wb[0] + (r << wy.sh[0]); sw[1] = wy.wb[1] + (r << wy.sh[1]);
  };
  const int p = wid & 3;
  unsigned sl = 0;
#define G2_W "s_add_u32 m0, %[l], %[ow]\n\ts_nop 0\n\tglobal_load_lds_dwordx4 %[v0], %[w0] sc1\n\t" \
             "s_add_u32 m0, m0, 1024\n\ts_nop 0\n\tglobal_load_lds_dwordx4 %[v1], %[w0] sc1\n\t" \
             "s_add_u32 m0, m0, 7168\n\ts_nop 0\n\tglobal_load_lds_dwordx4 %[v2], %[w1] sc1\n\t" \
             "s_add_u32 m0, m0, 1024\n\ts_nop 0\n\tglobal_load_lds_dwordx4 %[v3], %[w1] sc1"
#define G2_WOP(w) [ow] "s"(shm_base + p * 2048), [w0] "s"(sw[0]), [w1] "s"(sw[1]), [v0] "v"(w.vb[0][0]), [v1] "v"(w.vb[0][1]), [v2] "v"(w.vb[1][0]), [v3] "v"(w.vb[1][1])
  auto tile2 = [&](int t0) __attribute__((always_inline)) {
    int b = bid;
    asm volatile("" : "+s"(b));
    role = 1 - ((b >> 2) & 1);
    mt2 = ((role ? b : b ^ 5) & 7) * 4 + ((b >> 3) & 3), nt2 = b >> 5;
    sa = (const char*)abuf + (size_t)(mt2 * 72 + role * NY + t0) * 16384 + wm * 4096;
    asm volatile("" : "+s"(sa));
  };
  auto prep = [&](bool first) __attribute__((always_inline)) {
    int l = threadIdx.x;
    asm volatile("" : "+v"(l));
    l &= 63;
    if (first) {
      tile2(3);
      vxq = ((mt2 * 128 + (wm >> 1) * 64 + role * 32 + 2 * (l & 15)) * D_ + nt2 * 128 + (wm & 1) * 64) * 2 + ((l >> 4) & 1) * 32 + (l >> 5) * 16;
      const int t = min(l < NY ? l : l - DLT, NY - 1), w = wm >> 1;
      const int o = (0xffaa5500fae504ull >> (2 * t)) & 3;
      int u1 = (0x990990 >> ((2 * t) & 31)) & 3, u2 = 3 + (t & 3);
      asm volatile("" : "+v"(u1), "+v"(u2));
      const int u = t < 12 ? u1 : u2;
      vg = (__umul24((l < NY ? role : 1 - role) * 4 + (((o & 1) << 1) | (o >> 1)), 14) + ((0xb85140a >> (4 * u)) & 15) + (u == 5 ? w : 2 * w)) * 32;
      asm volatile("" : "+v"(vg), "+v"(vxq));
      sz = (const char*)abuf + (size_t)(mt2 * 72 + 56) * 16384 + (wm >> 1) * 8192 + role * 4096 + (wm & 1) * 2048;
      PIN_S(sz);
    }
    wy = wsetup(wd, 0, l, first);
    wz = wsetup(wo, F_, l, first);
    ws0 = 64 << wz.sh[0]; PIN_S(ws0);
    ws1 = 64 << wz.sh[1]; PIN_S(ws1);
    SB0();
  };
  auto start2 = [&]() __attribute__((always_inline)) {
    const __amdgpu_buffer_rsrc_t rx = __builtin_amdgcn_make_buffer_rsrc((void*)x, 0, S_ * D_ * 2, 0x00020000);
#pragma unroll
    for (int f = 0; f < 4; ++f) xq[f] = __builtin_bit_cast(u32x4, __builtin_amdgcn_raw_buffer_load_b128(rx, vxq + (f >> 1) * 2048 + (f & 1) * 64, 0, 0));
    cok = __builtin_amdgcn_readfirstlane(cnt[1] & cnt[2]) != 0u;
    if (__builtin_expect(!cok, 0)) prep(false);
    wsel();
    PIN_S(sw[0]); PIN_S(sw[1]);
    SB0();
  };
  auto kloop = [&](auto wmc) {
    constexpr bool LD = decltype(wmc)::value != 0;
    constexpr int DMP = 2 * G1_STAGE;
    const char* pa = nullptr;
    unsigned vob = 0;
    f32x4 ca = {0.f, 0.f, 0.f, 0.f}, cb = {0.f, 0.f, 0.f, 0.f}, cd = ca;
    bf16x8 sel = {0, 0, 0, 0, 0, 0, 0, 0};
    int tro = 0;
    const int psh = (wid == 2 && !tya) ? 1 : 0, pstr = wid < 3 ? qstr : F_ * 2;
    const char* const pbase = wid == 0 ? qb : (wid == 1 ? qb + dp1 : (wid == 2 ? mb0 : gb));
    int lane = threadIdx.x;
    asm volatile("; %1" : "+v"(lane) : "n"(LD ? 1 : 0));
    lane &= 63;
    const int fr = lane & 15, fq = lane >> 4, q4 = fr >> 2, pp = lane & 3;
    const int ff = ((q4 >> 1) & 1) | ((fq & 1) << 1);
    int aoff[2], boff[2];
#pragma unroll
    for (int ks = 0; ks < 2; ++ks) aoff[ks] = (wm * 64 + fr) * 128 + (((ks * 4 + fq) ^ (fr & 7)) * 16);
#pragma unroll
    for (int j = 0; j < 2; ++j) boff[j] = 32768 + (8 * fq + q4) * 128 + (((2 * wc + j) ^ ff) * 32) + pp * 8;
    unsigned vol = lane * 16;
    float sm = 0.f, sq = 0.f;
    const char* pf = (const char*)wd + (bid * 4 + wid) * 7168;
    long pstep = 64 * pstr;
    if (!LD) {
      const int rs = 256 + mtile * 8 + (lane >> 3), vs = ((lane & 7) >> 1) ^ (((rs >> 1) & 1) | ((mtile & 1) << 1));
      pa = pbase + ((psh & vs) ? dgu : 0) + (rs * pstr + (vs >> psh) * 32 + (lane & 1) * 16);
      vob = rs * (F_ * 2) + vs * 32 + (lane & 1) * 16;
      tro = DMP + wid * 1024 + q4 * 128 + ((fq ^ ((q4 >> 1) | ((mtile & 1) << 1))) * 32) + pp * 8;
      const short one = fr == fq ? 0x3f80 : 0;
      sel = bf16x8{one, one, one, one, one, one, one, one};
      asm volatile("" : "+v"(tro), "+v"(vol), "+v"(sel), "+v"(ca), "+v"(cb), "+v"(sm), "+v"(sq));
      PIN_S(pf); PIN_S(pstep);
    }
    auto accum = [&](lds_cptr p, int ob) {
      ca = __builtin_amdgcn_mfma_f32_16x16x32_bf16(tr_frag(p), sel, ca, 0, 0, 0);
      if (wid == 0) cb = __builtin_amdgcn_mfma_f32_16x16x32_bf16(tr_frag(p + ob), sel, cb, 0, 0, 0);
    };
    auto xrow = [&](int i) {
      u64 a = (u64)(x + (size_t)srow * D_ + i * 256), b = (u64)(wo + (bid * 4 + wid) * 1024);
      PIN_S(a); PIN_S(b);
      asm volatile("global_load_dwordx4 %0, %1, %2" : "+v"(cd) : "v"(vol + (vol & (i < 4 ? 512 : 0)) * 3), "s"(i < 4 ? a : b) : "memory");
    };
    const int q32 = 32 * qstr;
    const char *sq0 = qb + q32, *sq1 = sq0 + dp1, *sg0 = gb + 32 * F_ * 2, *sg1 = sg0 + dgu, *sx = ab + (long)(kz + 128 - 32 * D_ * 2);
    u32x2 mp[2] = {__builtin_bit_cast(u32x2, mptr + q32), __builtin_bit_cast(u32x2, mptr + 2 * q32)};
    u64 cy[2];
    unsigned dst = shm_base + G1_STAGE + wm * 1024;
    int dbuf = G1_STAGE;
    auto stage = [&](int i0, int i1) __attribute__((always_inline)) {
#pragma unroll
        for (int i = i0; i < i1; ++i) {
          const int q = i >> 1, h = i & 1;
          const unsigned ld = dst + h * 4096 + (q < 5 ? 32768 + q * 8192 : (q - 5) * 8192);
          if (q < 2) glds16_s((q ? sq1 : sq0) + h * q32, voq, ld);
          else if (q == 2) glds16_v(__builtin_bit_cast(const char*, mp[h]), ld);
          else if (q < 5) glds16_s((q == 4 ? sg1 : sg0) + h * 32 * F_ * 2, vog, ld);
          else glds16_s(sx + ((q - 5) * 64 + h * 32) * D_ * 2, voa, ld);
        }
    };
    u64 cy0 = 0, rt0 = 0;
#pragma unroll
    for (int a = 0; a < 4; ++a) { acc[a][9] = f32x4{0.f, 0.f, 0.f, 0.f}; asm volatile("" : "+v"(acc[a][9])); }
    if (!LD) {
#define SH 0
      if (SH & 1) {
        glds16_v(pa - 4 * pstep, shm_base + DMP + 7168);
        if (wid == 0) glds16_s(gb + dgu, vob - 256 * F_ * 2, shm_base + DMP + 7168);
      }
      if (SH & 2) {
        glds16_v(pa - 3 * pstep, shm_base + DMP + 7168);
        if (wid == 0) glds16_s(gb + dgu, vob - 192 * F_ * 2, shm_base + DMP + 7168);
      }
      xrow(0);
      if (wid == 0) WAIT_V(1 + 2 * ((SH & 1) + (SH >> 1))); else WAIT_V(1 + (SH & 1) + (SH >> 1));
    }
    asm volatile("" : "+v"(aoff[0]), "+v"(aoff[1]), "+v"(boff[0]), "+v"(boff[1]));
    bf16x8 kp[2];
    auto body = [&](bool upd, bool keep = false, bool first = false) __attribute__((always_inline)) {
      lds_cptr sb = shm3;
      auto rdb = [&](int g, int i) {
        lds_cptr p = sb + boff[g & 1] + (g >> 1) * 8192 + (i >> 1) * 4096;
        const int s = ((g + 1) & 1) * 2 + (i >> 1);
        if (i & 1) bh[s] = tr_read(p + 512); else bl[s] = tr_read(p);
      };
      auto rda = [&](int ks, int mf) { (ks ? af1 : af0)[mf] = *(const __attribute__((address_space(3))) bf16x8*)(sb + aoff[ks] + mf * 2048); };
      SB0();
      __builtin_amdgcn_s_waitcnt(0xC07F);
#pragma unroll
      for (int gg = 0; gg < 10; ++gg) {
        const int g = (gg + 9) % 10;
#pragma unroll
        for (int t = 0; t < 8; ++t) {
          const bool two = t >= 4 && !(upd && g >= 5 && g <= 8);
          SB0();
          if ((!two || (t & 1)) && !(first && g == 9)) mm(g, two ? t - 1 : t, two, first);
          SB0();
          if (first && !LD && (g == 3 || g == 7) && t == 0) BAR();
          if (g == 9) {
            if (t == 0 || t == 2) { rdb(0, t); rdb(0, t + 1); }
            if (t & 1) { rda((t >> 1) & 1, t >> 1); rda(((t >> 1) & 1) ^ 1, t >> 1); }
          } else if (t < 4) rdb(g + 1, t);
          else if (keep && g == 7) {
            if (t == 5) { WAIT_V(0); kp[0] = tr_frag(shm3 + tro); }
            if (t == 7) kp[1] = tr_frag(shm3 + tro + 4096);
          } else if (upd) {
            if (g == 5 && t == 4) { sq0 += 2 * q32; PIN_S(sq0); }
            if (g == 5 && t == 5) { sq1 += 2 * q32; PIN_S(sq1); }
            if (g == 5 && t == 6) { sg0 += 64 * F_ * 2; PIN_S(sg0); }
            if (g == 6 && t == 4) { sg1 += 64 * F_ * 2; PIN_S(sg1); }
            if (g == 6 && t == 5) { sx += 128; PIN_S(sx); }
            if (g == 6 && t == 6) { dst -= dbuf; PIN_S(dst); }
            if (g == 7 && t == 4) asm volatile("v_add_co_u32 %0, %2, %4, %0\n\tv_add_co_u32 %1, %3, %4, %1" : "+v"(mp[0][0]), "+v"(mp[1][0]), "=&s"(cy[0]), "=&s"(cy[1]) : "s"(2 * q32));
            if (g == 7 && t == 6) asm volatile("v_addc_co_u32 %0, vcc, 0, %0, %2\n\tv_addc_co_u32 %1, vcc, 0, %1, %3" : "+v"(mp[0][1]), "+v"(mp[1][1]) : "s"(cy[0]), "s"(cy[1]) : "vcc");
            if (g == 8 && t == 4) { aoff[0] += dbuf; aoff[1] += dbuf; asm volatile("" : "+v"(aoff[0]), "+v"(aoff[1])); }
            if (g == 8 && t == 5) { boff[0] += dbuf; boff[1] += dbuf; asm volatile("" : "+v"(boff[0]), "+v"(boff[1])); }
            if (g == 8 && t == 6) { dbuf = -dbuf; PIN_S(dbuf); }
          }
        }
      }
      SB0();
    };
    TS1(0);
#define ARRP(o, c) do {} while (0)
    if (LD) {
      stage(0, PA);
      WAIT_V(6 + PA);
      TS1(5);
      BARL();
      TS(5);
      stage(PA, PB);
      WAIT_V(2 + PB);
      BAR();
      stage(PB, PC);
      WAIT_V(PC);
      BAR();
      stage(PC, 18);
      body(true, false, true);
      WAIT_V(0);
      BARL();
#pragma clang loop unroll(disable)
      for (int kt = 1; kt < NT - 1; ++kt) {
        if (__builtin_expect(kt == 5 + wid, 0)) prep(true);
        stage(0, 18);
        body(true);
        WAIT_V(0);
        BARL();
        SB0();
      }
      start2();
      body(false);
    } else {
      char* wsx;
      auto tile = [&](int kt, bool keep) __attribute__((always_inline)) {
        SB0();
        ARRP(wid * 64 + kt * 4, true);
        BARL();
        body(false, keep);
        ARRP(320 + kt * 4 + 4, wid == 0);
        if (keep) asm volatile("" : "+v"(kp[0]), "+v"(kp[1])); else WAIT_V(0);
      };
      auto next = [&]() __attribute__((always_inline)) {
        aoff[0] += dbuf; aoff[1] += dbuf; boff[0] += dbuf; boff[1] += dbuf;
        dbuf = -dbuf;
        asm volatile("" : "+v"(aoff[0]), "+v"(aoff[1]), "+v"(boff[0]), "+v"(boff[1]), "+v"(vol), "+v"(tro) :: "memory");
        wsx = ws;
        PIN_S(wsx);
      };
      auto share = [&]() {
        glds16_v(pa, shm_base + DMP + wid * 1024);
        if (wid == 0) glds16_s(gb + dgu, vob, shm_base + DMP + 4096);
      };
      auto kept = [&]() {
        ca = __builtin_amdgcn_mfma_f32_16x16x32_bf16(kp[0], sel, ca, 0, 0, 0);
        if (wid == 0) cb = __builtin_amdgcn_mfma_f32_16x16x32_bf16(kp[1], sel, cb, 0, 0, 0);
      };
      auto cpub = [&](u32x4 a, u32x4 b) {
        if ((vol & 0xc0) == 0) {
          const char* const cs = wsx + 101 * MB_ + ntile * 10240 + mtile * 256;
          const unsigned co = (vol & 0x30) * 4 + (vol >> 8) * 16;
          gst16(cs + wid * 2048, co, a);
          if (wid == 0) gst16(cs + 8192, co, b);
        }
      };
      auto side0 = [&](int kt) __attribute__((always_inline)) {
        if (kt < 2 && kt + 2 >= NT0) {
          glds16_v(pa - 2 * pstep, shm_base + DMP + 7168);
          if (wid == 0) glds16_s(gb + dgu, vob - 128 * F_ * 2, shm_base + DMP + 7168);
        }
        share();
        u32x4 xw;
        asm volatile("v_mov_b32 %0, %4\n\tv_mov_b32 %1, %5\n\tv_mov_b32 %2, %6\n\tv_mov_b32 %3, %7" : "=&v"(xw[0]), "=&v"(xw[1]), "=&v"(xw[2]), "=&v"(xw[3])
                     : "v"(cd[0]), "v"(cd[1]), "v"(cd[2]), "v"(cd[3]));
        xrow(kt + 1);
        next();
        if (kt) kept();
        pa += pstep; vob += 64 * F_ * 2;
        accum(shm3 + tro + ((kt & 1) * G1_STAGE + 32768 + mtile * 1024 + wid * 7168 - DMP), 4 * 8192);
#pragma unroll
        for (int i = 0; i < 4; ++i) {
          float lo, hi;
          asm volatile("v_lshlrev_b32 %2, 16, %4\n\tv_and_b32 %3, 0xffff0000, %4\n\tv_add_f32 %0, %0, %2\n\tv_fmac_f32 %1, %2, %2\n\t"
                       "v_add_f32 %0, %0, %3\n\tv_fmac_f32 %1, %3, %3" : "+v"(sm), "+v"(sq), "=&v"(lo), "=&v"(hi) : "v"(xw[i]));
        }
      };
      BARL();
      body(false, false, true);
      WAIT_V(0);
      side0(0);
#pragma clang loop unroll(disable)
      for (int kt = 1; kt < 4; ++kt) {
        tile(kt, true);
        side0(kt);
      }
#pragma clang loop unroll(disable)
      for (int kt = 4; kt < 12; ++kt) {
        tile(kt, true);
        share();
        glds16_s(pf, vol, shm_base + DMP + 7168);
        next();
        kept();
        pa += pstep; vob += 64 * F_ * 2;
        pf = kt == 10 ? (const char*)wo + (bid * 4 + wid) * 2048 + 1024 : pf + 1024;
        if (kt < 8) {
          const unsigned q1 = bid * 4 + wid + (kt - 5) * 1024, q0 = q1 - 1024;
          if (q0 < 1152) gst16(wsx + 98 * MB_ + (q0 & 1) * 589824 + (q0 >> 1) * 1024, vol, *(const u32x4*)(shm + AUX + wid * 1024 + vol));
          if (q1 < 1152) {
            const char* src = (const char*)((q1 >> 1) < 448 ? wd + (size_t)(q1 >> 1) * 8 * D_ : wo + (size_t)((q1 >> 1) - 448) * 8 * D_);
            src += ((3 - ((unsigned)(uintptr_t)src >> 7)) & 7) * 128 + (q1 & 1) * 1024;
            glds16_s(src, (vol >> 7) * 2048 + (vol & 0x70), shm_base + AUX + wid * 1024);
          }
          u32x4 bk;
          asm volatile("v_mov_b32 %0, %1" : "=v"(bk[0]) : "s"(BLANK));
          bk[1] = bk[0]; bk[2] = bk[0]; bk[3] = bk[0];
          gst16(wsx + 64 * MB_ + bid * 16384 + wid * 4096 + (kt - 4) * 1024, vol, bk);
          if (tya && (kt == 4 || kt == 7)) gst16(wsx + 4 * MB_ + (mtile * 32 + ntile) * 4096 + wid * 2048 + (kt & 1) * 1024, vol, bk);
          if (kt == 4) cpub(bk, bk);
        } else {
          if (kt == 8) {
            const float ml = sm * (1.0f / 32), mean = half_sum(sm) * (1.0f / D_), dm = ml - mean;
            const float q = __builtin_fmaf(dm * 32.0f, dm, __builtin_fmaf(-ml, sm, sq));
            const unsigned ps = pack_stats(mean, fmaxf(half_sum(q) * (1.0f / D_), 0.f));
            const u64 now = __builtin_amdgcn_s_memrealtime();
            if ((vol & 0x1f0) == 0) gst16(wsx + 97 * MB_ + srow * 16, vol >> 5, u32x4{ps, (unsigned)now, ps, (unsigned)(now >> 32)});
          }
          if (kt == 9) {
            if (wid == 0) {
              u64 now;
              asm volatile("s_memrealtime %0\n\ts_waitcnt lgkmcnt(0)" : "=s"(now) :: "memory");
              if (vol == 0) asm volatile("global_store_dwordx2 %0, %1, %2 sc1" :: "v"(vol), "v"(now), "s"(wsx + 100 * MB_ + 131072 + bid * 8) : "memory");
            }
            if (wid == 2) glds16_s1(wsx + 102 * MB_ + (bid & ~1) * 512, vol, shm_base + DMP + 7168);
          }
        }
      }
      asm volatile("" :: "v"(cd));
      if (wid >= 2) glds16_s1(wsx + 100 * MB_ + 131072 + (wid - 2) * 1024, vol, shm_base + DMP + 3072 + wid * 1024);
      tile(12, true);
      next();
      kept();
      const int lane = vol >> 4;
      glds16_s1(wsx + 97 * MB_ + (m0 + wid * 64) * 16, ((lane & 32) + 2 * (lane & 15) + ((lane >> 4) & 1)) * 16, shm_base + AUX + wid * 1024);
      asm volatile("s_nop 7\n\ts_nop 3" : "+v"(ca), "+v"(cb));
      cpub(__builtin_bit_cast(u32x4, ca), __builtin_bit_cast(u32x4, cb));
      if (wid >= 2) {
        u32x4 c4 = *(const u32x4*)(shm + DMP + 3072 + wid * 1024 + vol);
        bool ok = __all(fresh(((u64)c4[1] << 32) | c4[0], t_own) & fresh(((u64)c4[3] << 32) | c4[2], t_own));
        while (__builtin_expect(!ok, 0) && __builtin_amdgcn_s_memrealtime() - t_own < TIMEOUT_ / 10) {
          const __amdgpu_buffer_rsrc_t rs = __builtin_amdgcn_make_buffer_rsrc((void*)(wsx + 100 * MB_ + 131072), 0, 2048, 0x00020000);
          asm volatile("" ::: "memory");
          c4 = __builtin_bit_cast(u32x4, __builtin_amdgcn_raw_buffer_load_b128(rs, (wid - 2) * 1024 + vol, 0, 16));
          ok = __all(fresh(((u64)c4[1] << 32) | c4[0], t_own) & fresh(((u64)c4[3] << 32) | c4[2], t_own));
        }
        *(unsigned*)(shm + CNT - 4 + wid * 4) = ok ? 1u : 0u;
      }
      tile(13, false);
      next();
      u32x4 cw[4];
      auto cgather = [&]() {
        const __amdgpu_buffer_rsrc_t rc = __builtin_amdgcn_make_buffer_rsrc((void*)(wsx + 101 * MB_ + ntile * 10240), 0, 5 * 2048, 0x00020000);
        const unsigned vcs = (vol < 512 ? wid * 2048 : 8192) + (vol & 256) * 4 + (vol & 0xf0);
#pragma unroll
        for (int i = 0; i < 4; ++i) cw[i] = __builtin_bit_cast(u32x4, __builtin_amdgcn_raw_buffer_load_b128(rc, vcs + i * 256, 0, 16));
      };
      {
        const int ln = wid * 64 + (vol >> 4);
        u32x4 gr = *(const u32x4*)(shm + AUX + ln * 16);
        if (__builtin_expect(!__all((gr[0] == gr[2]) & fresh(((u64)gr[3] << 32) | gr[1], t_own)), 0)) {
          const __amdgpu_buffer_rsrc_t rs = __builtin_amdgcn_make_buffer_rsrc((void*)(wsx + 97 * MB_ + m0 * 16), 0, 4096, 0x00020000);
          const int prow = (ln & 0xe0) + 2 * (ln & 15) + ((ln >> 4) & 1);
          for (;;) {
            gr = __builtin_bit_cast(u32x4, __builtin_amdgcn_raw_buffer_load_b128(rs, prow * 16, 0, 16));
            if (__all((gr[0] == gr[2]) & fresh(((u64)gr[3] << 32) | gr[1], t_own))) break;
            if (__builtin_amdgcn_s_memrealtime() - t_own > TIMEOUT_) { gr[0] = row_stats_serial(x + (size_t)(m0 + prow) * D_); break; }
            __builtin_amdgcn_s_sleep(2);
          }
        }
        cgather();
        const float nm = -lo_f(gr[0]), dn = hi_f(gr[0]), r = __builtin_amdgcn_rcpf(dn);
        auto mul = [](float a, float b) { float c; asm("v_mul_f32 %0, %1, %2" : "=v"(c) : "v"(a), "v"(b)); return c; };
        const f32x4 fc = {nm, -2.3022081986f * r, mul(mul(mul(-0.10294324f, r), r), r), mul(dn, dn)};
        *(f32x4*)(shm + AUX + ln * 16) = fc;
        float* const tw = (float*)(shm + 2 * G1_STAGE + 4096 + wid * 768) + ln;
#pragma unroll
        for (int i = 0; i < 4; ++i) tw[i * 64] = fc[i];
        ((float*)(shm + AUX + 4096))[384 + ln] = r;
        cok = __builtin_amdgcn_readfirstlane(cnt[1] & cnt[2]) != 0u;
        tile2(0);
        wy = wsetup(wd, 0, vol >> 4, cok);
      }
      tile(14, false);
      next();
      {
        while (__builtin_expect(blank(cw) && cok, 0)) {
          __builtin_amdgcn_s_sleep(2);
          asm volatile("" ::: "memory");
          cgather();
        }
        f32x4 t;
#pragma unroll
        for (int i = 0; i < 4; ++i) {
          float a, b;
          asm("v_add_f32 %0, %2, %3\n\tv_add_f32 %1, %4, %5\n\tv_add_f32 %0, %0, %1" : "=&v"(a), "=&v"(b) : "v"(cw[0][i]), "v"(cw[1][i]), "v"(cw[2][i]), "v"(cw[3][i]));
          const auto r2 = __builtin_amdgcn_permlane16_swap(__float_as_uint(a), __float_as_uint(a), false, false);
          asm("v_add_f32 %0, %1, %2" : "=v"(t[i]) : "v"(r2[0]), "v"(r2[1]));
        }
        const int l = vol >> 4;
        if (__builtin_expect(!cok, 0)) {
          const int gq = (l >> 2) & 3;
          const char* pc = l < 32 ? pbase + ((psh & gq) ? dgu : 0) + (gq >> psh) * 32 + (l & 3) * 8 : gb + dgu + (l & 15) * 8;
          t = f32x4{0.f, 0.f, 0.f, 0.f};
#pragma unroll 1
          for (int k = 0; k < D_; ++k) { const u32x2 w = *(const __attribute__((address_space(1))) u32x2*)(pc + (size_t)k * (l < 32 ? pstr : F_ * 2)); t += f32x4{lo_f(w[0]), hi_f(w[0]), lo_f(w[1]), hi_f(w[1])}; }
        }
        if (l < 32 || wid == 0)
          *(f32x4*)((float*)(shm + AUX + 4096) + ((l >> 3) & 1) * 192 + (l < 32 ? wid : 4) * 32 + (l & 7) * 4) = t;
        __builtin_amdgcn_s_setprio(2);
      }
      SB0();
      ARRP(wid * 64 + 60, true);
      BARL();
      body(false);
    }
#pragma unroll
    for (int t = 0; t < 8; t += 2) mm(9, t, true);
    asm volatile("s_nop 7\n\ts_nop 3");
    SB0();
    TS1(3);
    if (!LD) {
#pragma unroll
      for (int i = 0; i < 3; ++i) {
        const unsigned r = ((role * 4 + (i & 1) * 2) * 14 + (i < 2 ? 10 : 0) + (wm & 2)) * 32;
        sw[0] = wy.wb[0] + (r << wy.sh[0]); sw[1] = wy.wb[1] + (r << wy.sh[1]);
        asm volatile(G2_W :: [l] "s"((i + 1) * 16384), G2_WOP(wy) : "memory", "scc", "m0");
      }
    } else {
      __builtin_amdgcn_s_setprio(3);
      WAIT_V(0);
    }
  };
  if (wid >= 4) kloop(std::integral_constant<int, 1>{});
  else kloop(std::integral_constant<int, 0>{});
  __builtin_amdgcn_sched_barrier(0);
  int tz = threadIdx.x;
  asm volatile("" : "+v"(tz));
  __builtin_amdgcn_sched_barrier(0);
  const int li = tz & 15, lq = (tz >> 4) & 3, ln = tz & 63;
  const __amdgpu_buffer_rsrc_t ra = __builtin_amdgcn_make_buffer_rsrc((void*)abuf, 0, 16 * 72 * 16384, 0x00020000);
  float* const cfin = (float*)(shm + AUX + 4096);
  float* const svr = cfin + 384;
  float* const tb = (float*)(shm + 2 * G1_STAGE + 4096 + wm * 1024);
  auto signal = [&](unsigned v) {
    unsigned a, b;
    asm volatile("v_mov_b32 %0, %2\n\tv_mov_b32 %1, %3\n\ts_mov_b64 exec, 1\n\tds_add_u32 %0, %1\n\ts_mov_b64 exec, -1"
                 : "=&v"(a), "=&v"(b) : "s"(shm_base + CNT), "s"(v) : "memory");
  };
  auto await = [&](unsigned mask, unsigned val) {
    unsigned a, t;
    asm volatile("v_mov_b32 %0, %2\n1:\n\tds_read_b32 %1, %0\n\ts_waitcnt lgkmcnt(0)\n\tv_readfirstlane_b32 vcc_lo, %1\n\ts_and_b32 vcc_lo, vcc_lo, %3\n\t"
                 "s_cmp_eq_u32 vcc_lo, %4\n\ts_cbranch_scc1 2f\n\ts_sleep 1\n\ts_branch 1b\n2:" : "=&v"(a), "=&v"(t) : "s"(shm_base + CNT), "s"(mask), "s"(val) : "memory", "scc", "vcc");
  };
  auto client = [&](int l) { const int m128 = mtile * 2 + ((l >> 4) & 1); return (((l & 7) * 4 + (m128 & 3)) * 8) + ((m128 >> 2) ^ ((l & 8) ? 5 : 0)); };
  auto notify = [&](int l0, int nl) __attribute__((always_inline)) {
    u64 now;
    asm volatile("s_memrealtime %0\n\ts_waitcnt lgkmcnt(0)" : "=s"(now) :: "memory");
    int l = threadIdx.x;
    asm volatile("" : "+v"(l));
    l &= 63;
    const __amdgpu_buffer_rsrc_t rd1 = __builtin_amdgcn_make_buffer_rsrc((void*)(done1 + ntile), 0, 256 * 512, 0x00020000);
    if (l < nl) __builtin_amdgcn_raw_buffer_store_b64(u32x2{(unsigned)now, (unsigned)(now >> 32)}, rd1, client(l0 + l) * 512, 0, 16);
  };
  const int xo = ((xcd & 1) << 1) | ((xcd >> 1) & 1), te = (xcd >> 2) * 28 + 6 * (xo >> 1) + (xo & 1), tl = (xcd >> 2) * 28 + 12 + 4 * xo;
  const u64 te0 = __builtin_amdgcn_s_memrealtime();
  const __amdgpu_buffer_rsrc_t re = __builtin_amdgcn_make_buffer_rsrc((void*)(done1 + client((wm >> 1) * 16 + 8 - (xcd & 4) * 2) * 64 + 32 + (ntile & 15) * 2 + (wm & 1)),
                                                                      0, 7 * 16384 + 8, 0x00020000);
  auto early = [&]() {
    __builtin_amdgcn_raw_buffer_store_b64(u32x2{(unsigned)te0, (unsigned)(te0 >> 32)}, re, ln * 16384, 0, 16);
  };
  signal(1u << (8 * wc));
  if (wc) {
    await(0xffffu, 0x404u);
#pragma unroll
    for (int f = 0; f < 4; ++f) *(u32x4*)(shm + G2_X + wm * 4096 + f * 1024 + ln * 16) = xq[f];
    SB0();
    __builtin_amdgcn_s_setprio(1);
  }
  TS1(4);
  f32x4 rf[4];
#pragma unroll
  for (int mf = 0; mf < 4; ++mf) rf[mf] = *(const f32x4*)(shm + AUX + (wm * 64 + mf * 16 + li) * 16);
  const float* const cl = cfin + wc * 192 + lq * 4;
  const int sbase = ((mtile * 2 + (wm >> 1)) * 72 * 8 + (wm & 1) * 4) * 2048;
  auto putf = [&](int kg, int mf, u32x4 v) {
    __builtin_amdgcn_raw_buffer_store_b128(v, ra, ln * 16 + sbase + ((kg >> 1) * 8 + mf) * 2048 + (kg & 1) * 1024, 0, 16);
  };
  auto gelp = [&](f32x2 g, f32x2 u, f32x2 cg, f32x2 cu, f32x2 m2, f32x2 k12, f32x2 k22, f32x2 d2) {
    const f32x2 tg = __builtin_elementwise_fma(m2, cg, g), tu = __builtin_elementwise_fma(m2, cu, u);
    const f32x2 arg = tg * __builtin_elementwise_fma(tg * tg, k22, k12);
    const f32x2 dn = __builtin_elementwise_fma(f32x2{__builtin_amdgcn_exp2f(arg[0]), __builtin_amdgcn_exp2f(arg[1])}, d2, d2);
    const f32x2 r = (tg * tu) * f32x2{__builtin_amdgcn_rcpf(dn[0]), __builtin_amdgcn_rcpf(dn[1])};
    return pack2(r[0], r[1]);
  };
  auto gel2 = [&](int p, int kg, int mf0 = 0, int mf1 = 4) {
    f32x4 cg[2], cu[2];
#pragma unroll
    for (int j = 0; j < 2; ++j) { cg[j] = *(const f32x4*)(cl + p * 32 + j * 16); cu[j] = *(const f32x4*)(cl + p * 32 + 32 + j * 16); }
#pragma unroll
    for (int mf = mf0; mf < mf1; ++mf) {
      const f32x2 m2 = {rf[mf][0], rf[mf][0]}, k12 = {rf[mf][1], rf[mf][1]}, k22 = {rf[mf][2], rf[mf][2]}, d2 = {rf[mf][3], rf[mf][3]};
      u32x4 o;
#pragma unroll
      for (int i = 0; i < 4; ++i) {
        const int j = i >> 1, e = (i & 1) * 2;
        const f32x4 g = acc[mf][p * 2 + j], u = acc[mf][p * 2 + 2 + j];
        o[i] = gelp(f32x2{g[e], g[e + 1]}, f32x2{u[e], u[e + 1]}, f32x2{cg[j][e], cg[j][e + 1]}, f32x2{cu[j][e], cu[j][e + 1]}, m2, k12, k22, d2);
      }
      putf(kg, mf, o);
    }
  };
  int tsel = ntile & 1;
  asm volatile("" : "+s"(tsel));
  if (tsel != 0) {
    const int tq = (wc ? tl : te + 2 + (xo & 1)) + he;
    gel2(0, 2 * tq);
    gel2(3, 2 * tq + 1);
    const float cg = cfin[wc * 192 + 64 + li], cu = cfin[wc * 192 + 80 + li];
#pragma unroll
    for (int mf = 0; mf < 4; ++mf) {
      f32x4 t[4];
#pragma unroll
      for (int i = 0; i < 4; ++i) t[i] = *(const f32x4*)(tb + i * 64 + mf * 16 + lq * 4);
      const f32x4 g = acc[mf][4], u = acc[mf][5];
      u32x2 o;
#pragma unroll
      for (int e = 0; e < 4; e += 2)
        o[e >> 1] = gelp(f32x2{g[e], g[e + 1]}, f32x2{u[e], u[e + 1]}, f32x2{cg, cg}, f32x2{cu, cu}, f32x2{t[0][e], t[0][e + 1]},
                         f32x2{t[1][e], t[1][e + 1]}, f32x2{t[2][e], t[2][e + 1]}, f32x2{t[3][e], t[3][e + 1]});
      *(u32x2*)(shm + wm * 4096 + mf * 1024 + wc * 512 + li * 32 + lq * 8) = o;
      if (mf == EPOS && wc == 0) {
        TS1(1);
        WAIT_V(0);
        TS1(6);
        early();
      }
    }
    if (wc == 0) {
      signal(0x10000u << wid);
    } else {
      await(0x1000u << wid, 0x1000u << wid);
#pragma unroll
      for (int mf = 0; mf < 4; ++mf) putf(2 * (tl + 2) + he, mf, __builtin_bit_cast(u32x4, tr_frag(shm3 + wm * 4096 + mf * 1024 + ln * 8)));
    }
  } else {
    float rd[4];
#pragma unroll
    for (int mf = 0; mf < 4; ++mf) rd[mf] = svr[wm * 64 + mf * 16 + li];
    auto cvq = [&](int p, int mf, const f32x4& c0, const f32x4& c1) {
      u32x4 o;
#pragma unroll
      for (int j = 0; j < 2; ++j) {
        const f32x4 a = acc[mf][p * 2 + j], c4 = j ? c1 : c0;
        const f32x2 m2 = {rf[mf][0], rf[mf][0]}, r2 = {rd[mf], rd[mf]};
        f32x2 t0 = __builtin_elementwise_fma(m2, f32x2{c4[0], c4[1]}, f32x2{a[0], a[1]});
        f32x2 t1 = __builtin_elementwise_fma(m2, f32x2{c4[2], c4[3]}, f32x2{a[2], a[3]});
        if (p) { t0 *= r2; t1 *= r2; }
        o[2 * j] = pack2(t0[0], t0[1]); o[2 * j + 1] = pack2(t1[0], t1[1]);
      }
      return o;
    };
    const float cv0 = cfin[wc * 192 + 64 + li], cv1 = cfin[wc * 192 + 80 + li];
    auto cvv = [&](int mf) {
      const f32x4 n4 = *(const f32x4*)(tb + mf * 16 + lq * 4), r4 = *(const f32x4*)(svr + wm * 64 + mf * 16 + lq * 4);
      u32x4 o;
#pragma unroll
      for (int j = 0; j < 2; ++j) {
        const f32x4 a = acc[mf][4 + j];
        const f32x2 c2 = {j ? cv1 : cv0, j ? cv1 : cv0};
        const f32x2 t0 = __builtin_elementwise_fma(f32x2{n4[0], n4[1]}, c2, f32x2{a[0], a[1]}) * f32x2{r4[0], r4[1]};
        const f32x2 t1 = __builtin_elementwise_fma(f32x2{n4[2], n4[3]}, c2, f32x2{a[2], a[3]}) * f32x2{r4[2], r4[3]};
        o[2 * j] = pack2(t0[0], t0[1]); o[2 * j + 1] = pack2(t1[0], t1[1]);
      }
      return o;
    };
    u32x4 kx[4], vx[4], qx[4], hx[4];
    const bf16x4 ones = {0x3f80, 0x3f80, 0x3f80, 0x3f80};
    const f32x4 ck0 = *(const f32x4*)(cl + 32), ck1 = *(const f32x4*)(cl + 48);
    kx[2] = cvq(1, 2, ck0, ck1); kx[3] = cvq(1, 3, ck0, ck1);
    vx[2] = cvv(2); vx[3] = cvv(3);
    auto exo = [](int e) { return e * 4096 + (e >> 2) * (G2_A - 16384); };
    const bool needh = wm == 0 && mtile > 0;
    const __amdgpu_buffer_rsrc_t rh = __builtin_amdgcn_make_buffer_rsrc((void*)((char*)halo + ((needh ? mtile - 1 : mtile) * 32 + ntile + wc) * 4096), 0,
                                                                        needh || wm == 3 ? 4096 : 0, 0x00020000);
    if (wm < 3) {
      char* const ex = shm + exo(wc * 3 + wm) + ln * 16;
      *(u32x4*)ex = kx[2]; *(u32x4*)(ex + 1024) = kx[3]; *(u32x4*)(ex + 2048) = vx[2]; *(u32x4*)(ex + 3072) = vx[3];
      signal(0x10000u << wid);
    } else {
#pragma unroll
      for (int r = 0; r < 4; ++r) __builtin_amdgcn_raw_buffer_store_b128(r < 2 ? kx[2 + r] : vx[r], rh, ln * 16 + r * 1024, 0, 16);
    }
    auto hload = [&]() {
#pragma unroll
      for (int r = 0; r < 4; ++r) hx[r] = __builtin_bit_cast(u32x4, __builtin_amdgcn_raw_buffer_load_b128(rh, ln * 16 + r * 1024, 0, 16));
    };
    SB0();
    gel2(3, 2 * (wc ? tl + 3 : te) + he);
    SB0();
    if (wm == 0) hload();
    SB0();
    kx[0] = cvq(1, 0, ck0, ck1); kx[1] = cvq(1, 1, ck0, ck1);
    vx[0] = cvv(0); vx[1] = cvv(1);
    {
      const f32x4 cq0 = *(const f32x4*)(cl), cq1 = *(const f32x4*)(cl + 16);
#pragma unroll
      for (int mf = 0; mf < 4; ++mf) qx[mf] = cvq(0, mf, cq0, cq1);
    }
    f32x4 mk0, mk1;
    u32x2 pm;
#pragma unroll
    for (int j = 0; j < 4; ++j) {
      const int d = 4 * lq + j - li;
      mk0[j] = d >= 9 ? 0.f : -1.0e30f;
      mk1[j] = (unsigned)(d + 7) <= 7u ? 0.f : -1.0e30f;
    }
#pragma unroll
    for (int j = 0; j < 2; ++j) pm[j] = (4 * lq + 2 * j > li ? 0xffffu : 0u) | (4 * lq + 2 * j + 1 > li ? 0xffff0000u : 0u);
    auto max3 = [](float a, float b, float c) { float r; asm("v_max3_f32 %0, %1, %2, %3" : "=v"(r) : "v"(a), "v"(b), "v"(c)); return r; };
    auto max2 = [](float a, float b) { float r; asm("v_max_f32 %0, %1, %2" : "=v"(r) : "v"(a), "v"(b)); return r; };
    asm volatile("" : "+v"(kx[0]), "+v"(kx[1]), "+v"(vx[0]), "+v"(vx[1]), "+v"(qx[0]), "+v"(qx[1]), "+v"(qx[2]), "+v"(qx[3]), "+v"(mk0), "+v"(mk1), "+v"(pm));
    TS1(1);
    if (wc == 0) {
      if (wm == 0) WAIT_V(4); else WAIT_V(0);
      early();
    }
    if (wm > 0) {
      await(0x8000u << wid, 0x8000u << wid);
#pragma unroll
      for (int i = 0; i < 4; ++i) hx[i] = *(const u32x4*)(shm + exo(wc * 3 + wm - 1) + i * 1024 + ln * 16);
    } else if (__builtin_expect(cok || !needh, 1)) {
      while (__builtin_expect(blank(hx), 0)) {
        __builtin_amdgcn_s_sleep(1);
        asm volatile("" ::: "memory");
        hload();
      }
    } else {
      await(0xffffu, 0x404u);
      char* const fb = shm + exo(6 + wc) + ln * 16;
#pragma unroll 1
      for (int i = 0; i < 32; ++i) {
        const int rec = i >> 3, e = i & 7;
        const int row = m0 - 32 + 2 * (rec < 2 ? li : lq * 4 + (e & 3)) + (rec & 1);
        const int col = (rec < 2 ? D_ + lq * 4 + (e & 3) : 2 * D_ + li) + (ntile + wc) * 32 + (e >> 2) * 16;
        const u16* xr = x + (size_t)row * D_;
        const unsigned stt = row_stats_serial(xr);
        const float mb = lo_f(stt);
        float a = 0.f;
#pragma unroll 1
        for (int k = 0; k < D_; ++k) a += (bf2f(xr[k]) - mb) * bf2f(wqkv[(size_t)k * 3072 + col]);
        *(u16*)(fb + rec * 1024 + e * 2) = f2bf(a / hi_f(stt));
      }
#pragma unroll
      for (int i = 0; i < 4; ++i) hx[i] = *(const u32x4*)(fb + i * 1024);
    }
    TS1(6);
    {
      const f32x4 z = {0.f, 0.f, 0.f, 0.f};
      const float mz = mtile == 0 && wm == 0 ? -1.0e30f : 0.f;
      const f32x4 mkf = mk0 + f32x4{mz, mz, mz, mz};
      f32x4 s0[4], s1[4];
#pragma unroll
      for (int i = 0; i < 4; ++i) {
        const int n = i ^ 2;
        const bf16x8 qf = __builtin_bit_cast(bf16x8, qx[n]);
        s1[n] = __builtin_amdgcn_mfma_f32_16x16x32_bf16(__builtin_bit_cast(bf16x8, kx[n]), qf, mk1, 0, 0, 0);
        s0[n] = __builtin_amdgcn_mfma_f32_16x16x32_bf16(__builtin_bit_cast(bf16x8, n < 2 ? hx[n] : kx[n - 2]), qf, n < 2 ? mkf : mk0, 0, 0, 0);
      }
      SB0();
      asm volatile("s_nop 7\n\ts_nop 3" : "+v"(s0[0]), "+v"(s1[0]), "+v"(s0[1]), "+v"(s1[1]), "+v"(s0[2]), "+v"(s1[2]), "+v"(s0[3]), "+v"(s1[3]));
      float mx[4];
#pragma unroll
      for (int n = 0; n < 4; ++n)
#pragma unroll
        for (int j = 0; j < 4; ++j) s0[n][j] = max2(s0[n][j], s1[n][j]);
#pragma unroll
      for (int n = 0; n < 4; ++n) mx[n] = max3(s0[n][0], s0[n][1], s0[n][2]);
#pragma unroll
      for (int n = 0; n < 4; ++n) mx[n] = max2(mx[n], s0[n][3]);
#pragma unroll
      for (int n = 0; n < 4; ++n) { auto r = __builtin_amdgcn_permlane16_swap(__float_as_uint(mx[n]), __float_as_uint(mx[n]), false, false);
        mx[n] = max2(__uint_as_float(r[0]), __uint_as_float(r[1])); }
#pragma unroll
      for (int n = 0; n < 4; ++n) { auto r = __builtin_amdgcn_permlane32_swap(__float_as_uint(mx[n]), __float_as_uint(mx[n]), false, false);
        mx[n] = max2(__uint_as_float(r[0]), __uint_as_float(r[1])); }
      SB0();
      constexpr float cs2 = 0.17677669529663687f * 1.4426950408889634f;
      f32x4 os[4], o0[4], o1[4];
#pragma unroll
      for (int n = 0; n < 4; ++n) {
        const float c = cs2 * rd[n], nm = -mx[n] * c;
        const f32x4 ar = __builtin_elementwise_fma(s0[n], f32x4{c, c, c, c}, f32x4{nm, nm, nm, nm});
        const u32x2 pk = {pack2(__builtin_amdgcn_exp2f(ar[0]), __builtin_amdgcn_exp2f(ar[1])), pack2(__builtin_amdgcn_exp2f(ar[2]), __builtin_amdgcn_exp2f(ar[3]))};
        const bf16x4 pa = __builtin_bit_cast(bf16x4, pk), p0 = __builtin_bit_cast(bf16x4, pk & pm), p1 = __builtin_bit_cast(bf16x4, pk & ~pm);
        const u32x4 vp = n < 2 ? hx[2 + n] : vx[n - 2];
        auto half = [](const u32x4& v, int h) { return __builtin_bit_cast(bf16x4, u32x2{v[2 * h], v[2 * h + 1]}); };
        os[n] = __builtin_amdgcn_mfma_f32_16x16x16bf16_1k(ones, pa, z, 0, 0, 0);
        o0[n] = __builtin_amdgcn_mfma_f32_16x16x16bf16_1k(half(vx[n], 0), p1, __builtin_amdgcn_mfma_f32_16x16x16bf16_1k(half(vp, 0), p0, z, 0, 0, 0), 0, 0, 0);
        o1[n] = __builtin_amdgcn_mfma_f32_16x16x16bf16_1k(half(vx[n], 1), p1, __builtin_amdgcn_mfma_f32_16x16x16bf16_1k(half(vp, 1), p0, z, 0, 0, 0), 0, 0, 0);
      }
#pragma unroll
      for (int n = 0; n < 4; ++n) {
        const float inv = __builtin_amdgcn_rcpf(os[n][0]);
        const f32x4 n0 = o0[n] * inv, n1 = o1[n] * inv;
        putf(112 + ntile + wc, n, u32x4{pack2(n0[0], n0[1]), pack2(n0[2], n0[3]), pack2(n1[0], n1[1]), pack2(n1[2], n1[3])});
      }
    }
  }
  TS1(2);
  {
  __builtin_amdgcn_sched_barrier(0);
  int tid = threadIdx.x;
  asm volatile("" : "+v"(tid));
  __builtin_amdgcn_sched_barrier(0);
  const int lane = tid & 63;
  const int wm = wid >> 1, wn = wid & 1;
  const int m0 = mt2 * 128, n0 = nt2 * 128;
  constexpr int PD = G2_NSTAGE;
  const int ny = (xcd & 1) ? NY - DLT : NY + DLT;
  const unsigned va = lane * 16;
  auto stage_y = [&]() {
    asm volatile("s_add_u32 m0, %[l], %[oa]\n\ts_nop 0\n\tglobal_load_lds_dwordx4 %[va], %[a] sc1\n\tglobal_load_lds_dwordx4 %[va], %[a] offset:1024 sc1\n\t"
                 "global_load_lds_dwordx4 %[va], %[a] offset:2048 sc1\n\tglobal_load_lds_dwordx4 %[va], %[a] offset:3072 sc1\n\t" G2_W
                 :: [l] "s"(sl), [oa] "s"(shm_base + G2_A + (p ^ role) * 4096), [a] "s"(sa), [va] "v"(va), G2_WOP(wy) : "memory", "scc", "m0");
    ++st;
    sa += 16384;
    sl = (sl + 16384) & 65535;
    wsel();
  };
  u64 ready = 0;
  const u64* const mbox = &done1[bid * 64 + lane];
  auto seen = [&](u64 v) __attribute__((always_inline)) {
    const u64 r = __ballot(fresh(v, t_own));
    u64 d;
    asm("s_bitreplicate_b64_b32 %0, %1" : "=s"(d) : "s"((unsigned)r >> (16 * role)));
    ready |= r | (d << 32);
  };
  auto need = [&](u64 mask, bool renew) __attribute__((always_inline)) {
    if ((ready & mask) != mask) {
      seen(__hip_atomic_load(mbox, RLX_AGENT));
      while ((ready & mask) != mask) {
        __builtin_amdgcn_s_sleep(PSLEEP);
        if (renew && !cok) notify(0, 32);
        seen(__hip_atomic_load(mbox, RLX_AGENT));
      }
    }
    asm volatile("" ::: "memory");
  };
  u64 fl = 0;
  auto peek = [&]() { asm volatile("global_load_dwordx2 %0, %1, %2 sc1" : "+v"(fl) : "v"(va >> 1), "s"(done1 + bid * 64) : "memory"); };
  auto check = [&](u64 mask, bool renew) __attribute__((always_inline)) {
    asm volatile("" : "+v"(fl));
    seen(fl);
    if (__builtin_expect((ready & mask) != mask, 0)) need(mask, renew);
  };
  if (wid >= 4) {
    TS(4);
    __builtin_amdgcn_s_setprio(0);
    BARL();
    stage_y();
    stage_y();
    BAR();
    if (p == 1) peek();
    stage_y();
    WAIT_V(16);
    BAR();
    if (p == 0) notify(0, 32);
#define G2_A2 "global_load_lds_dwordx4 %[va], %[a] sc1\n\tglobal_load_lds_dwordx4 %[va], %[a] offset:1024 sc1"
#define G2_A4 G2_A2 "\n\tglobal_load_lds_dwordx4 %[va], %[a] offset:2048 sc1\n\tglobal_load_lds_dwordx4 %[va], %[a] offset:3072 sc1"
#define G2_SYNC "\n\ts_mov_b32 m0, %[la]\n\ts_waitcnt vmcnt(%[n])\n\ts_barrier\n\t"
    const unsigned oy = shm_base + G2_A + (p ^ role) * 4096, os = shm_base + G2_A + (p >> 1) * 8192 + (p & 1) * 2048;
    asm volatile("s_mov_b32 m0, %[la]\n\ts_nop 0\n\t" G2_A4 :: [la] "s"(sl + oy), [a] "s"(sa), [va] "v"(va) : "memory", "m0");
    auto step = [&](long adv = 16384) __attribute__((always_inline)) {
      wsel();
      ++st;
      sa += adv;
      const unsigned sn = (sl + 16384) & 65535;
      asm volatile(G2_W G2_SYNC G2_A4 :: [l] "s"(sl), [la] "s"(sn + oy), [n] "n"(16), [a] "s"(sa), [va] "v"(va), G2_WOP(wy) : "memory", "scc", "m0");
      sl = sn;
    };
    auto chk1 = [&](u64 mask) __attribute__((always_inline)) { if (p == 1) { WAIT_V(16); check(mask, true); } };
    step();
    chk1(0xcc00cc00ull << 32);
    step(); step();
    if (p == 1) peek();
    step(); step();
    chk1(0x0f0fu << (16 * role));
#pragma unroll 1
    for (int g = 7; g < 13; ++g) step();
    if (p == 1) peek();
    step(); step();
    chk1((0xffffu << (16 * role)) | 0xf555f555u);
#pragma unroll 1
    for (int g = 15; g < min(ny, NY) - 5; ++g) step();
    if (ny > NY) {
      step(((1 - 2 * role) * NY + 1 - DLT) * 16384);
#pragma unroll
      for (int i = 1; i < DLT; ++i) step();
    }
    wsel();
    sa = sz;
    {
      const unsigned sn = (sl + 16384) & 65535;
      asm volatile(G2_W G2_SYNC G2_A2 :: [l] "s"(sl), [la] "s"(sn + os), [n] "n"(16), [a] "s"(sa), [va] "v"(va), G2_WOP(wy) : "memory", "scc", "m0");
      sl = sn;
    }
    sw[0] = wz.wb[0]; sw[1] = wz.wb[1];
#pragma unroll
    for (int j = -3; j < 13; ++j) {
      if (j == GETP + 1 || j == GETP + 2)
        asm volatile("s_mov_b32 m0, %[l]\n\ts_nop 0\n\tglobal_load_lds_dwordx4 %[va], %[a] offset:%[o] sc1\n\tglobal_load_lds_dwordx4 %[va], %[a] offset:%[o]+1024 sc1"
                     :: [l] "s"(shm_base + G2_X + 16384 + p * 4096), [a] "s"((const char*)slab + (size_t)(bid ^ 5) * 16384 + p * 4096), [va] "v"(va),
                        [o] "n"((j - GETP - 1) * 2048) : "memory", "m0");
      sa += 16384;
      const unsigned sn = (sl + 16384) & 65535;
      constexpr int G = GETP;
      static_assert(GETP <= GETJ + 1, "");
      if (j < 12) asm volatile(G2_W G2_SYNC G2_A2 :: [l] "s"(sl), [la] "s"(sn + os), [n] "n"((j == -3 ? 14 : 12) + (j > G && j < G + 3 ? 2 : 0) + (j > G + 1 && j < G + 4 ? 2 : 0)),
                               [a] "s"(sa), [va] "v"(va), G2_WOP(wz) : "memory", "scc", "m0");
      else asm volatile(G2_W :: [l] "s"(sl), G2_WOP(wz) : "memory", "scc", "m0");
      sl = sn;
      sw[0] += ws0; sw[1] += ws1;
    }
    WAIT_V(12);
    BAR();
    WAIT_V(6);
    BAR();
    WAIT_V(0);
    BAR();
    asm volatile("" : "+v"(fl));
    if (tid < 258) __hip_atomic_store(lane ? done2 + bid * 8 : &acks[bid], __builtin_amdgcn_s_memrealtime(), RLX_AGENT);
    if (__builtin_expect(!cok, 0) && p == 0) {
      const bool mine = lane <= 32;
      const u64* const pa = &acks[lane < 32 ? client(lane) : bid ^ 5];
      while (!__all(!mine || fresh(__hip_atomic_load(pa, RLX_AGENT), t_own))) {
        notify(0, 32);
        if (lane == 0) __hip_atomic_store(done2 + bid * 8, __builtin_amdgcn_s_memrealtime(), RLX_AGENT);
        __builtin_amdgcn_s_sleep(32);
      }
    }
  } else {
    TS(6);
    f32x4 acc[4][4], sacc[2][4];
    const int fr = lane & 15, fq = lane >> 4, q4 = fr >> 2, pp = lane & 3;
    const int ff = ((q4 >> 1) & 1) | ((fq & 1) << 1);
    int aof = G2_A + wm * 8192 + lane * 16;
    int boff[4];
#pragma unroll
    for (int j = 0; j < 4; ++j) boff[j] = wn * 8192 + (8 * fq + q4) * 128 + ((j ^ ff) * 32) + pp * 8;
    int vo = wid * 4096 + lane * 16;
    int vx = ((m0 + wm * 64 + role * 32 + 2 * fr) * D_ + n0 + wn * 64) * 2 + (fq & 1) * 32 + (fq >> 1) * 16;
    lds_cptr px = shm3 + G2_X + wid * 4096 + (fq >> 1) * 512 + fr * 16 + (fq & 1) * 8;
    asm volatile("" : "+v"(vo), "+v"(vx), "+v"(px));
#define SB() __builtin_amdgcn_sched_barrier(0)
    struct Fr { bf16x8 a[4]; bf16x4 bl[4], bh[4]; };
    Fr fa[2], fb[2];
    auto rd = [&](Fr* f, int odd, int t) {
      constexpr int ord[12] = {0, 1, 8, 2, 3, 4, 5, 6, 7, 9, 10, 11};
      constexpr int seq[24] = {0, 1, 2, 12, 13, 14, 15, 16, 3, 4, 5, 6, 17, 18, 19, 20, 7, 8, 9, 21, 10, 22, 11, 23};
      lds_cptr sb = shm3 + odd * 16384;
      const int ks = seq[t] / 12, o = ord[seq[t] % 12];
      if (o >= 8) f[ks].a[o - 8] = *(const __attribute__((address_space(3))) bf16x8*)(sb + aof + (o - 8) * 2048 + ks * 1024);
      else if (o & 1) f[ks].bh[o >> 1] = tr_read(sb + boff[o >> 1] + ks * 4096 + 512);
      else f[ks].bl[o >> 1] = tr_read(sb + boff[o >> 1] + ks * 4096);
    };
    auto bfr = [&](const Fr& c, int j) { return __builtin_shufflevector(c.bl[j], c.bh[j], 0, 1, 2, 3, 4, 5, 6, 7); };
    auto mf1 = [](f32x4& c, bf16x8 b, bf16x8 a, bool z = false) {
      if (z) asm volatile("v_mfma_f32_16x16x32_bf16 %0, %1, %2, 0" : "=&v"(c) : "v"(b), "v"(a));
      else asm volatile("v_mfma_f32_16x16x32_bf16 %0, %1, %2, %0" : "+v"(c) : "v"(b), "v"(a));
    };
    auto mf2 = [](f32x4& c, bf16x8 b, bf16x8 a, bf16x8 b2, bf16x8 a2, bool z) {
      if (z) asm volatile("v_mfma_f32_16x16x32_bf16 %0, %1, %2, 0\n\tv_mfma_f32_16x16x32_bf16 %0, %3, %4, %0" : "=&v"(c) : "v"(b), "v"(a), "v"(b2), "v"(a2));
      else asm volatile("v_mfma_f32_16x16x32_bf16 %0, %1, %2, %0\n\tv_mfma_f32_16x16x32_bf16 %0, %3, %4, %0" : "+v"(c) : "v"(b), "v"(a), "v"(b2), "v"(a2));
    };
    auto pairs = [&](f32x4 (*ac)[4], const Fr* c, int nq, int mx, int nf, auto fill, bool z = false) {
#pragma unroll
      for (int q = 0; q < nq; ++q) {
        const int mf = (q >> 2) ^ mx, j = (q & 4) ? 3 - (q & 3) : q & 3, ks = q & 1;
        SB();
        if (2 * q >= nf) mf2(ac[mf][j], bfr(c[ks], j), c[ks].a[mf], bfr(c[ks ^ 1], j), c[ks ^ 1].a[mf], z);
        else {
          mf1(ac[mf][j], bfr(c[ks], j), c[ks].a[mf], z);
          SB();
          fill(2 * q);
          SB();
          mf1(ac[mf][j], bfr(c[ks ^ 1], j), c[ks ^ 1].a[mf]);
          SB();
          fill(2 * q + 1);
        }
      }
      SB();
    };
    auto flip = [&]() {
      aof ^= 32768; boff[0] ^= 32768; boff[1] ^= 32768; boff[2] ^= 32768; boff[3] ^= 32768;
      asm volatile("" : "+v"(aof), "+v"(boff[0]), "+v"(boff[1]), "+v"(boff[2]), "+v"(boff[3]));
    };
    auto bar = [&]() { __builtin_amdgcn_s_waitcnt(0xC07F); BAR(); };
    auto ystg = [&](const Fr* c, Fr* n, int odd, bool z = false) {
      pairs(acc, c, 16, 0, odd ? 30 : 24, [&](int t) {
        if (t < 24) rd(n, odd, t);
        else if (t == 24) asm volatile("s_nop 0");
        else { int& a = t == 29 ? aof : boff[t - 25]; a ^= 32768; asm volatile("" : "+v"(a)); }
      }, z);
    };
    auto stg = [&](const Fr* c, Fr* n, int odd, bool z = false) { pairs(sacc, c, 8, 0, 10, [&](int t) { rd(n, odd, 2 * t); rd(n, odd, 2 * t + 1); }, z); };
    asm volatile("" : "+v"(aof), "+v"(boff[0]), "+v"(boff[1]), "+v"(boff[2]), "+v"(boff[3]));
    await(0xffffu, 0x404u);
    auto lda = [&](int i) {
      asm volatile("s_mov_b32 m0, %[l]\n\ts_nop 0\n\tglobal_load_lds_dwordx4 %[va], %[a] sc1\n\tglobal_load_lds_dwordx4 %[va], %[a] offset:1024 sc1\n\t"
                   "global_load_lds_dwordx4 %[va], %[a] offset:2048 sc1\n\tglobal_load_lds_dwordx4 %[va], %[a] offset:3072 sc1"
                   :: [l] "s"(shm_base + G2_A + (i + 1) * 16384 + (p ^ role) * 4096), [a] "s"(sa + i * 16384), [va] "v"(va) : "memory", "m0");
    };
    need(0x00330033ull << 32, false);
    TS(4);
    lda(0);
    lda(1);
    need(0x00ff00ffull << 32, false);
    TS(0);
    lda(2);
    WAIT_V(8);
    signal(1u << 24);
    await(0xff000000u, 4u << 24);
    __builtin_amdgcn_s_waitcnt(0xC07F);
    SB();
#pragma unroll
    for (int t = 0; t < 24; ++t) rd(fa, 1, t);
    flip();
#define CPRIO 0
    __builtin_amdgcn_s_setprio(CPRIO);
    WAIT_V(4);
    peek();
    bar();
    TS(7);
    ystg(fa, fb, 0, true);
    WAIT_V(1);
    bar();
    ystg(fb, fa, 1);
    WAIT_V(0);
    check(0x33003300ull << 32, false);
    constexpr int PAR = (NY - DLT + 1) & 1;
#pragma unroll 1
    for (int g = 2; g < ny - 2; g += 2) {
      bar();
      ystg(fa, fb, 0);
      bar();
      ystg(fb, fa, 1);
    }
    if (PAR) {
      bar();
      ystg(fa, fb, 0);
    }
    Fr* const f0 = PAR ? fb : fa;
    Fr* const f1 = PAR ? fa : fb;
    bar();
    {
      const __amdgpu_buffer_rsrc_t rs = __builtin_amdgcn_make_buffer_rsrc((void*)((char*)slab + (size_t)bid * 16384), 0, 16384, 0x00020000);
      u32x4 pk;
      pairs(acc, f0, 16, 2, 32, [&](int t) {
        if (t < 20) rd(f1, PAR, t);
        else {
          const int f = (t - 20) / 3, e = (t - 20) % 3;
          if (e < 2) { const f32x4 a = acc[2 + (f >> 1)][(f & 1) * 2 + e]; pk[2 * e] = pack2(a[0], a[1]); pk[2 * e + 1] = pack2(a[2], a[3]); }
          else __builtin_amdgcn_raw_buffer_store_b128(pk, rs, vo + f * 1024, 0, 16);
        }
      });
    }
    TS(2);
    static_assert(GETJ % 2 == 1 && GETJ >= 3 && GETJ <= 7, "");
    f32x4 xf[2][4];
#pragma unroll
    for (int j = 0; j < GETJ + 5; j += 2) {
      if (PAR) flip();
      bar();
      stg(f1, f0, PAR ^ 1, j == 0);
      if (!PAR) flip();
      bar();
      u32x2 xv[2];
      pairs(sacc, f0, 8, 0, 12, [&](int t) {
        if (t < 10) { rd(f1, PAR, 2 * t); rd(f1, PAR, 2 * t + 1); }
        else if (j < 8) xv[t - 10] = *(const __attribute__((address_space(3))) u32x2*)(px + (j >> 1) * 1024 + (t - 10) * 256);
      });
      if (j < 8) {
#pragma unroll
        for (int e = 0; e < 2; ++e) xf[j >> 2][(j & 2) + e] = f32x4{lo_f(xv[e][0]), hi_f(xv[e][0]), lo_f(xv[e][1]), hi_f(xv[e][1])};
        asm volatile("" : "+v"(xf[j >> 2][j & 2]), "+v"(xf[j >> 2][(j & 2) + 1]));
        SB();
      }
    }
    if (PAR) flip();
    WAIT_V(0);
    bar();
    u32x4 pv[4];
    {
      lds_cptr pp_ = shm3 + G2_X + 16384 + vo;
      pairs(sacc, f1, 8, 0, 12, [&](int t) {
        if (t < 2) {
#pragma unroll
          for (int f = 2 * t; f < 2 * t + 2; ++f) pv[f] = *(const __attribute__((address_space(3))) u32x4*)(pp_ + f * 1024);
        } else { rd(f0, PAR ^ 1, 2 * t - 4); rd(f0, PAR ^ 1, 2 * t - 3); }
      });
      if (__builtin_expect(blank(pv) || !cok, 0)) {
        const __amdgpu_buffer_rsrc_t rp = __builtin_amdgcn_make_buffer_rsrc((void*)((char*)slab + (size_t)(bid ^ 5) * 16384), 0, 16384, 0x00020000);
        while (!cok && !__all(fresh(__hip_atomic_load(done2 + (bid ^ 5) * 8, RLX_AGENT), t_own))) {
          notify(0, 32);
          if (lane == 0) __hip_atomic_store(done2 + bid * 8, __builtin_amdgcn_s_memrealtime(), RLX_AGENT);
          __builtin_amdgcn_s_sleep(8);
        }
        do {
          asm volatile("" ::: "memory");
#pragma unroll
          for (int f = 0; f < 4; ++f) pv[f] = __builtin_bit_cast(u32x4, __builtin_amdgcn_raw_buffer_load_b128(rp, vo + f * 1024, 0, 16));
        } while (blank(pv));
      }
    }
    auto silu = [&](int g0, int g1) {
#pragma unroll
      for (int f = g0; f < g1; ++f) {
        const int mf = f >> 2, j = f & 3;
        const u32x4 pw = pv[mf * 2 + (j >> 1)];
        const f32x4 ya = acc[mf][j] + f32x4{lo_f(pw[2 * (j & 1)]), hi_f(pw[2 * (j & 1)]), lo_f(pw[2 * (j & 1) + 1]), hi_f(pw[2 * (j & 1) + 1])};
#pragma unroll
        for (int e = 0; e < 2; ++e) {
          const f32x2 y2 = {ya[2 * e], ya[2 * e + 1]};
          const f32x2 ar = y2 * f32x2{-1.4426950408889634f, -1.4426950408889634f};
          const f32x2 dn = f32x2{__builtin_amdgcn_exp2f(ar[0]), __builtin_amdgcn_exp2f(ar[1])} + f32x2{1.0f, 1.0f};
          const f32x2 t = y2 * f32x2{__builtin_amdgcn_rcpf(dn[0]), __builtin_amdgcn_rcpf(dn[1])};
          acc[mf][j][2 * e] = t[0]; acc[mf][j][2 * e + 1] = t[1];
        }
        asm volatile("" : "+v"(acc[mf][j]));
      }
      SB();
    };
#define SPT 0, 0, 1, 2, 4, 6, 8, 8
    constexpr int spt[] = {SPT};
    silu(0, spt[1]);
#pragma unroll
    for (int j = GETJ + 6; j < 15; ++j) {
      if ((j ^ PAR) & 1) flip();
      bar();
      if (j & 1) stg(f0, f1, PAR); else stg(f1, f0, PAR ^ 1);
      silu(spt[j - GETJ - 5], j == 14 ? 8 : spt[j - GETJ - 4]);
    }
    pairs(sacc, f0, 8, 0, 0, [](int) {});
    asm volatile("s_nop 7\n\ts_nop 3" : "+v"(sacc[1][0]), "+v"(sacc[1][1]), "+v"(sacc[1][2]), "+v"(sacc[1][3]));
    SB();
    TS(1);
    {
      const __amdgpu_buffer_rsrc_t ro = __builtin_amdgcn_make_buffer_rsrc((void*)out, 0, S_ * D_ * 2, 0x00020000);
#pragma unroll
      for (int f = 0; f < 4; ++f) {
        const int mf = f >> 1, j = (f & 1) * 2;
        const f32x4 a = __builtin_elementwise_fma(sacc[mf][j], acc[mf][j], xf[mf][j]), b = __builtin_elementwise_fma(sacc[mf][j + 1], acc[mf][j + 1], xf[mf][j + 1]);
        const auto r0 = __builtin_amdgcn_permlane16_swap(pack2(a[0], a[1]), pack2(b[0], b[1]), false, false);
        const auto r1 = __builtin_amdgcn_permlane16_swap(pack2(a[2], a[3]), pack2(b[2], b[3]), false, false);
        __builtin_amdgcn_raw_buffer_store_b128(u32x4{r0[0], r1[0], r0[1], r1[1]}, ro, vx + mf * 2048 + j * 32, 0, 2);
      }
    }
    TS(3);
  }
  if (bid == 73 && wid == 4) { unsigned d_ = (ts_r[13] - (unsigned)t_own) * 10u; if (d_ > 9000u) d_ = 0; while (__builtin_amdgcn_s_memrealtime() - t_own < 6000ull + d_) {} }
  }
  asm volatile(".Lcode_end:");
}

extern "C" void kernel_launch(void* const* d_in, const int* in_sizes, int n_in, void* d_out, int out_size, void* d_ws,
                              size_t ws_size, hipStream_t stream) {
  const u16* A_x = (const u16*)d_in[0];
  const u16* A_wqkv = (const u16*)d_in[1];
  const u16* A_wo = (const u16*)d_in[2];
  const u16* B_wg = (const u16*)d_in[4];
  const u16* B_wu = (const u16*)d_in[5];
  const u16* B_wd = (const u16*)d_in[6];
  u16* out = (u16*)d_out;
  fused_kernel<<<256, 512, 0, stream>>>(A_x, A_wqkv, B_wg, B_wu, B_wd, A_wo, out, (char*)d_ws);
}
```

```cpp
#define ALL_TS 1
#define TSMASK 4104
#include <hip/hip_runtime.h>
#include <hip/hip_bf16.h>
#include <stdint.h>
#include <type_traits>

typedef unsigned short u16;
using bf16x8 = __attribute__((ext_vector_type(8))) short;
using bf16x4 = __attribute__((ext_vector_type(4))) short;
using f32x4 = __attribute__((ext_vector_type(4))) float;
using u32x2 = __attribute__((ext_vector_type(2))) unsigned;
using u32x4 = __attribute__((ext_vector_type(4))) unsigned;
typedef __attribute__((address_space(3))) const char* lds_cptr;

constexpr int S_ = 2048, D_ = 1024, F_ = 3584;

__device__ __forceinline__ float bf2f(u16 h) { return __uint_as_float((unsigned)h << 16); }
__device__ __forceinline__ u16 f2bf(float f) {
  unsigned u = __float_as_uint(f);
  u += 0x7fffu + ((u >> 16) & 1u);
  return (u16)(u >> 16);
}
__device__ __forceinline__ float rbf(float f) { return bf2f(f2bf(f)); }
typedef __attribute__((ext_vector_type(2))) __bf16 bf16x2_t;
typedef __attribute__((ext_vector_type(2))) float f32x2;
__device__ __forceinline__ unsigned pack2(float a, float b) {
  bf16x2_t r = __builtin_convertvector((f32x2){a, b}, bf16x2_t);
  return __builtin_bit_cast(unsigned, r);
}
__device__ __forceinline__ float lo_f(unsigned p) { return __uint_as_float(p << 16); }
__device__ __forceinline__ float hi_f(unsigned p) { return __uint_as_float(p & 0xffff0000u); }

#define TSDECL unsigned ts_r[16] = {0, 0, 0, 0, 0, 0, 0, 0, 0, 0, 0, 0, 0, 0, 0, 0}
#define TSS(i) do { if ((TSMASK >> (i)) & 1) { unsigned long long t_; asm volatile("s_memrealtime %0\n\ts_waitcnt lgkmcnt(0)" : "=s"(t_)); ts_r[i] = (unsigned)t_; } } while (0)
#define TSFLUSH() do { if ((threadIdx.x & 63) == 0) { for (int i_ = 0; i_ < 16; ++i_) flags[(blockIdx.x * 8 + (threadIdx.x >> 6)) * 16 + i_] = ts_r[i_]; } } while (0)
#define TS1(i) TSS(i)
#define TS(i) TSS(8 + (i))
#define PIN_S(v) asm volatile("" : "+s"(v))
#define WAIT_V(n) asm volatile("s_waitcnt vmcnt(%0)" ::"n"(n) : "memory")
#define WAIT_L(n) asm volatile("s_waitcnt lgkmcnt(%0)" ::"n"(n) : "memory")
#define BARL() do { WAIT_L(0); BAR(); } while (0)
#define BAR() do { asm volatile("" ::: "memory"); __builtin_amdgcn_s_barrier(); asm volatile("" ::: "memory"); } while (0)

__device__ __forceinline__ bf16x4 tr_read(lds_cptr p) {
  return __builtin_amdgcn_ds_read_tr16_b64_v4i16((__attribute__((address_space(3))) bf16x4*)p);
}
__device__ __forceinline__ bf16x8 tr_frag(lds_cptr p) {
  bf16x4 lo = tr_read(p), hi = tr_read(p + 512);
  bf16x8 r;
  r[0] = lo[0]; r[1] = lo[1]; r[2] = lo[2]; r[3] = lo[3];
  r[4] = hi[0]; r[5] = hi[1]; r[6] = hi[2]; r[7] = hi[3];
  return r;
}
__device__ __forceinline__ void glds16_s(const void* sbase, unsigned voff, unsigned lds_dst) {
  asm volatile("s_mov_b32 m0, %2\n\ts_nop 0\n\tglobal_load_lds_dwordx4 %0, %1" :: "v"(voff), "s"(sbase), "s"(lds_dst) : "m0");
}
__device__ __forceinline__ void glds16_s1(const void* sbase, unsigned voff, unsigned lds_dst) {
  asm volatile("s_mov_b32 m0, %2\n\ts_nop 0\n\tglobal_load_lds_dwordx4 %0, %1 sc1" :: "v"(voff), "s"(sbase), "s"(lds_dst) : "memory", "m0");
}
__device__ __forceinline__ void glds16_v(const void* p, unsigned lds_dst) {
  asm volatile("s_mov_b32 m0, %1\n\ts_nop 0\n\tglobal_load_lds_dwordx4 %0, off" :: "v"(p), "s"(lds_dst) : "m0");
}
__device__ __forceinline__ void gst16(const void* sbase, unsigned voff, u32x4 v) {
  asm volatile("global_store_dwordx4 %0, %1, %2 offset:0 sc1\n\ts_nop 1" :: "v"(voff), "v"(v), "s"(sbase) : "memory");
}

typedef unsigned long long u64;
__device__ __forceinline__ bool fresh(u64 stamp, u64 t_own) {
  const u64 b = t_own + 100ull;
  const unsigned lo = (unsigned)stamp - (unsigned)b, hi = (unsigned)(stamp >> 32) - (unsigned)(b >> 32) - ((unsigned)stamp < (unsigned)b ? 1u : 0u);
  return (hi == 0u) & (lo < (unsigned)(__builtin_amdgcn_s_memrealtime() - t_own));
}
constexpr u64 TIMEOUT_ = 200000;
__device__ __forceinline__ float half_sum(float v) {
  v += __builtin_bit_cast(float, __builtin_amdgcn_update_dpp(0, __builtin_bit_cast(int, v), 0xB1, 0xf, 0xf, false));
  v += __builtin_bit_cast(float, __builtin_amdgcn_update_dpp(0, __builtin_bit_cast(int, v), 0x4E, 0xf, 0xf, false));
  v += __builtin_bit_cast(float, __builtin_amdgcn_update_dpp(0, __builtin_bit_cast(int, v), 0x141, 0xf, 0xf, false));
  v += __builtin_bit_cast(float, __builtin_amdgcn_update_dpp(0, __builtin_bit_cast(int, v), 0x140, 0xf, 0xf, false));
  { auto r = __builtin_amdgcn_permlane16_swap(__float_as_uint(v), __float_as_uint(v), false, false); v = __uint_as_float(r[0]) + __uint_as_float(r[1]); }
  return v;
}
__device__ __forceinline__ unsigned pack_stats(float mean, float var) {
  return (unsigned)f2bf(mean) | ((unsigned)f2bf(sqrtf(rbf(rbf(var) + 1e-5f))) << 16);
}
__device__ __forceinline__ unsigned row_stats_serial(const u16* xr) {
  float s = 0.f, q = 0.f;
#pragma unroll 1
  for (int k = 0; k < D_; ++k) s += bf2f(xr[k]);
  const float mean = s * (1.0f / D_);
#pragma unroll 1
  for (int k = 0; k < D_; ++k) { const float d = bf2f(xr[k]) - mean; q += d * d; }
  return pack_stats(mean, q * (1.0f / D_));
}
constexpr int G1_STAGE = 32768 + 5 * 8192;
constexpr int PA = 4, PB = 8, PC = 12;
constexpr int DLT = 2;
constexpr int GETP = 4, GETJ = 3;
constexpr unsigned BLANK = 0x7f817f81u;
__device__ __forceinline__ bool blank(const u32x4* pv) {
  u64 m;
#define CX(i) "v_cmpx_ne_u32 vcc, %1, %" #i "\n\t"
  asm volatile(CX(2) CX(3) CX(4) CX(5) CX(6) CX(7) CX(8) CX(9) CX(10) CX(11) CX(12) CX(13) CX(14) CX(15) CX(16) CX(17)
               "s_mov_b64 %0, exec\n\ts_mov_b64 exec, -1\n\ts_nop 3" : "=s"(m) : "s"(BLANK), "v"(pv[0][0]), "v"(pv[0][1]), "v"(pv[0][2]), "v"(pv[0][3]),
               "v"(pv[1][0]), "v"(pv[1][1]), "v"(pv[1][2]), "v"(pv[1][3]), "v"(pv[2][0]), "v"(pv[2][1]), "v"(pv[2][2]), "v"(pv[2][3]),
               "v"(pv[3][0]), "v"(pv[3][1]), "v"(pv[3][2]), "v"(pv[3][3]) : "vcc");
  return m != ~0ull;
}
#define RLX_AGENT __ATOMIC_RELAXED, __HIP_MEMORY_SCOPE_AGENT

constexpr int G2_A = 65536, G2_X = 131072;
constexpr size_t MB_ = 1024 * 1024;
__global__ __launch_bounds__(512, 2) __attribute__((aligned(4096))) void fused_kernel(const u16* __restrict__ x, const u16* __restrict__ wqkv,
                                                       const u16* __restrict__ wg, const u16* __restrict__ wu,
                                                       const u16* __restrict__ wd, const u16* __restrict__ wo,
                                                       u16* __restrict__ out, char* __restrict__ ws) {
  u16* const halo = (u16*)(ws + 4 * MB_);
  u16* const abuf = (u16*)(ws + 16 * MB_);
  char* const slab = ws + 64 * MB_;
  unsigned* const flags = (unsigned*)(ws + 96 * MB_);
  char* const wcopy = ws + 98 * MB_;
  u64* const done1 = (u64*)(ws + 102 * MB_);
  u64* const done2 = (u64*)(ws + 100 * MB_ + 65536);
  u64* const acks = (u64*)(ws + 100 * MB_ + 196608);
  __shared__ __attribute__((aligned(1024))) char shm[163840];
  constexpr int AUX = 2 * G1_STAGE + 8192;
  {
    u64 pc; int len;
    asm volatile("s_getpc_b64 %0\n.Lcode_beg:\n\ts_mov_b32 %1, .Lcode_end-.Lcode_beg" : "=s"(pc), "=s"(len));
    const int ol = ((blockIdx.x >> 3) + 32 * (threadIdx.x >> 6)) * 1024 + (threadIdx.x & 63) * 16;
    asm volatile("s_mov_b32 m0, %2\n\ts_nop 0\n\tglobal_load_lds_dwordx4 %0, %1"
                 :: "v"(ol < len - 16 ? ol : 0), "s"(pc & ~15ull), "s"((unsigned)(uintptr_t)(lds_cptr)shm + 2 * G1_STAGE + 7168) : "m0");
    __builtin_amdgcn_sched_barrier(0);
  }
  const u64 t_own = __builtin_amdgcn_s_memrealtime();
  TSDECL;
  TS1(7);
  const int tid = threadIdx.x, lane = tid & 63;
  const int wid = __builtin_amdgcn_readfirstlane(tid >> 6);
  const int wm = wid & 3, wc = wid >> 2;
  int bid_ = blockIdx.x;
  PIN_S(bid_);
  const int bid = bid_ & 255, xcd = bid & 7, idx = bid >> 3;
  const int ntile = xcd * 4 + (idx & 3), mtile = idx >> 2;
  const int m0 = mtile * 256;
  const int hg = ntile >> 1;
  const bool tya = (ntile & 1) == 0;
  const int srow = m0 + ntile * 8 + wm * 2;
  lds_cptr shm3 = (lds_cptr)shm;
  const unsigned shm_base = (unsigned)(uintptr_t)shm3;
  const int r8 = tid >> 3, pos = tid & 7;
  const int phys = pos >> 1, half = pos & 1;
  const int fk = ((r8 >> 1) & 1) | (((r8 >> 3) & 1) << 1);
  const int vsrc = phys ^ fk;
  const int qstr = tya ? 3072 * 2 : F_ * 2;
  unsigned voa = (((r8 & 32) + 2 * (r8 & 15) + ((r8 >> 4) & 1)) * D_ + ((pos ^ (r8 & 7)) * 8)) * 2;
  unsigned vog = r8 * (F_ * 2) + vsrc * 32 + half * 16, voq = r8 * qstr + vsrc * 32 + half * 16;
  unsigned vom = r8 * qstr + (vsrc >> (tya ? 0 : 1)) * 32 + half * 16;
  asm volatile("" : "+v"(voa), "+v"(vog), "+v"(voq), "+v"(vom));
  const int kt0 = (hg >> 1) * 7, he = hg & 1;
  unsigned oq = (tya ? hg : kt0 + 2 * he) * 128, og = (kt0 + (tya ? 5 + he : 2 * he + 1)) * 128, om = tya ? 4096 : 512 - 192 * he;
  unsigned d0 = shm_base + wid * 1024 + 32768, ox = m0 * D_ * 2;
  long uq = -32 * qstr;
  PIN_S(oq); PIN_S(og); PIN_S(om); PIN_S(d0); PIN_S(ox); PIN_S(uq);
  __builtin_amdgcn_sched_barrier(0);
  asm volatile("" :: "s"(x), "s"(wqkv), "s"(wg), "s"(wu), "s"(wd), "s"(wo), "s"(out), "s"(ws));
  const char* const ab = (const char*)x + ox;
  int kz = 0;
  asm volatile("" : "+s"(kz));
  u64 pq = (u64)wqkv, pg = (u64)wg;
  PIN_S(pq); PIN_S(pg);
  const char* const qb = (const char*)(tya ? pq : pg) + oq;
  if (wid < 4) {
#pragma unroll
    for (int i = 0; i < 8; ++i) glds16_s(ab + i * 32 * D_ * 2, voa, d0 - 32768 + i * 4096);
  } else {
    glds16_s(qb, voq, d0); glds16_s(qb + uq, voq, d0 - 4096);
  }
  __builtin_amdgcn_sched_barrier(0);
  const char* const gb = (const char*)wg + og;
  const long dgu = (const char*)wu - (const char*)wg, dp1 = tya ? 2048 : dgu;
  const char* const mb0 = qb + om;
  const char* mptr = mb0 + (((vsrc & 1) && !tya) ? dgu : 0) + vom;
  if (wid >= 4) {
    const long ug = kz - 32 * F_ * 2;
    glds16_s(qb + dp1, voq, d0 + 8192); glds16_s(qb + dp1 + uq, voq, d0 + 4096);
    glds16_v(mptr, d0 + 2 * 8192); glds16_v(mptr + uq, d0 + 2 * 8192 - 4096);
    glds16_s(gb, vog, d0 + 3 * 8192); glds16_s(gb + ug, vog, d0 + 3 * 8192 - 4096);
    glds16_s(gb + dgu, vog, d0 + 4 * 8192); glds16_s(gb + dgu + ug, vog, d0 + 4 * 8192 - 4096);
  }
  __builtin_amdgcn_sched_barrier(0);
  f32x4 acc[4][10];

  constexpr int NT = D_ / 64;
  constexpr int CNT = AUX + 4096 + 3584;
  if (tid < 8) *(unsigned*)(shm + CNT + tid * 4) = 0u;
  bf16x8 af0[4], af1[4];
  bf16x4 bl[4], bh[4];
#define SB0() __builtin_amdgcn_sched_barrier(0)
  auto mm = [&](int g, int t, bool two, bool z = false) {
    const int mi = t >> 1, mf = ((g & 1) && g != 9) ? 3 - mi : mi, ks = (t & 1) ^ (mi & 1), s = ((g + 1) & 1) * 2;
    const bf16x8 b0 = __builtin_shufflevector(bl[s + ks], bh[s + ks], 0, 1, 2, 3, 4, 5, 6, 7), a0 = ks ? af1[mf] : af0[mf];
    const bf16x8 b1 = __builtin_shufflevector(bl[s + 1 - ks], bh[s + 1 - ks], 0, 1, 2, 3, 4, 5, 6, 7), a1 = ks ? af0[mf] : af1[mf];
    const bool tr = (g >> 1) == 2;
    if (z && two) asm volatile("v_mfma_f32_16x16x32_bf16 %0, %1, %2, 0\n\tv_mfma_f32_16x16x32_bf16 %0, %3, %4, %0" : "=&v"(acc[mf][g])
                               : "v"(tr ? a0 : b0), "v"(tr ? b0 : a0), "v"(tr ? a1 : b1), "v"(tr ? b1 : a1));
    else if (z && !(t & 1)) asm volatile("v_mfma_f32_16x16x32_bf16 %0, %1, %2, 0" : "=&v"(acc[mf][g]) : "v"(tr ? a0 : b0), "v"(tr ? b0 : a0));
    else if (two) asm volatile("v_mfma_f32_16x16x32_bf16 %0, %1, %2, %0\n\tv_mfma_f32_16x16x32_bf16 %0, %3, %4, %0" : "+v"(acc[mf][g])
                          : "v"(tr ? a0 : b0), "v"(tr ? b0 : a0), "v"(tr ? a1 : b1), "v"(tr ? b1 : a1));
    else asm volatile("v_mfma_f32_16x16x32_bf16 %0, %1, %2, %0" : "+v"(acc[mf][g]) : "v"(tr ? a0 : b0), "v"(tr ? b0 : a0));
  };
  typedef __attribute__((address_space(3))) unsigned lds_u32;
  lds_u32* const cnt = (lds_u32*)(shm3 + CNT);
  bool cok;
  constexpr int NY = 28;
  int role, mt2, nt2;
  struct WS { unsigned vb[2][2]; const char* wb[2]; int sh[2]; };
  auto wsetup = [&](const u16* w, int crow0, int l, bool ck) __attribute__((always_inline)) {
    WS r;
    const unsigned vr = ((l >> 5) & 1) * 16 + (wm & 1) * 8 + ((l >> 3) & 3), vc = (((l >> 1) & 3) ^ ((l >> 4) & 1)) * 32 + (l & 1) * 16;
    u64 cp = (u64)(wcopy + (nt2 >> 2) * 589824 + crow0 * 128);
    asm volatile("" : "+s"(cp));
#pragma unroll
    for (int i = 0; i < 2; ++i) {
      u64 dp = (u64)(w + nt2 * 128 + i * 64);
      asm volatile("" : "+s"(dp));
      const bool slow = ck & ((((unsigned)dp >> 7) & 7) == 3);
      r.sh[i] = slow ? 7 : 11;
      r.wb[i] = (const char*)(slow ? cp : dp) + (crow0 ? (wm >> 1) * 32 << r.sh[i] : 0);
      r.vb[i][0] = (vr << r.sh[i]) + vc; r.vb[i][1] = ((vr + 4) << r.sh[i]) + (vc ^ 64);
    }
    asm volatile("" : "+v"(r.vb[0][0]), "+v"(r.vb[0][1]), "+v"(r.vb[1][0]), "+v"(r.vb[1][1]));
    PIN_S(r.wb[0]); PIN_S(r.wb[1]); PIN_S(r.sh[0]); PIN_S(r.sh[1]);
    return r;
  };
  WS wy, wz;
  int vxq, ws0, ws1;
  const char* sz;
  u32x4 xq[4];
  unsigned vg;
  int st = 3;
  const char *sa, *sw[2];
  auto wsel = [&]() {
    const unsigned r = __builtin_amdgcn_readlane(vg, st);
    sw[0] = wy.wb[0] + (r << wy.sh[0]); sw[1] = wy.wb[1] + (r << wy.sh[1]);
  };
  const int p = wid & 3;
  unsigned sl = 0;
#define G2_W "s_add_u32 m0, %[l], %[ow]\n\ts_nop 0\n\tglobal_load_lds_dwordx4 %[v0], %[w0] sc1\n\t" \
             "s_add_u32 m0, m0, 1024\n\ts_nop 0\n\tglobal_load_lds_dwordx4 %[v1], %[w0] sc1\n\t" \
             "s_add_u32 m0, m0, 7168\n\ts_nop 0\n\tglobal_load_lds_dwordx4 %[v2], %[w1] sc1\n\t" \
             "s_add_u32 m0, m0, 1024\n\ts_nop 0\n\tglobal_load_lds_dwordx4 %[v3], %[w1] sc1"
#define G2_WOP(w) [ow] "s"(shm_base + p * 2048), [w0] "s"(sw[0]), [w1] "s"(sw[1]), [v0] "v"(w.vb[0][0]), [v1] "v"(w.vb[0][1]), [v2] "v"(w.vb[1][0]), [v3] "v"(w.vb[1][1])
  auto tile2 = [&](int t0) __attribute__((always_inline)) {
    int b = bid;
    asm volatile("" : "+s"(b));
    role = 1 - ((b >> 2) & 1);
    mt2 = ((role ? b : b ^ 5) & 7) * 4 + ((b >> 3) & 3), nt2 = b >> 5;
    sa = (const char*)abuf + (size_t)(mt2 * 72 + role * NY + t0) * 16384 + wm * 4096;
    asm volatile("" : "+s"(sa));
  };
  auto prep = [&](bool first) __attribute__((always_inline)) {
    int l = threadIdx.x;
    asm volatile("" : "+v"(l));
    l &= 63;
    if (first) {
      tile2(3);
      vxq = ((mt2 * 128 + (wm >> 1) * 64 + role * 32 + 2 * (l & 15)) * D_ + nt2 * 128 + (wm & 1) * 64) * 2 + ((l >> 4) & 1) * 32 + (l >> 5) * 16;
      const int t = min(l < NY ? l : l - DLT, NY - 1), w = wm >> 1;
      const int o = (0xffaa5500fae504ull >> (2 * t)) & 3;
      int u1 = (0x990990 >> ((2 * t) & 31)) & 3, u2 = 3 + (t & 3);
      asm volatile("" : "+v"(u1), "+v"(u2));
      const int u = t < 12 ? u1 : u2;
      vg = (__umul24((l < NY ? role : 1 - role) * 4 + (((o & 1) << 1) | (o >> 1)), 14) + ((0xb85140a >> (4 * u)) & 15) + (u == 5 ? w : 2 * w)) * 32;
      asm volatile("" : "+v"(vg), "+v"(vxq));
      sz = (const char*)abuf + (size_t)(mt2 * 72 + 56) * 16384 + (wm >> 1) * 8192 + role * 4096 + (wm & 1) * 2048;
      PIN_S(sz);
    }
    wy = wsetup(wd, 0, l, first);
    wz = wsetup(wo, F_, l, first);
    ws0 = 64 << wz.sh[0]; PIN_S(ws0);
    ws1 = 64 << wz.sh[1]; PIN_S(ws1);
    SB0();
  };
  auto start2 = [&]() __attribute__((always_inline)) {
    const __amdgpu_buffer_rsrc_t rx = __builtin_amdgcn_make_buffer_rsrc((void*)x, 0, S_ * D_ * 2, 0x00020000);
#pragma unroll
    for (int f = 0; f < 4; ++f) xq[f] = __builtin_bit_cast(u32x4, __builtin_amdgcn_raw_buffer_load_b128(rx, vxq + (f >> 1) * 2048 + (f & 1) * 64, 0, 0));
    cok = __builtin_amdgcn_readfirstlane(cnt[1] & cnt[2]) != 0u;
    if (__builtin_expect(!cok, 0)) prep(false);
    wsel();
    PIN_S(sw[0]); PIN_S(sw[1]);
    SB0();
  };
  auto kloop = [&](auto wmc) {
    constexpr bool LD = decltype(wmc)::value != 0;
    constexpr int DMP = 2 * G1_STAGE;
    const char* pa = nullptr;
    unsigned vob = 0;
    f32x4 ca = {0.f, 0.f, 0.f, 0.f}, cb = {0.f, 0.f, 0.f, 0.f}, cd = ca;
    bf16x8 sel = {0, 0, 0, 0, 0, 0, 0, 0};
    int tro = 0;
    const int psh = (wid == 2 && !tya) ? 1 : 0, pstr = wid < 3 ? qstr : F_ * 2;
    const char* const pbase = wid == 0 ? qb : (wid == 1 ? qb + dp1 : (wid == 2 ? mb0 : gb));
    int lane = threadIdx.x;
    asm volatile("; %1" : "+v"(lane) : "n"(LD ? 1 : 0));
    lane &= 63;
    const int fr = lane & 15, fq = lane >> 4, q4 = fr >> 2, pp = lane & 3;
    const int ff = ((q4 >> 1) & 1) | ((fq & 1) << 1);
    int aoff[2], boff[2];
#pragma unroll
    for (int ks = 0; ks < 2; ++ks) aoff[ks] = (wm * 64 + fr) * 128 + (((ks * 4 + fq) ^ (fr & 7)) * 16);
#pragma unroll
    for (int j = 0; j < 2; ++j) boff[j] = 32768 + (8 * fq + q4) * 128 + (((2 * wc + j) ^ ff) * 32) + pp * 8;
    unsigned vol = lane * 16;
    float sm = 0.f, sq = 0.f;
    const char* pf = (const char*)wd + (bid * 4 + wid) * 7168;
    long pstep = 64 * pstr;
    if (!LD) {
      const int rs = 256 + mtile * 8 + (lane >> 3), vs = ((lane & 7) >> 1) ^ (((rs >> 1) & 1) | ((mtile & 1) << 1));
      pa = pbase + ((psh & vs) ? dgu : 0) + (rs * pstr + (vs >> psh) * 32 + (lane & 1) * 16);
      vob = rs * (F_ * 2) + vs * 32 + (lane & 1) * 16;
      tro = DMP + wid * 1024 + q4 * 128 + ((fq ^ ((q4 >> 1) | ((mtile & 1) << 1))) * 32) + pp * 8;
      const short one = fr == fq ? 0x3f80 : 0;
      sel = bf16x8{one, one, one, one, one, one, one, one};
      asm volatile("" : "+v"(tro), "+v"(vol), "+v"(sel), "+v"(ca), "+v"(cb), "+v"(sm), "+v"(sq));
      PIN_S(pf); PIN_S(pstep);
    }
    auto accum = [&](lds_cptr p, int ob) {
      ca = __builtin_amdgcn_mfma_f32_16x16x32_bf16(tr_frag(p), sel, ca, 0, 0, 0);
      if (wid == 0) cb = __builtin_amdgcn_mfma_f32_16x16x32_bf16(tr_frag(p + ob), sel, cb, 0, 0, 0);
    };
    auto xrow = [&](int i) {
      u64 a = (u64)(x + (size_t)srow * D_ + i * 256), b = (u64)(wo + (bid * 4 + wid) * 1024);
      PIN_S(a); PIN_S(b);
      asm volatile("global_load_dwordx4 %0, %1, %2" : "+v"(cd) : "v"(vol + (vol & (i < 4 ? 512 : 0)) * 3), "s"(i < 4 ? a : b) : "memory");
    };
    const int q32 = 32 * qstr;
    const char *sq0 = qb + q32, *sq1 = sq0 + dp1, *sg0 = gb + 32 * F_ * 2, *sg1 = sg0 + dgu, *sx = ab + (long)(kz + 128 - 32 * D_ * 2);
    u32x2 mp[2] = {__builtin_bit_cast(u32x2, mptr + q32), __builtin_bit_cast(u32x2, mptr + 2 * q32)};
    u64 cy[2];
    unsigned dst = shm_base + G1_STAGE + wm * 1024;
    int dbuf = G1_STAGE;
    auto stage = [&](int i0, int i1) __attribute__((always_inline)) {
#pragma unroll
        for (int i = i0; i < i1; ++i) {
          const int q = i >> 1, h = i & 1;
          const unsigned ld = dst + h * 4096 + (q < 5 ? 32768 + q * 8192 : (q - 5) * 8192);
          if (q < 2) glds16_s((q ? sq1 : sq0) + h * q32, voq, ld);
          else if (q == 2) glds16_v(__builtin_bit_cast(const char*, mp[h]), ld);
          else if (q < 5) glds16_s((q == 4 ? sg1 : sg0) + h * 32 * F_ * 2, vog, ld);
          else glds16_s(sx + ((q - 5) * 64 + h * 32) * D_ * 2, voa, ld);
        }
    };
#pragma unroll
    for (int a = 0; a < 4; ++a) { acc[a][9] = f32x4{0.f, 0.f, 0.f, 0.f}; asm volatile("" : "+v"(acc[a][9])); }
    if (!LD) {
      xrow(0);
      WAIT_V(1);
    }
    asm volatile("" : "+v"(aoff[0]), "+v"(aoff[1]), "+v"(boff[0]), "+v"(boff[1]));
    bf16x8 kp[2];
    auto body = [&](bool upd, bool keep = false, bool first = false) __attribute__((always_inline)) {
      lds_cptr sb = shm3;
      auto rdb = [&](int g, int i) {
        lds_cptr p = sb + boff[g & 1] + (g >> 1) * 8192 + (i >> 1) * 4096;
        const int s = ((g + 1) & 1) * 2 + (i >> 1);
        if (i & 1) bh[s] = tr_read(p + 512); else bl[s] = tr_read(p);
      };
      auto rda = [&](int ks, int mf) { (ks ? af1 : af0)[mf] = *(const __attribute__((address_space(3))) bf16x8*)(sb + aoff[ks] + mf * 2048); };
      SB0();
      __builtin_amdgcn_s_waitcnt(0xC07F);
#pragma unroll
      for (int gg = 0; gg < 10; ++gg) {
        const int g = (gg + 9) % 10;
#pragma unroll
        for (int t = 0; t < 8; ++t) {
          const bool two = t >= 4 && !(upd && g >= 5 && g <= 8);
          SB0();
          if ((!two || (t & 1)) && !(first && g == 9)) mm(g, two ? t - 1 : t, two, first);
          SB0();
          if (first && !LD && (g == 3 || g == 7) && t == 0) BAR();
          if (g == 9) {
            if (t == 0 || t == 2) { rdb(0, t); rdb(0, t + 1); }
            if (t & 1) { rda((t >> 1) & 1, t >> 1); rda(((t >> 1) & 1) ^ 1, t >> 1); }
          } else if (t < 4) rdb(g + 1, t);
          else if (keep && g == 7) {
            if (t == 5) { WAIT_V(0); kp[0] = tr_frag(shm3 + tro); }
            if (t == 7) kp[1] = tr_frag(shm3 + tro + 4096);
          } else if (upd) {
            if (g == 5 && t == 4) { sq0 += 2 * q32; PIN_S(sq0); }
            if (g == 5 && t == 5) { sq1 += 2 * q32; PIN_S(sq1); }
            if (g == 5 && t == 6) { sg0 += 64 * F_ * 2; PIN_S(sg0); }
            if (g == 6 && t == 4) { sg1 += 64 * F_ * 2; PIN_S(sg1); }
            if (g == 6 && t == 5) { sx += 128; PIN_S(sx); }
            if (g == 6 && t == 6) { dst -= dbuf; PIN_S(dst); }
            if (g == 7 && t == 4) asm volatile("v_add_co_u32 %0, %2, %4, %0\n\tv_add_co_u32 %1, %3, %4, %1" : "+v"(mp[0][0]), "+v"(mp[1][0]), "=&s"(cy[0]), "=&s"(cy[1]) : "s"(2 * q32));
            if (g == 7 && t == 6) asm volatile("v_addc_co_u32 %0, vcc, 0, %0, %2\n\tv_addc_co_u32 %1, vcc, 0, %1, %3" : "+v"(mp[0][1]), "+v"(mp[1][1]) : "s"(cy[0]), "s"(cy[1]) : "vcc");
            if (g == 8 && t == 4) { aoff[0] += dbuf; aoff[1] += dbuf; asm volatile("" : "+v"(aoff[0]), "+v"(aoff[1])); }
            if (g == 8 && t == 5) { boff[0] += dbuf; boff[1] += dbuf; asm volatile("" : "+v"(boff[0]), "+v"(boff[1])); }
            if (g == 8 && t == 6) { dbuf = -dbuf; PIN_S(dbuf); }
          }
        }
      }
      SB0();
    };
    TS1(0);
    if (LD) {
      stage(0, PA);
      WAIT_V(6 + PA);
      TS1(5);
      BARL();
      stage(PA, PB);
      WAIT_V(2 + PB);
      BAR();
      stage(PB, PC);
      WAIT_V(PC);
      BAR();
      stage(PC, 18);
      body(true, false, true);
      WAIT_V(0);
      BARL();
#pragma clang loop unroll(disable)
      for (int kt = 1; kt < NT - 1; ++kt) {
        if (__builtin_expect(kt == 5 + wid, 0)) prep(true);
        stage(0, 18);
        body(true);
        WAIT_V(0);
        BARL();
        SB0();
      }
      start2();
      body(false);
    } else {
      char* wsx;
      auto tile = [&](int kt, bool keep) __attribute__((always_inline)) {
        SB0();
        BARL();
        body(false, keep);
        if (keep) asm volatile("" : "+v"(kp[0]), "+v"(kp[1])); else WAIT_V(0);
      };
      auto next = [&]() __attribute__((always_inline)) {
        aoff[0] += dbuf; aoff[1] += dbuf; boff[0] += dbuf; boff[1] += dbuf;
        dbuf = -dbuf;
        asm volatile("" : "+v"(aoff[0]), "+v"(aoff[1]), "+v"(boff[0]), "+v"(boff[1]), "+v"(vol), "+v"(tro) :: "memory");
        wsx = ws;
        PIN_S(wsx);
      };
      auto share = [&]() {
        glds16_v(pa, shm_base + DMP + wid * 1024);
        if (wid == 0) glds16_s(gb + dgu, vob, shm_base + DMP + 4096);
      };
      auto kept = [&]() {
        ca = __builtin_amdgcn_mfma_f32_16x16x32_bf16(kp[0], sel, ca, 0, 0, 0);
        if (wid == 0) cb = __builtin_amdgcn_mfma_f32_16x16x32_bf16(kp[1], sel, cb, 0, 0, 0);
      };
      auto cpub = [&](u32x4 a, u32x4 b) {
        if ((vol & 0xc0) == 0) {
          const char* const cs = wsx + 101 * MB_ + ntile * 10240 + mtile * 256;
          const unsigned co = (vol & 0x30) * 4 + (vol >> 8) * 16;
          gst16(cs + wid * 2048, co, a);
          if (wid == 0) gst16(cs + 8192, co, b);
        }
      };
      auto side0 = [&](int kt) __attribute__((always_inline)) {
        if (kt < 2) {
          glds16_v(pa - 2 * pstep, shm_base + DMP + 7168);
          if (wid == 0) glds16_s(gb + dgu, vob - 128 * F_ * 2, shm_base + DMP + 7168);
        }
        share();
        u32x4 xw;
        asm volatile("v_mov_b32 %0, %4\n\tv_mov_b32 %1, %5\n\tv_mov_b32 %2, %6\n\tv_mov_b32 %3, %7" : "=&v"(xw[0]), "=&v"(xw[1]), "=&v"(xw[2]), "=&v"(xw[3])
                     : "v"(cd[0]), "v"(cd[1]), "v"(cd[2]), "v"(cd[3]));
        xrow(kt + 1);
        next();
        if (kt) kept();
        pa += pstep; vob += 64 * F_ * 2;
        accum(shm3 + tro + ((kt & 1) * G1_STAGE + 32768 + mtile * 1024 + wid * 7168 - DMP), 4 * 8192);
#pragma unroll
        for (int i = 0; i < 4; ++i) {
          float lo, hi;
          asm volatile("v_lshlrev_b32 %2, 16, %4\n\tv_and_b32 %3, 0xffff0000, %4\n\tv_add_f32 %0, %0, %2\n\tv_fmac_f32 %1, %2, %2\n\t"
                       "v_add_f32 %0, %0, %3\n\tv_fmac_f32 %1, %3, %3" : "+v"(sm), "+v"(sq), "=&v"(lo), "=&v"(hi) : "v"(xw[i]));
        }
      };
      BARL();
      body(false, false, true);
      WAIT_V(0);
      side0(0);
#pragma clang loop unroll(disable)
      for (int kt = 1; kt < 4; ++kt) {
        tile(kt, true);
        side0(kt);
      }
#pragma clang loop unroll(disable)
      for (int kt = 4; kt < 12; ++kt) {
        tile(kt, true);
        share();
        glds16_s(pf, vol, shm_base + DMP + 7168);
        next();
        kept();
        pa += pstep; vob += 64 * F_ * 2;
        pf = kt == 10 ? (const char*)wo + (bid * 4 + wid) * 2048 + 1024 : pf + 1024;
        if (kt < 8) {
          const unsigned q1 = bid * 4 + wid + (kt - 5) * 1024, q0 = q1 - 1024;
          if (q0 < 1152) gst16(wsx + 98 * MB_ + (q0 & 1) * 589824 + (q0 >> 1) * 1024, vol, *(const u32x4*)(shm + AUX + wid * 1024 + vol));
          if (q1 < 1152) {
            const char* src = (const char*)((q1 >> 1) < 448 ? wd + (size_t)(q1 >> 1) * 8 * D_ : wo + (size_t)((q1 >> 1) - 448) * 8 * D_);
            src += ((3 - ((unsigned)(uintptr_t)src >> 7)) & 7) * 128 + (q1 & 1) * 1024;
            glds16_s(src, (vol >> 7) * 2048 + (vol & 0x70), shm_base + AUX + wid * 1024);
          }
          u32x4 bk;
          asm volatile("v_mov_b32 %0, %1" : "=v"(bk[0]) : "s"(BLANK));
          bk[1] = bk[0]; bk[2] = bk[0]; bk[3] = bk[0];
          gst16(wsx + 64 * MB_ + bid * 16384 + wid * 4096 + (kt - 4) * 1024, vol, bk);
          if (tya && (kt == 4 || kt == 7)) gst16(wsx + 4 * MB_ + (mtile * 32 + ntile) * 4096 + wid * 2048 + (kt & 1) * 1024, vol, bk);
          if (kt == 4) cpub(bk, bk);
        } else {
          if (kt == 8) {
            const float ml = sm * (1.0f / 32), mean = half_sum(sm) * (1.0f / D_), dm = ml - mean;
            const float q = __builtin_fmaf(dm * 32.0f, dm, __builtin_fmaf(-ml, sm, sq));
            const unsigned ps = pack_stats(mean, fmaxf(half_sum(q) * (1.0f / D_), 0.f));
            const u64 now = __builtin_amdgcn_s_memrealtime();
            if ((vol & 0x1f0) == 0) gst16(wsx + 97 * MB_ + srow * 16, vol >> 5, u32x4{ps, (unsigned)now, ps, (unsigned)(now >> 32)});
          }
          if (kt == 9) {
            if (wid == 0) {
              u64 now;
              asm volatile("s_memrealtime %0\n\ts_waitcnt lgkmcnt(0)" : "=s"(now) :: "memory");
              if (vol == 0) asm volatile("global_store_dwordx2 %0, %1, %2 sc1" :: "v"(vol), "v"(now), "s"(wsx + 100 * MB_ + 131072 + bid * 8) : "memory");
            }
            if (wid == 2) glds16_s1(wsx + 102 * MB_ + (bid & ~1) * 512, vol, shm_base + DMP + 7168);
          }
        }
      }
      asm volatile("" :: "v"(cd));
      if (wid >= 2) glds16_s1(wsx + 100 * MB_ + 131072 + (wid - 2) * 1024, vol, shm_base + DMP + 3072 + wid * 1024);
      tile(12, true);
      next();
      kept();
      const int lane = vol >> 4;
      glds16_s1(wsx + 97 * MB_ + (m0 + wid * 64) * 16, ((lane & 32) + 2 * (lane & 15) + ((lane >> 4) & 1)) * 16, shm_base + AUX + wid * 1024);
      asm volatile("s_nop 7\n\ts_nop 3" : "+v"(ca), "+v"(cb));
      cpub(__builtin_bit_cast(u32x4, ca), __builtin_bit_cast(u32x4, cb));
      if (wid >= 2) {
        u32x4 c4 = *(const u32x4*)(shm + DMP + 3072 + wid * 1024 + vol);
        bool ok = __all(fresh(((u64)c4[1] << 32) | c4[0], t_own) & fresh(((u64)c4[3] << 32) | c4[2], t_own));
        while (__builtin_expect(!ok, 0) && __builtin_amdgcn_s_memrealtime() - t_own < TIMEOUT_ / 10) {
          const __amdgpu_buffer_rsrc_t rs = __builtin_amdgcn_make_buffer_rsrc((void*)(wsx + 100 * MB_ + 131072), 0, 2048, 0x00020000);
          asm volatile("" ::: "memory");
          c4 = __builtin_bit_cast(u32x4, __builtin_amdgcn_raw_buffer_load_b128(rs, (wid - 2) * 1024 + vol, 0, 16));
          ok = __all(fresh(((u64)c4[1] << 32) | c4[0], t_own) & fresh(((u64)c4[3] << 32) | c4[2], t_own));
        }
        *(unsigned*)(shm + CNT - 4 + wid * 4) = ok ? 1u : 0u;
      }
      tile(13, false);
      next();
      u32x4 cw[4];
      auto cgather = [&]() {
        const __amdgpu_buffer_rsrc_t rc = __builtin_amdgcn_make_buffer_rsrc((void*)(wsx + 101 * MB_ + ntile * 10240), 0, 5 * 2048, 0x00020000);
        const unsigned vcs = (vol < 512 ? wid * 2048 : 8192) + (vol & 256) * 4 + (vol & 0xf0);
#pragma unroll
        for (int i = 0; i < 4; ++i) cw[i] = __builtin_bit_cast(u32x4, __builtin_amdgcn_raw_buffer_load_b128(rc, vcs + i * 256, 0, 16));
      };
      {
        const int ln = wid * 64 + (vol >> 4);
        u32x4 gr = *(const u32x4*)(shm + AUX + ln * 16);
        if (__builtin_expect(!__all((gr[0] == gr[2]) & fresh(((u64)gr[3] << 32) | gr[1], t_own)), 0)) {
          const __amdgpu_buffer_rsrc_t rs = __builtin_amdgcn_make_buffer_rsrc((void*)(wsx + 97 * MB_ + m0 * 16), 0, 4096, 0x00020000);
          const int prow = (ln & 0xe0) + 2 * (ln & 15) + ((ln >> 4) & 1);
          for (;;) {
            gr = __builtin_bit_cast(u32x4, __builtin_amdgcn_raw_buffer_load_b128(rs, prow * 16, 0, 16));
            if (__all((gr[0] == gr[2]) & fresh(((u64)gr[3] << 32) | gr[1], t_own))) break;
            if (__builtin_amdgcn_s_memrealtime() - t_own > TIMEOUT_) { gr[0] = row_stats_serial(x + (size_t)(m0 + prow) * D_); break; }
            __builtin_amdgcn_s_sleep(2);
          }
        }
        cgather();
        const float nm = -lo_f(gr[0]), dn = hi_f(gr[0]), r = __builtin_amdgcn_rcpf(dn);
        auto mul = [](float a, float b) { float c; asm("v_mul_f32 %0, %1, %2" : "=v"(c) : "v"(a), "v"(b)); return c; };
        const f32x4 fc = {nm, -2.3022081986f * r, mul(mul(mul(-0.10294324f, r), r), r), mul(dn, dn)};
        *(f32x4*)(shm + AUX + ln * 16) = fc;
        float* const tw = (float*)(shm + 2 * G1_STAGE + 4096 + wid * 768) + ln;
#pragma unroll
        for (int i = 0; i < 4; ++i) tw[i * 64] = fc[i];
        ((float*)(shm + AUX + 4096))[384 + ln] = r;
        cok = __builtin_amdgcn_readfirstlane(cnt[1] & cnt[2]) != 0u;
        tile2(0);
        wy = wsetup(wd, 0, vol >> 4, cok);
      }
      tile(14, false);
      next();
      {
        while (__builtin_expect(blank(cw) && cok, 0)) {
          __builtin_amdgcn_s_sleep(2);
          asm volatile("" ::: "memory");
          cgather();
        }
        f32x4 t;
#pragma unroll
        for (int i = 0; i < 4; ++i) {
          float a, b;
          asm("v_add_f32 %0, %2, %3\n\tv_add_f32 %1, %4, %5\n\tv_add_f32 %0, %0, %1" : "=&v"(a), "=&v"(b) : "v"(cw[0][i]), "v"(cw[1][i]), "v"(cw[2][i]), "v"(cw[3][i]));
          const auto r2 = __builtin_amdgcn_permlane16_swap(__float_as_uint(a), __float_as_uint(a), false, false);
          asm("v_add_f32 %0, %1, %2" : "=v"(t[i]) : "v"(r2[0]), "v"(r2[1]));
        }
        const int l = vol >> 4;
        if (__builtin_expect(!cok, 0)) {
          const int gq = (l >> 2) & 3;
          const char* pc = l < 32 ? pbase + ((psh & gq) ? dgu : 0) + (gq >> psh) * 32 + (l & 3) * 8 : gb + dgu + (l & 15) * 8;
          t = f32x4{0.f, 0.f, 0.f, 0.f};
#pragma unroll 1
          for (int k = 0; k < D_; ++k) { const u32x2 w = *(const __attribute__((address_space(1))) u32x2*)(pc + (size_t)k * (l < 32 ? pstr : F_ * 2)); t += f32x4{lo_f(w[0]), hi_f(w[0]), lo_f(w[1]), hi_f(w[1])}; }
        }
        if (l < 32 || wid == 0)
          *(f32x4*)((float*)(shm + AUX + 4096) + ((l >> 3) & 1) * 192 + (l < 32 ? wid : 4) * 32 + (l & 7) * 4) = t;
        __builtin_amdgcn_s_setprio(2);
      }
      SB0();
      BARL();
      body(false);
    }
#pragma unroll
    for (int t = 0; t < 8; t += 2) mm(9, t, true);
    asm volatile("s_nop 7\n\ts_nop 3");
    SB0();
    TS1(3);
    if (!LD) {
#pragma unroll
      for (int i = 0; i < 3; ++i) {
        const unsigned r = ((role * 4 + (i & 1) * 2) * 14 + (i < 2 ? 10 : 0) + (wm & 2)) * 32;
        sw[0] = wy.wb[0] + (r << wy.sh[0]); sw[1] = wy.wb[1] + (r << wy.sh[1]);
        asm volatile(G2_W :: [l] "s"((i + 1) * 16384), G2_WOP(wy) : "memory", "scc", "m0");
      }
    } else {
      __builtin_amdgcn_s_setprio(3);
      WAIT_V(0);
    }
  };
  if (wid >= 4) kloop(std::integral_constant<int, 1>{});
  else kloop(std::integral_constant<int, 0>{});
  __builtin_amdgcn_sched_barrier(0);
  int tz = threadIdx.x;
  asm volatile("" : "+v"(tz));
  __builtin_amdgcn_sched_barrier(0);
  const int li = tz & 15, lq = (tz >> 4) & 3, ln = tz & 63;
  const __amdgpu_buffer_rsrc_t ra = __builtin_amdgcn_make_buffer_rsrc((void*)abuf, 0, 16 * 72 * 16384, 0x00020000);
  float* const cfin = (float*)(shm + AUX + 4096);
  float* const svr = cfin + 384;
  float* const tb = (float*)(shm + 2 * G1_STAGE + 4096 + wm * 1024);
  auto signal = [&](unsigned v) {
    unsigned a, b;
    asm volatile("v_mov_b32 %0, %2\n\tv_mov_b32 %1, %3\n\ts_mov_b64 exec, 1\n\tds_add_u32 %0, %1\n\ts_mov_b64 exec, -1"
                 : "=&v"(a), "=&v"(b) : "s"(shm_base + CNT), "s"(v) : "memory");
  };
  auto await = [&](unsigned mask, unsigned val) {
    unsigned a, t;
    asm volatile("v_mov_b32 %0, %2\n1:\n\tds_read_b32 %1, %0\n\ts_waitcnt lgkmcnt(0)\n\tv_readfirstlane_b32 vcc_lo, %1\n\ts_and_b32 vcc_lo, vcc_lo, %3\n\t"
                 "s_cmp_eq_u32 vcc_lo, %4\n\ts_cbranch_scc1 2f\n\ts_sleep 1\n\ts_branch 1b\n2:" : "=&v"(a), "=&v"(t) : "s"(shm_base + CNT), "s"(mask), "s"(val) : "memory", "scc", "vcc");
  };
  auto client = [&](int l) { const int m128 = mtile * 2 + ((l >> 4) & 1); return (((l & 7) * 4 + (m128 & 3)) * 8) + ((m128 >> 2) ^ ((l & 8) ? 5 : 0)); };
  auto notify = [&](int l0, int nl) __attribute__((always_inline)) {
    u64 now;
    asm volatile("s_memrealtime %0\n\ts_waitcnt lgkmcnt(0)" : "=s"(now) :: "memory");
    int l = threadIdx.x;
    asm volatile("" : "+v"(l));
    l &= 63;
    const __amdgpu_buffer_rsrc_t rd1 = __builtin_amdgcn_make_buffer_rsrc((void*)(done1 + ntile), 0, 256 * 512, 0x00020000);
    if (l < nl) __builtin_amdgcn_raw_buffer_store_b64(u32x2{(unsigned)now, (unsigned)(now >> 32)}, rd1, client(l0 + l) * 512, 0, 16);
  };
  const int xo = ((xcd & 1) << 1) | ((xcd >> 1) & 1), te = (xcd >> 2) * 28 + 6 * (xo >> 1) + (xo & 1), tl = (xcd >> 2) * 28 + 12 + 4 * xo;
  const u64 te0 = __builtin_amdgcn_s_memrealtime();
  const __amdgpu_buffer_rsrc_t re = __builtin_amdgcn_make_buffer_rsrc((void*)(done1 + client((wm >> 1) * 16 + 8 - (xcd & 4) * 2) * 64 + 32 + (ntile & 15) * 2 + (wm & 1)),
                                                                      0, 7 * 16384 + 8, 0x00020000);
  auto early = [&]() {
    __builtin_amdgcn_raw_buffer_store_b64(u32x2{(unsigned)te0, (unsigned)(te0 >> 32)}, re, ln * 16384, 0, 16);
  };
  signal(1u << (8 * wc));
  if (wc) {
    await(0xffffu, 0x404u);
#pragma unroll
    for (int f = 0; f < 4; ++f) *(u32x4*)(shm + G2_X + wm * 4096 + f * 1024 + ln * 16) = xq[f];
    SB0();
    __builtin_amdgcn_s_setprio(1);
  }
  TS1(4);
  f32x4 rf[4];
#pragma unroll
  for (int mf = 0; mf < 4; ++mf) rf[mf] = *(const f32x4*)(shm + AUX + (wm * 64 + mf * 16 + li) * 16);
  const float* const cl = cfin + wc * 192 + lq * 4;
  const int sbase = ((mtile * 2 + (wm >> 1)) * 72 * 8 + (wm & 1) * 4) * 2048;
  auto putf = [&](int kg, int mf, u32x4 v) {
    __builtin_amdgcn_raw_buffer_store_b128(v, ra, ln * 16 + sbase + ((kg >> 1) * 8 + mf) * 2048 + (kg & 1) * 1024, 0, 16);
  };
  auto gelp = [&](f32x2 g, f32x2 u, f32x2 cg, f32x2 cu, f32x2 m2, f32x2 k12, f32x2 k22, f32x2 d2) {
    const f32x2 tg = __builtin_elementwise_fma(m2, cg, g), tu = __builtin_elementwise_fma(m2, cu, u);
    const f32x2 arg = tg * __builtin_elementwise_fma(tg * tg, k22, k12);
    const f32x2 dn = __builtin_elementwise_fma(f32x2{__builtin_amdgcn_exp2f(arg[0]), __builtin_amdgcn_exp2f(arg[1])}, d2, d2);
    const f32x2 r = (tg * tu) * f32x2{__builtin_amdgcn_rcpf(dn[0]), __builtin_amdgcn_rcpf(dn[1])};
    return pack2(r[0], r[1]);
  };
  auto gel2 = [&](int p, int kg) {
    f32x4 cg[2], cu[2];
#pragma unroll
    for (int j = 0; j < 2; ++j) { cg[j] = *(const f32x4*)(cl + p * 32 + j * 16); cu[j] = *(const f32x4*)(cl + p * 32 + 32 + j * 16); }
#pragma unroll
    for (int mf = 0; mf < 4; ++mf) {
      const f32x2 m2 = {rf[mf][0], rf[mf][0]}, k12 = {rf[mf][1], rf[mf][1]}, k22 = {rf[mf][2], rf[mf][2]}, d2 = {rf[mf][3], rf[mf][3]};
      u32x4 o;
#pragma unroll
      for (int i = 0; i < 4; ++i) {
        const int j = i >> 1, e = (i & 1) * 2;
        const f32x4 g = acc[mf][p * 2 + j], u = acc[mf][p * 2 + 2 + j];
        o[i] = gelp(f32x2{g[e], g[e + 1]}, f32x2{u[e], u[e + 1]}, f32x2{cg[j][e], cg[j][e + 1]}, f32x2{cu[j][e], cu[j][e + 1]}, m2, k12, k22, d2);
      }
      putf(kg, mf, o);
    }
  };
  int tsel = ntile & 1;
  asm volatile("" : "+s"(tsel));
  if (tsel != 0) {
    const int tq = (wc ? tl : te + 2 + (xo & 1)) + he;
    gel2(0, 2 * tq);
    gel2(3, 2 * tq + 1);
    const float cg = cfin[wc * 192 + 64 + li], cu = cfin[wc * 192 + 80 + li];
#pragma unroll
    for (int mf = 0; mf < 4; ++mf) {
      f32x4 t[4];
#pragma unroll
      for (int i = 0; i < 4; ++i) t[i] = *(const f32x4*)(tb + i * 64 + mf * 16 + lq * 4);
      const f32x4 g = acc[mf][4], u = acc[mf][5];
      u32x2 o;
#pragma unroll
      for (int e = 0; e < 4; e += 2)
        o[e >> 1] = gelp(f32x2{g[e], g[e + 1]}, f32x2{u[e], u[e + 1]}, f32x2{cg, cg}, f32x2{cu, cu}, f32x2{t[0][e], t[0][e + 1]},
                         f32x2{t[1][e], t[1][e + 1]}, f32x2{t[2][e], t[2][e + 1]}, f32x2{t[3][e], t[3][e + 1]});
      *(u32x2*)(shm + wm * 4096 + mf * 1024 + wc * 512 + li * 32 + lq * 8) = o;
      if (mf == 2 && wc == 0) {
        TS1(1);
        WAIT_V(0);
        TS1(6);
        early();
      }
    }
    if (wc == 0) {
      signal(0x10000u << wid);
    } else {
      await(0x1000u << wid, 0x1000u << wid);
#pragma unroll
      for (int mf = 0; mf < 4; ++mf) putf(2 * (tl + 2) + he, mf, __builtin_bit_cast(u32x4, tr_frag(shm3 + wm * 4096 + mf * 1024 + ln * 8)));
    }
  } else {
    float rd[4];
#pragma unroll
    for (int mf = 0; mf < 4; ++mf) rd[mf] = svr[wm * 64 + mf * 16 + li];
    auto cvq = [&](int p, int mf, const f32x4& c0, const f32x4& c1) {
      u32x4 o;
#pragma unroll
      for (int j = 0; j < 2; ++j) {
        const f32x4 a = acc[mf][p * 2 + j], c4 = j ? c1 : c0;
        const f32x2 m2 = {rf[mf][0], rf[mf][0]}, r2 = {rd[mf], rd[mf]};
        f32x2 t0 = __builtin_elementwise_fma(m2, f32x2{c4[0], c4[1]}, f32x2{a[0], a[1]});
        f32x2 t1 = __builtin_elementwise_fma(m2, f32x2{c4[2], c4[3]}, f32x2{a[2], a[3]});
        if (p) { t0 *= r2; t1 *= r2; }
        o[2 * j] = pack2(t0[0], t0[1]); o[2 * j + 1] = pack2(t1[0], t1[1]);
      }
      return o;
    };
    const float cv0 = cfin[wc * 192 + 64 + li], cv1 = cfin[wc * 192 + 80 + li];
    auto cvv = [&](int mf) {
      const f32x4 n4 = *(const f32x4*)(tb + mf * 16 + lq * 4), r4 = *(const f32x4*)(svr + wm * 64 + mf * 16 + lq * 4);
      u32x4 o;
#pragma unroll
      for (int j = 0; j < 2; ++j) {
        const f32x4 a = acc[mf][4 + j];
        const f32x2 c2 = {j ? cv1 : cv0, j ? cv1 : cv0};
        const f32x2 t0 = __builtin_elementwise_fma(f32x2{n4[0], n4[1]}, c2, f32x2{a[0], a[1]}) * f32x2{r4[0], r4[1]};
        const f32x2 t1 = __builtin_elementwise_fma(f32x2{n4[2], n4[3]}, c2, f32x2{a[2], a[3]}) * f32x2{r4[2], r4[3]};
        o[2 * j] = pack2(t0[0], t0[1]); o[2 * j + 1] = pack2(t1[0], t1[1]);
      }
      return o;
    };
    u32x4 kx[4], vx[4], qx[4], hx[4];
    const bf16x4 ones = {0x3f80, 0x3f80, 0x3f80, 0x3f80};
    const f32x4 ck0 = *(const f32x4*)(cl + 32), ck1 = *(const f32x4*)(cl + 48);
    kx[2] = cvq(1, 2, ck0, ck1); kx[3] = cvq(1, 3, ck0, ck1);
    vx[2] = cvv(2); vx[3] = cvv(3);
    auto exo = [](int e) { return e * 4096 + (e >> 2) * (G2_A - 16384); };
    const bool needh = wm == 0 && mtile > 0;
    const __amdgpu_buffer_rsrc_t rh = __builtin_amdgcn_make_buffer_rsrc((void*)((char*)halo + ((needh ? mtile - 1 : mtile) * 32 + ntile + wc) * 4096), 0,
                                                                        needh || wm == 3 ? 4096 : 0, 0x00020000);
    if (wm < 3) {
      char* const ex = shm + exo(wc * 3 + wm) + ln * 16;
      *(u32x4*)ex = kx[2]; *(u32x4*)(ex + 1024) = kx[3]; *(u32x4*)(ex + 2048) = vx[2]; *(u32x4*)(ex + 3072) = vx[3];
      signal(0x10000u << wid);
    } else {
#pragma unroll
      for (int r = 0; r < 4; ++r) __builtin_amdgcn_raw_buffer_store_b128(r < 2 ? kx[2 + r] : vx[r], rh, ln * 16 + r * 1024, 0, 16);
    }
    auto hload = [&]() {
#pragma unroll
      for (int r = 0; r < 4; ++r) hx[r] = __builtin_bit_cast(u32x4, __builtin_amdgcn_raw_buffer_load_b128(rh, ln * 16 + r * 1024, 0, 16));
    };
    SB0();
    gel2(3, 2 * (wc ? tl + 3 : te) + he);
    SB0();
    if (wm == 0) hload();
    SB0();
    kx[0] = cvq(1, 0, ck0, ck1); kx[1] = cvq(1, 1, ck0, ck1);
    vx[0] = cvv(0); vx[1] = cvv(1);
    {
      const f32x4 cq0 = *(const f32x4*)(cl), cq1 = *(const f32x4*)(cl + 16);
#pragma unroll
      for (int mf = 0; mf < 4; ++mf) qx[mf] = cvq(0, mf, cq0, cq1);
    }
    f32x4 mk0, mk1;
    u32x2 pm;
#pragma unroll
    for (int j = 0; j < 4; ++j) {
      const int d = 4 * lq + j - li;
      mk0[j] = d >= 9 ? 0.f : -1.0e30f;
      mk1[j] = (unsigned)(d + 7) <= 7u ? 0.f : -1.0e30f;
    }
#pragma unroll
    for (int j = 0; j < 2; ++j) pm[j] = (4 * lq + 2 * j > li ? 0xffffu : 0u) | (4 * lq + 2 * j + 1 > li ? 0xffff0000u : 0u);
    auto max3 = [](float a, float b, float c) { float r; asm("v_max3_f32 %0, %1, %2, %3" : "=v"(r) : "v"(a), "v"(b), "v"(c)); return r; };
    auto max2 = [](float a, float b) { float r; asm("v_max_f32 %0, %1, %2" : "=v"(r) : "v"(a), "v"(b)); return r; };
    asm volatile("" : "+v"(kx[0]), "+v"(kx[1]), "+v"(vx[0]), "+v"(vx[1]), "+v"(qx[0]), "+v"(qx[1]), "+v"(qx[2]), "+v"(qx[3]), "+v"(mk0), "+v"(mk1), "+v"(pm));
    TS1(1);
    if (wc == 0) {
      if (wm == 0) WAIT_V(4); else WAIT_V(0);
      early();
    }
    if (wm > 0) {
      await(0x8000u << wid, 0x8000u << wid);
#pragma unroll
      for (int i = 0; i < 4; ++i) hx[i] = *(const u32x4*)(shm + exo(wc * 3 + wm - 1) + i * 1024 + ln * 16);
    } else if (__builtin_expect(cok || !needh, 1)) {
      while (__builtin_expect(blank(hx), 0)) {
        __builtin_amdgcn_s_sleep(1);
        asm volatile("" ::: "memory");
        hload();
      }
    } else {
      await(0xffffu, 0x404u);
      char* const fb = shm + exo(6 + wc) + ln * 16;
#pragma unroll 1
      for (int i = 0; i < 32; ++i) {
        const int rec = i >> 3, e = i & 7;
        const int row = m0 - 32 + 2 * (rec < 2 ? li : lq * 4 + (e & 3)) + (rec & 1);
        const int col = (rec < 2 ? D_ + lq * 4 + (e & 3) : 2 * D_ + li) + (ntile + wc) * 32 + (e >> 2) * 16;
        const u16* xr = x + (size_t)row * D_;
        const unsigned stt = row_stats_serial(xr);
        const float mb = lo_f(stt);
        float a = 0.f;
#pragma unroll 1
        for (int k = 0; k < D_; ++k) a += (bf2f(xr[k]) - mb) * bf2f(wqkv[(size_t)k * 3072 + col]);
        *(u16*)(fb + rec * 1024 + e * 2) = f2bf(a / hi_f(stt));
      }
#pragma unroll
      for (int i = 0; i < 4; ++i) hx[i] = *(const u32x4*)(fb + i * 1024);
    }
    TS1(6);
    {
      const f32x4 z = {0.f, 0.f, 0.f, 0.f};
      const float mz = mtile == 0 && wm == 0 ? -1.0e30f : 0.f;
      const f32x4 mkf = mk0 + f32x4{mz, mz, mz, mz};
      f32x4 s0[4], s1[4];
#pragma unroll
      for (int i = 0; i < 4; ++i) {
        const int n = i ^ 2;
        const bf16x8 qf = __builtin_bit_cast(bf16x8, qx[n]);
        s1[n] = __builtin_amdgcn_mfma_f32_16x16x32_bf16(__builtin_bit_cast(bf16x8, kx[n]), qf, mk1, 0, 0, 0);
        s0[n] = __builtin_amdgcn_mfma_f32_16x16x32_bf16(__builtin_bit_cast(bf16x8, n < 2 ? hx[n] : kx[n - 2]), qf, n < 2 ? mkf : mk0, 0, 0, 0);
      }
      SB0();
      asm volatile("s_nop 7\n\ts_nop 3" : "+v"(s0[0]), "+v"(s1[0]), "+v"(s0[1]), "+v"(s1[1]), "+v"(s0[2]), "+v"(s1[2]), "+v"(s0[3]), "+v"(s1[3]));
      float mx[4];
#pragma unroll
      for (int n = 0; n < 4; ++n)
#pragma unroll
        for (int j = 0; j < 4; ++j) s0[n][j] = max2(s0[n][j], s1[n][j]);
#pragma unroll
      for (int n = 0; n < 4; ++n) mx[n] = max3(s0[n][0], s0[n][1], s0[n][2]);
#pragma unroll
      for (int n = 0; n < 4; ++n) mx[n] = max2(mx[n], s0[n][3]);
#pragma unroll
      for (int n = 0; n < 4; ++n) { auto r = __builtin_amdgcn_permlane16_swap(__float_as_uint(mx[n]), __float_as_uint(mx[n]), false, false);
        mx[n] = max2(__uint_as_float(r[0]), __uint_as_float(r[1])); }
#pragma unroll
      for (int n = 0; n < 4; ++n) { auto r = __builtin_amdgcn_permlane32_swap(__float_as_uint(mx[n]), __float_as_uint(mx[n]), false, false);
        mx[n] = max2(__uint_as_float(r[0]), __uint_as_float(r[1])); }
      SB0();
      constexpr float cs2 = 0.17677669529663687f * 1.4426950408889634f;
      f32x4 os[4], o0[4], o1[4];
#pragma unroll
      for (int n = 0; n < 4; ++n) {
        const float c = cs2 * rd[n], nm = -mx[n] * c;
        const f32x4 ar = __builtin_elementwise_fma(s0[n], f32x4{c, c, c, c}, f32x4{nm, nm, nm, nm});
        const u32x2 pk = {pack2(__builtin_amdgcn_exp2f(ar[0]), __builtin_amdgcn_exp2f(ar[1])), pack2(__builtin_amdgcn_exp2f(ar[2]), __builtin_amdgcn_exp2f(ar[3]))};
        const bf16x4 pa = __builtin_bit_cast(bf16x4, pk), p0 = __builtin_bit_cast(bf16x4, pk & pm), p1 = __builtin_bit_cast(bf16x4, pk & ~pm);
        const u32x4 vp = n < 2 ? hx[2 + n] : vx[n - 2];
        auto half = [](const u32x4& v, int h) { return __builtin_bit_cast(bf16x4, u32x2{v[2 * h], v[2 * h + 1]}); };
        os[n] = __builtin_amdgcn_mfma_f32_16x16x16bf16_1k(ones, pa, z, 0, 0, 0);
        o0[n] = __builtin_amdgcn_mfma_f32_16x16x16bf16_1k(half(vx[n], 0), p1, __builtin_amdgcn_mfma_f32_16x16x16bf16_1k(half(vp, 0), p0, z, 0, 0, 0), 0, 0, 0);
        o1[n] = __builtin_amdgcn_mfma_f32_16x16x16bf16_1k(half(vx[n], 1), p1, __builtin_amdgcn_mfma_f32_16x16x16bf16_1k(half(vp, 1), p0, z, 0, 0, 0), 0, 0, 0);
      }
#pragma unroll
      for (int n = 0; n < 4; ++n) {
        const float inv = __builtin_amdgcn_rcpf(os[n][0]);
        const f32x4 n0 = o0[n] * inv, n1 = o1[n] * inv;
        putf(112 + ntile + wc, n, u32x4{pack2(n0[0], n0[1]), pack2(n0[2], n0[3]), pack2(n1[0], n1[1]), pack2(n1[2], n1[3])});
      }
    }
  }
  TS1(2);
  {
  __builtin_amdgcn_sched_barrier(0);
  int tid = threadIdx.x;
  asm volatile("" : "+v"(tid));
  __builtin_amdgcn_sched_barrier(0);
  const int lane = tid & 63;
  const int wm = wid >> 1, wn = wid & 1;
  const int m0 = mt2 * 128, n0 = nt2 * 128;
  const int ny = (xcd & 1) ? NY - DLT : NY + DLT;
  const unsigned va = lane * 16;
  auto stage_y = [&]() {
    asm volatile("s_add_u32 m0, %[l], %[oa]\n\ts_nop 0\n\tglobal_load_lds_dwordx4 %[va], %[a] sc1\n\tglobal_load_lds_dwordx4 %[va], %[a] offset:1024 sc1\n\t"
                 "global_load_lds_dwordx4 %[va], %[a] offset:2048 sc1\n\tglobal_load_lds_dwordx4 %[va], %[a] offset:3072 sc1\n\t" G2_W
                 :: [l] "s"(sl), [oa] "s"(shm_base + G2_A + (p ^ role) * 4096), [a] "s"(sa), [va] "v"(va), G2_WOP(wy) : "memory", "scc", "m0");
    ++st;
    sa += 16384;
    sl = (sl + 16384) & 65535;
    wsel();
  };
  u64 ready = 0;
  const u64* const mbox = &done1[bid * 64 + lane];
  auto seen = [&](u64 v) __attribute__((always_inline)) {
    const u64 r = __ballot(fresh(v, t_own));
    u64 d;
    asm("s_bitreplicate_b64_b32 %0, %1" : "=s"(d) : "s"((unsigned)r >> (16 * role)));
    ready |= r | (d << 32);
  };
  auto need = [&](u64 mask, bool renew) __attribute__((always_inline)) {
    if ((ready & mask) != mask) {
      seen(__hip_atomic_load(mbox, RLX_AGENT));
      while ((ready & mask) != mask) {
        __builtin_amdgcn_s_sleep(2);
        if (renew && !cok) notify(0, 32);
        seen(__hip_atomic_load(mbox, RLX_AGENT));
      }
    }
    asm volatile("" ::: "memory");
  };
  u64 fl = 0;
  auto peek = [&]() { asm volatile("global_load_dwordx2 %0, %1, %2 sc1" : "+v"(fl) : "v"(va >> 1), "s"(done1 + bid * 64) : "memory"); };
  auto check = [&](u64 mask, bool renew) __attribute__((always_inline)) {
    asm volatile("" : "+v"(fl));
    seen(fl);
    if (__builtin_expect((ready & mask) != mask, 0)) need(mask, renew);
  };
  if (wid >= 4) {
    TS(4);
    __builtin_amdgcn_s_setprio(0);
    BARL();
    stage_y();
    stage_y();
    BAR();
    if (p == 1) peek();
    stage_y();
    WAIT_V(16);
    BAR();
    if (p == 0) notify(0, 32);
#define G2_A2 "global_load_lds_dwordx4 %[va], %[a] sc1\n\tglobal_load_lds_dwordx4 %[va], %[a] offset:1024 sc1"
#define G2_A4 G2_A2 "\n\tglobal_load_lds_dwordx4 %[va], %[a] offset:2048 sc1\n\tglobal_load_lds_dwordx4 %[va], %[a] offset:3072 sc1"
#define G2_SYNC "\n\ts_mov_b32 m0, %[la]\n\ts_waitcnt vmcnt(%[n])\n\ts_barrier\n\t"
    const unsigned oy = shm_base + G2_A + (p ^ role) * 4096, os = shm_base + G2_A + (p >> 1) * 8192 + (p & 1) * 2048;
    asm volatile("s_mov_b32 m0, %[la]\n\ts_nop 0\n\t" G2_A4 :: [la] "s"(sl + oy), [a] "s"(sa), [va] "v"(va) : "memory", "m0");
    auto step = [&](long adv = 16384) __attribute__((always_inline)) {
      wsel();
      ++st;
      sa += adv;
      const unsigned sn = (sl + 16384) & 65535;
      asm volatile(G2_W G2_SYNC G2_A4 :: [l] "s"(sl), [la] "s"(sn + oy), [n] "n"(16), [a] "s"(sa), [va] "v"(va), G2_WOP(wy) : "memory", "scc", "m0");
      sl = sn;
    };
    auto chk1 = [&](u64 mask) __attribute__((always_inline)) { if (p == 1) { WAIT_V(16); check(mask, true); } };
    step();
    chk1(0xcc00cc00ull << 32);
    step(); step();
    if (p == 1) peek();
    step(); step();
    chk1(0x0f0fu << (16 * role));
#pragma unroll 1
    for (int g = 7; g < 13; ++g) step();
    if (p == 1) peek();
    step(); step();
    constexpr unsigned BOTH = 0xf555f555u;
    chk1((0xffffu << (16 * role)) | BOTH);
#pragma unroll 1
    for (int g = 15; g < min(ny, NY) - 5; ++g) step();
    if (ny > NY) {
      step(((1 - 2 * role) * NY + 1 - DLT) * 16384);
#pragma unroll
      for (int i = 1; i < DLT; ++i) step();
    }
    wsel();
    sa = sz;
    {
      const unsigned sn = (sl + 16384) & 65535;
      asm volatile(G2_W G2_SYNC G2_A2 :: [l] "s"(sl), [la] "s"(sn + os), [n] "n"(16), [a] "s"(sa), [va] "v"(va), G2_WOP(wy) : "memory", "scc", "m0");
      sl = sn;
    }
    sw[0] = wz.wb[0]; sw[1] = wz.wb[1];
#pragma unroll
    for (int j = -3; j < 13; ++j) {
      if (j == GETP + 1 || j == GETP + 2)
        asm volatile("s_mov_b32 m0, %[l]\n\ts_nop 0\n\tglobal_load_lds_dwordx4 %[va], %[a] offset:%[o] sc1\n\tglobal_load_lds_dwordx4 %[va], %[a] offset:%[o]+1024 sc1"
                     :: [l] "s"(shm_base + G2_X + 16384 + p * 4096), [a] "s"((const char*)slab + (size_t)(bid ^ 5) * 16384 + p * 4096), [va] "v"(va),
                        [o] "n"((j - GETP - 1) * 2048) : "memory", "m0");
      sa += 16384;
      const unsigned sn = (sl + 16384) & 65535;
      constexpr int G = GETP;
      static_assert(GETP <= GETJ + 1, "");
      if (j < 12) asm volatile(G2_W G2_SYNC G2_A2 :: [l] "s"(sl), [la] "s"(sn + os), [n] "n"((j == -3 ? 14 : 12) + (j > G && j < G + 3 ? 2 : 0) + (j > G + 1 && j < G + 4 ? 2 : 0)),
                               [a] "s"(sa), [va] "v"(va), G2_WOP(wz) : "memory", "scc", "m0");
      else asm volatile(G2_W :: [l] "s"(sl), G2_WOP(wz) : "memory", "scc", "m0");
      sl = sn;
      sw[0] += ws0; sw[1] += ws1;
    }
    WAIT_V(12);
    BAR();
    WAIT_V(6);
    BAR();
    WAIT_V(0);
    BAR();
    asm volatile("" : "+v"(fl));
    TS(5);
    if (tid < 258) __hip_atomic_store(lane ? done2 + bid * 8 : &acks[bid], __builtin_amdgcn_s_memrealtime(), RLX_AGENT);
    if (__builtin_expect(!cok, 0) && p == 0) {
      const bool mine = lane == 32 || (lane < 32 && (((BOTH >> ntile) & 1) || ((lane >> 3) & 1) != (xcd >> 2)));
      const u64* const pa = &acks[lane < 32 ? client(lane) : bid ^ 5];
      while (!__all(!mine || fresh(__hip_atomic_load(pa, RLX_AGENT), t_own))) {
        notify(0, 32);
        if (lane == 0) __hip_atomic_store(done2 + bid * 8, __builtin_amdgcn_s_memrealtime(), RLX_AGENT);
        __builtin_amdgcn_s_sleep(32);
      }
    }
  } else {
    TS(6);
    f32x4 acc[4][4], sacc[2][4];
    const int fr = lane & 15, fq = lane >> 4, q4 = fr >> 2, pp = lane & 3;
    const int ff = ((q4 >> 1) & 1) | ((fq & 1) << 1);
    int aof = G2_A + wm * 8192 + lane * 16;
    int boff[4];
#pragma unroll
    for (int j = 0; j < 4; ++j) boff[j] = wn * 8192 + (8 * fq + q4) * 128 + ((j ^ ff) * 32) + pp * 8;
    int vo = wid * 4096 + lane * 16;
    int vx = ((m0 + wm * 64 + role * 32 + 2 * fr) * D_ + n0 + wn * 64) * 2 + (fq & 1) * 32 + (fq >> 1) * 16;
    lds_cptr px = shm3 + G2_X + wid * 4096 + (fq >> 1) * 512 + fr * 16 + (fq & 1) * 8;
    asm volatile("" : "+v"(vo), "+v"(vx), "+v"(px));
#define SB() __builtin_amdgcn_sched_barrier(0)
    struct Fr { bf16x8 a[4]; bf16x4 bl[4], bh[4]; };
    Fr fa[2], fb[2];
    auto rd = [&](Fr* f, int odd, int t) {
      constexpr int ord[12] = {0, 1, 8, 2, 3, 4, 5, 6, 7, 9, 10, 11};
      constexpr int seq[24] = {0, 1, 2, 12, 13, 14, 15, 16, 3, 4, 5, 6, 17, 18, 19, 20, 7, 8, 9, 21, 10, 22, 11, 23};
      lds_cptr sb = shm3 + odd * 16384;
      const int ks = seq[t] / 12, o = ord[seq[t] % 12];
      if (o >= 8) f[ks].a[o - 8] = *(const __attribute__((address_space(3))) bf16x8*)(sb + aof + (o - 8) * 2048 + ks * 1024);
      else if (o & 1) f[ks].bh[o >> 1] = tr_read(sb + boff[o >> 1] + ks * 4096 + 512);
      else f[ks].bl[o >> 1] = tr_read(sb + boff[o >> 1] + ks * 4096);
    };
    auto bfr = [&](const Fr& c, int j) { return __builtin_shufflevector(c.bl[j], c.bh[j], 0, 1, 2, 3, 4, 5, 6, 7); };
    auto mf1 = [](f32x4& c, bf16x8 b, bf16x8 a, bool z = false) {
      if (z) asm volatile("v_mfma_f32_16x16x32_bf16 %0, %1, %2, 0" : "=&v"(c) : "v"(b), "v"(a));
      else asm volatile("v_mfma_f32_16x16x32_bf16 %0, %1, %2, %0" : "+v"(c) : "v"(b), "v"(a));
    };
    auto mf2 = [](f32x4& c, bf16x8 b, bf16x8 a, bf16x8 b2, bf16x8 a2, bool z) {
      if (z) asm volatile("v_mfma_f32_16x16x32_bf16 %0, %1, %2, 0\n\tv_mfma_f32_16x16x32_bf16 %0, %3, %4, %0" : "=&v"(c) : "v"(b), "v"(a), "v"(b2), "v"(a2));
      else asm volatile("v_mfma_f32_16x16x32_bf16 %0, %1, %2, %0\n\tv_mfma_f32_16x16x32_bf16 %0, %3, %4, %0" : "+v"(c) : "v"(b), "v"(a), "v"(b2), "v"(a2));
    };
    auto pairs = [&](f32x4 (*ac)[4], const Fr* c, int nq, int mx, int nf, auto fill, bool z = false) {
#pragma unroll
      for (int q = 0; q < nq; ++q) {
        const int mf = (q >> 2) ^ mx, j = (q & 4) ? 3 - (q & 3) : q & 3, ks = q & 1;
        SB();
        if (2 * q >= nf) mf2(ac[mf][j], bfr(c[ks], j), c[ks].a[mf], bfr(c[ks ^ 1], j), c[ks ^ 1].a[mf], z);
        else {
          mf1(ac[mf][j], bfr(c[ks], j), c[ks].a[mf], z);
          SB();
          fill(2 * q);
          SB();
          mf1(ac[mf][j], bfr(c[ks ^ 1], j), c[ks ^ 1].a[mf]);
          SB();
          fill(2 * q + 1);
        }
      }
      SB();
    };
    auto flip = [&]() {
      aof ^= 32768; boff[0] ^= 32768; boff[1] ^= 32768; boff[2] ^= 32768; boff[3] ^= 32768;
      asm volatile("" : "+v"(aof), "+v"(boff[0]), "+v"(boff[1]), "+v"(boff[2]), "+v"(boff[3]));
    };
    auto bar = [&]() { __builtin_amdgcn_s_waitcnt(0xC07F); BAR(); };
    auto ystg = [&](const Fr* c, Fr* n, int odd, bool z = false) {
      pairs(acc, c, 16, 0, odd ? 30 : 24, [&](int t) {
        if (t < 24) rd(n, odd, t);
        else if (t == 24) asm volatile("s_nop 0");
        else { int& a = t == 29 ? aof : boff[t - 25]; a ^= 32768; asm volatile("" : "+v"(a)); }
      }, z);
    };
    auto stg = [&](const Fr* c, Fr* n, int odd, bool z = false) { pairs(sacc, c, 8, 0, 10, [&](int t) { rd(n, odd, 2 * t); rd(n, odd, 2 * t + 1); }, z); };
    asm volatile("" : "+v"(aof), "+v"(boff[0]), "+v"(boff[1]), "+v"(boff[2]), "+v"(boff[3]));
    await(0xffffu, 0x404u);
    auto lda = [&](int i) {
      asm volatile("s_mov_b32 m0, %[l]\n\ts_nop 0\n\tglobal_load_lds_dwordx4 %[va], %[a] sc1\n\tglobal_load_lds_dwordx4 %[va], %[a] offset:1024 sc1\n\t"
                   "global_load_lds_dwordx4 %[va], %[a] offset:2048 sc1\n\tglobal_load_lds_dwordx4 %[va], %[a] offset:3072 sc1"
                   :: [l] "s"(shm_base + G2_A + (i + 1) * 16384 + (p ^ role) * 4096), [a] "s"(sa + i * 16384), [va] "v"(va) : "memory", "m0");
    };
    need(0x00330033ull << 32, false);
    TS(4);
    lda(0);
    lda(1);
    need(0x00ff00ffull << 32, false);
    TS(0);
    lda(2);
    WAIT_V(8);
    signal(1u << 24);
    await(0xff000000u, 4u << 24);
    __builtin_amdgcn_s_waitcnt(0xC07F);
    SB();
#pragma unroll
    for (int t = 0; t < 24; ++t) rd(fa, 1, t);
    flip();
    __builtin_amdgcn_s_setprio(0);
    WAIT_V(4);
    TS(5);
    peek();
    bar();
    TS(7);
    ystg(fa, fb, 0, true);
    WAIT_V(1);
    bar();
    ystg(fb, fa, 1);
    WAIT_V(0);
    check(0x33003300ull << 32, false);
    constexpr int PAR = (NY - DLT + 1) & 1;
#pragma unroll 1
    for (int g = 2; g < ny - 2; g += 2) {
      bar();
      ystg(fa, fb, 0);
      bar();
      ystg(fb, fa, 1);
    }
    if (PAR) {
      bar();
      ystg(fa, fb, 0);
    }
    Fr* const f0 = PAR ? fb : fa;
    Fr* const f1 = PAR ? fa : fb;
    bar();
    {
      const __amdgpu_buffer_rsrc_t rs = __builtin_amdgcn_make_buffer_rsrc((void*)((char*)slab + (size_t)bid * 16384), 0, 16384, 0x00020000);
      u32x4 pk;
      pairs(acc, f0, 16, 2, 32, [&](int t) {
        if (t < 20) rd(f1, PAR, t);
        else {
          const int f = (t - 20) / 3, e = (t - 20) % 3;
          if (e < 2) { const f32x4 a = acc[2 + (f >> 1)][(f & 1) * 2 + e]; pk[2 * e] = pack2(a[0], a[1]); pk[2 * e + 1] = pack2(a[2], a[3]); }
          else __builtin_amdgcn_raw_buffer_store_b128(pk, rs, vo + f * 1024, 0, 16);
        }
      });
    }
    TS(2);
    static_assert(GETJ % 2 == 1 && GETJ >= 3 && GETJ <= 7, "");
    f32x4 xf[2][4];
#pragma unroll
    for (int j = 0; j < GETJ + 5; j += 2) {
      if (PAR) flip();
      bar();
      stg(f1, f0, PAR ^ 1, j == 0);
      if (!PAR) flip();
      bar();
      u32x2 xv[2];
      pairs(sacc, f0, 8, 0, 12, [&](int t) {
        if (t < 10) { rd(f1, PAR, 2 * t); rd(f1, PAR, 2 * t + 1); }
        else if (j < 8) xv[t - 10] = *(const __attribute__((address_space(3))) u32x2*)(px + (j >> 1) * 1024 + (t - 10) * 256);
      });
      if (j < 8) {
#pragma unroll
        for (int e = 0; e < 2; ++e) xf[j >> 2][(j & 2) + e] = f32x4{lo_f(xv[e][0]), hi_f(xv[e][0]), lo_f(xv[e][1]), hi_f(xv[e][1])};
        asm volatile("" : "+v"(xf[j >> 2][j & 2]), "+v"(xf[j >> 2][(j & 2) + 1]));
        SB();
      }
    }
    if (PAR) flip();
    WAIT_V(0);
    bar();
    u32x4 pv[4];
    {
      lds_cptr pp_ = shm3 + G2_X + 16384 + vo;
      pairs(sacc, f1, 8, 0, 12, [&](int t) {
        if (t < 2) {
#pragma unroll
          for (int f = 2 * t; f < 2 * t + 2; ++f) pv[f] = *(const __attribute__((address_space(3))) u32x4*)(pp_ + f * 1024);
        } else { rd(f0, PAR ^ 1, 2 * t - 4); rd(f0, PAR ^ 1, 2 * t - 3); }
      });
      if (__builtin_expect(blank(pv) || !cok, 0)) {
        const __amdgpu_buffer_rsrc_t rp = __builtin_amdgcn_make_buffer_rsrc((void*)((char*)slab + (size_t)(bid ^ 5) * 16384), 0, 16384, 0x00020000);
        while (!cok && !__all(fresh(__hip_atomic_load(done2 + (bid ^ 5) * 8, RLX_AGENT), t_own))) {
          notify(0, 32);
          if (lane == 0) __hip_atomic_store(done2 + bid * 8, __builtin_amdgcn_s_memrealtime(), RLX_AGENT);
          __builtin_amdgcn_s_sleep(8);
        }
        do {
          asm volatile("" ::: "memory");
#pragma unroll
          for (int f = 0; f < 4; ++f) pv[f] = __builtin_bit_cast(u32x4, __builtin_amdgcn_raw_buffer_load_b128(rp, vo + f * 1024, 0, 16));
        } while (blank(pv));
      }
    }
    auto silu = [&](int g0, int g1) {
#pragma unroll
      for (int f = g0; f < g1; ++f) {
        const int mf = f >> 2, j = f & 3;
        const u32x4 pw = pv[mf * 2 + (j >> 1)];
        const f32x4 ya = acc[mf][j] + f32x4{lo_f(pw[2 * (j & 1)]), hi_f(pw[2 * (j & 1)]), lo_f(pw[2 * (j & 1) + 1]), hi_f(pw[2 * (j & 1) + 1])};
#pragma unroll
        for (int e = 0; e < 2; ++e) {
          const f32x2 y2 = {ya[2 * e], ya[2 * e + 1]};
          const f32x2 ar = y2 * f32x2{-1.4426950408889634f, -1.4426950408889634f};
          const f32x2 dn = f32x2{__builtin_amdgcn_exp2f(ar[0]), __builtin_amdgcn_exp2f(ar[1])} + f32x2{1.0f, 1.0f};
          const f32x2 t = y2 * f32x2{__builtin_amdgcn_rcpf(dn[0]), __builtin_amdgcn_rcpf(dn[1])};
          acc[mf][j][2 * e] = t[0]; acc[mf][j][2 * e + 1] = t[1];
        }
        asm volatile("" : "+v"(acc[mf][j]));
      }
      SB();
    };
    constexpr int spt[] = {0, 0, 1, 2, 4, 6, 8, 8};
    silu(0, spt[1]);
#pragma unroll
    for (int j = GETJ + 6; j < 15; ++j) {
      if ((j ^ PAR) & 1) flip();
      bar();
      if (j & 1) stg(f0, f1, PAR); else stg(f1, f0, PAR ^ 1);
      silu(spt[j - GETJ - 5], j == 14 ? 8 : spt[j - GETJ - 4]);
    }
    pairs(sacc, f0, 8, 0, 0, [](int) {});
    asm volatile("s_nop 7\n\ts_nop 3" : "+v"(sacc[1][0]), "+v"(sacc[1][1]), "+v"(sacc[1][2]), "+v"(sacc[1][3]));
    SB();
    TS(1);
    {
      const __amdgpu_buffer_rsrc_t ro = __builtin_amdgcn_make_buffer_rsrc((void*)out, 0, S_ * D_ * 2, 0x00020000);
#pragma unroll
      for (int f = 0; f < 4; ++f) {
        const int mf = f >> 1, j = (f & 1) * 2;
        const f32x4 a = __builtin_elementwise_fma(sacc[mf][j], acc[mf][j], xf[mf][j]), b = __builtin_elementwise_fma(sacc[mf][j + 1], acc[mf][j + 1], xf[mf][j + 1]);
        const auto r0 = __builtin_amdgcn_permlane16_swap(pack2(a[0], a[1]), pack2(b[0], b[1]), false, false);
        const auto r1 = __builtin_amdgcn_permlane16_swap(pack2(a[2], a[3]), pack2(b[2], b[3]), false, false);
        __builtin_amdgcn_raw_buffer_store_b128(u32x4{r0[0], r1[0], r0[1], r1[1]}, ro, vx + mf * 2048 + j * 32, 0, 2);
      }
    }
    TS(3);
  }
  if (bid == 73 && wid == 4) { unsigned d_ = (ts_r[12] - ts_r[3]) * 5u; if (d_ > 12000u) d_ = 0; while (__builtin_amdgcn_s_memrealtime() - t_own < 6000ull + d_) {} }
  }
  asm volatile(".Lcode_end:");
}

extern "C" void kernel_launch(void* const* d_in, const int* in_sizes, int n_in, void* d_out, int out_size, void* d_ws,
                              size_t ws_size, hipStream_t stream) {
  const u16* A_x = (const u16*)d_in[0];
  const u16* A_wqkv = (const u16*)d_in[1];
  const u16* A_wo = (const u16*)d_in[2];
  const u16* B_wg = (const u16*)d_in[4];
  const u16* B_wu = (const u16*)d_in[5];
  const u16* B_wd = (const u16*)d_in[6];
  u16* out = (u16*)d_out;
  fused_kernel<<<256, 512, 0, stream>>>(A_x, A_wqkv, B_wg, B_wu, B_wd, A_wo, out, (char*)d_ws);
}
```

```cpp
#include <hip/hip_runtime.h>
#include <hip/hip_bf16.h>
#include <stdint.h>
#include <type_traits>

typedef unsigned short u16;
using bf16x8 = __attribute__((ext_vector_type(8))) short;
using bf16x4 = __attribute__((ext_vector_type(4))) short;
using f32x4 = __attribute__((ext_vector_type(4))) float;
using u32x2 = __attribute__((ext_vector_type(2))) unsigned;
using u32x4 = __attribute__((ext_vector_type(4))) unsigned;
typedef __attribute__((address_space(3))) const char* lds_cptr;

constexpr int S_ = 2048, D_ = 1024, F_ = 3584;

__device__ __forceinline__ float bf2f(u16 h) { return __uint_as_float((unsigned)h << 16); }
__device__ __forceinline__ u16 f2bf(float f) {
  unsigned u = __float_as_uint(f);
  u += 0x7fffu + ((u >> 16) & 1u);
  return (u16)(u >> 16);
}
__device__ __forceinline__ float rbf(float f) { return bf2f(f2bf(f)); }
typedef __attribute__((ext_vector_type(2))) __bf16 bf16x2_t;
typedef __attribute__((ext_vector_type(2))) float f32x2;
__device__ __forceinline__ unsigned pack2(float a, float b) {
  bf16x2_t r = __builtin_convertvector((f32x2){a, b}, bf16x2_t);
  return __builtin_bit_cast(unsigned, r);
}
__device__ __forceinline__ float lo_f(unsigned p) { return __uint_as_float(p << 16); }
__device__ __forceinline__ float hi_f(unsigned p) { return __uint_as_float(p & 0xffff0000u); }

#define TSDECL
#define TSS(i) do {} while (0)
#define TSFLUSH() do {} while (0)
#define TS1(i) TSS(i)
#define TS(i) TSS(8 + (i))
#define PIN_S(v) asm volatile("" : "+s"(v))
#define WAIT_V(n) asm volatile("s_waitcnt vmcnt(%0)" ::"n"(n) : "memory")
#define WAIT_L(n) asm volatile("s_waitcnt lgkmcnt(%0)" ::"n"(n) : "memory")
#define BARL() do { WAIT_L(0); BAR(); } while (0)
#define BAR() do { asm volatile("" ::: "memory"); __builtin_amdgcn_s_barrier(); asm volatile("" ::: "memory"); } while (0)

__device__ __forceinline__ bf16x4 tr_read(lds_cptr p) {
  return __builtin_amdgcn_ds_read_tr16_b64_v4i16((__attribute__((address_space(3))) bf16x4*)p);
}
__device__ __forceinline__ bf16x8 tr_frag(lds_cptr p) {
  bf16x4 lo = tr_read(p), hi = tr_read(p + 512);
  bf16x8 r;
  r[0] = lo[0]; r[1] = lo[1]; r[2] = lo[2]; r[3] = lo[3];
  r[4] = hi[0]; r[5] = hi[1]; r[6] = hi[2]; r[7] = hi[3];
  return r;
}
__device__ __forceinline__ void glds16_s(const void* sbase, unsigned voff, unsigned lds_dst) {
  asm volatile("s_mov_b32 m0, %2\n\ts_nop 0\n\tglobal_load_lds_dwordx4 %0, %1" :: "v"(voff), "s"(sbase), "s"(lds_dst) : "m0");
}
__device__ __forceinline__ void glds16_s1(const void* sbase, unsigned voff, unsigned lds_dst) {
  asm volatile("s_mov_b32 m0, %2\n\ts_nop 0\n\tglobal_load_lds_dwordx4 %0, %1 sc1" :: "v"(voff), "s"(sbase), "s"(lds_dst) : "memory", "m0");
}
__device__ __forceinline__ void glds16_v(const void* p, unsigned lds_dst) {
  asm volatile("s_mov_b32 m0, %1\n\ts_nop 0\n\tglobal_load_lds_dwordx4 %0, off" :: "v"(p), "s"(lds_dst) : "m0");
}
__device__ __forceinline__ void gst16(const void* sbase, unsigned voff, u32x4 v) {
  asm volatile("global_store_dwordx4 %0, %1, %2 offset:0 sc1\n\ts_nop 1" :: "v"(voff), "v"(v), "s"(sbase) : "memory");
}

typedef unsigned long long u64;
__device__ __forceinline__ bool fresh(u64 stamp, u64 t_own) {
  const u64 b = t_own + 100ull;
  const unsigned lo = (unsigned)stamp - (unsigned)b, hi = (unsigned)(stamp >> 32) - (unsigned)(b >> 32) - ((unsigned)stamp < (unsigned)b ? 1u : 0u);
  return (hi == 0u) & (lo < (unsigned)(__builtin_amdgcn_s_memrealtime() - t_own));
}
constexpr u64 TIMEOUT_ = 200000;
__device__ __forceinline__ float half_sum(float v) {
  v += __builtin_bit_cast(float, __builtin_amdgcn_update_dpp(0, __builtin_bit_cast(int, v), 0xB1, 0xf, 0xf, false));
  v += __builtin_bit_cast(float, __builtin_amdgcn_update_dpp(0, __builtin_bit_cast(int, v), 0x4E, 0xf, 0xf, false));
  v += __builtin_bit_cast(float, __builtin_amdgcn_update_dpp(0, __builtin_bit_cast(int, v), 0x141, 0xf, 0xf, false));
  v += __builtin_bit_cast(float, __builtin_amdgcn_update_dpp(0, __builtin_bit_cast(int, v), 0x140, 0xf, 0xf, false));
  { auto r = __builtin_amdgcn_permlane16_swap(__float_as_uint(v), __float_as_uint(v), false, false); v = __uint_as_float(r[0]) + __uint_as_float(r[1]); }
  return v;
}
__device__ __forceinline__ unsigned pack_stats(float mean, float var) {
  return (unsigned)f2bf(mean) | ((unsigned)f2bf(sqrtf(rbf(rbf(var) + 1e-5f))) << 16);
}
__device__ __forceinline__ unsigned row_stats_serial(const u16* xr) {
  float s = 0.f, q = 0.f;
#pragma unroll 1
  for (int k = 0; k < D_; ++k) s += bf2f(xr[k]);
  const float mean = s * (1.0f / D_);
#pragma unroll 1
  for (int k = 0; k < D_; ++k) { const float d = bf2f(xr[k]) - mean; q += d * d; }
  return pack_stats(mean, q * (1.0f / D_));
}
constexpr int G1_STAGE = 32768 + 5 * 8192;
constexpr int PA = 4, PB = 8, PC = 12;
constexpr int DLT = 2;
constexpr int GETP = 4, GETJ = 3;
constexpr unsigned BLANK = 0x7f817f81u;
__device__ __forceinline__ bool blank(const u32x4* pv) {
  u64 m;
#define CX(i) "v_cmpx_ne_u32 vcc, %1, %" #i "\n\t"
  asm volatile(CX(2) CX(3) CX(4) CX(5) CX(6) CX(7) CX(8) CX(9) CX(10) CX(11) CX(12) CX(13) CX(14) CX(15) CX(16) CX(17)
               "s_mov_b64 %0, exec\n\ts_mov_b64 exec, -1\n\ts_nop 3" : "=s"(m) : "s"(BLANK), "v"(pv[0][0]), "v"(pv[0][1]), "v"(pv[0][2]), "v"(pv[0][3]),
               "v"(pv[1][0]), "v"(pv[1][1]), "v"(pv[1][2]), "v"(pv[1][3]), "v"(pv[2][0]), "v"(pv[2][1]), "v"(pv[2][2]), "v"(pv[2][3]),
               "v"(pv[3][0]), "v"(pv[3][1]), "v"(pv[3][2]), "v"(pv[3][3]) : "vcc");
  return m != ~0ull;
}
#define RLX_AGENT __ATOMIC_RELAXED, __HIP_MEMORY_SCOPE_AGENT

constexpr int G2_A = 65536, G2_X = 131072;
constexpr size_t MB_ = 1024 * 1024;
__global__ __launch_bounds__(512, 2) __attribute__((aligned(4096))) void fused_kernel(const u16* __restrict__ x, const u16* __restrict__ wqkv,
                                                       const u16* __restrict__ wg, const u16* __restrict__ wu,
                                                       const u16* __restrict__ wd, const u16* __restrict__ wo,
                                                       u16* __restrict__ out, char* __restrict__ ws) {
  u16* const halo = (u16*)(ws + 4 * MB_);
  u16* const abuf = (u16*)(ws + 16 * MB_);
  char* const slab = ws + 64 * MB_;
  unsigned* const flags = (unsigned*)(ws + 96 * MB_);
  char* const wcopy = ws + 98 * MB_;
  u64* const done1 = (u64*)(ws + 102 * MB_);
  u64* const done2 = (u64*)(ws + 100 * MB_ + 65536);
  u64* const acks = (u64*)(ws + 100 * MB_ + 196608);
  __shared__ __attribute__((aligned(1024))) char shm[163840];
  constexpr int AUX = 2 * G1_STAGE + 8192;
  {
    u64 pc; int len;
    asm volatile("s_getpc_b64 %0\n.Lcode_beg:\n\ts_mov_b32 %1, .Lcode_end-.Lcode_beg" : "=s"(pc), "=s"(len));
    const int ol = ((blockIdx.x >> 3) + 32 * (threadIdx.x >> 6)) * 1024 + (threadIdx.x & 63) * 16;
    asm volatile("s_mov_b32 m0, %2\n\ts_nop 0\n\tglobal_load_lds_dwordx4 %0, %1"
                 :: "v"(ol < len - 16 ? ol : 0), "s"(pc & ~15ull), "s"((unsigned)(uintptr_t)(lds_cptr)shm + 2 * G1_STAGE + 7168) : "m0");
    __builtin_amdgcn_sched_barrier(0);
  }
  const u64 t_own = __builtin_amdgcn_s_memrealtime();
  TSDECL;
  TS1(7);
  const int tid = threadIdx.x, lane = tid & 63;
  const int wid = __builtin_amdgcn_readfirstlane(tid >> 6);
  const int wm = wid & 3, wc = wid >> 2;
  int bid_ = blockIdx.x;
  PIN_S(bid_);
  const int bid = bid_ & 255, xcd = bid & 7, idx = bid >> 3;
  const int ntile = xcd * 4 + (idx & 3), mtile = idx >> 2;
  const int m0 = mtile * 256;
  const int hg = ntile >> 1;
  const bool tya = (ntile & 1) == 0;
  const int srow = m0 + ntile * 8 + wm * 2;
  lds_cptr shm3 = (lds_cptr)shm;
  const unsigned shm_base = (unsigned)(uintptr_t)shm3;
  const int r8 = tid >> 3, pos = tid & 7;
  const int phys = pos >> 1, half = pos & 1;
  const int fk = ((r8 >> 1) & 1) | (((r8 >> 3) & 1) << 1);
  const int vsrc = phys ^ fk;
  const int qstr = tya ? 3072 * 2 : F_ * 2;
  unsigned voa = (((r8 & 32) + 2 * (r8 & 15) + ((r8 >> 4) & 1)) * D_ + ((pos ^ (r8 & 7)) * 8)) * 2;
  unsigned vog = r8 * (F_ * 2) + vsrc * 32 + half * 16, voq = r8 * qstr + vsrc * 32 + half * 16;
  unsigned vom = r8 * qstr + (vsrc >> (tya ? 0 : 1)) * 32 + half * 16;
  asm volatile("" : "+v"(voa), "+v"(vog), "+v"(voq), "+v"(vom));
  const int kt0 = (hg >> 1) * 7, he = hg & 1;
  unsigned oq = (tya ? hg : kt0 + 2 * he) * 128, og = (kt0 + (tya ? 5 + he : 2 * he + 1)) * 128, om = tya ? 4096 : 512 - 192 * he;
  unsigned d0 = shm_base + wid * 1024 + 32768, ox = m0 * D_ * 2;
  long uq = -32 * qstr;
  PIN_S(oq); PIN_S(og); PIN_S(om); PIN_S(d0); PIN_S(ox); PIN_S(uq);
  __builtin_amdgcn_sched_barrier(0);
  asm volatile("" :: "s"(x), "s"(wqkv), "s"(wg), "s"(wu), "s"(wd), "s"(wo), "s"(out), "s"(ws));
  const char* const ab = (const char*)x + ox;
  int kz = 0;
  asm volatile("" : "+s"(kz));
  u64 pq = (u64)wqkv, pg = (u64)wg;
  PIN_S(pq); PIN_S(pg);
  const char* const qb = (const char*)(tya ? pq : pg) + oq;
  if (wid < 4) {
#pragma unroll
    for (int i = 0; i < 8; ++i) glds16_s(ab + i * 32 * D_ * 2, voa, d0 - 32768 + i * 4096);
  } else {
    glds16_s(qb, voq, d0); glds16_s(qb + uq, voq, d0 - 4096);
  }
  __builtin_amdgcn_sched_barrier(0);
  const char* const gb = (const char*)wg + og;
  const long dgu = (const char*)wu - (const char*)wg, dp1 = tya ? 2048 : dgu;
  const char* const mb0 = qb + om;
  const char* mptr = mb0 + (((vsrc & 1) && !tya) ? dgu : 0) + vom;
  if (wid >= 4) {
    const long ug = kz - 32 * F_ * 2;
    glds16_s(qb + dp1, voq, d0 + 8192); glds16_s(qb + dp1 + uq, voq, d0 + 4096);
    glds16_v(mptr, d0 + 2 * 8192); glds16_v(mptr + uq, d0 + 2 * 8192 - 4096);
    glds16_s(gb, vog, d0 + 3 * 8192); glds16_s(gb + ug, vog, d0 + 3 * 8192 - 4096);
    glds16_s(gb + dgu, vog, d0 + 4 * 8192); glds16_s(gb + dgu + ug, vog, d0 + 4 * 8192 - 4096);
  }
  __builtin_amdgcn_sched_barrier(0);
  f32x4 acc[4][10];

  constexpr int NT = D_ / 64;
  constexpr int CNT = AUX + 4096 + 3584;
  if (tid < 8) *(unsigned*)(shm + CNT + tid * 4) = 0u;
  bf16x8 af0[4], af1[4];
  bf16x4 bl[4], bh[4];
#define SB0() __builtin_amdgcn_sched_barrier(0)
  auto mm = [&](int g, int t, bool two, bool z = false) {
    const int mi = t >> 1, mf = ((g & 1) && g != 9) ? 3 - mi : mi, ks = (t & 1) ^ (mi & 1), s = ((g + 1) & 1) * 2;
    const bf16x8 b0 = __builtin_shufflevector(bl[s + ks], bh[s + ks], 0, 1, 2, 3, 4, 5, 6, 7), a0 = ks ? af1[mf] : af0[mf];
    const bf16x8 b1 = __builtin_shufflevector(bl[s + 1 - ks], bh[s + 1 - ks], 0, 1, 2, 3, 4, 5, 6, 7), a1 = ks ? af0[mf] : af1[mf];
    const bool tr = (g >> 1) == 2;
    if (z && two) asm volatile("v_mfma_f32_16x16x32_bf16 %0, %1, %2, 0\n\tv_mfma_f32_16x16x32_bf16 %0, %3, %4, %0" : "=&v"(acc[mf][g])
                               : "v"(tr ? a0 : b0), "v"(tr ? b0 : a0), "v"(tr ? a1 : b1), "v"(tr ? b1 : a1));
    else if (z && !(t & 1)) asm volatile("v_mfma_f32_16x16x32_bf16 %0, %1, %2, 0" : "=&v"(acc[mf][g]) : "v"(tr ? a0 : b0), "v"(tr ? b0 : a0));
    else if (two) asm volatile("v_mfma_f32_16x16x32_bf16 %0, %1, %2, %0\n\tv_mfma_f32_16x16x32_bf16 %0, %3, %4, %0" : "+v"(acc[mf][g])
                          : "v"(tr ? a0 : b0), "v"(tr ? b0 : a0), "v"(tr ? a1 : b1), "v"(tr ? b1 : a1));
    else asm volatile("v_mfma_f32_16x16x32_bf16 %0, %1, %2, %0" : "+v"(acc[mf][g]) : "v"(tr ? a0 : b0), "v"(tr ? b0 : a0));
  };
  typedef __attribute__((address_space(3))) unsigned lds_u32;
  lds_u32* const cnt = (lds_u32*)(shm3 + CNT);
  bool cok;
  constexpr int NY = 28;
  int role, mt2, nt2;
  struct WS { unsigned vb[2][2]; const char* wb[2]; int sh[2]; };
  auto wsetup = [&](const u16* w, int crow0, int l, bool ck) __attribute__((always_inline)) {
    WS r;
    const unsigned vr = ((l >> 5) & 1) * 16 + (wm & 1) * 8 + ((l >> 3) & 3), vc = (((l >> 1) & 3) ^ ((l >> 4) & 1)) * 32 + (l & 1) * 16;
    u64 cp = (u64)(wcopy + (nt2 >> 2) * 589824 + crow0 * 128);
    asm volatile("" : "+s"(cp));
#pragma unroll
    for (int i = 0; i < 2; ++i) {
      u64 dp = (u64)(w + nt2 * 128 + i * 64);
      asm volatile("" : "+s"(dp));
      const bool slow = ck & ((((unsigned)dp >> 7) & 7) == 3);
      r.sh[i] = slow ? 7 : 11;
      r.wb[i] = (const char*)(slow ? cp : dp) + (crow0 ? (wm >> 1) * 32 << r.sh[i] : 0);
      r.vb[i][0] = (vr << r.sh[i]) + vc; r.vb[i][1] = ((vr + 4) << r.sh[i]) + (vc ^ 64);
    }
    asm volatile("" : "+v"(r.vb[0][0]), "+v"(r.vb[0][1]), "+v"(r.vb[1][0]), "+v"(r.vb[1][1]));
    PIN_S(r.wb[0]); PIN_S(r.wb[1]); PIN_S(r.sh[0]); PIN_S(r.sh[1]);
    return r;
  };
  WS wy, wz;
  int vxq, ws0, ws1;
  const char* sz;
  u32x4 xq[4];
  unsigned vg;
  int st = 3;
  const char *sa, *sw[2];
  auto wsel = [&]() {
    const unsigned r = __builtin_amdgcn_readlane(vg, st);
    sw[0] = wy.wb[0] + (r << wy.sh[0]); sw[1] = wy.wb[1] + (r << wy.sh[1]);
  };
  const int p = wid & 3;
  unsigned sl = 0;
#define G2_W "s_add_u32 m0, %[l], %[ow]\n\ts_nop 0\n\tglobal_load_lds_dwordx4 %[v0], %[w0] sc1\n\t" \
             "s_add_u32 m0, m0, 1024\n\ts_nop 0\n\tglobal_load_lds_dwordx4 %[v1], %[w0] sc1\n\t" \
             "s_add_u32 m0, m0, 7168\n\ts_nop 0\n\tglobal_load_lds_dwordx4 %[v2], %[w1] sc1\n\t" \
             "s_add_u32 m0, m0, 1024\n\ts_nop 0\n\tglobal_load_lds_dwordx4 %[v3], %[w1] sc1"
#define G2_WOP(w) [ow] "s"(shm_base + p * 2048), [w0] "s"(sw[0]), [w1] "s"(sw[1]), [v0] "v"(w.vb[0][0]), [v1] "v"(w.vb[0][1]), [v2] "v"(w.vb[1][0]), [v3] "v"(w.vb[1][1])
  auto tile2 = [&](int t0) __attribute__((always_inline)) {
    int b = bid;
    asm volatile("" : "+s"(b));
    role = 1 - ((b >> 2) & 1);
    mt2 = ((role ? b : b ^ 5) & 7) * 4 + ((b >> 3) & 3), nt2 = b >> 5;
    sa = (const char*)abuf + (size_t)(mt2 * 72 + role * NY + t0) * 16384 + wm * 4096;
    asm volatile("" : "+s"(sa));
  };
  auto prep = [&](bool first) __attribute__((always_inline)) {
    int l = threadIdx.x;
    asm volatile("" : "+v"(l));
    l &= 63;
    if (first) {
      tile2(3);
      vxq = ((mt2 * 128 + (wm >> 1) * 64 + role * 32 + 2 * (l & 15)) * D_ + nt2 * 128 + (wm & 1) * 64) * 2 + ((l >> 4) & 1) * 32 + (l >> 5) * 16;
      const int t = min(l < NY ? l : l - DLT, NY - 1), w = wm >> 1;
      const int o = (0xffaa5500fae504ull >> (2 * t)) & 3;
      int u1 = (0x990990 >> ((2 * t) & 31)) & 3, u2 = 3 + (t & 3);
      asm volatile("" : "+v"(u1), "+v"(u2));
      const int u = t < 12 ? u1 : u2;
      vg = (__umul24((l < NY ? role : 1 - role) * 4 + (((o & 1) << 1) | (o >> 1)), 14) + ((0xb85140a >> (4 * u)) & 15) + (u == 5 ? w : 2 * w)) * 32;
      asm volatile("" : "+v"(vg), "+v"(vxq));
      sz = (const char*)abuf + (size_t)(mt2 * 72 + 56) * 16384 + (wm >> 1) * 8192 + role * 4096 + (wm & 1) * 2048;
      PIN_S(sz);
    }
    wy = wsetup(wd, 0, l, first);
    wz = wsetup(wo, F_, l, first);
    ws0 = 64 << wz.sh[0]; PIN_S(ws0);
    ws1 = 64 << wz.sh[1]; PIN_S(ws1);
    SB0();
  };
  auto start2 = [&]() __attribute__((always_inline)) {
    const __amdgpu_buffer_rsrc_t rx = __builtin_amdgcn_make_buffer_rsrc((void*)x, 0, S_ * D_ * 2, 0x00020000);
#pragma unroll
    for (int f = 0; f < 4; ++f) xq[f] = __builtin_bit_cast(u32x4, __builtin_amdgcn_raw_buffer_load_b128(rx, vxq + (f >> 1) * 2048 + (f & 1) * 64, 0, 0));
    cok = __builtin_amdgcn_readfirstlane(cnt[1] & cnt[2]) != 0u;
    if (__builtin_expect(!cok, 0)) prep(false);
    wsel();
    PIN_S(sw[0]); PIN_S(sw[1]);
    SB0();
  };
  auto kloop = [&](auto wmc) {
    constexpr bool LD = decltype(wmc)::value != 0;
    constexpr int DMP = 2 * G1_STAGE;
    const char* pa = nullptr;
    unsigned vob = 0;
    f32x4 ca = {0.f, 0.f, 0.f, 0.f}, cb = {0.f, 0.f, 0.f, 0.f}, cd = ca;
    bf16x8 sel = {0, 0, 0, 0, 0, 0, 0, 0};
    int tro = 0;
    const int psh = (wid == 2 && !tya) ? 1 : 0, pstr = wid < 3 ? qstr : F_ * 2;
    const char* const pbase = wid == 0 ? qb : (wid == 1 ? qb + dp1 : (wid == 2 ? mb0 : gb));
    int lane = threadIdx.x;
    asm volatile("; %1" : "+v"(lane) : "n"(LD ? 1 : 0));
    lane &= 63;
    const int fr = lane & 15, fq = lane >> 4, q4 = fr >> 2, pp = lane & 3;
    const int ff = ((q4 >> 1) & 1) | ((fq & 1) << 1);
    int aoff[2], boff[2];
#pragma unroll
    for (int ks = 0; ks < 2; ++ks) aoff[ks] = (wm * 64 + fr) * 128 + (((ks * 4 + fq) ^ (fr & 7)) * 16);
#pragma unroll
    for (int j = 0; j < 2; ++j) boff[j] = 32768 + (8 * fq + q4) * 128 + (((2 * wc + j) ^ ff) * 32) + pp * 8;
    unsigned vol = lane * 16;
    float sm = 0.f, sq = 0.f;
    const char* pf = (const char*)wd + (bid * 4 + wid) * 7168;
    long pstep = 64 * pstr;
    if (!LD) {
      const int rs = 256 + mtile * 8 + (lane >> 3), vs = ((lane & 7) >> 1) ^ (((rs >> 1) & 1) | ((mtile & 1) << 1));
      pa = pbase + ((psh & vs) ? dgu : 0) + (rs * pstr + (vs >> psh) * 32 + (lane & 1) * 16);
      vob = rs * (F_ * 2) + vs * 32 + (lane & 1) * 16;
      tro = DMP + wid * 1024 + q4 * 128 + ((fq ^ ((q4 >> 1) | ((mtile & 1) << 1))) * 32) + pp * 8;
      const short one = fr == fq ? 0x3f80 : 0;
      sel = bf16x8{one, one, one, one, one, one, one, one};
      asm volatile("" : "+v"(tro), "+v"(vol), "+v"(sel), "+v"(ca), "+v"(cb), "+v"(sm), "+v"(sq));
      PIN_S(pf); PIN_S(pstep);
    }
    auto accum = [&](lds_cptr p, int ob) {
      ca = __builtin_amdgcn_mfma_f32_16x16x32_bf16(tr_frag(p), sel, ca, 0, 0, 0);
      if (wid == 0) cb = __builtin_amdgcn_mfma_f32_16x16x32_bf16(tr_frag(p + ob), sel, cb, 0, 0, 0);
    };
    auto xrow = [&](int i) {
      u64 a = (u64)(x + (size_t)srow * D_ + i * 256), b = (u64)(wo + (bid * 4 + wid) * 1024);
      PIN_S(a); PIN_S(b);
      asm volatile("global_load_dwordx4 %0, %1, %2" : "+v"(cd) : "v"(vol + (vol & (i < 4 ? 512 : 0)) * 3), "s"(i < 4 ? a : b) : "memory");
    };
    const int q32 = 32 * qstr;
    const char *sq0 = qb + q32, *sq1 = sq0 + dp1, *sg0 = gb + 32 * F_ * 2, *sg1 = sg0 + dgu, *sx = ab + (long)(kz + 128 - 32 * D_ * 2);
    u32x2 mp[2] = {__builtin_bit_cast(u32x2, mptr + q32), __builtin_bit_cast(u32x2, mptr + 2 * q32)};
    u64 cy[2];
    unsigned dst = shm_base + G1_STAGE + wm * 1024;
    int dbuf = G1_STAGE;
    auto stage = [&](int i0, int i1) __attribute__((always_inline)) {
#pragma unroll
        for (int i = i0; i < i1; ++i) {
          const int q = i >> 1, h = i & 1;
          const unsigned ld = dst + h * 4096 + (q < 5 ? 32768 + q * 8192 : (q - 5) * 8192);
          if (q < 2) glds16_s((q ? sq1 : sq0) + h * q32, voq, ld);
          else if (q == 2) glds16_v(__builtin_bit_cast(const char*, mp[h]), ld);
          else if (q < 5) glds16_s((q == 4 ? sg1 : sg0) + h * 32 * F_ * 2, vog, ld);
          else glds16_s(sx + ((q - 5) * 64 + h * 32) * D_ * 2, voa, ld);
        }
    };
#pragma unroll
    for (int a = 0; a < 4; ++a) { acc[a][9] = f32x4{0.f, 0.f, 0.f, 0.f}; asm volatile("" : "+v"(acc[a][9])); }
    if (!LD) {
      xrow(0);
      WAIT_V(1);
    }
    asm volatile("" : "+v"(aoff[0]), "+v"(aoff[1]), "+v"(boff[0]), "+v"(boff[1]));
    bf16x8 kp[2];
    auto body = [&](bool upd, bool keep = false, bool first = false) __attribute__((always_inline)) {
      lds_cptr sb = shm3;
      auto rdb = [&](int g, int i) {
        lds_cptr p = sb + boff[g & 1] + (g >> 1) * 8192 + (i >> 1) * 4096;
        const int s = ((g + 1) & 1) * 2 + (i >> 1);
        if (i & 1) bh[s] = tr_read(p + 512); else bl[s] = tr_read(p);
      };
      auto rda = [&](int ks, int mf) { (ks ? af1 : af0)[mf] = *(const __attribute__((address_space(3))) bf16x8*)(sb + aoff[ks] + mf * 2048); };
      SB0();
      __builtin_amdgcn_s_waitcnt(0xC07F);
#pragma unroll
      for (int gg = 0; gg < 10; ++gg) {
        const int g = (gg + 9) % 10;
#pragma unroll
        for (int t = 0; t < 8; ++t) {
          const bool two = t >= 4 && !(upd && g >= 5 && g <= 8);
          SB0();
          if ((!two || (t & 1)) && !(first && g == 9)) mm(g, two ? t - 1 : t, two, first);
          SB0();
          if (first && !LD && (g == 3 || g == 7) && t == 0) BAR();
          if (g == 9) {
            if (t == 0 || t == 2) { rdb(0, t); rdb(0, t + 1); }
            if (t & 1) { rda((t >> 1) & 1, t >> 1); rda(((t >> 1) & 1) ^ 1, t >> 1); }
          } else if (t < 4) rdb(g + 1, t);
          else if (keep && g == 7) {
            if (t == 5) { WAIT_V(0); kp[0] = tr_frag(shm3 + tro); }
            if (t == 7) kp[1] = tr_frag(shm3 + tro + 4096);
          } else if (upd) {
            if (g == 5 && t == 4) { sq0 += 2 * q32; PIN_S(sq0); }
            if (g == 5 && t == 5) { sq1 += 2 * q32; PIN_S(sq1); }
            if (g == 5 && t == 6) { sg0 += 64 * F_ * 2; PIN_S(sg0); }
            if (g == 6 && t == 4) { sg1 += 64 * F_ * 2; PIN_S(sg1); }
            if (g == 6 && t == 5) { sx += 128; PIN_S(sx); }
            if (g == 6 && t == 6) { dst -= dbuf; PIN_S(dst); }
            if (g == 7 && t == 4) asm volatile("v_add_co_u32 %0, %2, %4, %0\n\tv_add_co_u32 %1, %3, %4, %1" : "+v"(mp[0][0]), "+v"(mp[1][0]), "=&s"(cy[0]), "=&s"(cy[1]) : "s"(2 * q32));
            if (g == 7 && t == 6) asm volatile("v_addc_co_u32 %0, vcc, 0, %0, %2\n\tv_addc_co_u32 %1, vcc, 0, %1, %3" : "+v"(mp[0][1]), "+v"(mp[1][1]) : "s"(cy[0]), "s"(cy[1]) : "vcc");
            if (g == 8 && t == 4) { aoff[0] += dbuf; aoff[1] += dbuf; asm volatile("" : "+v"(aoff[0]), "+v"(aoff[1])); }
            if (g == 8 && t == 5) { boff[0] += dbuf; boff[1] += dbuf; asm volatile("" : "+v"(boff[0]), "+v"(boff[1])); }
            if (g == 8 && t == 6) { dbuf = -dbuf; PIN_S(dbuf); }
          }
        }
      }
      SB0();
    };
    TS1(0);
    if (LD) {
      stage(0, PA);
      WAIT_V(6 + PA);
      TS1(5);
      BARL();
      stage(PA, PB);
      WAIT_V(2 + PB);
      BAR();
      stage(PB, PC);
      WAIT_V(PC);
      BAR();
      stage(PC, 18);
      body(true, false, true);
      WAIT_V(0);
      BARL();
#pragma clang loop unroll(disable)
      for (int kt = 1; kt < NT - 1; ++kt) {
        if (__builtin_expect(kt == 5 + wid, 0)) prep(true);
        stage(0, 18);
        body(true);
        WAIT_V(0);
        BARL();
        SB0();
      }
      start2();
      body(false);
    } else {
      char* wsx;
      auto tile = [&](int kt, bool keep) __attribute__((always_inline)) {
        SB0();
        BARL();
        body(false, keep);
        if (keep) asm volatile("" : "+v"(kp[0]), "+v"(kp[1])); else WAIT_V(0);
      };
      auto next = [&]() __attribute__((always_inline)) {
        aoff[0] += dbuf; aoff[1] += dbuf; boff[0] += dbuf; boff[1] += dbuf;
        dbuf = -dbuf;
        asm volatile("" : "+v"(aoff[0]), "+v"(aoff[1]), "+v"(boff[0]), "+v"(boff[1]), "+v"(vol), "+v"(tro) :: "memory");
        wsx = ws;
        PIN_S(wsx);
      };
      auto share = [&]() {
        glds16_v(pa, shm_base + DMP + wid * 1024);
        if (wid == 0) glds16_s(gb + dgu, vob, shm_base + DMP + 4096);
      };
      auto kept = [&]() {
        ca = __builtin_amdgcn_mfma_f32_16x16x32_bf16(kp[0], sel, ca, 0, 0, 0);
        if (wid == 0) cb = __builtin_amdgcn_mfma_f32_16x16x32_bf16(kp[1], sel, cb, 0, 0, 0);
      };
      auto cpub = [&](u32x4 a, u32x4 b) {
        if ((vol & 0xc0) == 0) {
          const char* const cs = wsx + 101 * MB_ + ntile * 10240 + mtile * 256;
          const unsigned co = (vol & 0x30) * 4 + (vol >> 8) * 16;
          gst16(cs + wid * 2048, co, a);
          if (wid == 0) gst16(cs + 8192, co, b);
        }
      };
      auto side0 = [&](int kt) __attribute__((always_inline)) {
        if (kt < 2) {
          glds16_v(pa - 2 * pstep, shm_base + DMP + 7168);
          if (wid == 0) glds16_s(gb + dgu, vob - 128 * F_ * 2, shm_base + DMP + 7168);
        }
        share();
        u32x4 xw;
        asm volatile("v_mov_b32 %0, %4\n\tv_mov_b32 %1, %5\n\tv_mov_b32 %2, %6\n\tv_mov_b32 %3, %7" : "=&v"(xw[0]), "=&v"(xw[1]), "=&v"(xw[2]), "=&v"(xw[3])
                     : "v"(cd[0]), "v"(cd[1]), "v"(cd[2]), "v"(cd[3]));
        xrow(kt + 1);
        next();
        if (kt) kept();
        pa += pstep; vob += 64 * F_ * 2;
        accum(shm3 + tro + ((kt & 1) * G1_STAGE + 32768 + mtile * 1024 + wid * 7168 - DMP), 4 * 8192);
#pragma unroll
        for (int i = 0; i < 4; ++i) {
          float lo, hi;
          asm volatile("v_lshlrev_b32 %2, 16, %4\n\tv_and_b32 %3, 0xffff0000, %4\n\tv_add_f32 %0, %0, %2\n\tv_fmac_f32 %1, %2, %2\n\t"
                       "v_add_f32 %0, %0, %3\n\tv_fmac_f32 %1, %3, %3" : "+v"(sm), "+v"(sq), "=&v"(lo), "=&v"(hi) : "v"(xw[i]));
        }
      };
      BARL();
      body(false, false, true);
      WAIT_V(0);
      side0(0);
#pragma clang loop unroll(disable)
      for (int kt = 1; kt < 4; ++kt) {
        tile(kt, true);
        side0(kt);
      }
#pragma clang loop unroll(disable)
      for (int kt = 4; kt < 12; ++kt) {
        tile(kt, true);
        share();
        glds16_s(pf, vol, shm_base + DMP + 7168);
        next();
        kept();
        pa += pstep; vob += 64 * F_ * 2;
        pf = kt == 10 ? (const char*)wo + (bid * 4 + wid) * 2048 + 1024 : pf + 1024;
        if (kt < 8) {
          const unsigned q1 = bid * 4 + wid + (kt - 5) * 1024, q0 = q1 - 1024;
          if (q0 < 1152) gst16(wsx + 98 * MB_ + (q0 & 1) * 589824 + (q0 >> 1) * 1024, vol, *(const u32x4*)(shm + AUX + wid * 1024 + vol));
          if (q1 < 1152) {
            const char* src = (const char*)((q1 >> 1) < 448 ? wd + (size_t)(q1 >> 1) * 8 * D_ : wo + (size_t)((q1 >> 1) - 448) * 8 * D_);
            src += ((3 - ((unsigned)(uintptr_t)src >> 7)) & 7) * 128 + (q1 & 1) * 1024;
            glds16_s(src, (vol >> 7) * 2048 + (vol & 0x70), shm_base + AUX + wid * 1024);
          }
          u32x4 bk;
          asm volatile("v_mov_b32 %0, %1" : "=v"(bk[0]) : "s"(BLANK));
          bk[1] = bk[0]; bk[2] = bk[0]; bk[3] = bk[0];
          gst16(wsx + 64 * MB_ + bid * 16384 + wid * 4096 + (kt - 4) * 1024, vol, bk);
          if (tya && (kt == 4 || kt == 7)) gst16(wsx + 4 * MB_ + (mtile * 32 + ntile) * 4096 + wid * 2048 + (kt & 1) * 1024, vol, bk);
          if (kt == 4) cpub(bk, bk);
        } else {
          if (kt == 8) {
            const float ml = sm * (1.0f / 32), mean = half_sum(sm) * (1.0f / D_), dm = ml - mean;
            const float q = __builtin_fmaf(dm * 32.0f, dm, __builtin_fmaf(-ml, sm, sq));
            const unsigned ps = pack_stats(mean, fmaxf(half_sum(q) * (1.0f / D_), 0.f));
            const u64 now = __builtin_amdgcn_s_memrealtime();
            if ((vol & 0x1f0) == 0) gst16(wsx + 97 * MB_ + srow * 16, vol >> 5, u32x4{ps, (unsigned)now, ps, (unsigned)(now >> 32)});
          }
          if (kt == 9) {
            if (wid == 0) {
              u64 now;
              asm volatile("s_memrealtime %0\n\ts_waitcnt lgkmcnt(0)" : "=s"(now) :: "memory");
              if (vol == 0) asm volatile("global_store_dwordx2 %0, %1, %2 sc1" :: "v"(vol), "v"(now), "s"(wsx + 100 * MB_ + 131072 + bid * 8) : "memory");
            }
            if (wid == 2) glds16_s1(wsx + 102 * MB_ + (bid & ~1) * 512, vol, shm_base + DMP + 7168);
          }
        }
      }
      asm volatile("" :: "v"(cd));
      if (wid >= 2) glds16_s1(wsx + 100 * MB_ + 131072 + (wid - 2) * 1024, vol, shm_base + DMP + 3072 + wid * 1024);
      tile(12, true);
      next();
      kept();
      const int lane = vol >> 4;
      glds16_s1(wsx + 97 * MB_ + (m0 + wid * 64) * 16, ((lane & 32) + 2 * (lane & 15) + ((lane >> 4) & 1)) * 16, shm_base + AUX + wid * 1024);
      asm volatile("s_nop 7\n\ts_nop 3" : "+v"(ca), "+v"(cb));
      cpub(__builtin_bit_cast(u32x4, ca), __builtin_bit_cast(u32x4, cb));
      if (wid >= 2) {
        u32x4 c4 = *(const u32x4*)(shm + DMP + 3072 + wid * 1024 + vol);
        bool ok = __all(fresh(((u64)c4[1] << 32) | c4[0], t_own) & fresh(((u64)c4[3] << 32) | c4[2], t_own));
        while (__builtin_expect(!ok, 0) && __builtin_amdgcn_s_memrealtime() - t_own < TIMEOUT_ / 10) {
          const __amdgpu_buffer_rsrc_t rs = __builtin_amdgcn_make_buffer_rsrc((void*)(wsx + 100 * MB_ + 131072), 0, 2048, 0x00020000);
          asm volatile("" ::: "memory");
          c4 = __builtin_bit_cast(u32x4, __builtin_amdgcn_raw_buffer_load_b128(rs, (wid - 2) * 1024 + vol, 0, 16));
          ok = __all(fresh(((u64)c4[1] << 32) | c4[0], t_own) & fresh(((u64)c4[3] << 32) | c4[2], t_own));
        }
        *(unsigned*)(shm + CNT - 4 + wid * 4) = ok ? 1u : 0u;
      }
      tile(13, false);
      next();
      u32x4 cw[4];
      auto cgather = [&]() {
        const __amdgpu_buffer_rsrc_t rc = __builtin_amdgcn_make_buffer_rsrc((void*)(wsx + 101 * MB_ + ntile * 10240), 0, 5 * 2048, 0x00020000);
        const unsigned vcs = (vol < 512 ? wid * 2048 : 8192) + (vol & 256) * 4 + (vol & 0xf0);
#pragma unroll
        for (int i = 0; i < 4; ++i) cw[i] = __builtin_bit_cast(u32x4, __builtin_amdgcn_raw_buffer_load_b128(rc, vcs + i * 256, 0, 16));
      };
      {
        const int ln = wid * 64 + (vol >> 4);
        u32x4 gr = *(const u32x4*)(shm + AUX + ln * 16);
        if (__builtin_expect(!__all((gr[0] == gr[2]) & fresh(((u64)gr[3] << 32) | gr[1], t_own)), 0)) {
          const __amdgpu_buffer_rsrc_t rs = __builtin_amdgcn_make_buffer_rsrc((void*)(wsx + 97 * MB_ + m0 * 16), 0, 4096, 0x00020000);
          const int prow = (ln & 0xe0) + 2 * (ln & 15) + ((ln >> 4) & 1);
          for (;;) {
            gr = __builtin_bit_cast(u32x4, __builtin_amdgcn_raw_buffer_load_b128(rs, prow * 16, 0, 16));
            if (__all((gr[0] == gr[2]) & fresh(((u64)gr[3] << 32) | gr[1], t_own))) break;
            if (__builtin_amdgcn_s_memrealtime() - t_own > TIMEOUT_) { gr[0] = row_stats_serial(x + (size_t)(m0 + prow) * D_); break; }
            __builtin_amdgcn_s_sleep(2);
          }
        }
        cgather();
        const float nm = -lo_f(gr[0]), dn = hi_f(gr[0]), r = __builtin_amdgcn_rcpf(dn);
        auto mul = [](float a, float b) { float c; asm("v_mul_f32 %0, %1, %2" : "=v"(c) : "v"(a), "v"(b)); return c; };
        const f32x4 fc = {nm, -2.3022081986f * r, mul(mul(mul(-0.10294324f, r), r), r), mul(dn, dn)};
        *(f32x4*)(shm + AUX + ln * 16) = fc;
        float* const tw = (float*)(shm + 2 * G1_STAGE + 4096 + wid * 768) + ln;
#pragma unroll
        for (int i = 0; i < 4; ++i) tw[i * 64] = fc[i];
        ((float*)(shm + AUX + 4096))[384 + ln] = r;
        cok = __builtin_amdgcn_readfirstlane(cnt[1] & cnt[2]) != 0u;
        tile2(0);
        wy = wsetup(wd, 0, vol >> 4, cok);
      }
      tile(14, false);
      next();
      {
        while (__builtin_expect(blank(cw) && cok, 0)) {
          __builtin_amdgcn_s_sleep(2);
          asm volatile("" ::: "memory");
          cgather();
        }
        f32x4 t;
#pragma unroll
        for (int i = 0; i < 4; ++i) {
          float a, b;
          asm("v_add_f32 %0, %2, %3\n\tv_add_f32 %1, %4, %5\n\tv_add_f32 %0, %0, %1" : "=&v"(a), "=&v"(b) : "v"(cw[0][i]), "v"(cw[1][i]), "v"(cw[2][i]), "v"(cw[3][i]));
          const auto r2 = __builtin_amdgcn_permlane16_swap(__float_as_uint(a), __float_as_uint(a), false, false);
          asm("v_add_f32 %0, %1, %2" : "=v"(t[i]) : "v"(r2[0]), "v"(r2[1]));
        }
        const int l = vol >> 4;
        if (__builtin_expect(!cok, 0)) {
          const int gq = (l >> 2) & 3;
          const char* pc = l < 32 ? pbase + ((psh & gq) ? dgu : 0) + (gq >> psh) * 32 + (l & 3) * 8 : gb + dgu + (l & 15) * 8;
          t = f32x4{0.f, 0.f, 0.f, 0.f};
#pragma unroll 1
          for (int k = 0; k < D_; ++k) { const u32x2 w = *(const __attribute__((address_space(1))) u32x2*)(pc + (size_t)k * (l < 32 ? pstr : F_ * 2)); t += f32x4{lo_f(w[0]), hi_f(w[0]), lo_f(w[1]), hi_f(w[1])}; }
        }
        if (l < 32 || wid == 0)
          *(f32x4*)((float*)(shm + AUX + 4096) + ((l >> 3) & 1) * 192 + (l < 32 ? wid : 4) * 32 + (l & 7) * 4) = t;
        __builtin_amdgcn_s_setprio(2);
      }
      SB0();
      BARL();
      body(false);
    }
#pragma unroll
    for (int t = 0; t < 8; t += 2) mm(9, t, true);
    asm volatile("s_nop 7\n\ts_nop 3");
    SB0();
    TS1(3);
    if (!LD) {
#pragma unroll
      for (int i = 0; i < 3; ++i) {
        const unsigned r = ((role * 4 + (i & 1) * 2) * 14 + (i < 2 ? 10 : 0) + (wm & 2)) * 32;
        sw[0] = wy.wb[0] + (r << wy.sh[0]); sw[1] = wy.wb[1] + (r << wy.sh[1]);
        asm volatile(G2_W :: [l] "s"((i + 1) * 16384), G2_WOP(wy) : "memory", "scc", "m0");
      }
    } else {
      __builtin_amdgcn_s_setprio(3);
      WAIT_V(0);
    }
  };
  if (wid >= 4) kloop(std::integral_constant<int, 1>{});
  else kloop(std::integral_constant<int, 0>{});
  __builtin_amdgcn_sched_barrier(0);
  int tz = threadIdx.x;
  asm volatile("" : "+v"(tz));
  __builtin_amdgcn_sched_barrier(0);
  const int li = tz & 15, lq = (tz >> 4) & 3, ln = tz & 63;
  const __amdgpu_buffer_rsrc_t ra = __builtin_amdgcn_make_buffer_rsrc((void*)abuf, 0, 16 * 72 * 16384, 0x00020000);
  float* const cfin = (float*)(shm + AUX + 4096);
  float* const svr = cfin + 384;
  float* const tb = (float*)(shm + 2 * G1_STAGE + 4096 + wm * 1024);
  auto signal = [&](unsigned v) {
    unsigned a, b;
    asm volatile("v_mov_b32 %0, %2\n\tv_mov_b32 %1, %3\n\ts_mov_b64 exec, 1\n\tds_add_u32 %0, %1\n\ts_mov_b64 exec, -1"
                 : "=&v"(a), "=&v"(b) : "s"(shm_base + CNT), "s"(v) : "memory");
  };
  auto await = [&](unsigned mask, unsigned val) {
    unsigned a, t;
    asm volatile("v_mov_b32 %0, %2\n1:\n\tds_read_b32 %1, %0\n\ts_waitcnt lgkmcnt(0)\n\tv_readfirstlane_b32 vcc_lo, %1\n\ts_and_b32 vcc_lo, vcc_lo, %3\n\t"
                 "s_cmp_eq_u32 vcc_lo, %4\n\ts_cbranch_scc1 2f\n\ts_sleep 1\n\ts_branch 1b\n2:" : "=&v"(a), "=&v"(t) : "s"(shm_base + CNT), "s"(mask), "s"(val) : "memory", "scc", "vcc");
  };
  auto client = [&](int l) { const int m128 = mtile * 2 + ((l >> 4) & 1); return (((l & 7) * 4 + (m128 & 3)) * 8) + ((m128 >> 2) ^ ((l & 8) ? 5 : 0)); };
  auto notify = [&](int l0, int nl) __attribute__((always_inline)) {
    u64 now;
    asm volatile("s_memrealtime %0\n\ts_waitcnt lgkmcnt(0)" : "=s"(now) :: "memory");
    int l = threadIdx.x;
    asm volatile("" : "+v"(l));
    l &= 63;
    const __amdgpu_buffer_rsrc_t rd1 = __builtin_amdgcn_make_buffer_rsrc((void*)(done1 + ntile), 0, 256 * 512, 0x00020000);
    if (l < nl) __builtin_amdgcn_raw_buffer_store_b64(u32x2{(unsigned)now, (unsigned)(now >> 32)}, rd1, client(l0 + l) * 512, 0, 16);
  };
  const int xo = ((xcd & 1) << 1) | ((xcd >> 1) & 1), te = (xcd >> 2) * 28 + 6 * (xo >> 1) + (xo & 1), tl = (xcd >> 2) * 28 + 12 + 4 * xo;
  const u64 te0 = __builtin_amdgcn_s_memrealtime();
  const __amdgpu_buffer_rsrc_t re = __builtin_amdgcn_make_buffer_rsrc((void*)(done1 + client((wm >> 1) * 16 + 8 - (xcd & 4) * 2) * 64 + 32 + (ntile & 15) * 2 + (wm & 1)),
                                                                      0, 7 * 16384 + 8, 0x00020000);
  auto early = [&]() {
    __builtin_amdgcn_raw_buffer_store_b64(u32x2{(unsigned)te0, (unsigned)(te0 >> 32)}, re, ln * 16384, 0, 16);
  };
  signal(1u << (8 * wc));
  if (wc) {
    await(0xffffu, 0x404u);
#pragma unroll
    for (int f = 0; f < 4; ++f) *(u32x4*)(shm + G2_X + wm * 4096 + f * 1024 + ln * 16) = xq[f];
    SB0();
    __builtin_amdgcn_s_setprio(1);
  }
  TS1(4);
  f32x4 rf[4];
#pragma unroll
  for (int mf = 0; mf < 4; ++mf) rf[mf] = *(const f32x4*)(shm + AUX + (wm * 64 + mf * 16 + li) * 16);
  const float* const cl = cfin + wc * 192 + lq * 4;
  const int sbase = ((mtile * 2 + (wm >> 1)) * 72 * 8 + (wm & 1) * 4) * 2048;
  auto putf = [&](int kg, int mf, u32x4 v) {
    __builtin_amdgcn_raw_buffer_store_b128(v, ra, ln * 16 + sbase + ((kg >> 1) * 8 + mf) * 2048 + (kg & 1) * 1024, 0, 16);
  };
  auto gelp = [&](f32x2 g, f32x2 u, f32x2 cg, f32x2 cu, f32x2 m2, f32x2 k12, f32x2 k22, f32x2 d2) {
    const f32x2 tg = __builtin_elementwise_fma(m2, cg, g), tu = __builtin_elementwise_fma(m2, cu, u);
    const f32x2 arg = tg * __builtin_elementwise_fma(tg * tg, k22, k12);
    const f32x2 dn = __builtin_elementwise_fma(f32x2{__builtin_amdgcn_exp2f(arg[0]), __builtin_amdgcn_exp2f(arg[1])}, d2, d2);
    const f32x2 r = (tg * tu) * f32x2{__builtin_amdgcn_rcpf(dn[0]), __builtin_amdgcn_rcpf(dn[1])};
    return pack2(r[0], r[1]);
  };
  auto gel2 = [&](int p, int kg) {
    f32x4 cg[2], cu[2];
#pragma unroll
    for (int j = 0; j < 2; ++j) { cg[j] = *(const f32x4*)(cl + p * 32 + j * 16); cu[j] = *(const f32x4*)(cl + p * 32 + 32 + j * 16); }
#pragma unroll
    for (int mf = 0; mf < 4; ++mf) {
      const f32x2 m2 = {rf[mf][0], rf[mf][0]}, k12 = {rf[mf][1], rf[mf][1]}, k22 = {rf[mf][2], rf[mf][2]}, d2 = {rf[mf][3], rf[mf][3]};
      u32x4 o;
#pragma unroll
      for (int i = 0; i < 4; ++i) {
        const int j = i >> 1, e = (i & 1) * 2;
        const f32x4 g = acc[mf][p * 2 + j], u = acc[mf][p * 2 + 2 + j];
        o[i] = gelp(f32x2{g[e], g[e + 1]}, f32x2{u[e], u[e + 1]}, f32x2{cg[j][e], cg[j][e + 1]}, f32x2{cu[j][e], cu[j][e + 1]}, m2, k12, k22, d2);
      }
      putf(kg, mf, o);
    }
  };
  int tsel = ntile & 1;
  asm volatile("" : "+s"(tsel));
  if (tsel != 0) {
    const int tq = (wc ? tl : te + 2 + (xo & 1)) + he;
    gel2(0, 2 * tq);
    gel2(3, 2 * tq + 1);
    const float cg = cfin[wc * 192 + 64 + li], cu = cfin[wc * 192 + 80 + li];
#pragma unroll
    for (int mf = 0; mf < 4; ++mf) {
      f32x4 t[4];
#pragma unroll
      for (int i = 0; i < 4; ++i) t[i] = *(const f32x4*)(tb + i * 64 + mf * 16 + lq * 4);
      const f32x4 g = acc[mf][4], u = acc[mf][5];
      u32x2 o;
#pragma unroll
      for (int e = 0; e < 4; e += 2)
        o[e >> 1] = gelp(f32x2{g[e], g[e + 1]}, f32x2{u[e], u[e + 1]}, f32x2{cg, cg}, f32x2{cu, cu}, f32x2{t[0][e], t[0][e + 1]},
                         f32x2{t[1][e], t[1][e + 1]}, f32x2{t[2][e], t[2][e + 1]}, f32x2{t[3][e], t[3][e + 1]});
      *(u32x2*)(shm + wm * 4096 + mf * 1024 + wc * 512 + li * 32 + lq * 8) = o;
      if (mf == 2 && wc == 0) {
        TS1(1);
        WAIT_V(0);
        TS1(6);
        early();
      }
    }
    if (wc == 0) {
      signal(0x10000u << wid);
    } else {
      await(0x1000u << wid, 0x1000u << wid);
#pragma unroll
      for (int mf = 0; mf < 4; ++mf) putf(2 * (tl + 2) + he, mf, __builtin_bit_cast(u32x4, tr_frag(shm3 + wm * 4096 + mf * 1024 + ln * 8)));
    }
  } else {
    float rd[4];
#pragma unroll
    for (int mf = 0; mf < 4; ++mf) rd[mf] = svr[wm * 64 + mf * 16 + li];
    auto cvq = [&](int p, int mf, const f32x4& c0, const f32x4& c1) {
      u32x4 o;
#pragma unroll
      for (int j = 0; j < 2; ++j) {
        const f32x4 a = acc[mf][p * 2 + j], c4 = j ? c1 : c0;
        const f32x2 m2 = {rf[mf][0], rf[mf][0]}, r2 = {rd[mf], rd[mf]};
        f32x2 t0 = __builtin_elementwise_fma(m2, f32x2{c4[0], c4[1]}, f32x2{a[0], a[1]});
        f32x2 t1 = __builtin_elementwise_fma(m2, f32x2{c4[2], c4[3]}, f32x2{a[2], a[3]});
        if (p) { t0 *= r2; t1 *= r2; }
        o[2 * j] = pack2(t0[0], t0[1]); o[2 * j + 1] = pack2(t1[0], t1[1]);
      }
      return o;
    };
    const float cv0 = cfin[wc * 192 + 64 + li], cv1 = cfin[wc * 192 + 80 + li];
    auto cvv = [&](int mf) {
      const f32x4 n4 = *(const f32x4*)(tb + mf * 16 + lq * 4), r4 = *(const f32x4*)(svr + wm * 64 + mf * 16 + lq * 4);
      u32x4 o;
#pragma unroll
      for (int j = 0; j < 2; ++j) {
        const f32x4 a = acc[mf][4 + j];
        const f32x2 c2 = {j ? cv1 : cv0, j ? cv1 : cv0};
        const f32x2 t0 = __builtin_elementwise_fma(f32x2{n4[0], n4[1]}, c2, f32x2{a[0], a[1]}) * f32x2{r4[0], r4[1]};
        const f32x2 t1 = __builtin_elementwise_fma(f32x2{n4[2], n4[3]}, c2, f32x2{a[2], a[3]}) * f32x2{r4[2], r4[3]};
        o[2 * j] = pack2(t0[0], t0[1]); o[2 * j + 1] = pack2(t1[0], t1[1]);
      }
      return o;
    };
    u32x4 kx[4], vx[4], qx[4], hx[4];
    const bf16x4 ones = {0x3f80, 0x3f80, 0x3f80, 0x3f80};
    const f32x4 ck0 = *(const f32x4*)(cl + 32), ck1 = *(const f32x4*)(cl + 48);
    kx[2] = cvq(1, 2, ck0, ck1); kx[3] = cvq(1, 3, ck0, ck1);
    vx[2] = cvv(2); vx[3] = cvv(3);
    auto exo = [](int e) { return e * 4096 + (e >> 2) * (G2_A - 16384); };
    const bool needh = wm == 0 && mtile > 0;
    const __amdgpu_buffer_rsrc_t rh = __builtin_amdgcn_make_buffer_rsrc((void*)((char*)halo + ((needh ? mtile - 1 : mtile) * 32 + ntile + wc) * 4096), 0,
                                                                        needh || wm == 3 ? 4096 : 0, 0x00020000);
    if (wm < 3) {
      char* const ex = shm + exo(wc * 3 + wm) + ln * 16;
      *(u32x4*)ex = kx[2]; *(u32x4*)(ex + 1024) = kx[3]; *(u32x4*)(ex + 2048) = vx[2]; *(u32x4*)(ex + 3072) = vx[3];
      signal(0x10000u << wid);
    } else {
#pragma unroll
      for (int r = 0; r < 4; ++r) __builtin_amdgcn_raw_buffer_store_b128(r < 2 ? kx[2 + r] : vx[r], rh, ln * 16 + r * 1024, 0, 16);
    }
    auto hload = [&]() {
#pragma unroll
      for (int r = 0; r < 4; ++r) hx[r] = __builtin_bit_cast(u32x4, __builtin_amdgcn_raw_buffer_load_b128(rh, ln * 16 + r * 1024, 0, 16));
    };
    SB0();
    gel2(3, 2 * (wc ? tl + 3 : te) + he);
    SB0();
    if (wm == 0) hload();
    SB0();
    kx[0] = cvq(1, 0, ck0, ck1); kx[1] = cvq(1, 1, ck0, ck1);
    vx[0] = cvv(0); vx[1] = cvv(1);
    {
      const f32x4 cq0 = *(const f32x4*)(cl), cq1 = *(const f32x4*)(cl + 16);
#pragma unroll
      for (int mf = 0; mf < 4; ++mf) qx[mf] = cvq(0, mf, cq0, cq1);
    }
    f32x4 mk0, mk1;
    u32x2 pm;
#pragma unroll
    for (int j = 0; j < 4; ++j) {
      const int d = 4 * lq + j - li;
      mk0[j] = d >= 9 ? 0.f : -1.0e30f;
      mk1[j] = (unsigned)(d + 7) <= 7u ? 0.f : -1.0e30f;
    }
#pragma unroll
    for (int j = 0; j < 2; ++j) pm[j] = (4 * lq + 2 * j > li ? 0xffffu : 0u) | (4 * lq + 2 * j + 1 > li ? 0xffff0000u : 0u);
    auto max3 = [](float a, float b, float c) { float r; asm("v_max3_f32 %0, %1, %2, %3" : "=v"(r) : "v"(a), "v"(b), "v"(c)); return r; };
    auto max2 = [](float a, float b) { float r; asm("v_max_f32 %0, %1, %2" : "=v"(r) : "v"(a), "v"(b)); return r; };
    asm volatile("" : "+v"(kx[0]), "+v"(kx[1]), "+v"(vx[0]), "+v"(vx[1]), "+v"(qx[0]), "+v"(qx[1]), "+v"(qx[2]), "+v"(qx[3]), "+v"(mk0), "+v"(mk1), "+v"(pm));
    TS1(1);
    if (wc == 0) {
      if (wm == 0) WAIT_V(4); else WAIT_V(0);
      early();
    }
    if (wm > 0) {
      await(0x8000u << wid, 0x8000u << wid);
#pragma unroll
      for (int i = 0; i < 4; ++i) hx[i] = *(const u32x4*)(shm + exo(wc * 3 + wm - 1) + i * 1024 + ln * 16);
    } else if (__builtin_expect(cok || !needh, 1)) {
      while (__builtin_expect(blank(hx), 0)) {
        __builtin_amdgcn_s_sleep(1);
        asm volatile("" ::: "memory");
        hload();
      }
    } else {
      await(0xffffu, 0x404u);
      char* const fb = shm + exo(6 + wc) + ln * 16;
#pragma unroll 1
      for (int i = 0; i < 32; ++i) {
        const int rec = i >> 3, e = i & 7;
        const int row = m0 - 32 + 2 * (rec < 2 ? li : lq * 4 + (e & 3)) + (rec & 1);
        const int col = (rec < 2 ? D_ + lq * 4 + (e & 3) : 2 * D_ + li) + (ntile + wc) * 32 + (e >> 2) * 16;
        const u16* xr = x + (size_t)row * D_;
        const unsigned stt = row_stats_serial(xr);
        const float mb = lo_f(stt);
        float a = 0.f;
#pragma unroll 1
        for (int k = 0; k < D_; ++k) a += (bf2f(xr[k]) - mb) * bf2f(wqkv[(size_t)k * 3072 + col]);
        *(u16*)(fb + rec * 1024 + e * 2) = f2bf(a / hi_f(stt));
      }
#pragma unroll
      for (int i = 0; i < 4; ++i) hx[i] = *(const u32x4*)(fb + i * 1024);
    }
    TS1(6);
    {
      const f32x4 z = {0.f, 0.f, 0.f, 0.f};
      const float mz = mtile == 0 && wm == 0 ? -1.0e30f : 0.f;
      const f32x4 mkf = mk0 + f32x4{mz, mz, mz, mz};
      f32x4 s0[4], s1[4];
#pragma unroll
      for (int i = 0; i < 4; ++i) {
        const int n = i ^ 2;
        const bf16x8 qf = __builtin_bit_cast(bf16x8, qx[n]);
        s1[n] = __builtin_amdgcn_mfma_f32_16x16x32_bf16(__builtin_bit_cast(bf16x8, kx[n]), qf, mk1, 0, 0, 0);
        s0[n] = __builtin_amdgcn_mfma_f32_16x16x32_bf16(__builtin_bit_cast(bf16x8, n < 2 ? hx[n] : kx[n - 2]), qf, n < 2 ? mkf : mk0, 0, 0, 0);
      }
      SB0();
      asm volatile("s_nop 7\n\ts_nop 3" : "+v"(s0[0]), "+v"(s1[0]), "+v"(s0[1]), "+v"(s1[1]), "+v"(s0[2]), "+v"(s1[2]), "+v"(s0[3]), "+v"(s1[3]));
      float mx[4];
#pragma unroll
      for (int n = 0; n < 4; ++n)
#pragma unroll
        for (int j = 0; j < 4; ++j) s0[n][j] = max2(s0[n][j], s1[n][j]);
#pragma unroll
      for (int n = 0; n < 4; ++n) mx[n] = max3(s0[n][0], s0[n][1], s0[n][2]);
#pragma unroll
      for (int n = 0; n < 4; ++n) mx[n] = max2(mx[n], s0[n][3]);
#pragma unroll
      for (int n = 0; n < 4; ++n) { auto r = __builtin_amdgcn_permlane16_swap(__float_as_uint(mx[n]), __float_as_uint(mx[n]), false, false);
        mx[n] = max2(__uint_as_float(r[0]), __uint_as_float(r[1])); }
#pragma unroll
      for (int n = 0; n < 4; ++n) { auto r = __builtin_amdgcn_permlane32_swap(__float_as_uint(mx[n]), __float_as_uint(mx[n]), false, false);
        mx[n] = max2(__uint_as_float(r[0]), __uint_as_float(r[1])); }
      SB0();
      constexpr float cs2 = 0.17677669529663687f * 1.4426950408889634f;
      f32x4 os[4], o0[4], o1[4];
#pragma unroll
      for (int n = 0; n < 4; ++n) {
        const float c = cs2 * rd[n], nm = -mx[n] * c;
        const f32x4 ar = __builtin_elementwise_fma(s0[n], f32x4{c, c, c, c}, f32x4{nm, nm, nm, nm});
        const u32x2 pk = {pack2(__builtin_amdgcn_exp2f(ar[0]), __builtin_amdgcn_exp2f(ar[1])), pack2(__builtin_amdgcn_exp2f(ar[2]), __builtin_amdgcn_exp2f(ar[3]))};
        const bf16x4 pa = __builtin_bit_cast(bf16x4, pk), p0 = __builtin_bit_cast(bf16x4, pk & pm), p1 = __builtin_bit_cast(bf16x4, pk & ~pm);
        const u32x4 vp = n < 2 ? hx[2 + n] : vx[n - 2];
        auto half = [](const u32x4& v, int h) { return __builtin_bit_cast(bf16x4, u32x2{v[2 * h], v[2 * h + 1]}); };
        os[n] = __builtin_amdgcn_mfma_f32_16x16x16bf16_1k(ones, pa, z, 0, 0, 0);
        o0[n] = __builtin_amdgcn_mfma_f32_16x16x16bf16_1k(half(vx[n], 0), p1, __builtin_amdgcn_mfma_f32_16x16x16bf16_1k(half(vp, 0), p0, z, 0, 0, 0), 0, 0, 0);
        o1[n] = __builtin_amdgcn_mfma_f32_16x16x16bf16_1k(half(vx[n], 1), p1, __builtin_amdgcn_mfma_f32_16x16x16bf16_1k(half(vp, 1), p0, z, 0, 0, 0), 0, 0, 0);
      }
#pragma unroll
      for (int n = 0; n < 4; ++n) {
        const float inv = __builtin_amdgcn_rcpf(os[n][0]);
        const f32x4 n0 = o0[n] * inv, n1 = o1[n] * inv;
        putf(112 + ntile + wc, n, u32x4{pack2(n0[0], n0[1]), pack2(n0[2], n0[3]), pack2(n1[0], n1[1]), pack2(n1[2], n1[3])});
      }
    }
  }
  TS1(2);
  {
  __builtin_amdgcn_sched_barrier(0);
  int tid = threadIdx.x;
  asm volatile("" : "+v"(tid));
  __builtin_amdgcn_sched_barrier(0);
  const int lane = tid & 63;
  const int wm = wid >> 1, wn = wid & 1;
  const int m0 = mt2 * 128, n0 = nt2 * 128;
  const int ny = (xcd & 1) ? NY - DLT : NY + DLT;
  const unsigned va = lane * 16;
  auto stage_y = [&]() {
    asm volatile("s_add_u32 m0, %[l], %[oa]\n\ts_nop 0\n\tglobal_load_lds_dwordx4 %[va], %[a] sc1\n\tglobal_load_lds_dwordx4 %[va], %[a] offset:1024 sc1\n\t"
                 "global_load_lds_dwordx4 %[va], %[a] offset:2048 sc1\n\tglobal_load_lds_dwordx4 %[va], %[a] offset:3072 sc1\n\t" G2_W
                 :: [l] "s"(sl), [oa] "s"(shm_base + G2_A + (p ^ role) * 4096), [a] "s"(sa), [va] "v"(va), G2_WOP(wy) : "memory", "scc", "m0");
    ++st;
    sa += 16384;
    sl = (sl + 16384) & 65535;
    wsel();
  };
  u64 ready = 0;
  const u64* const mbox = &done1[bid * 64 + lane];
  auto seen = [&](u64 v) __attribute__((always_inline)) {
    const u64 r = __ballot(fresh(v, t_own));
    u64 d;
    asm("s_bitreplicate_b64_b32 %0, %1" : "=s"(d) : "s"((unsigned)r >> (16 * role)));
    ready |= r | (d << 32);
  };
  auto need = [&](u64 mask, bool renew) __attribute__((always_inline)) {
    if ((ready & mask) != mask) {
      seen(__hip_atomic_load(mbox, RLX_AGENT));
      while ((ready & mask) != mask) {
        __builtin_amdgcn_s_sleep(2);
        if (renew && !cok) notify(0, 32);
        seen(__hip_atomic_load(mbox, RLX_AGENT));
      }
    }
    asm volatile("" ::: "memory");
  };
  u64 fl = 0;
  auto peek = [&]() { asm volatile("global_load_dwordx2 %0, %1, %2 sc1" : "+v"(fl) : "v"(va >> 1), "s"(done1 + bid * 64) : "memory"); };
  auto check = [&](u64 mask, bool renew) __attribute__((always_inline)) {
    asm volatile("" : "+v"(fl));
    seen(fl);
    if (__builtin_expect((ready & mask) != mask, 0)) need(mask, renew);
  };
  if (wid >= 4) {
    TS(4);
    __builtin_amdgcn_s_setprio(0);
    BARL();
    stage_y();
    stage_y();
    BAR();
    if (p == 1) peek();
    stage_y();
    WAIT_V(16);
    BAR();
    if (p == 0) notify(0, 32);
#define G2_A2 "global_load_lds_dwordx4 %[va], %[a] sc1\n\tglobal_load_lds_dwordx4 %[va], %[a] offset:1024 sc1"
#define G2_A4 G2_A2 "\n\tglobal_load_lds_dwordx4 %[va], %[a] offset:2048 sc1\n\tglobal_load_lds_dwordx4 %[va], %[a] offset:3072 sc1"
#define G2_SYNC "\n\ts_mov_b32 m0, %[la]\n\ts_waitcnt vmcnt(%[n])\n\ts_barrier\n\t"
    const unsigned oy = shm_base + G2_A + (p ^ role) * 4096, os = shm_base + G2_A + (p >> 1) * 8192 + (p & 1) * 2048;
    asm volatile("s_mov_b32 m0, %[la]\n\ts_nop 0\n\t" G2_A4 :: [la] "s"(sl + oy), [a] "s"(sa), [va] "v"(va) : "memory", "m0");
    auto step = [&](long adv = 16384) __attribute__((always_inline)) {
      wsel();
      ++st;
      sa += adv;
      const unsigned sn = (sl + 16384) & 65535;
      asm volatile(G2_W G2_SYNC G2_A4 :: [l] "s"(sl), [la] "s"(sn + oy), [n] "n"(16), [a] "s"(sa), [va] "v"(va), G2_WOP(wy) : "memory", "scc", "m0");
      sl = sn;
    };
    auto chk1 = [&](u64 mask) __attribute__((always_inline)) { if (p == 1) { WAIT_V(16); check(mask, true); } };
    step();
    chk1(0xcc00cc00ull << 32);
    step(); step();
    if (p == 1) peek();
    step(); step();
    chk1(0x0f0fu << (16 * role));
#pragma unroll 1
    for (int g = 7; g < 13; ++g) step();
    if (p == 1) peek();
    step(); step();
    constexpr unsigned BOTH = 0xf555f555u;
    chk1((0xffffu << (16 * role)) | BOTH);
#pragma unroll 1
    for (int g = 15; g < min(ny, NY) - 5; ++g) step();
    if (ny > NY) {
      step(((1 - 2 * role) * NY + 1 - DLT) * 16384);
#pragma unroll
      for (int i = 1; i < DLT; ++i) step();
    }
    wsel();
    sa = sz;
    {
      const unsigned sn = (sl + 16384) & 65535;
      asm volatile(G2_W G2_SYNC G2_A2 :: [l] "s"(sl), [la] "s"(sn + os), [n] "n"(16), [a] "s"(sa), [va] "v"(va), G2_WOP(wy) : "memory", "scc", "m0");
      sl = sn;
    }
    sw[0] = wz.wb[0]; sw[1] = wz.wb[1];
#pragma unroll
    for (int j = -3; j < 13; ++j) {
      if (j == GETP + 1 || j == GETP + 2)
        asm volatile("s_mov_b32 m0, %[l]\n\ts_nop 0\n\tglobal_load_lds_dwordx4 %[va], %[a] offset:%[o] sc1\n\tglobal_load_lds_dwordx4 %[va], %[a] offset:%[o]+1024 sc1"
                     :: [l] "s"(shm_base + G2_X + 16384 + p * 4096), [a] "s"((const char*)slab + (size_t)(bid ^ 5) * 16384 + p * 4096), [va] "v"(va),
                        [o] "n"((j - GETP - 1) * 2048) : "memory", "m0");
      sa += 16384;
      const unsigned sn = (sl + 16384) & 65535;
      constexpr int G = GETP;
      static_assert(GETP <= GETJ + 1, "");
      if (j < 12) asm volatile(G2_W G2_SYNC G2_A2 :: [l] "s"(sl), [la] "s"(sn + os), [n] "n"((j == -3 ? 14 : 12) + (j > G && j < G + 3 ? 2 : 0) + (j > G + 1 && j < G + 4 ? 2 : 0)),
                               [a] "s"(sa), [va] "v"(va), G2_WOP(wz) : "memory", "scc", "m0");
      else asm volatile(G2_W :: [l] "s"(sl), G2_WOP(wz) : "memory", "scc", "m0");
      sl = sn;
      sw[0] += ws0; sw[1] += ws1;
    }
    WAIT_V(12);
    BAR();
    WAIT_V(6);
    BAR();
    WAIT_V(0);
    BAR();
    asm volatile("" : "+v"(fl));
    TS(5);
    if (tid < 258) __hip_atomic_store(lane ? done2 + bid * 8 : &acks[bid], __builtin_amdgcn_s_memrealtime(), RLX_AGENT);
    if (__builtin_expect(!cok, 0) && p == 0) {
      const bool mine = lane == 32 || (lane < 32 && (((BOTH >> ntile) & 1) || ((lane >> 3) & 1) != (xcd >> 2)));
      const u64* const pa = &acks[lane < 32 ? client(lane) : bid ^ 5];
      while (!__all(!mine || fresh(__hip_atomic_load(pa, RLX_AGENT), t_own))) {
        notify(0, 32);
        if (lane == 0) __hip_atomic_store(done2 + bid * 8, __builtin_amdgcn_s_memrealtime(), RLX_AGENT);
        __builtin_amdgcn_s_sleep(32);
      }
    }
  } else {
    TS(6);
    f32x4 acc[4][4], sacc[2][4];
    const int fr = lane & 15, fq = lane >> 4, q4 = fr >> 2, pp = lane & 3;
    const int ff = ((q4 >> 1) & 1) | ((fq & 1) << 1);
    int aof = G2_A + wm * 8192 + lane * 16;
    int boff[4];
#pragma unroll
    for (int j = 0; j < 4; ++j) boff[j] = wn * 8192 + (8 * fq + q4) * 128 + ((j ^ ff) * 32) + pp * 8;
    int vo = wid * 4096 + lane * 16;
    int vx = ((m0 + wm * 64 + role * 32 + 2 * fr) * D_ + n0 + wn * 64) * 2 + (fq & 1) * 32 + (fq >> 1) * 16;
    lds_cptr px = shm3 + G2_X + wid * 4096 + (fq >> 1) * 512 + fr * 16 + (fq & 1) * 8;
    asm volatile("" : "+v"(vo), "+v"(vx), "+v"(px));
#define SB() __builtin_amdgcn_sched_barrier(0)
    struct Fr { bf16x8 a[4]; bf16x4 bl[4], bh[4]; };
    Fr fa[2], fb[2];
    auto rd = [&](Fr* f, int odd, int t) {
      constexpr int ord[12] = {0, 1, 8, 2, 3, 4, 5, 6, 7, 9, 10, 11};
      constexpr int seq[24] = {0, 1, 2, 12, 13, 14, 15, 16, 3, 4, 5, 6, 17, 18, 19, 20, 7, 8, 9, 21, 10, 22, 11, 23};
      lds_cptr sb = shm3 + odd * 16384;
      const int ks = seq[t] / 12, o = ord[seq[t] % 12];
      if (o >= 8) f[ks].a[o - 8] = *(const __attribute__((address_space(3))) bf16x8*)(sb + aof + (o - 8) * 2048 + ks * 1024);
      else if (o & 1) f[ks].bh[o >> 1] = tr_read(sb + boff[o >> 1] + ks * 4096 + 512);
      else f[ks].bl[o >> 1] = tr_read(sb + boff[o >> 1] + ks * 4096);
    };
    auto bfr = [&](const Fr& c, int j) { return __builtin_shufflevector(c.bl[j], c.bh[j], 0, 1, 2, 3, 4, 5, 6, 7); };
    auto mf1 = [](f32x4& c, bf16x8 b, bf16x8 a, bool z = false) {
      if (z) asm volatile("v_mfma_f32_16x16x32_bf16 %0, %1, %2, 0" : "=&v"(c) : "v"(b), "v"(a));
      else asm volatile("v_mfma_f32_16x16x32_bf16 %0, %1, %2, %0" : "+v"(c) : "v"(b), "v"(a));
    };
    auto mf2 = [](f32x4& c, bf16x8 b, bf16x8 a, bf16x8 b2, bf16x8 a2, bool z) {
      if (z) asm volatile("v_mfma_f32_16x16x32_bf16 %0, %1, %2, 0\n\tv_mfma_f32_16x16x32_bf16 %0, %3, %4, %0" : "=&v"(c) : "v"(b), "v"(a), "v"(b2), "v"(a2));
      else asm volatile("v_mfma_f32_16x16x32_bf16 %0, %1, %2, %0\n\tv_mfma_f32_16x16x32_bf16 %0, %3, %4, %0" : "+v"(c) : "v"(b), "v"(a), "v"(b2), "v"(a2));
    };
    auto pairs = [&](f32x4 (*ac)[4], const Fr* c, int nq, int mx, int nf, auto fill, bool z = false) {
#pragma unroll
      for (int q = 0; q < nq; ++q) {
        const int mf = (q >> 2) ^ mx, j = (q & 4) ? 3 - (q & 3) : q & 3, ks = q & 1;
        SB();
        if (2 * q >= nf) mf2(ac[mf][j], bfr(c[ks], j), c[ks].a[mf], bfr(c[ks ^ 1], j), c[ks ^ 1].a[mf], z);
        else {
          mf1(ac[mf][j], bfr(c[ks], j), c[ks].a[mf], z);
          SB();
          fill(2 * q);
          SB();
          mf1(ac[mf][j], bfr(c[ks ^ 1], j), c[ks ^ 1].a[mf]);
          SB();
          fill(2 * q + 1);
        }
      }
      SB();
    };
    auto flip = [&]() {
      aof ^= 32768; boff[0] ^= 32768; boff[1] ^= 32768; boff[2] ^= 32768; boff[3] ^= 32768;
      asm volatile("" : "+v"(aof), "+v"(boff[0]), "+v"(boff[1]), "+v"(boff[2]), "+v"(boff[3]));
    };
    auto bar = [&]() { __builtin_amdgcn_s_waitcnt(0xC07F); BAR(); };
    auto ystg = [&](const Fr* c, Fr* n, int odd, bool z = false) {
      pairs(acc, c, 16, 0, odd ? 30 : 24, [&](int t) {
        if (t < 24) rd(n, odd, t);
        else if (t == 24) asm volatile("s_nop 0");
        else { int& a = t == 29 ? aof : boff[t - 25]; a ^= 32768; asm volatile("" : "+v"(a)); }
      }, z);
    };
    auto stg = [&](const Fr* c, Fr* n, int odd, bool z = false) { pairs(sacc, c, 8, 0, 10, [&](int t) { rd(n, odd, 2 * t); rd(n, odd, 2 * t + 1); }, z); };
    asm volatile("" : "+v"(aof), "+v"(boff[0]), "+v"(boff[1]), "+v"(boff[2]), "+v"(boff[3]));
    await(0xffffu, 0x404u);
    auto lda = [&](int i) {
      asm volatile("s_mov_b32 m0, %[l]\n\ts_nop 0\n\tglobal_load_lds_dwordx4 %[va], %[a] sc1\n\tglobal_load_lds_dwordx4 %[va], %[a] offset:1024 sc1\n\t"
                   "global_load_lds_dwordx4 %[va], %[a] offset:2048 sc1\n\tglobal_load_lds_dwordx4 %[va], %[a] offset:3072 sc1"
                   :: [l] "s"(shm_base + G2_A + (i + 1) * 16384 + (p ^ role) * 4096), [a] "s"(sa + i * 16384), [va] "v"(va) : "memory", "m0");
    };
    need(0x00330033ull << 32, false);
    TS(4);
    lda(0);
    lda(1);
    need(0x00ff00ffull << 32, false);
    TS(0);
    lda(2);
    WAIT_V(8);
    signal(1u << 24);
    await(0xff000000u, 4u << 24);
    __builtin_amdgcn_s_waitcnt(0xC07F);
    SB();
#pragma unroll
    for (int t = 0; t < 24; ++t) rd(fa, 1, t);
    flip();
    __builtin_amdgcn_s_setprio(0);
    WAIT_V(4);
    TS(5);
    peek();
    bar();
    TS(7);
    ystg(fa, fb, 0, true);
    WAIT_V(1);
    bar();
    ystg(fb, fa, 1);
    WAIT_V(0);
    check(0x33003300ull << 32, false);
    constexpr int PAR = (NY - DLT + 1) & 1;
#pragma unroll 1
    for (int g = 2; g < ny - 2; g += 2) {
      bar();
      ystg(fa, fb, 0);
      bar();
      ystg(fb, fa, 1);
    }
    if (PAR) {
      bar();
      ystg(fa, fb, 0);
    }
    Fr* const f0 = PAR ? fb : fa;
    Fr* const f1 = PAR ? fa : fb;
    bar();
    {
      const __amdgpu_buffer_rsrc_t rs = __builtin_amdgcn_make_buffer_rsrc((void*)((char*)slab + (size_t)bid * 16384), 0, 16384, 0x00020000);
      u32x4 pk;
      pairs(acc, f0, 16, 2, 32, [&](int t) {
        if (t < 20) rd(f1, PAR, t);
        else {
          const int f = (t - 20) / 3, e = (t - 20) % 3;
          if (e < 2) { const f32x4 a = acc[2 + (f >> 1)][(f & 1) * 2 + e]; pk[2 * e] = pack2(a[0], a[1]); pk[2 * e + 1] = pack2(a[2], a[3]); }
          else __builtin_amdgcn_raw_buffer_store_b128(pk, rs, vo + f * 1024, 0, 16);
        }
      });
    }
    TS(2);
    static_assert(GETJ % 2 == 1 && GETJ >= 3 && GETJ <= 7, "");
    f32x4 xf[2][4];
#pragma unroll
    for (int j = 0; j < GETJ + 5; j += 2) {
      if (PAR) flip();
      bar();
      stg(f1, f0, PAR ^ 1, j == 0);
      if (!PAR) flip();
      bar();
      u32x2 xv[2];
      pairs(sacc, f0, 8, 0, 12, [&](int t) {
        if (t < 10) { rd(f1, PAR, 2 * t); rd(f1, PAR, 2 * t + 1); }
        else if (j < 8) xv[t - 10] = *(const __attribute__((address_space(3))) u32x2*)(px + (j >> 1) * 1024 + (t - 10) * 256);
      });
      if (j < 8) {
#pragma unroll
        for (int e = 0; e < 2; ++e) xf[j >> 2][(j & 2) + e] = f32x4{lo_f(xv[e][0]), hi_f(xv[e][0]), lo_f(xv[e][1]), hi_f(xv[e][1])};
        asm volatile("" : "+v"(xf[j >> 2][j & 2]), "+v"(xf[j >> 2][(j & 2) + 1]));
        SB();
      }
    }
    if (PAR) flip();
    WAIT_V(0);
    bar();
    u32x4 pv[4];
    {
      lds_cptr pp_ = shm3 + G2_X + 16384 + vo;
      pairs(sacc, f1, 8, 0, 12, [&](int t) {
        if (t < 2) {
#pragma unroll
          for (int f = 2 * t; f < 2 * t + 2; ++f) pv[f] = *(const __attribute__((address_space(3))) u32x4*)(pp_ + f * 1024);
        } else { rd(f0, PAR ^ 1, 2 * t - 4); rd(f0, PAR ^ 1, 2 * t - 3); }
      });
      if (__builtin_expect(blank(pv) || !cok, 0)) {
        const __amdgpu_buffer_rsrc_t rp = __builtin_amdgcn_make_buffer_rsrc((void*)((char*)slab + (size_t)(bid ^ 5) * 16384), 0, 16384, 0x00020000);
        while (!cok && !__all(fresh(__hip_atomic_load(done2 + (bid ^ 5) * 8, RLX_AGENT), t_own))) {
          notify(0, 32);
          if (lane == 0) __hip_atomic_store(done2 + bid * 8, __builtin_amdgcn_s_memrealtime(), RLX_AGENT);
          __builtin_amdgcn_s_sleep(8);
        }
        do {
          asm volatile("" ::: "memory");
#pragma unroll
          for (int f = 0; f < 4; ++f) pv[f] = __builtin_bit_cast(u32x4, __builtin_amdgcn_raw_buffer_load_b128(rp, vo + f * 1024, 0, 16));
        } while (blank(pv));
      }
    }
    auto silu = [&](int g0, int g1) {
#pragma unroll
      for (int f = g0; f < g1; ++f) {
        const int mf = f >> 2, j = f & 3;
        const u32x4 pw = pv[mf * 2 + (j >> 1)];
        const f32x4 ya = acc[mf][j] + f32x4{lo_f(pw[2 * (j & 1)]), hi_f(pw[2 * (j & 1)]), lo_f(pw[2 * (j & 1) + 1]), hi_f(pw[2 * (j & 1) + 1])};
#pragma unroll
        for (int e = 0; e < 2; ++e) {
          const f32x2 y2 = {ya[2 * e], ya[2 * e + 1]};
          const f32x2 ar = y2 * f32x2{-1.4426950408889634f, -1.4426950408889634f};
          const f32x2 dn = f32x2{__builtin_amdgcn_exp2f(ar[0]), __builtin_amdgcn_exp2f(ar[1])} + f32x2{1.0f, 1.0f};
          const f32x2 t = y2 * f32x2{__builtin_amdgcn_rcpf(dn[0]), __builtin_amdgcn_rcpf(dn[1])};
          acc[mf][j][2 * e] = t[0]; acc[mf][j][2 * e + 1] = t[1];
        }
        asm volatile("" : "+v"(acc[mf][j]));
      }
      SB();
    };
    constexpr int spt[] = {0, 0, 1, 2, 4, 6, 8, 8};
    silu(0, spt[1]);
#pragma unroll
    for (int j = GETJ + 6; j < 15; ++j) {
      if ((j ^ PAR) & 1) flip();
      bar();
      if (j & 1) stg(f0, f1, PAR); else stg(f1, f0, PAR ^ 1);
      silu(spt[j - GETJ - 5], j == 14 ? 8 : spt[j - GETJ - 4]);
    }
    pairs(sacc, f0, 8, 0, 0, [](int) {});
    asm volatile("s_nop 7\n\ts_nop 3" : "+v"(sacc[1][0]), "+v"(sacc[1][1]), "+v"(sacc[1][2]), "+v"(sacc[1][3]));
    SB();
    TS(1);
    {
      const __amdgpu_buffer_rsrc_t ro = __builtin_amdgcn_make_buffer_rsrc((void*)out, 0, S_ * D_ * 2, 0x00020000);
#pragma unroll
      for (int f = 0; f < 4; ++f) {
        const int mf = f >> 1, j = (f & 1) * 2;
        const f32x4 a = __builtin_elementwise_fma(sacc[mf][j], acc[mf][j], xf[mf][j]), b = __builtin_elementwise_fma(sacc[mf][j + 1], acc[mf][j + 1], xf[mf][j + 1]);
        const auto r0 = __builtin_amdgcn_permlane16_swap(pack2(a[0], a[1]), pack2(b[0], b[1]), false, false);
        const auto r1 = __builtin_amdgcn_permlane16_swap(pack2(a[2], a[3]), pack2(b[2], b[3]), false, false);
        __builtin_amdgcn_raw_buffer_store_b128(u32x4{r0[0], r1[0], r0[1], r1[1]}, ro, vx + mf * 2048 + j * 32, 0, 2);
      }
    }
    TS(3);
  }
  TSFLUSH();
  }
  asm volatile(".Lcode_end:");
}

extern "C" void kernel_launch(void* const* d_in, const int* in_sizes, int n_in, void* d_out, int out_size, void* d_ws,
                              size_t ws_size, hipStream_t stream) {
  const u16* A_x = (const u16*)d_in[0];
  const u16* A_wqkv = (const u16*)d_in[1];
  const u16* A_wo = (const u16*)d_in[2];
  const u16* B_wg = (const u16*)d_in[4];
  const u16* B_wu = (const u16*)d_in[5];
  const u16* B_wd = (const u16*)d_in[6];
  u16* out = (u16*)d_out;
  fused_kernel<<<256, 512, 0, stream>>>(A_x, A_wqkv, B_wg, B_wu, B_wd, A_wo, out, (char*)d_ws);
}
```
